```python
import torch

def _softmax(x, axis=-1):
    return torch.softmax(x, dim=axis)
def _gelu(x):
    return torch.nn.functional.gelu(x, approximate="tanh")
def _silu(x):
    return torch.nn.functional.silu(x)
def _sigmoid(x):
    return torch.sigmoid(x)
def _layernorm(x, axis=-1, eps=1e-5):
    m = x.mean(dim=axis, keepdim=True)
    v = x.var(dim=axis, keepdim=True, unbiased=False)
    return (x - m) / torch.sqrt(v + eps)
def _scatter_add(tgt, idx, src, axis=0):
    return tgt.clone().index_add_(axis, idx, src)
def _segment_sum(src, seg, n):
    out = torch.zeros((n,) + src.shape[1:], dtype=src.dtype)
    return out.index_add_(0, seg, src)
def _pad(x, pw):
    flat = tuple(v for pair in reversed(pw) for v in pair)
    return torch.nn.functional.pad(x, flat)

kernel_name = 'sequential_preLN__sliding_local_dilated__moe_arch_mod__prompt'

def setup_inputs():

    _w = (lambda a: (lambda t: t.to(torch.bfloat16) if t.is_floating_point() else t)(torch.as_tensor(a)))
    def _A_setup():
        B, S, D = 1, 2048, 640
        return {
            "x": _w((lambda *s: torch.randn(*s, dtype=torch.bfloat16) * 0.05)(B, S, D)),
            "wqkv": _w((lambda *s: torch.randn(*s, dtype=torch.bfloat16) * 0.05)(D, 3 * D)),
            "wo": _w((lambda *s: torch.randn(*s, dtype=torch.bfloat16) * 0.05)(D, D)),
        }
    _A_in = _A_setup()

    def _B_setup():
        B, S, D, F, C = 1, 2048, 640, 1536, 1
        return {
            "x": _w((lambda *s: torch.randn(*s, dtype=torch.bfloat16) * 0.05)(B, S, D)),
            "w_r": _w((lambda *s: torch.randn(*s, dtype=torch.bfloat16) * 0.05)(D)),
            "w_gate": _w((lambda *s: torch.randn(*s, dtype=torch.bfloat16) * 0.05)(D, F)),
            "w_up": _w((lambda *s: torch.randn(*s, dtype=torch.bfloat16) * 0.05)(D, F)),
            "w_down": _w((lambda *s: torch.randn(*s, dtype=torch.bfloat16) * 0.05)(F, D)),
        }
    _B_in = _B_setup()

    return {
        "A_x": _A_in["x"],
        "A_wqkv": _A_in["wqkv"],
        "A_wo": _A_in["wo"],
        "B_x": _B_in["x"],
        "B_w_r": _B_in["w_r"],
        "B_w_gate": _B_in["w_gate"],
        "B_w_up": _B_in["w_up"],
        "B_w_down": _B_in["w_down"],
    }

def reference(A_x, A_wqkv, A_wo, B_x, B_w_r, B_w_gate, B_w_up, B_w_down):
    def _A_ref(x, wqkv, wo):
        B, S, D = x.shape
        H = 32; Dh = D // H
        W, dil = 8, 2
        qkv = (lambda x, *d: x.permute(*d))((x @ wqkv).reshape(B, S, 3, H, Dh), 2, 0, 3, 1, 4)
        q, k, v = qkv[0], qkv[1], qkv[2]
        scale = Dh ** -0.5
        pos = torch.arange(S)
        off = torch.arange(W) * dil
        idx = pos[:, None] - off[None, :]
        mask = idx >= 0
        idx_c = torch.where(mask, idx, 0).reshape(-1)
        kg = k[:, :, idx_c, :].reshape(B, H, S, W, Dh)
        vg = v[:, :, idx_c, :].reshape(B, H, S, W, Dh)
        logits = torch.einsum("bhsd,bhswd->bhsw", q, kg) * scale
        logits = torch.where(mask[None, None, :, :], logits, -1e9)
        attn = _softmax(logits, axis=-1)
        o = torch.einsum("bhsw,bhswd->bhsd", attn, vg)
        return (lambda x, *d: x.permute(*d))(o, 0, 2, 1, 3).reshape(B, S, D) @ wo

    def _B_ref(x, w_r, w_gate, w_up, w_down):
        B, S, D = x.shape
        C = 1
        r = x @ w_r
        idx = torch.argsort(-r, axis=-1)[:, :C]
        idx_e = (lambda x, n, axis: torch.repeat_interleave(x, n, dim=axis))(idx.reshape(B, C, 1), D, axis=2)
        x_sel = (lambda x, i, axis: torch.take_along_dim(x, i, dim=axis))(x, idx_e, axis=1)
        g = _silu(x_sel @ w_gate)
        u = x_sel @ w_up
        y = (g * u) @ w_down
        r_sel = _sigmoid((lambda x, i, axis: torch.take_along_dim(x, i, dim=axis))(r, idx, axis=1))
        y = y * r_sel.reshape(B, C, 1)
        b_off = (torch.arange(B) * S).reshape(B, 1)
        flat_idx = (idx + b_off).reshape(B * C)
        zero = (x * 0.0).reshape(B * S, D)
        upd = _scatter_add(zero, flat_idx, y.reshape(B * C, D), axis=0)
        return x + upd.reshape(B, S, D)

    x = A_x
    h = x + _A_ref(_layernorm(x, axis=-1), A_wqkv, A_wo)
    h2 = h + _B_ref(_layernorm(h, axis=-1), B_w_r, B_w_gate, B_w_up, B_w_down)
    return h2
```

```cpp
#include <hip/hip_runtime.h>
#include <stdint.h>


typedef unsigned short u16;
typedef unsigned int u32;
#define LDS_TOTAL 163840
#define CL8(T) "v" #T "0", "v" #T "1", "v" #T "2", "v" #T "3", "v" #T "4", "v" #T "5", "v" #T "6", "v" #T "7"
#define CL10(T) CL8(T), "v" #T "8", "v" #T "9"
#define SL10(T) "s" #T "0", "s" #T "1", "s" #T "2", "s" #T "3", "s" #T "4", "s" #T "5", "s" #T "6", "s" #T "7", "s" #T "8", "s" #T "9"

#define BODY \
  ".Lst_%=:\n\t" "s_load_dwordx16 s[8:23], %[ka], 0x0\n\t" "s_load_dwordx2 s[24:25], %[ka], 0x40\n\t" "v_readfirstlane_b32 s26, v0\n\t" "s_lshr_b32 s76, %[bid], 3\n\t" "v_and_b32 v8, 63, v0\n\t" \
  "s_mul_i32 s77, s76, 205\n\t" "s_lshr_b32 s26, s26, 6\n\t" "s_lshr_b32 s77, s77, 10\n\t" "v_lshrrev_b32 v1, 3, v8\n\t" "s_mul_i32 s27, s77, 5\n\t" "s_and_b32 s33, s26, 3\n\t" \
  "s_sub_u32 s27, s76, s27\n\t" "s_and_b32 s78, %[bid], 1\n\t" "s_bfe_u32 s79, %[bid], 0x20001\n\t" "s_mul_i32 s28, s78, 5\n\t" "s_mul_i32 s29, s79, 6\n\t" "s_add_u32 s28, s28, s27\n\t" \
  "s_add_u32 s29, s29, s77\n\t" "s_cmp_ge_u32 s76, 30\n\t" "s_cselect_b32 s29, 24, s29\n\t" "s_cmp_ge_u32 s29, 23\n\t" "s_cbranch_scc1 .Lsp_%=\n\t" "s_lshl_b32 s77, s77, 2\n\t" \
  "s_lshl1_add_u32 s31, s27, s78\n\t" "s_lshr_b32 s77, 0x875320, s77\n\t" "s_add_u32 s31, s31, s79\n\t" "s_and_b32 s77, s77, 15\n\t" "s_mul_i32 s30, s29, 89\n\t" "s_add_u32 s31, s31, s77\n\t" \
  "s_mul_i32 s77, s31, 205\n\t" "s_lshr_b32 s77, s77, 11\n\t" "s_mul_i32 s77, s77, 10\n\t" "s_sub_u32 s31, s31, s77\n\t" "s_cmp_ge_u32 s26, 4\n\t" "s_cbranch_scc1 .Lnl_%=\n\t" "s_setprio 2\n\t" \
  "v_lshl_or_b32 v2, s33, 4, v1\n\t" "v_and_b32 v3, 2, v1\n\t" "v_mul_u32_u24 v2, 0xf00, v2\n\t" "v_xor_b32 v3, v3, v8\n\t" "s_lshl_b32 s76, s28, 7\n\t" "v_and_b32 v3, 7, v3\n\t" \
  "s_add_u32 s76, s76, 3072\n\t" "v_lshlrev_b32 v3, 4, v3\n\t" "s_mul_i32 s86, s33, 6144\n\t" "v_add3_u32 v40, v2, v3, s76\n\t" "s_add_u32 s86, s86, 3072\n\t" "v_add_u32 v41, 256, v40\n\t" \
  "v_add_u32 v42, 512, v40\n\t" "v_xor_b32 v43, 64, v40\n\t" "s_mov_b32 m0, s86\n\t" "v_add_u32 v43, 27648, v43\n\t" "s_waitcnt lgkmcnt(0)\n\t" "s_mul_i32 s76, s31, 245760\n\t" \
  "s_lshl_b32 s77, s31, 7\n\t" "s_add_u32 s78, s10, s76\n\t" "s_addc_u32 s79, s11, 0\n\t" "s_add_u32 s80, s8, s77\n\t" "s_addc_u32 s81, s9, 0\n\t" "v_add_u32 v44, 256, v43\n\t" \
  "v_add_u32 v45, 512, v43\n\t" "global_load_lds_dwordx4 v40, s[78:79] offset:-3072\n\t" "global_load_lds_dwordx4 v41, s[78:79] offset:-2048\n\t" \
  "global_load_lds_dwordx4 v42, s[78:79] offset:-1024\n\t" "global_load_lds_dwordx4 v43, s[78:79] offset:0\n\t" "global_load_lds_dwordx4 v44, s[78:79] offset:1024\n\t" \
  "global_load_lds_dwordx4 v45, s[78:79] offset:2048\n\t" "s_mul_i32 s76, s33, 24\n\t" "s_mul_i32 s87, s33, 3072\n\t" "v_add_u32 v2, s76, v1\n\t" "s_movk_i32 s77, 2048\n\t" \
  "v_lshrrev_b32 v3, 1, v2\n\t" "v_add_u32 v2, s30, v2\n\t" "v_xor_b32 v3, v3, v8\n\t" "s_add_u32 s87, s87, 26624\n\t" "v_and_b32 v3, 7, v3\n\t" "s_mov_b32 m0, s87\n\t" \
  "v_lshl_add_u32 v3, v3, 4, s77\n\t" "s_movk_i32 s77, 1280\n\t" "v_add_u32 v4, 0, v2\n\t" "v_sub_u32 v4, v4, 6 clamp\n\t" "v_lshrrev_b32 v5, 10, v4\n\t" "v_lshl_or_b32 v4, v4, 1, v5\n\t" \
  "v_and_b32 v4, 0x7ff, v4\n\t" "v_mad_u32_u24 v46, v4, s77, v3\n\t" "global_load_lds_dwordx4 v46, s[80:81] offset:-2048\n\t" "v_xor_b32 v3, 64, v3\n\t" "v_subrev_u32 v3, 1024, v3\n\t" \
  "v_add_u32 v4, 8, v2\n\t" "v_sub_u32 v4, v4, 6 clamp\n\t" "v_lshrrev_b32 v5, 10, v4\n\t" "v_lshl_or_b32 v4, v4, 1, v5\n\t" "v_and_b32 v4, 0x7ff, v4\n\t" "v_mad_u32_u24 v47, v4, s77, v3\n\t" \
  "global_load_lds_dwordx4 v47, s[80:81] offset:-1024\n\t" "v_xor_b32 v3, 64, v3\n\t" "v_subrev_u32 v3, 1024, v3\n\t" "v_add_u32 v4, 16, v2\n\t" "v_sub_u32 v4, v4, 6 clamp\n\t" \
  "v_lshrrev_b32 v5, 10, v4\n\t" "v_lshl_or_b32 v4, v4, 1, v5\n\t" "v_and_b32 v4, 0x7ff, v4\n\t" "v_mad_u32_u24 v48, v4, s77, v3\n\t" "global_load_lds_dwordx4 v48, s[80:81] offset:0\n\t" \
  ".Lnl_%=:\n\t" "s_cmp_lg_u32 s26, 11\n\t" "s_cbranch_scc1 .Lnc_%=\n\t" "s_getpc_b64 s[76:77]\n\t" ".Lpc_%=:\n\t" "s_and_b32 s78, %[bid], -8\n\t" "s_sub_u32 s76, s76, .Lpc_%=-.Lst_%=\n\t" \
  "s_subb_u32 s77, s77, 0\n\t" "v_add_u32 v1, s78, v8\n\t" "v_min_u32 v1, 136, v1\n\t" "s_mov_b32 m0, 163072\n\t" "v_lshlrev_b32 v1, 7, v1\n\t" "s_mov_b64 exec, 0xff\n\t" \
  "global_load_lds_dword v1, s[76:77]\n\t" "s_mov_b64 exec, -1\n\t" ".Lnc_%=:\n\t" "s_cmp_ge_u32 s26, 6\n\t" "s_cselect_b32 s34, 1, 0\n\t" "s_cselect_b32 s76, 6, 0\n\t" "v_and_b32 v9, 15, v8\n\t" \
  "s_sub_u32 s35, s26, s76\n\t" "v_lshrrev_b32 v10, 2, v8\n\t" "s_and_b32 s36, s26, 1\n\t" "s_lshr_b32 s37, s26, 1\n\t" "s_lshl4_add_u32 s76, s35, s30\n\t" "s_sub_u32 s76, s76, 6\n\t" \
  "v_lshlrev_b32 v32, 4, v8\n\t" "v_add_u32 v2, s76, v9\n\t" "s_mov_b32 s76, 0x1fe00\n\t" "v_and_b32 v2, 0x3ff, v2\n\t" "v_lshlrev_b32 v1, v9, s76\n\t" "v_min_u32 v2, 7, v2\n\t" \
  "v_lshlrev_b32 v35, 7, v10\n\t" "v_sub_u32 v2, v9, v2\n\t" "v_and_b32 v10, 12, v10\n\t" "v_add_u32 v2, 16, v2\n\t" "s_cmp_eq_u32 s35, 0\n\t" "s_cselect_b32 s76, 0xffff0000, -1\n\t" \
  "s_cselect_b32 s81, 0, 1024\n\t" "v_lshlrev_b32 v2, v2, -1\n\t" "v_and_b32 v1, s76, v1\n\t" "s_mov_b32 s77, 0xff80ff80\n\t" "s_cmp_eq_u32 s29, 0\n\t" "s_cselect_b32 s77, 0xffc0ffc0, s77\n\t" \
  "v_and_b32 v1, v1, v2\n\t" "s_cmp_eq_u32 s37, 0\n\t" "s_cselect_b32 s72, s77, -1\n\t" "v_not_b32 v1, v1\n\t" "s_mov_b32 s73, s72\n\t" "v_lshrrev_b32 v1, v10, v1\n\t" \
  "s_lshl4_add_u32 s76, s37, s30\n\t" "s_mov_b32 s77, 0xffff\n\t" "v_bfe_i32 v20, v1, 0, 1\n\t" "v_bfe_i32 v21, v1, 1, 1\n\t" "v_bfe_i32 v22, v1, 2, 1\n\t" "v_bfe_i32 v23, v1, 3, 1\n\t" \
  "v_add_u32 v2, s76, v9\n\t" "v_bfi_b32 v28, s77, v20, v21\n\t" "v_bfi_b32 v29, s77, v22, v23\n\t" "v_bfe_i32 v24, v1, 16, 1\n\t" "v_bfe_i32 v25, v1, 17, 1\n\t" "v_bfe_i32 v26, v1, 18, 1\n\t" \
  "v_bfe_i32 v27, v1, 19, 1\n\t" "v_sub_u32 v2, v2, 6 clamp\n\t" "v_lshrrev_b32 v3, 10, v2\n\t" "v_lshl_or_b32 v2, v2, 1, v3\n\t" "v_and_b32 v2, 0x7ff, v2\n\t" "s_lshl_b32 s76, s28, 7\n\t" \
  "s_lshl_b32 s77, s36, 6\n\t" "v_and_b32 v20, 0xff800000, v20\n\t" "v_and_b32 v21, 0xff800000, v21\n\t" "v_and_b32 v22, 0xff800000, v22\n\t" "v_and_b32 v23, 0xff800000, v23\n\t" \
  "v_and_b32 v24, 0xff800000, v24\n\t" "v_and_b32 v25, 0xff800000, v25\n\t" "v_and_b32 v26, 0xff800000, v26\n\t" "v_and_b32 v27, 0xff800000, v27\n\t" "s_add_u32 s76, s76, s77\n\t" \
  "v_lshlrev_b32 v1, 1, v8\n\t" "v_lshrrev_b32 v3, 1, v8\n\t" "v_and_b32 v1, 32, v1\n\t" "v_and_b32 v3, 16, v3\n\t" "s_movk_i32 s78, 1280\n\t" "v_or3_b32 v34, v1, v3, s76\n\t" \
  "v_lshlrev_b32 v5, 2, v8\n\t" "v_mad_u32_u24 v33, v2, s78, v34\n\t" "v_and_b32 v6, 0x60, v5\n\t" "s_mul_i32 s76, s36, 40960\n\t" "v_xor_b32 v1, s77, v6\n\t" "s_add_u32 s76, s76, 73728\n\t" \
  "v_and_b32 v5, 12, v5\n\t" "v_add3_u32 v35, v35, v1, s76\n\t" "v_mov_b32 v37, 156416\n\t" "v_lshl_add_u32 v35, v5, 1, v35\n\t" "v_mov_b32 v38, 1\n\t" "s_mul_i32 s78, s27, 12\n\t" \
  "s_mov_b32 s76, 0x10321320\n\t" "s_mov_b32 s77, 0xbe0f102\n\t" "s_lshr_b64 s[76:77], s[76:77], s78\n\t" "s_and_b32 s74, s76, 0xfff\n\t" "s_mov_b32 s76, 0xedfe0f41\n\t" "s_mov_b32 s77, 0xfdcedcf\n\t" \
  "s_lshr_b64 s[76:77], s[76:77], s78\n\t" "s_pack_ll_b32_b16 s74, s74, s76\n\t" "s_mul_i32 s76, s27, 0x10001\n\t" "s_sub_u32 s76, 0xe0009, s76\n\t" "s_lshl4_add_u32 s74, s74, s76\n\t" \
  "s_mul_i32 s78, s34, 3072\n\t" "s_lshr_b32 s79, s74, 16\n\t" "v_bfe_u32 v1, s74, v10, 4\n\t" "s_lshl_b32 s76, s35, 9\n\t" "v_bfe_u32 v2, s79, v10, 4\n\t" "v_and_b32 v4, 12, v8\n\t" \
  "s_sub_u32 s76, s76, s78\n\t" "v_bfe_u32 v3, s74, v4, 4\n\t" "s_add_u32 s76, s76, 3072\n\t" "v_bfe_u32 v4, s79, v4, 4\n\t" "v_lshl_add_u32 v30, v8, 3, s76\n\t" "s_sub_u32 s76, 5, s27\n\t" \
  "v_lshl_add_u32 v31, s78, 1, v30\n\t" "s_sub_u32 s77, 15, s27\n\t" "v_cmp_gt_u32 s[40:41], s76, v1\n\t" "v_cmp_gt_u32 s[42:43], s76, v2\n\t" "v_cmp_le_u32 s[44:45], s77, v1\n\t" \
  "v_cmp_le_u32 s[46:47], s77, v2\n\t" "v_cmp_gt_u32 s[50:51], s76, v3\n\t" "v_cmp_gt_u32 s[52:53], s76, v4\n\t" "s_lshl_b32 s76, s27, 2\n\t" "v_cmp_gt_u32 s[48:49], s76, v10\n\t" \
  "s_mul_i32 s76, s29, 10\n\t" "s_add_u32 s76, s76, s28\n\t" "s_mul_i32 s76, s76, 6144\n\t" "s_add_u32 s76, s76, 585728\n\t" "s_waitcnt lgkmcnt(0)\n\t" "s_add_u32 s66, s24, s76\n\t" \
  "s_addc_u32 s67, s25, 0\n\t" "s_lshl_b32 s76, s35, 10\n\t" "s_add_u32 s76, s76, 4096\n\t" "s_add_u32 s56, s66, s76\n\t" "s_addc_u32 s57, s67, 0\n\t" "s_sub_u32 s58, s56, s81\n\t" \
  "s_subb_u32 s59, s57, 0\n\t" "s_mul_i32 s76, s29, 122880\n\t" "s_add_u32 s76, s76, 2004992\n\t" "s_mul_i32 s77, s26, 10240\n\t" "s_add_u32 s77, s77, s76\n\t" "s_add_u32 s62, s24, s77\n\t" \
  "s_addc_u32 s63, s25, 0\n\t" "s_add_u32 s64, s62, 5120\n\t" "s_addc_u32 s65, s63, 0\n\t" "s_mul_i32 s77, s35, 10\n\t" "s_add_u32 s77, s77, s28\n\t" "s_lshl_b32 s77, s77, 1\n\t" \
  "s_sub_u32 s77, s77, s27\n\t" "s_lshl_b32 s77, s77, 10\n\t" "s_add_u32 s77, s77, s76\n\t" "s_add_u32 s77, s77, 512\n\t" "s_add_u32 s60, s24, s77\n\t" "s_addc_u32 s61, s25, 0\n\t" \
  "s_mov_b64 s[76:77], 0xffff\n\t" "s_not_b64 s[54:55], s[76:77]\n\t" "s_cmp_lg_u32 s34, 0\n\t" "s_cselect_b64 s[54:55], s[76:77], s[54:55]\n\t" "s_mov_b32 s39, 400000\n\t" "s_mov_b32 s75, 0\n\t" \
  "s_cmp_lt_u32 s26, 4\n\t" "s_cbranch_scc1 .Ll_%=\n\t" "s_mul_i32 s77, s27, 0x1111\n\t" "s_sub_u32 s80, 0x8765, s77\n\t" "s_sub_u32 s77, 0xdcba, s77\n\t" "s_cmp_eq_u32 s33, 1\n\t" \
  "s_cselect_b32 s80, s74, s80\n\t" "s_cmp_eq_u32 s33, 2\n\t" "s_cselect_b32 s80, s79, s80\n\t" "s_cmp_eq_u32 s33, 3\n\t" "s_cselect_b32 s80, s77, s80\n\t" "v_mul_u32_u24 v1, 768, v10\n\t" \
  "v_bfe_u32 v5, s80, v5, 4\n\t" "v_lshlrev_b32 v2, 5, v9\n\t" "v_lshlrev_b32 v5, 3, v5\n\t" "v_and_b32 v2, 0x180, v2\n\t" "v_xor_b32 v5, v5, v6\n\t" "s_cmp_ge_u32 s26, 8\n\t" \
  "v_add3_u32 v1, v1, v5, v2\n\t" "s_cbranch_scc1 .Lg_%=\n\t" "v_mov_b32 v7, 0\n\t" "s_setprio 3\n\t" "global_load_dwordx2 v[40:41], v7, s[24:25] sc1\n\t" "s_cmp_lg_u32 s33, 2\n\t" \
  "s_cbranch_scc1 .Lre0_%=\n\t" "v_lshlrev_b32 v5, 2, v8\n\t" "global_load_dwordx4 v[64:67], v32, s[14:15]\n\t" "global_load_dword v68, v5, s[14:15] offset:1024\n\t" ".Lre0_%=:\n\t" \
  "v_lshl_add_u32 v3, v8, 2, v37\n\t" "s_mov_b32 exec_hi, 0\n\t" "ds_write_b32 v3, v7\n\t" "s_mov_b32 exec_hi, -1\n\t" "s_mul_i32 s76, s29, 15360\n\t" "s_mul_i32 s77, s33, 384\n\t" \
  "s_add_u32 s76, s76, s77\n\t" "s_add_u32 s76, s76, 8192\n\t" "s_add_u32 s88, s24, s76\n\t" "s_addc_u32 s89, s25, 0\n\t" "s_add_u32 s86, s88, 3072\n\t" "s_addc_u32 s87, s89, 0\n\t" \
  "s_mul_i32 s76, s28, 1536\n\t" "s_add_u32 s88, s88, s76\n\t" "s_addc_u32 s89, s89, 0\n\t" "s_lshl2_add_u32 s76, s28, s33\n\t" "s_mul_i32 s76, s76, 5120\n\t" "s_add_u32 s76, s76, 361472\n\t" \
  "s_add_u32 s90, s24, s76\n\t" "s_addc_u32 s91, s25, 0\n\t" "s_lshl_b32 s76, s31, 9\n\t" "s_add_u32 s92, s90, s76\n\t" "s_addc_u32 s93, s91, 0\n\t" "v_mov_b32 v116, -1\n\t" "v_mov_b32 v117, -1\n\t" \
  "v_mov_b32 v118, -1\n\t" "v_mov_b32 v119, -1\n\t" "s_mul_i32 s76, s33, 1536\n\t" "s_add_u32 s76, s76, 4096\n\t" "s_add_u32 s94, s66, s76\n\t" "s_addc_u32 s95, s67, 0\n\t" \
  "s_mul_i32 s76, s33, 3072\n\t" "v_add_u32 v2, s76, v32\n\t" "s_mov_b32 s77, 0x3f803f80\n\t" "v_mov_b32 v96, s77\n\t" "v_mov_b32 v97, s77\n\t" "v_mov_b32 v98, s77\n\t" "v_mov_b32 v99, s77\n\t" \
  "v_and_b32 v3, 7, v8\n\t" "v_lshrrev_b32 v12, 3, v8\n\t" "v_lshl_add_u32 v12, v3, 3, v12\n\t" "v_lshlrev_b32 v12, 4, v12\n\t" "v_cmp_eq_u32 s[70:71], 1, v3\n\t" "v_cmp_eq_u32 s[96:97], 2, v3\n\t" \
  "v_cmp_gt_u32 s[98:99], 3, v3\n\t" "v_and_b32 v5, 31, v8\n\t" "v_lshrrev_b32 v13, 5, v8\n\t" "v_min_u32 v7, 23, v5\n\t" "v_mul_u32_u24 v3, 7680, v13\n\t" "v_lshl_add_u32 v3, v7, 4, v3\n\t" \
  "v_mul_u32_u24 v6, 2560, v13\n\t" "v_lshl_add_u32 v6, v5, 4, v6\n\t" "v_mul_u32_u24 v13, 384, v13\n\t" "v_lshl_add_u32 v13, v7, 2, v13\n\t" "s_mul_i32 s76, s33, 96\n\t" \
  "s_add_u32 s76, s76, 155648\n\t" "v_add_u32 v13, s76, v13\n\t" "v_min_u32 v4, 22, v8\n\t" "s_movk_i32 s76, 4096\n\t" "v_lshl_add_u32 v4, v4, 7, s76\n\t" "v_and_b32 v5, 16, v8\n\t" \
  "v_lshl_add_u32 v5, v5, 1, v9\n\t" "s_lshl_b32 s76, s33, 8\n\t" "s_add_u32 s76, s76, 156672\n\t" "v_lshl_add_u32 v5, v5, 2, s76\n\t" "s_mov_b32 s76, 0x3acccccd\n\t" "s_bitcmp0_b32 0x3f6c0, s29\n\t" \
  "s_waitcnt lgkmcnt(0)\n\t" "s_barrier\n\t" "ds_read_b128 v[44:47], v2 offset:24576\n\t" "ds_read_b128 v[48:51], v2 offset:25600\n\t" "ds_read_b128 v[52:55], v2 offset:26624\n\t" \
  "s_cbranch_scc1 .Lc0_%=\n\t" "ds_read_b64_tr_b16 v[72:73], v1 offset:0\n\t" "ds_read_b64_tr_b16 v[74:75], v1 offset:512\n\t" "ds_read_b64_tr_b16 v[76:77], v1 offset:1024\n\t" \
  "ds_read_b64_tr_b16 v[78:79], v1 offset:1536\n\t" "ds_read_b64_tr_b16 v[80:81], v1 offset:2048\n\t" "ds_read_b64_tr_b16 v[82:83], v1 offset:2560\n\t" \
  "ds_read_b64_tr_b16 v[84:85], v1 offset:12288\n\t" "ds_read_b64_tr_b16 v[86:87], v1 offset:12800\n\t" "ds_read_b64_tr_b16 v[88:89], v1 offset:13312\n\t" \
  "ds_read_b64_tr_b16 v[90:91], v1 offset:13824\n\t" "ds_read_b64_tr_b16 v[92:93], v1 offset:14336\n\t" "ds_read_b64_tr_b16 v[94:95], v1 offset:14848\n\t" ".Lc0_%=:\n\t" "s_waitcnt lgkmcnt(0)\n\t" \
  "s_cbranch_scc1 .Lc1_%=\n\t" "v_mfma_f32_16x16x32_bf16 v[100:103], v[96:99], v[72:75], 0\n\t" "v_mfma_f32_16x16x32_bf16 v[104:107], v[96:99], v[76:79], 0\n\t" \
  "v_mfma_f32_16x16x32_bf16 v[108:111], v[96:99], v[80:83], 0\n\t" "v_mfma_f32_16x16x32_bf16 v[100:103], v[96:99], v[84:87], v[100:103]\n\t" \
  "v_mfma_f32_16x16x32_bf16 v[104:107], v[96:99], v[88:91], v[104:107]\n\t" "v_mfma_f32_16x16x32_bf16 v[108:111], v[96:99], v[92:95], v[108:111]\n\t" ".Lc1_%=:\n\t" "s_barrier\n\t" \
  "global_store_dwordx4 v32, v[116:119], s[94:95] sc1\n\t" "s_mov_b32 exec_hi, 0\n\t" "global_store_dwordx4 v32, v[116:119], s[94:95] offset:1024 sc1\n\t" "s_mov_b32 exec_hi, -1\n\t" \
  "v_dot2_f32_bf16 v56, v44, s77, 0\n\t" "v_dot2_f32_bf16 v57, v44, v44, 0\n\t" "v_dot2_f32_bf16 v58, v48, s77, 0\n\t" "v_dot2_f32_bf16 v59, v48, v48, 0\n\t" "v_dot2_f32_bf16 v60, v52, s77, 0\n\t" \
  "v_dot2_f32_bf16 v61, v52, v52, 0\n\t" "v_dot2_f32_bf16 v56, v45, s77, v56\n\t" "v_dot2_f32_bf16 v57, v45, v45, v57\n\t" "v_dot2_f32_bf16 v58, v49, s77, v58\n\t" \
  "v_dot2_f32_bf16 v59, v49, v49, v59\n\t" "v_dot2_f32_bf16 v60, v53, s77, v60\n\t" "v_dot2_f32_bf16 v61, v53, v53, v61\n\t" "s_barrier\n\t" "v_dot2_f32_bf16 v56, v46, s77, v56\n\t" \
  "v_dot2_f32_bf16 v57, v46, v46, v57\n\t" "v_dot2_f32_bf16 v58, v50, s77, v58\n\t" "v_dot2_f32_bf16 v59, v50, v50, v59\n\t" "v_dot2_f32_bf16 v60, v54, s77, v60\n\t" \
  "v_dot2_f32_bf16 v61, v54, v54, v61\n\t" "v_dot2_f32_bf16 v56, v47, s77, v56\n\t" "v_dot2_f32_bf16 v57, v47, v47, v57\n\t" "v_dot2_f32_bf16 v58, v51, s77, v58\n\t" \
  "v_dot2_f32_bf16 v59, v51, v51, v59\n\t" "v_dot2_f32_bf16 v60, v55, s77, v60\n\t" "v_dot2_f32_bf16 v61, v55, v55, v61\n\t" "s_barrier\n\t" "s_nop 1\n\t" \
  "v_add_f32_dpp v56, v56, v56 quad_perm:[1,0,3,2] row_mask:0xf bank_mask:0xf\n\t" "v_add_f32_dpp v57, v57, v57 quad_perm:[1,0,3,2] row_mask:0xf bank_mask:0xf\n\t" \
  "v_add_f32_dpp v58, v58, v58 quad_perm:[1,0,3,2] row_mask:0xf bank_mask:0xf\n\t" "v_add_f32_dpp v59, v59, v59 quad_perm:[1,0,3,2] row_mask:0xf bank_mask:0xf\n\t" \
  "v_add_f32_dpp v60, v60, v60 quad_perm:[1,0,3,2] row_mask:0xf bank_mask:0xf\n\t" "v_add_f32_dpp v61, v61, v61 quad_perm:[1,0,3,2] row_mask:0xf bank_mask:0xf\n\t" \
  "v_add_f32_dpp v56, v56, v56 quad_perm:[2,3,0,1] row_mask:0xf bank_mask:0xf\n\t" "v_add_f32_dpp v57, v57, v57 quad_perm:[2,3,0,1] row_mask:0xf bank_mask:0xf\n\t" \
  "v_add_f32_dpp v58, v58, v58 quad_perm:[2,3,0,1] row_mask:0xf bank_mask:0xf\n\t" "v_add_f32_dpp v59, v59, v59 quad_perm:[2,3,0,1] row_mask:0xf bank_mask:0xf\n\t" \
  "v_add_f32_dpp v60, v60, v60 quad_perm:[2,3,0,1] row_mask:0xf bank_mask:0xf\n\t" "v_add_f32_dpp v61, v61, v61 quad_perm:[2,3,0,1] row_mask:0xf bank_mask:0xf\n\t" \
  "v_add_f32_dpp v56, v56, v56 row_half_mirror row_mask:0xf bank_mask:0xf\n\t" "v_add_f32_dpp v57, v57, v57 row_half_mirror row_mask:0xf bank_mask:0xf\n\t" \
  "v_add_f32_dpp v58, v58, v58 row_half_mirror row_mask:0xf bank_mask:0xf\n\t" "v_add_f32_dpp v59, v59, v59 row_half_mirror row_mask:0xf bank_mask:0xf\n\t" \
  "v_add_f32_dpp v60, v60, v60 row_half_mirror row_mask:0xf bank_mask:0xf\n\t" "v_add_f32_dpp v61, v61, v61 row_half_mirror row_mask:0xf bank_mask:0xf\n\t" "s_barrier\n\t" "s_mov_b64 vcc, 0xffff\n\t" \
  "v_cndmask_b32 v56, v56, v58, s[70:71]\n\t" "v_cndmask_b32 v57, v57, v59, s[70:71]\n\t" "v_cndmask_b32 v56, v56, v60, s[96:97]\n\t" "v_cndmask_b32 v57, v57, v61, s[96:97]\n\t" \
  "v_mov_b32 v61, v104\n\t" "v_cndmask_b32 v60, v108, v100, vcc\n\t" "s_waitcnt vmcnt(0)\n\t" "v_add_co_u32 v58, vcc, 1, v40\n\t" "s_nop 1\n\t" "v_addc_co_u32 v59, vcc, 0, v41, vcc\n\t" \
  "s_mov_b64 exec, s[98:99]\n\t" "global_store_dwordx4 v12, v[56:59], s[88:89] sc1\n\t" "s_mov_b64 exec, -1\n\t" "s_cbranch_scc1 .Lc2_%=\n\t" "s_mov_b32 exec_hi, 0\n\t" \
  "global_store_dwordx4 v32, v[58:61], s[92:93] sc1\n\t" "s_mov_b32 exec_hi, -1\n\t" ".Lc2_%=:\n\t" "s_barrier\n\t" "s_cmp_lg_u32 s33, 1\n\t" "s_cbranch_scc1 .Ltp_%=\n\t" "s_memrealtime s[96:97]\n\t" \
  "s_mul_i32 s98, s29, 1280\n\t" "s_add_u32 s98, s98, 5368320\n\t" "v_min_u32 v4, 9, v9\n\t" "v_lshl_add_u32 v4, v4, 7, s98\n\t" "s_lshl_b32 s99, s28, 7\n\t" "s_add_u32 s98, s98, s99\n\t" \
  "v_lshl_add_u32 v7, v9, 3, s98\n\t" "v_mov_b32 v57, v58\n\t" "s_waitcnt lgkmcnt(0)\n\t" "v_mov_b32 v56, s96\n\t" "s_mov_b64 exec, 0xffff\n\t" "global_store_dwordx2 v7, v[56:57], s[24:25] sc1\n\t" \
  "s_mov_b64 exec, -1\n\t" ".Ltp_%=:\n\t" "s_barrier\n\t" "s_barrier\n\t" "global_load_dwordx4 v[72:75], v3, s[86:87] offset:-3072 sc1\n\t" \
  "global_load_dwordx4 v[76:79], v3, s[86:87] offset:-1536 sc1\n\t" "global_load_dwordx4 v[80:83], v3, s[86:87] offset:0 sc1\n\t" "global_load_dwordx4 v[84:87], v3, s[86:87] offset:1536 sc1\n\t" \
  "global_load_dwordx4 v[88:91], v3, s[86:87] offset:3072 sc1\n\t" "global_load_dwordx4 v[92:95], v6, s[90:91] offset:0 sc1\n\t" "global_load_dwordx4 v[96:99], v6, s[90:91] offset:512 sc1\n\t" \
  "global_load_dwordx4 v[100:103], v6, s[90:91] offset:1024 sc1\n\t" "global_load_dwordx4 v[104:107], v6, s[90:91] offset:1536 sc1\n\t" \
  "global_load_dwordx4 v[108:111], v6, s[90:91] offset:2048 sc1\n\t" "s_bitcmp0_b32 s33, 0\n\t" "s_cbranch_scc1 .Lfl0_%=\n\t" "global_load_dwordx2 v[112:113], v4, s[24:25] sc1\n\t" ".Lfl0_%=:\n\t" \
  "s_barrier\n\t" "s_mov_b32 s70, 0\n\t" ".Lrg_%=:\n\t" "s_waitcnt vmcnt(0)\n\t" "v_pk_add_f32 v[120:121], v[72:73], v[76:77]\n\t" "v_pk_add_f32 v[126:127], v[94:95], v[98:99]\n\t" \
  "v_pk_add_f32 v[122:123], v[80:81], v[84:85]\n\t" "v_pk_add_f32 v[128:129], v[102:103], v[106:107]\n\t" "v_pk_add_f32 v[120:121], v[120:121], v[88:89]\n\t" \
  "v_pk_add_f32 v[126:127], v[126:127], v[110:111]\n\t" "v_pk_add_f32 v[124:125], v[120:121], v[122:123]\n\t" "v_pk_add_f32 v[130:131], v[126:127], v[128:129]\n\t" \
  "v_pk_add_f32 v[120:121], v[120:121], v[122:123]\n\t" "v_pk_add_f32 v[126:127], v[126:127], v[128:129]\n\t" "s_nop 0\n\t" "v_permlane32_swap_b32 v120, v124\n\t" \
  "v_cmp_ne_u64 vcc, v[74:75], v[58:59]\n\t" "v_permlane32_swap_b32 v121, v125\n\t" "v_permlane32_swap_b32 v126, v130\n\t" "v_pk_add_f32 v[120:121], v[120:121], v[124:125]\n\t" \
  "v_permlane32_swap_b32 v127, v131\n\t" "v_mul_f32 v120, s76, v120\n\t" "v_pk_add_f32 v[126:127], v[126:127], v[130:131]\n\t" "v_mul_f32 v122, v120, v120\n\t" \
  "v_cmp_ne_u64 s[80:81], v[78:79], v[58:59]\n\t" "s_or_b64 vcc, vcc, s[80:81]\n\t" "v_fma_f32 v121, v121, s76, -v122\n\t" "v_cmp_ne_u64 s[78:79], v[82:83], v[58:59]\n\t" \
  "s_or_b64 vcc, vcc, s[78:79]\n\t" "v_max_f32 v121, 0, v121\n\t" "v_cmp_ne_u64 s[80:81], v[86:87], v[58:59]\n\t" "s_or_b64 vcc, vcc, s[80:81]\n\t" "s_cmp_lg_u32 s70, 0\n\t" \
  "s_cbranch_scc1 .Lb8_%=\n\t" "s_barrier\n\t" ".Lb8_%=:\n\t" "s_mov_b32 s70, 1\n\t" "v_cvt_pk_bf16_f32 v120, v121, v120\n\t" "v_cmp_ne_u64 s[78:79], v[90:91], v[58:59]\n\t" \
  "s_or_b64 vcc, vcc, s[78:79]\n\t" "v_lshlrev_b32 v121, 16, v120\n\t" "v_cmp_ne_u64 s[80:81], v[92:93], v[58:59]\n\t" "s_or_b64 vcc, vcc, s[80:81]\n\t" "v_add_f32 v121, 0x3727c5ac, v121\n\t" \
  "v_cmp_ne_u64 s[78:79], v[96:97], v[58:59]\n\t" "s_or_b64 vcc, vcc, s[78:79]\n\t" "v_cvt_pk_bf16_f32 v121, v121, v121\n\t" "v_cmp_ne_u64 s[80:81], v[100:101], v[58:59]\n\t" \
  "s_or_b64 vcc, vcc, s[80:81]\n\t" "v_lshlrev_b32 v121, 16, v121\n\t" "v_cmp_ne_u64 s[78:79], v[104:105], v[58:59]\n\t" "s_or_b64 vcc, vcc, s[78:79]\n\t" "v_sqrt_f32 v121, v121\n\t" \
  "v_cmp_ne_u64 s[80:81], v[108:109], v[58:59]\n\t" "s_or_b64 vcc, vcc, s[80:81]\n\t" "v_cvt_pk_bf16_f32 v121, v121, v121\n\t" "v_and_b32 v120, 0xffff0000, v120\n\t" "v_lshlrev_b32 v121, 16, v121\n\t" \
  "s_cbranch_vccnz .Lsg_%=\n\t" ".Lrh_%=:\n\t" "s_mov_b32 exec_lo, 0\n\t" "v_rcp_f32 v120, v121\n\t" "s_mov_b32 exec_lo, -1\n\t" "ds_write2_b32 v5, v126, v127 offset1:16\n\t" \
  "ds_write_b32 v13, v120\n\t" "s_mov_b64 exec, 1\n\t" "ds_add_u32 v37, v38 offset:12\n\t" "s_mov_b64 exec, -1\n\t" "s_setprio 0\n\t" "s_cmp_lg_u32 s33, 3\n\t" "s_cbranch_scc1 .Lnfl_%=\n\t" \
  "v_cmp_ne_u64 vcc, v[112:113], v[58:59]\n\t" "s_cbranch_vccz .Lsfm_%=\n\t" ".Lsfl_%=:\n\t" "global_load_dwordx2 v[112:113], v4, s[24:25] sc1\n\t" "s_sub_u32 s39, s39, 1\n\t" \
  "s_cmp_lt_i32 s39, 0\n\t" "s_waitcnt vmcnt(0)\n\t" "s_cbranch_scc1 .Lsfm_%=\n\t" "v_cmp_ne_u64 vcc, v[112:113], v[58:59]\n\t" "s_cbranch_vccnz .Lsfl_%=\n\t" ".Lsfm_%=:\n\t" "s_mov_b64 exec, 1\n\t" \
  "ds_add_u32 v37, v38 offset:4\n\t" "s_mov_b64 exec, -1\n\t" ".Lnfl_%=:\n\t" "s_cmp_lg_u32 s33, 1\n\t" "s_cbranch_scc1 .Lnlg_%=\n\t" "v_subrev_u32 v112, s96, v112\n\t" \
  "v_cmp_eq_u32 vcc, v113, v58\n\t" "s_lshl_b32 s98, 7, s27\n\t" "s_lshr_b32 s98, s98, 1\n\t" "s_and_b32 s98, s98, 31\n\t" "v_cndmask_b32 v112, 60, v112, vcc\n\t" "s_sub_u32 s99, s28, s27\n\t" \
  "s_lshl_b32 s98, s98, s99\n\t" "s_or_b32 s98, s98, 0xffff0000\n\t" "s_mov_b32 s99, 0\n\t" "v_med3_i32 v112, v112, 0, 64\n\t" "v_cndmask_b32 v112, 0, v112, s[98:99]\n\t" "s_nop 1\n\t" \
  "v_max_u32_dpp v112, v112, v112 quad_perm:[1,0,3,2] row_mask:0xf bank_mask:0xf\n\t" "s_nop 1\n\t" "v_max_u32_dpp v112, v112, v112 quad_perm:[2,3,0,1] row_mask:0xf bank_mask:0xf\n\t" "s_nop 1\n\t" \
  "v_max_u32_dpp v112, v112, v112 row_half_mirror row_mask:0xf bank_mask:0xf\n\t" "s_nop 1\n\t" "v_max_u32_dpp v112, v112, v112 row_mirror row_mask:0xf bank_mask:0xf\n\t" "v_add_u32 v7, v10, v37\n\t" \
  "ds_write_b32 v7, v112 offset:80\n\t" ".Lnlg_%=:\n\t" "s_cmp_lg_u32 s33, 2\n\t" "s_cbranch_scc1 .Latt_%=\n\t" "v_dot2_f32_bf16 v1, v68, s77, 0\n\t" "v_dot2_f32_bf16 v1, v64, s77, v1\n\t" \
  "v_dot2_f32_bf16 v1, v65, s77, v1\n\t" "v_dot2_f32_bf16 v1, v66, s77, v1\n\t" "v_dot2_f32_bf16 v1, v67, s77, v1\n\t" "s_nop 2\n\t" \
  "v_add_f32_dpp v1, v1, v1 quad_perm:[1,0,3,2] row_mask:0xf bank_mask:0xf\n\t" "s_nop 1\n\t" "v_add_f32_dpp v1, v1, v1 quad_perm:[2,3,0,1] row_mask:0xf bank_mask:0xf\n\t" "s_nop 1\n\t" \
  "v_add_f32_dpp v1, v1, v1 row_half_mirror row_mask:0xf bank_mask:0xf\n\t" "s_nop 1\n\t" "v_add_f32_dpp v1, v1, v1 row_mirror row_mask:0xf bank_mask:0xf\n\t" "v_mov_b32 v2, v1\n\t" "s_nop 1\n\t" \
  "v_permlane32_swap_b32 v1, v2\n\t" "v_add_f32 v1, v1, v2\n\t" "v_mov_b32 v2, v1\n\t" "s_nop 1\n\t" "v_permlane16_swap_b32 v1, v2\n\t" "v_add_f32 v1, v1, v2\n\t" "ds_write_b32 v37, v1 offset:40\n\t" \
  "s_branch .Latt_%=\n\t" ".Ll_%=:\n\t" "s_movk_i32 s38, 1\n\t" "s_add_u32 s31, s31, 1\n\t" "s_cmp_eq_u32 s31, 10\n\t" "s_cselect_b32 s31, 0, s31\n\t" "s_mul_i32 s76, s31, 245760\n\t" \
  "s_lshl_b32 s77, s31, 7\n\t" "s_add_u32 s78, s10, s76\n\t" "s_addc_u32 s79, s11, 0\n\t" "s_add_u32 s80, s8, s77\n\t" "s_addc_u32 s81, s9, 0\n\t" "s_and_b32 s76, s38, 3\n\t" \
  "s_mul_i32 s76, s76, 36864\n\t" "s_add_u32 m0, s76, s87\n\t" "s_branch .Ll1_%=\n\t" ".Ll0_%=:\n\t" "s_add_u32 s31, s31, 1\n\t" "s_cmp_eq_u32 s31, 10\n\t" "s_cselect_b32 s31, 0, s31\n\t" \
  "s_mul_i32 s76, s31, 245760\n\t" "s_lshl_b32 s77, s31, 7\n\t" "s_add_u32 s78, s10, s76\n\t" "s_addc_u32 s79, s11, 0\n\t" "s_add_u32 s80, s8, s77\n\t" "s_addc_u32 s81, s9, 0\n\t" \
  "s_and_b32 s76, s38, 3\n\t" "s_mul_i32 s76, s76, 36864\n\t" "s_add_u32 m0, s76, s87\n\t" "s_waitcnt vmcnt(9)\n\t" "s_barrier\n\t" ".Ll1_%=:\n\t" \
  "global_load_lds_dwordx4 v46, s[80:81] offset:-2048\n\t" "global_load_lds_dwordx4 v47, s[80:81] offset:-1024\n\t" "global_load_lds_dwordx4 v48, s[80:81] offset:0\n\t" "s_add_u32 m0, s76, s86\n\t" \
  "s_add_u32 s38, s38, 1\n\t" "global_load_lds_dwordx4 v40, s[78:79] offset:-3072\n\t" "global_load_lds_dwordx4 v41, s[78:79] offset:-2048\n\t" "global_load_lds_dwordx4 v42, s[78:79] offset:-1024\n\t" \
  "global_load_lds_dwordx4 v43, s[78:79] offset:0\n\t" "global_load_lds_dwordx4 v44, s[78:79] offset:1024\n\t" "global_load_lds_dwordx4 v45, s[78:79] offset:2048\n\t" "s_cmp_lt_u32 s38, 9\n\t" \
  "s_cbranch_scc1 .Ll0_%=\n\t" "s_add_u32 s31, s31, 1\n\t" "s_cmp_eq_u32 s31, 10\n\t" "s_cselect_b32 s31, 0, s31\n\t" "s_mul_i32 s76, s31, 245760\n\t" "s_lshl_b32 s77, s31, 7\n\t" \
  "s_add_u32 s78, s10, s76\n\t" "s_addc_u32 s79, s11, 0\n\t" "s_add_u32 s80, s8, s77\n\t" "s_addc_u32 s81, s9, 0\n\t" "s_and_b32 s76, s38, 3\n\t" "s_mul_i32 s76, s76, 36864\n\t" \
  "s_add_u32 m0, s76, s87\n\t" "v_lshrrev_b32 v1, 3, v8\n\t" "v_lshrrev_b32 v4, 5, v8\n\t" "v_and_b32 v2, 6, v1\n\t" "v_and_b32 v1, 3, v1\n\t" "v_xor_b32 v2, v2, v8\n\t" \
  "v_mul_u32_u24 v1, 1280, v1\n\t" "v_and_b32 v2, 7, v2\n\t" "s_lshl_b32 s92, s28, 7\n\t" "v_lshl_add_u32 v1, v2, 4, v1\n\t" "s_and_b32 s93, s33, 1\n\t" "s_and_b32 s94, s33, 2\n\t" \
  "v_add_u32 v1, s92, v1\n\t" "s_mul_i32 s92, s93, 5\n\t" "s_add_u32 s92, s92, s94\n\t" "s_add_u32 s92, s92, 5\n\t" "v_add_u32 v2, s92, v4\n\t" "s_lshl2_add_u32 s92, s93, s94\n\t" \
  "s_movk_i32 s93, 5120\n\t" "v_add_lshl_u32 v50, v4, s92, 2\n\t" "v_mad_u32_u24 v49, v2, s93, v1\n\t" "s_lshl_b32 s92, s92, 9\n\t" "s_add_u32 s92, s92, 73728\n\t" "s_waitcnt vmcnt(9)\n\t" \
  "s_barrier\n\t" "global_load_lds_dwordx4 v46, s[80:81] offset:-2048\n\t" "global_load_lds_dwordx4 v47, s[80:81] offset:-1024\n\t" "global_load_lds_dwordx4 v48, s[80:81] offset:0\n\t" \
  "s_add_u32 m0, s76, s86\n\t" "s_nop 0\n\t" "global_load_lds_dwordx4 v40, s[78:79] offset:-3072\n\t" "global_load_lds_dwordx4 v41, s[78:79] offset:-2048\n\t" \
  "global_load_lds_dwordx4 v42, s[78:79] offset:-1024\n\t" "global_load_lds_dwordx4 v43, s[78:79] offset:0\n\t" "global_load_lds_dwordx4 v44, s[78:79] offset:1024\n\t" \
  "global_load_lds_dwordx4 v45, s[78:79] offset:2048\n\t" "s_waitcnt vmcnt(9)\n\t" "s_barrier\n\t" "s_sleep 2\n\t" "s_mov_b64 s[78:79], s[12:13]\n\t" "s_add_u32 m0, s92, 0\n\t" "s_nop 0\n\t" \
  "global_load_lds_dwordx4 v49, s[78:79]\n\t" "s_add_u32 s78, s78, 76800\n\t" "s_addc_u32 s79, s79, 0\n\t" "s_add_u32 m0, s92, 4096\n\t" "s_nop 0\n\t" "global_load_lds_dwordx4 v49, s[78:79]\n\t" \
  "s_add_u32 s78, s78, 76800\n\t" "s_addc_u32 s79, s79, 0\n\t" "s_add_u32 m0, s92, 8192\n\t" "s_nop 0\n\t" "global_load_lds_dwordx4 v49, s[78:79]\n\t" "s_add_u32 s78, s78, 76800\n\t" \
  "s_addc_u32 s79, s79, 0\n\t" "s_add_u32 m0, s92, 12288\n\t" "s_nop 0\n\t" "global_load_lds_dwordx4 v49, s[78:79]\n\t" "s_add_u32 s78, s78, 76800\n\t" "s_addc_u32 s79, s79, 0\n\t" \
  "s_add_u32 m0, s92, 16384\n\t" "s_waitcnt vmcnt(4)\n\t" "s_barrier\n\t" "global_load_lds_dwordx4 v49, s[78:79]\n\t" "s_add_u32 s78, s78, 102400\n\t" "s_addc_u32 s79, s79, 0\n\t" \
  "s_add_u32 m0, s92, 40960\n\t" "s_nop 0\n\t" "global_load_lds_dwordx4 v49, s[78:79]\n\t" "s_add_u32 s78, s78, 76800\n\t" "s_addc_u32 s79, s79, 0\n\t" "s_add_u32 m0, s92, 45056\n\t" "s_nop 0\n\t" \
  "global_load_lds_dwordx4 v49, s[78:79]\n\t" "s_add_u32 s78, s78, 76800\n\t" "s_addc_u32 s79, s79, 0\n\t" "s_add_u32 m0, s92, 49152\n\t" "s_nop 0\n\t" "global_load_lds_dwordx4 v49, s[78:79]\n\t" \
  "s_add_u32 s78, s78, 76800\n\t" "s_addc_u32 s79, s79, 0\n\t" "s_add_u32 m0, s92, 53248\n\t" "s_nop 0\n\t" "global_load_lds_dwordx4 v49, s[78:79]\n\t" "s_add_u32 s78, s78, 76800\n\t" \
  "s_addc_u32 s79, s79, 0\n\t" "s_add_u32 m0, s92, 57344\n\t" "s_nop 0\n\t" "global_load_lds_dwordx4 v49, s[78:79]\n\t" "s_setprio 0\n\t" "s_mov_b32 s94, 0xf41e3209\n\t" \
  "v_bfe_u32 v2, s94, v50, 4\n\t" "v_mad_u32_u24 v3, v2, s93, v1\n\t" "s_mov_b32 s94, 0xfe0d3218\n\t" "v_bfe_u32 v2, s94, v50, 4\n\t" "v_mad_u32_u24 v4, v2, s93, v1\n\t" \
  "s_mov_b32 s94, 0xfedc2107\n\t" "v_bfe_u32 v2, s94, v50, 4\n\t" "v_mad_u32_u24 v5, v2, s93, v1\n\t" "s_mov_b32 s94, 0xedcbf106\n\t" "v_bfe_u32 v2, s94, v50, 4\n\t" \
  "v_mad_u32_u24 v6, v2, s93, v1\n\t" "s_mov_b32 s94, 0xfdcabe05\n\t" "v_bfe_u32 v2, s94, v50, 4\n\t" "v_mad_u32_u24 v7, v2, s93, v1\n\t" "s_waitcnt vmcnt(0)\n\t" "s_mov_b64 exec, 1\n\t" \
  "ds_add_u32 v37, v38 offset:4\n\t" "s_mov_b64 exec, -1\n\t" "s_branch .Latt_%=\n\t" ".Lsg_%=:\n\t" "s_sub_u32 s39, s39, 1\n\t" "s_cmp_lt_i32 s39, 0\n\t" "s_cbranch_scc1 .Lrh_%=\n\t" \
  "global_load_dwordx4 v[72:75], v3, s[86:87] offset:-3072 sc1\n\t" "global_load_dwordx4 v[76:79], v3, s[86:87] offset:-1536 sc1\n\t" "global_load_dwordx4 v[80:83], v3, s[86:87] offset:0 sc1\n\t" \
  "global_load_dwordx4 v[84:87], v3, s[86:87] offset:1536 sc1\n\t" "global_load_dwordx4 v[88:91], v3, s[86:87] offset:3072 sc1\n\t" "global_load_dwordx4 v[92:95], v6, s[90:91] offset:0 sc1\n\t" \
  "global_load_dwordx4 v[96:99], v6, s[90:91] offset:512 sc1\n\t" "global_load_dwordx4 v[100:103], v6, s[90:91] offset:1024 sc1\n\t" "global_load_dwordx4 v[104:107], v6, s[90:91] offset:1536 sc1\n\t" \
  "global_load_dwordx4 v[108:111], v6, s[90:91] offset:2048 sc1\n\t" "s_branch .Lrg_%=\n\t" ".Lts_%=:\n\t" "ds_read_b32 v39, v37 offset:12\n\t" "ds_read_b128 v[160:163], v12 offset:1024\n\t" \
  "ds_read_b128 v[164:167], v12 offset:1088\n\t" "ds_read2_b32 v[136:137], v4 offset0:0 offset1:96\n\t" "ds_read2_b32 v[138:139], v4 offset0:16 offset1:112\n\t" \
  "ds_read2_b32 v[140:141], v4 offset0:32 offset1:128\n\t" "ds_read2_b32 v[142:143], v4 offset0:48 offset1:144\n\t" "ds_read2_b32 v[144:145], v4 offset0:64 offset1:160\n\t" \
  "ds_read2_b32 v[146:147], v4 offset0:80 offset1:176\n\t" "ds_read_b32 v14, v13 offset:1152\n\t" "s_sub_u32 s39, s39, 1\n\t" "s_cmp_lt_i32 s39, 0\n\t" "s_waitcnt lgkmcnt(0)\n\t" \
  "v_readfirstlane_b32 s38, v39\n\t" "s_cbranch_scc1 .Ltr_%=\n\t" "s_cmp_ge_u32 s38, 4\n\t" "s_cbranch_scc0 .Lts_%=\n\t" "s_branch .Ltr_%=\n\t" ".Lsq_%=:\n\t" "ds_read_b32 v39, v37 offset:0\n\t" \
  "s_sub_u32 s39, s39, 1\n\t" "s_cmp_lt_i32 s39, 0\n\t" "s_waitcnt lgkmcnt(0)\n\t" "v_readfirstlane_b32 s38, v39\n\t" "s_cbranch_scc1 .Lrq_%=\n\t" "s_cmp_ge_u32 s38, 4\n\t" \
  "s_cbranch_scc0 .Lsq_%=\n\t" "s_branch .Lq_%=\n\t" ".Lsqm_%=:\n\t" "ds_read_b32 v39, v37 offset:8\n\t" "s_sub_u32 s39, s39, 1\n\t" "s_cmp_lt_i32 s39, 0\n\t" "s_waitcnt lgkmcnt(0)\n\t" \
  "v_readfirstlane_b32 s38, v39\n\t" "s_cbranch_scc1 .Lrqm_%=\n\t" "s_cmp_ge_u32 s38, 4\n\t" "s_cbranch_scc0 .Lsqm_%=\n\t" "s_branch .Lrqm_%=\n\t" ".Lsq0_%=:\n\t" "ds_read_b32 v39, v37 offset:0\n\t" \
  "s_sub_u32 s39, s39, 1\n\t" "s_cmp_lt_i32 s39, 0\n\t" "s_waitcnt lgkmcnt(0)\n\t" "v_readfirstlane_b32 s38, v39\n\t" "s_cbranch_scc1 .Lrq0_%=\n\t" "s_cmp_ge_u32 s38, 4\n\t" \
  "s_cbranch_scc0 .Lsq0_%=\n\t" "s_branch .Lq0_%=\n\t" ".Lsv0_%=:\n\t" "ds_read_b32 v39, v37 offset:4\n\t" "s_sub_u32 s39, s39, 1\n\t" "s_cmp_lt_i32 s39, 0\n\t" "s_waitcnt lgkmcnt(0)\n\t" \
  "v_readfirstlane_b32 s38, v39\n\t" "s_cbranch_scc1 .Lrv0_%=\n\t" "s_cmp_ge_u32 s38, 9\n\t" "s_cbranch_scc0 .Lsv0_%=\n\t" "s_branch .Lv0_%=\n\t" ".Lsr_%=:\n\t" \
  "global_load_dwordx4 v[148:151], v32, s[58:59] sc1\n\t" "global_load_dwordx4 v[144:147], v32, s[56:57] sc1\n\t" "s_sub_u32 s39, s39, 1\n\t" "s_cmp_lt_i32 s39, 0\n\t" "s_waitcnt vmcnt(0)\n\t" \
  "s_cbranch_scc1 .Lrr_%=\n\t" "v_max_u32 v124, v144, v148\n\t" "v_cmp_eq_u32 vcc, -1, v124\n\t" "s_cbranch_vccnz .Lsr_%=\n\t" "s_branch .Lrr_%=\n\t" ".Lk0_%=:\n\t" "ds_read_b32 v39, v37 offset:8\n\t" \
  "s_waitcnt lgkmcnt(0)\n\t" "v_readfirstlane_b32 s38, v39\n\t" "s_cmp_ge_u32 s38, 4\n\t" "s_cbranch_scc0 .Lsqm_%=\n\t" ".Lrqm_%=:\n\t" "ds_read_b32 v39, v37 offset:80\n\t" \
  "s_memrealtime s[76:77]\n\t" "s_mul_i32 s80, s35, 5\n\t" "s_add_u32 s80, s80, 24\n\t" "s_waitcnt lgkmcnt(0)\n\t" "v_readfirstlane_b32 s38, v39\n\t" "s_add_u32 s80, s80, s38\n\t" ".Lqd_%=:\n\t" \
  "s_memrealtime s[78:79]\n\t" "s_waitcnt lgkmcnt(0)\n\t" "s_sub_u32 s78, s78, s76\n\t" "s_cmp_lt_u32 s78, s80\n\t" "s_cbranch_scc1 .Lqd_%=\n\t" "global_load_dwordx4 v[148:151], v32, s[58:59] sc1\n\t" \
  "global_load_dwordx4 v[144:147], v32, s[56:57] sc1\n\t" ".Lq0_%=:\n\t" "ds_read_b32 v39, v37\n\t" "ds_read_b64 v[40:41], v30 offset:0\n\t" "ds_read_b64 v[42:43], v30 offset:3072\n\t" \
  "ds_read_b64 v[44:45], v30 offset:11776\n\t" "ds_read_b64 v[46:47], v30 offset:14848\n\t" "ds_read_b64 v[48:49], v30 offset:12288\n\t" "ds_read_b64 v[50:51], v30 offset:15360\n\t" \
  "s_waitcnt lgkmcnt(6)\n\t" "v_readfirstlane_b32 s38, v39\n\t" "s_cmp_ge_u32 s38, 4\n\t" "s_cbranch_scc0 .Lsq0_%=\n\t" ".Lrq0_%=:\n\t" "s_waitcnt lgkmcnt(0)\n\t" \
  "v_cndmask_b32 v52, 0, v40, s[44:45]\n\t" "v_cndmask_b32 v53, 0, v41, s[44:45]\n\t" "v_cndmask_b32 v54, 0, v42, s[46:47]\n\t" "v_cndmask_b32 v55, 0, v43, s[46:47]\n\t" \
  "v_cndmask_b32 v40, 0, v40, s[40:41]\n\t" "v_cndmask_b32 v41, 0, v41, s[40:41]\n\t" "v_cndmask_b32 v42, 0, v42, s[42:43]\n\t" "v_cndmask_b32 v43, 0, v43, s[42:43]\n\t" "s_nop 1\n\t" \
  "v_mfma_f32_16x16x32_bf16 v[108:111], v[44:47], v[40:43], v[20:23]\n\t" "v_mfma_f32_16x16x32_bf16 v[112:115], v[48:51], v[40:43], v[24:27]\n\t" \
  "v_mfma_f32_16x16x32_bf16 v[116:119], v[44:47], v[52:55], v[20:23]\n\t" "v_mfma_f32_16x16x32_bf16 v[120:123], v[48:51], v[52:55], v[24:27]\n\t" ".Lv0_%=:\n\t" "ds_read_b32 v39, v37 offset:4\n\t" \
  "ds_read_b64 v[128:129], v30 offset:24064\n\t" "ds_read_b64 v[130:131], v30 offset:24576\n\t" "ds_read_b64 v[132:133], v30 offset:27136\n\t" "ds_read_b64 v[134:135], v30 offset:27648\n\t" \
  "s_waitcnt lgkmcnt(4)\n\t" "v_readfirstlane_b32 s38, v39\n\t" "s_cmp_ge_u32 s38, 9\n\t" "s_cbranch_scc0 .Lsv0_%=\n\t" ".Lrv0_%=:\n\t" "s_waitcnt lgkmcnt(0)\n\t" \
  "v_cndmask_b32 v136, v128, 0, s[50:51]\n\t" "v_cndmask_b32 v128, 0, v128, s[50:51]\n\t" "v_cndmask_b32 v137, v129, 0, s[50:51]\n\t" "v_cndmask_b32 v129, 0, v129, s[50:51]\n\t" \
  "v_cndmask_b32 v138, v130, 0, s[50:51]\n\t" "v_cndmask_b32 v130, 0, v130, s[50:51]\n\t" "v_cndmask_b32 v139, v131, 0, s[50:51]\n\t" "v_cndmask_b32 v131, 0, v131, s[50:51]\n\t" \
  "v_cndmask_b32 v140, v132, 0, s[52:53]\n\t" "v_cndmask_b32 v132, 0, v132, s[52:53]\n\t" "v_cndmask_b32 v141, v133, 0, s[52:53]\n\t" "v_cndmask_b32 v133, 0, v133, s[52:53]\n\t" \
  "v_cndmask_b32 v142, v134, 0, s[52:53]\n\t" "v_cndmask_b32 v134, 0, v134, s[52:53]\n\t" "v_cndmask_b32 v143, v135, 0, s[52:53]\n\t" "v_cndmask_b32 v135, 0, v135, s[52:53]\n\t" \
  "s_cmp_ge_u32 s26, 4\n\t" "s_cbranch_scc1 .Lnw2_%=\n\t" "s_add_u32 m0, s92, 20480\n\t" "s_nop 0\n\t" "global_load_lds_dwordx4 v3, s[12:13]\n\t" "s_add_u32 s12, s12, 81920\n\t" \
  "s_addc_u32 s13, s13, 0\n\t" "s_add_u32 m0, s92, 24576\n\t" "s_nop 0\n\t" "global_load_lds_dwordx4 v4, s[12:13]\n\t" "s_add_u32 s12, s12, 81920\n\t" "s_addc_u32 s13, s13, 0\n\t" \
  "s_add_u32 m0, s92, 28672\n\t" "s_nop 0\n\t" "global_load_lds_dwordx4 v5, s[12:13]\n\t" "s_add_u32 s12, s12, 81920\n\t" "s_addc_u32 s13, s13, 0\n\t" "s_add_u32 m0, s92, 32768\n\t" "s_nop 0\n\t" \
  "global_load_lds_dwordx4 v6, s[12:13]\n\t" "s_add_u32 s12, s12, 81920\n\t" "s_addc_u32 s13, s13, 0\n\t" "s_add_u32 m0, s92, 36864\n\t" "s_nop 0\n\t" "global_load_lds_dwordx4 v7, s[12:13]\n\t" \
  "s_add_u32 s12, s12, 81920\n\t" "s_addc_u32 s13, s13, 0\n\t" "s_add_u32 m0, s92, 61440\n\t" "s_nop 0\n\t" "global_load_lds_dwordx4 v3, s[12:13]\n\t" "s_add_u32 s12, s12, 81920\n\t" \
  "s_addc_u32 s13, s13, 0\n\t" "s_add_u32 m0, s92, 65536\n\t" "s_nop 0\n\t" "global_load_lds_dwordx4 v4, s[12:13]\n\t" "s_add_u32 s12, s12, 81920\n\t" "s_addc_u32 s13, s13, 0\n\t" \
  "s_add_u32 m0, s92, 69632\n\t" "s_nop 0\n\t" "global_load_lds_dwordx4 v5, s[12:13]\n\t" "s_add_u32 s12, s12, 81920\n\t" "s_addc_u32 s13, s13, 0\n\t" "s_add_u32 m0, s92, 73728\n\t" "s_nop 0\n\t" \
  "global_load_lds_dwordx4 v6, s[12:13]\n\t" "s_add_u32 s12, s12, 81920\n\t" "s_addc_u32 s13, s13, 0\n\t" "s_add_u32 m0, s92, 77824\n\t" "s_nop 0\n\t" "global_load_lds_dwordx4 v7, s[12:13]\n\t" \
  ".Lnw2_%=:\n\t" "s_cmp_ge_u32 s26, 4\n\t" "s_waitcnt vmcnt(10)\n\t" "s_cbranch_scc0 .Lnw3_%=\n\t" "s_waitcnt vmcnt(0)\n\t" ".Lnw3_%=:\n\t" "s_setprio 2\n\t" "v_max_u32 v124, v144, v148\n\t" \
  "v_cmp_eq_u32 vcc, -1, v124\n\t" "s_cbranch_vccnz .Lsr_%=\n\t" ".Lrr_%=:\n\t" "v_cndmask_b32 v124, 0, v144, s[48:49]\n\t" "v_cndmask_b32 v125, 0, v145, s[48:49]\n\t" \
  "v_cndmask_b32 v126, v144, 0, s[48:49]\n\t" "v_cndmask_b32 v127, v145, 0, s[48:49]\n\t" "v_mfma_f32_16x16x16_bf16 v[108:111], v[150:151], v[124:125], v[108:111]\n\t" \
  "v_mfma_f32_16x16x16_bf16 v[112:115], v[146:147], v[124:125], v[112:115]\n\t" "v_mfma_f32_16x16x16_bf16 v[116:119], v[150:151], v[126:127], v[116:119]\n\t" \
  "v_mfma_f32_16x16x16_bf16 v[120:123], v[146:147], v[126:127], v[120:123]\n\t" "s_branch .Lk1_%=\n\t" ".Lsv1_%=:\n\t" "ds_read_b32 v39, v37 offset:4\n\t" "s_sub_u32 s39, s39, 1\n\t" \
  "s_cmp_lt_i32 s39, 0\n\t" "s_waitcnt lgkmcnt(0)\n\t" "v_readfirstlane_b32 s38, v39\n\t" "s_cbranch_scc1 .Lrv1_%=\n\t" "s_cmp_ge_u32 s38, 9\n\t" "s_cbranch_scc0 .Lsv1_%=\n\t" \
  "ds_read_b64 v[128:129], v30 offset:24064\n\t" "ds_read_b64 v[130:131], v30 offset:24576\n\t" "ds_read_b64 v[132:133], v30 offset:27136\n\t" "ds_read_b64 v[134:135], v30 offset:27648\n\t" \
  "ds_read_b64 v[136:137], v31 offset:24064\n\t" "ds_read_b64 v[138:139], v31 offset:24576\n\t" "ds_read_b64 v[140:141], v31 offset:27136\n\t" "ds_read_b64 v[142:143], v31 offset:27648\n\t" \
  "s_branch .Lrv1_%=\n\t" ".Lk2_%=:\n\t" "s_waitcnt lgkmcnt(8)\n\t" "v_readfirstlane_b32 s38, v39\n\t" "s_cmp_ge_u32 s38, 9\n\t" "s_cbranch_scc0 .Lsv1_%=\n\t" ".Lrv1_%=:\n\t" \
  "s_waitcnt lgkmcnt(0)\n\t" "v_mfma_f32_16x16x32_bf16 v[152:155], v[128:131], v[112:115], 0\n\t" "v_mfma_f32_16x16x32_bf16 v[164:167], v[140:143], v[120:123], 0\n\t" \
  "v_mfma_f32_16x16x32_bf16 v[156:159], v[132:135], v[112:115], 0\n\t" "v_mfma_f32_16x16x32_bf16 v[160:163], v[136:139], v[120:123], 0\n\t" "v_xor_b32 v20, 32, v35\n\t" "v_xor_b32 v21, 64, v35\n\t" \
  "v_xor_b32 v22, 96, v35\n\t" "s_nop 1\n\t" "v_cvt_pk_bf16_f32 v144, v152, v153\n\t" "v_cvt_pk_bf16_f32 v145, v154, v155\n\t" "v_cvt_pk_bf16_f32 v146, v164, v165\n\t" \
  "v_cvt_pk_bf16_f32 v147, v166, v167\n\t" "global_store_dwordx4 v32, v[144:147], s[60:61] offset:-2560 sc1\n\t" "v_cvt_pk_bf16_f32 v148, v156, v157\n\t" "v_cvt_pk_bf16_f32 v149, v158, v159\n\t" \
  "v_cvt_pk_bf16_f32 v150, v160, v161\n\t" "v_cvt_pk_bf16_f32 v151, v162, v163\n\t" "s_branch .Lk3_%=\n\t" ".Lso0_%=:\n\t" "global_load_dwordx4 v[152:155], v32, s[62:63] offset:-2048 sc1\n\t" \
  "s_sub_u32 s39, s39, 1\n\t" "s_cmp_lt_i32 s39, 0\n\t" "s_waitcnt vmcnt(0)\n\t" "s_cbranch_scc1 .Lro0_%=\n\t" "v_cmp_eq_u32 vcc, -1, v152\n\t" "s_cbranch_vccnz .Lso0_%=\n\t" "s_branch .Lro0_%=\n\t" \
  ".Lso1_%=:\n\t" "global_load_dwordx4 v[156:159], v32, s[62:63] offset:-1024 sc1\n\t" "s_sub_u32 s39, s39, 1\n\t" "s_cmp_lt_i32 s39, 0\n\t" "s_waitcnt vmcnt(0)\n\t" "s_cbranch_scc1 .Lro1_%=\n\t" \
  "v_cmp_eq_u32 vcc, -1, v156\n\t" "s_cbranch_vccnz .Lso1_%=\n\t" "s_branch .Lro1_%=\n\t" ".Lso2_%=:\n\t" "global_load_dwordx4 v[160:163], v32, s[62:63] offset:0 sc1\n\t" "s_sub_u32 s39, s39, 1\n\t" \
  "s_cmp_lt_i32 s39, 0\n\t" "s_waitcnt vmcnt(0)\n\t" "s_cbranch_scc1 .Lro2_%=\n\t" "v_cmp_eq_u32 vcc, -1, v160\n\t" "s_cbranch_vccnz .Lso2_%=\n\t" "s_branch .Lro2_%=\n\t" ".Lso3_%=:\n\t" \
  "global_load_dwordx4 v[164:167], v32, s[62:63] offset:1024 sc1\n\t" "s_sub_u32 s39, s39, 1\n\t" "s_cmp_lt_i32 s39, 0\n\t" "s_waitcnt vmcnt(0)\n\t" "s_cbranch_scc1 .Lro3_%=\n\t" \
  "v_cmp_eq_u32 vcc, -1, v164\n\t" "s_cbranch_vccnz .Lso3_%=\n\t" "s_branch .Lro3_%=\n\t" ".Lso4_%=:\n\t" "global_load_dwordx4 v[152:155], v32, s[62:63] offset:2048 sc1\n\t" \
  "s_sub_u32 s39, s39, 1\n\t" "s_cmp_lt_i32 s39, 0\n\t" "s_waitcnt vmcnt(0)\n\t" "s_cbranch_scc1 .Lro4_%=\n\t" "v_cmp_eq_u32 vcc, -1, v152\n\t" "s_cbranch_vccnz .Lso4_%=\n\t" "s_branch .Lro4_%=\n\t" \
  ".Lso5_%=:\n\t" "global_load_dwordx4 v[156:159], v32, s[64:65] offset:-2048 sc1\n\t" "s_sub_u32 s39, s39, 1\n\t" "s_cmp_lt_i32 s39, 0\n\t" "s_waitcnt vmcnt(0)\n\t" "s_cbranch_scc1 .Lro5_%=\n\t" \
  "v_cmp_eq_u32 vcc, -1, v156\n\t" "s_cbranch_vccnz .Lso5_%=\n\t" "s_branch .Lro5_%=\n\t" ".Lso6_%=:\n\t" "global_load_dwordx4 v[160:163], v32, s[64:65] offset:-1024 sc1\n\t" \
  "s_sub_u32 s39, s39, 1\n\t" "s_cmp_lt_i32 s39, 0\n\t" "s_waitcnt vmcnt(0)\n\t" "s_cbranch_scc1 .Lro6_%=\n\t" "v_cmp_eq_u32 vcc, -1, v160\n\t" "s_cbranch_vccnz .Lso6_%=\n\t" "s_branch .Lro6_%=\n\t" \
  ".Lso7_%=:\n\t" "global_load_dwordx4 v[164:167], v32, s[64:65] offset:0 sc1\n\t" "s_sub_u32 s39, s39, 1\n\t" "s_cmp_lt_i32 s39, 0\n\t" "s_waitcnt vmcnt(0)\n\t" "s_cbranch_scc1 .Lro7_%=\n\t" \
  "v_cmp_eq_u32 vcc, -1, v164\n\t" "s_cbranch_vccnz .Lso7_%=\n\t" "s_branch .Lro7_%=\n\t" ".Lso8_%=:\n\t" "global_load_dwordx4 v[152:155], v32, s[64:65] offset:1024 sc1\n\t" \
  "s_sub_u32 s39, s39, 1\n\t" "s_cmp_lt_i32 s39, 0\n\t" "s_waitcnt vmcnt(0)\n\t" "s_cbranch_scc1 .Lro8_%=\n\t" "v_cmp_eq_u32 vcc, -1, v152\n\t" "s_cbranch_vccnz .Lso8_%=\n\t" "s_branch .Lro8_%=\n\t" \
  ".Lso9_%=:\n\t" "global_load_dwordx4 v[156:159], v32, s[64:65] offset:2048 sc1\n\t" "s_sub_u32 s39, s39, 1\n\t" "s_cmp_lt_i32 s39, 0\n\t" "s_waitcnt vmcnt(0)\n\t" "s_cbranch_scc1 .Lro9_%=\n\t" \
  "v_cmp_eq_u32 vcc, -1, v156\n\t" "s_cbranch_vccnz .Lso9_%=\n\t" "s_branch .Lro9_%=\n\t" ".Lg_%=:\n\t" "v_bfe_u32 v13, s80, v10, 4\n\t" "v_lshlrev_b32 v12, 1, v9\n\t" "s_movk_i32 s76, 24576\n\t" \
  "v_and_b32 v12, 28, v12\n\t" "v_lshl_add_u32 v2, v9, 7, s76\n\t" "v_xor_b32 v12, v12, v10\n\t" "s_mov_b32 s76, 155648\n\t" "v_lshl_add_u32 v2, v12, 2, v2\n\t" "v_lshl_add_u32 v4, v9, 2, s76\n\t" \
  "v_xor_b32 v3, 64, v2\n\t" "v_lshl_add_u32 v5, v10, 2, s76\n\t" "s_mul_i32 s76, s33, 3072\n\t" "v_lshl_add_u32 v6, v8, 3, s76\n\t" "s_sub_u32 s76, 5, s27\n\t" "s_cmp_eq_u32 s27, 0\n\t" \
  "s_cselect_b32 s76, 0, s76\n\t" "s_sub_u32 s77, 15, s27\n\t" "s_cmp_eq_u32 s27, 4\n\t" "s_cselect_b32 s77, 16, s77\n\t" "v_cmp_gt_u32 s[68:69], s76, v13\n\t" "v_cmp_le_u32 vcc, s77, v13\n\t" \
  "v_add3_u32 v13, v13, s27, -1\n\t" "s_or_b64 s[68:69], s[68:69], vcc\n\t" "v_and_b32 v13, 3, v13\n\t" "v_cndmask_b32 v12, 0, 50, vcc\n\t" "v_xor_b32 v13, v13, v12\n\t" "v_lshlrev_b32 v7, 4, v9\n\t" \
  "v_lshl_add_u32 v7, v13, 8, v7\n\t" "v_lshl_add_u32 v12, s33, 8, v5\n\t" "v_lshl_add_u32 v13, s33, 8, v4\n\t" "s_setprio 1\n\t" "s_movk_i32 s38, 9\n\t" "s_mov_b32 s77, 36864\n\t" \
  "v_mov_b64 v[40:41], 0\n\t" "v_mov_b64 v[42:43], 0\n\t" "v_mov_b64 v[44:45], 0\n\t" "v_mov_b64 v[46:47], 0\n\t" "v_mov_b64 v[48:49], 0\n\t" "v_mov_b64 v[50:51], 0\n\t" "v_mov_b64 v[52:53], 0\n\t" \
  "v_mov_b64 v[54:55], 0\n\t" "v_mov_b64 v[56:57], 0\n\t" "v_mov_b64 v[58:59], 0\n\t" "v_mov_b64 v[60:61], 0\n\t" "v_mov_b64 v[62:63], 0\n\t" "v_mov_b64 v[64:65], 0\n\t" "v_mov_b64 v[66:67], 0\n\t" \
  "v_mov_b64 v[68:69], 0\n\t" "v_mov_b64 v[70:71], 0\n\t" "v_mov_b64 v[72:73], 0\n\t" "v_mov_b64 v[74:75], 0\n\t" "v_mov_b64 v[76:77], 0\n\t" "v_mov_b64 v[78:79], 0\n\t" "v_mov_b64 v[80:81], 0\n\t" \
  "v_mov_b64 v[82:83], 0\n\t" "v_mov_b64 v[84:85], 0\n\t" "v_mov_b64 v[86:87], 0\n\t" "v_mov_b64 v[88:89], 0\n\t" "v_mov_b64 v[90:91], 0\n\t" "v_mov_b64 v[92:93], 0\n\t" "v_mov_b64 v[94:95], 0\n\t" \
  "v_mov_b64 v[96:97], 0\n\t" "v_mov_b64 v[98:99], 0\n\t" "v_mov_b64 v[100:101], 0\n\t" "v_mov_b64 v[102:103], 0\n\t" "v_mov_b64 v[104:105], 0\n\t" "v_mov_b64 v[106:107], 0\n\t" \
  "v_mov_b64 v[108:109], 0\n\t" "v_mov_b64 v[110:111], 0\n\t" "s_barrier\n\t" "ds_read_b64_tr_b16 v[136:137], v1 offset:0\n\t" "ds_read_b64_tr_b16 v[138:139], v1 offset:512\n\t" \
  "ds_read_b128 v[112:115], v2 offset:0\n\t" "ds_read_b64_tr_b16 v[140:141], v1 offset:1024\n\t" "ds_read_b64_tr_b16 v[142:143], v1 offset:1536\n\t" "ds_read_b64_tr_b16 v[144:145], v1 offset:2048\n\t" \
  "ds_read_b64_tr_b16 v[146:147], v1 offset:2560\n\t" "ds_read_b128 v[116:119], v2 offset:2048\n\t" "ds_read_b128 v[120:123], v2 offset:4096\n\t" "ds_read_b128 v[124:127], v2 offset:6144\n\t" \
  "ds_read_b128 v[128:131], v2 offset:8192\n\t" "ds_read_b128 v[132:135], v2 offset:10240\n\t" "s_branch .Lb_%=\n\t" ".La_%=:\n\t" "s_waitcnt lgkmcnt(9)\n\t" \
  "v_mfma_f32_16x16x32_bf16 v[40:43], v[148:151], v[112:115], v[40:43]\n\t" "v_xor_b32 v3, 64, v2\n\t" "s_waitcnt lgkmcnt(7)\n\t" \
  "v_mfma_f32_16x16x32_bf16 v[64:67], v[152:155], v[112:115], v[64:67]\n\t" "s_waitcnt lgkmcnt(5)\n\t" "v_mfma_f32_16x16x32_bf16 v[88:91], v[112:115], v[156:159], v[88:91]\n\t" \
  "s_waitcnt lgkmcnt(4)\n\t" "v_mfma_f32_16x16x32_bf16 v[44:47], v[148:151], v[116:119], v[44:47]\n\t" "v_mfma_f32_16x16x32_bf16 v[68:71], v[152:155], v[116:119], v[68:71]\n\t" \
  "v_mfma_f32_16x16x32_bf16 v[92:95], v[116:119], v[156:159], v[92:95]\n\t" "s_waitcnt lgkmcnt(0)\n\t" "s_barrier\n\t" "v_mfma_f32_16x16x32_bf16 v[48:51], v[148:151], v[120:123], v[48:51]\n\t" \
  "ds_read_b64_tr_b16 v[136:137], v1 offset:0\n\t" "ds_read_b64_tr_b16 v[138:139], v1 offset:512\n\t" "v_mfma_f32_16x16x32_bf16 v[72:75], v[152:155], v[120:123], v[72:75]\n\t" \
  "ds_read_b128 v[112:115], v2 offset:0\n\t" "ds_read_b64_tr_b16 v[140:141], v1 offset:1024\n\t" "v_mfma_f32_16x16x32_bf16 v[96:99], v[120:123], v[156:159], v[96:99]\n\t" \
  "ds_read_b64_tr_b16 v[142:143], v1 offset:1536\n\t" "ds_read_b64_tr_b16 v[144:145], v1 offset:2048\n\t" "v_mfma_f32_16x16x32_bf16 v[52:55], v[148:151], v[124:127], v[52:55]\n\t" \
  "ds_read_b64_tr_b16 v[146:147], v1 offset:2560\n\t" "ds_read_b128 v[116:119], v2 offset:2048\n\t" "v_mfma_f32_16x16x32_bf16 v[76:79], v[152:155], v[124:127], v[76:79]\n\t" \
  "ds_read_b128 v[120:123], v2 offset:4096\n\t" "v_mfma_f32_16x16x32_bf16 v[100:103], v[124:127], v[156:159], v[100:103]\n\t" "ds_read_b128 v[124:127], v2 offset:6144\n\t" \
  "v_mfma_f32_16x16x32_bf16 v[56:59], v[148:151], v[128:131], v[56:59]\n\t" "v_mfma_f32_16x16x32_bf16 v[80:83], v[152:155], v[128:131], v[80:83]\n\t" \
  "v_mfma_f32_16x16x32_bf16 v[104:107], v[128:131], v[156:159], v[104:107]\n\t" "ds_read_b128 v[128:131], v2 offset:8192\n\t" "v_mfma_f32_16x16x32_bf16 v[60:63], v[148:151], v[132:135], v[60:63]\n\t" \
  "v_mfma_f32_16x16x32_bf16 v[84:87], v[152:155], v[132:135], v[84:87]\n\t" "v_mfma_f32_16x16x32_bf16 v[108:111], v[132:135], v[156:159], v[108:111]\n\t" "ds_read_b128 v[132:135], v2 offset:10240\n\t" \
  ".Lb_%=:\n\t" "s_waitcnt lgkmcnt(9)\n\t" "v_mfma_f32_16x16x32_bf16 v[40:43], v[136:139], v[112:115], v[40:43]\n\t" "ds_read_b64_tr_b16 v[148:149], v1 offset:12288\n\t" "s_waitcnt lgkmcnt(8)\n\t" \
  "v_mfma_f32_16x16x32_bf16 v[64:67], v[140:143], v[112:115], v[64:67]\n\t" "ds_read_b64_tr_b16 v[150:151], v1 offset:12800\n\t" "s_waitcnt lgkmcnt(7)\n\t" \
  "v_mfma_f32_16x16x32_bf16 v[88:91], v[112:115], v[144:147], v[88:91]\n\t" "ds_read_b128 v[112:115], v3 offset:0\n\t" "s_waitcnt lgkmcnt(7)\n\t" \
  "v_mfma_f32_16x16x32_bf16 v[44:47], v[136:139], v[116:119], v[44:47]\n\t" "ds_read_b64_tr_b16 v[152:153], v1 offset:13312\n\t" \
  "v_mfma_f32_16x16x32_bf16 v[68:71], v[140:143], v[116:119], v[68:71]\n\t" "ds_read_b64_tr_b16 v[154:155], v1 offset:13824\n\t" \
  "v_mfma_f32_16x16x32_bf16 v[92:95], v[116:119], v[144:147], v[92:95]\n\t" "ds_read_b64_tr_b16 v[156:157], v1 offset:14336\n\t" "s_waitcnt lgkmcnt(9)\n\t" \
  "v_mfma_f32_16x16x32_bf16 v[48:51], v[136:139], v[120:123], v[48:51]\n\t" "ds_read_b64_tr_b16 v[158:159], v1 offset:14848\n\t" \
  "v_mfma_f32_16x16x32_bf16 v[72:75], v[140:143], v[120:123], v[72:75]\n\t" "ds_read_b128 v[116:119], v3 offset:2048\n\t" "v_mfma_f32_16x16x32_bf16 v[96:99], v[120:123], v[144:147], v[96:99]\n\t" \
  "ds_read_b128 v[120:123], v3 offset:4096\n\t" "s_waitcnt lgkmcnt(11)\n\t" "v_mfma_f32_16x16x32_bf16 v[52:55], v[136:139], v[124:127], v[52:55]\n\t" "s_bitcmp1_b32 0x44, s38\n\t" \
  "v_mfma_f32_16x16x32_bf16 v[76:79], v[140:143], v[124:127], v[76:79]\n\t" "s_cselect_b32 s76, -110592, s77\n\t" "v_add_u32 v1, s76, v1\n\t" \
  "v_mfma_f32_16x16x32_bf16 v[100:103], v[124:127], v[144:147], v[100:103]\n\t" "ds_read_b128 v[124:127], v3 offset:6144\n\t" "s_waitcnt lgkmcnt(11)\n\t" \
  "v_mfma_f32_16x16x32_bf16 v[56:59], v[136:139], v[128:131], v[56:59]\n\t" "v_add_u32 v2, s76, v2\n\t" "v_mfma_f32_16x16x32_bf16 v[80:83], v[140:143], v[128:131], v[80:83]\n\t" \
  "s_sub_u32 s38, s38, 1\n\t" "v_mfma_f32_16x16x32_bf16 v[104:107], v[128:131], v[144:147], v[104:107]\n\t" "ds_read_b128 v[128:131], v3 offset:8192\n\t" "s_waitcnt lgkmcnt(11)\n\t" \
  "v_mfma_f32_16x16x32_bf16 v[60:63], v[136:139], v[132:135], v[60:63]\n\t" "v_mfma_f32_16x16x32_bf16 v[84:87], v[140:143], v[132:135], v[84:87]\n\t" \
  "v_mfma_f32_16x16x32_bf16 v[108:111], v[132:135], v[144:147], v[108:111]\n\t" "ds_read_b128 v[132:135], v3 offset:10240\n\t" "s_cbranch_scc0 .La_%=\n\t" "s_waitcnt lgkmcnt(9)\n\t" \
  "v_mfma_f32_16x16x32_bf16 v[40:43], v[148:151], v[112:115], v[40:43]\n\t" "s_waitcnt lgkmcnt(7)\n\t" "v_mfma_f32_16x16x32_bf16 v[64:67], v[152:155], v[112:115], v[64:67]\n\t" \
  "s_waitcnt lgkmcnt(4)\n\t" "v_mfma_f32_16x16x32_bf16 v[44:47], v[148:151], v[116:119], v[44:47]\n\t" "ds_read_b32 v39, v37 offset:12\n\t" "ds_read_b128 v[160:163], v12 offset:1024\n\t" \
  "v_mfma_f32_16x16x32_bf16 v[68:71], v[152:155], v[116:119], v[68:71]\n\t" "ds_read_b128 v[164:167], v12 offset:1088\n\t" "ds_read2_b32 v[136:137], v4 offset0:0 offset1:96\n\t" \
  "s_waitcnt lgkmcnt(7)\n\t" "v_mfma_f32_16x16x32_bf16 v[48:51], v[148:151], v[120:123], v[48:51]\n\t" "ds_read2_b32 v[138:139], v4 offset0:16 offset1:112\n\t" \
  "ds_read2_b32 v[140:141], v4 offset0:32 offset1:128\n\t" "v_mfma_f32_16x16x32_bf16 v[72:75], v[152:155], v[120:123], v[72:75]\n\t" "ds_read2_b32 v[142:143], v4 offset0:48 offset1:144\n\t" \
  "ds_read2_b32 v[144:145], v4 offset0:64 offset1:160\n\t" "s_waitcnt lgkmcnt(10)\n\t" "v_mfma_f32_16x16x32_bf16 v[52:55], v[148:151], v[124:127], v[52:55]\n\t" \
  "ds_read2_b32 v[146:147], v4 offset0:80 offset1:176\n\t" "ds_read_b32 v14, v13 offset:1152\n\t" "v_mfma_f32_16x16x32_bf16 v[76:79], v[152:155], v[124:127], v[76:79]\n\t" "s_waitcnt lgkmcnt(11)\n\t" \
  "v_mfma_f32_16x16x32_bf16 v[56:59], v[148:151], v[128:131], v[56:59]\n\t" "s_waitcnt lgkmcnt(0)\n\t" "v_readfirstlane_b32 s38, v39\n\t" "s_cmp_ge_u32 s38, 4\n\t" "s_cbranch_scc0 .Lts_%=\n\t" \
  ".Ltr_%=:\n\t" "v_fma_f32 v40, -v160, v136, v40\n\t" "v_fma_f32 v41, -v161, v136, v41\n\t" "v_mfma_f32_16x16x32_bf16 v[80:83], v[152:155], v[128:131], v[80:83]\n\t" \
  "v_fma_f32 v42, -v162, v136, v42\n\t" "v_fma_f32 v43, -v163, v136, v43\n\t" "v_mfma_f32_16x16x32_bf16 v[60:63], v[148:151], v[132:135], v[60:63]\n\t" "v_fma_f32 v64, -v164, v136, v64\n\t" \
  "v_fma_f32 v65, -v165, v136, v65\n\t" "v_mfma_f32_16x16x32_bf16 v[84:87], v[152:155], v[132:135], v[84:87]\n\t" "v_fma_f32 v66, -v166, v136, v66\n\t" "v_fma_f32 v67, -v167, v136, v67\n\t" \
  "v_mfma_f32_16x16x32_bf16 v[88:91], v[112:115], v[156:159], v[88:91]\n\t" "v_mul_f32 v40, v40, v137\n\t" "v_mul_f32 v41, v41, v137\n\t" \
  "v_mfma_f32_16x16x32_bf16 v[92:95], v[116:119], v[156:159], v[92:95]\n\t" "v_mul_f32 v42, v42, v137\n\t" "v_mul_f32 v43, v43, v137\n\t" \
  "v_mfma_f32_16x16x32_bf16 v[96:99], v[120:123], v[156:159], v[96:99]\n\t" "v_mul_f32 v64, v64, v137\n\t" "v_mul_f32 v65, v65, v137\n\t" \
  "v_mfma_f32_16x16x32_bf16 v[100:103], v[124:127], v[156:159], v[100:103]\n\t" "v_mul_f32 v66, v66, v137\n\t" "v_mul_f32 v67, v67, v137\n\t" \
  "v_mfma_f32_16x16x32_bf16 v[104:107], v[128:131], v[156:159], v[104:107]\n\t" "v_cvt_pk_bf16_f32 v40, v40, v41\n\t" "v_cvt_pk_bf16_f32 v41, v42, v43\n\t" \
  "v_mfma_f32_16x16x32_bf16 v[108:111], v[132:135], v[156:159], v[108:111]\n\t" "v_cvt_pk_bf16_f32 v42, v64, v65\n\t" "v_cvt_pk_bf16_f32 v43, v66, v67\n\t" "ds_read_b128 v[112:115], v5 offset:0\n\t" \
  "ds_read_b128 v[116:119], v5 offset:384\n\t" "ds_read_b128 v[120:123], v5 offset:64\n\t" "ds_read_b128 v[124:127], v5 offset:448\n\t" "ds_read_b128 v[128:131], v5 offset:128\n\t" \
  "ds_read_b128 v[132:135], v5 offset:512\n\t" "v_fma_f32 v44, -v160, v138, v44\n\t" "v_fma_f32 v45, -v161, v138, v45\n\t" "v_fma_f32 v46, -v162, v138, v46\n\t" "v_fma_f32 v47, -v163, v138, v47\n\t" \
  "s_mov_b64 exec, s[68:69]\n\t" "global_store_dwordx4 v7, v[40:43], s[66:67] offset:-2048 sc1\n\t" "s_mov_b64 exec, -1\n\t" "ds_write2st64_b64 v6, v[40:41], v[42:43] offset0:0 offset1:24\n\t" \
  "s_mov_b64 exec, 1\n\t" "ds_add_u32 v37, v38 offset:8\n\t" "s_mov_b64 exec, -1\n\t" "v_pk_fma_f32 v[68:69], v[164:165], v[138:139], v[68:69] op_sel_hi:[1,0,1] neg_lo:[1,0,0] neg_hi:[1,0,0]\n\t" \
  "v_pk_fma_f32 v[70:71], v[166:167], v[138:139], v[70:71] op_sel_hi:[1,0,1] neg_lo:[1,0,0] neg_hi:[1,0,0]\n\t" "v_pk_mul_f32 v[44:45], v[44:45], v[138:139] op_sel:[0,1] op_sel_hi:[1,1]\n\t" \
  "v_pk_mul_f32 v[46:47], v[46:47], v[138:139] op_sel:[0,1] op_sel_hi:[1,1]\n\t" "v_pk_mul_f32 v[68:69], v[68:69], v[138:139] op_sel:[0,1] op_sel_hi:[1,1]\n\t" \
  "v_pk_mul_f32 v[70:71], v[70:71], v[138:139] op_sel:[0,1] op_sel_hi:[1,1]\n\t" "v_cvt_pk_bf16_f32 v44, v44, v45\n\t" "v_cvt_pk_bf16_f32 v45, v46, v47\n\t" "v_cvt_pk_bf16_f32 v46, v68, v69\n\t" \
  "v_cvt_pk_bf16_f32 v47, v70, v71\n\t" "s_mov_b64 exec, s[68:69]\n\t" "global_store_dwordx4 v7, v[44:47], s[66:67] offset:-1024 sc1\n\t" "s_mov_b64 exec, -1\n\t" \
  "ds_write2st64_b64 v6, v[44:45], v[46:47] offset0:1 offset1:25\n\t" "ds_read_b128 v[64:67], v5 offset:192\n\t" "ds_read_b128 v[68:71], v5 offset:576\n\t" \
  "v_pk_fma_f32 v[48:49], v[160:161], v[140:141], v[48:49] op_sel_hi:[1,0,1] neg_lo:[1,0,0] neg_hi:[1,0,0]\n\t" \
  "v_pk_fma_f32 v[50:51], v[162:163], v[140:141], v[50:51] op_sel_hi:[1,0,1] neg_lo:[1,0,0] neg_hi:[1,0,0]\n\t" \
  "v_pk_fma_f32 v[72:73], v[164:165], v[140:141], v[72:73] op_sel_hi:[1,0,1] neg_lo:[1,0,0] neg_hi:[1,0,0]\n\t" \
  "v_pk_fma_f32 v[74:75], v[166:167], v[140:141], v[74:75] op_sel_hi:[1,0,1] neg_lo:[1,0,0] neg_hi:[1,0,0]\n\t" "v_pk_mul_f32 v[48:49], v[48:49], v[140:141] op_sel:[0,1] op_sel_hi:[1,1]\n\t" \
  "v_pk_mul_f32 v[50:51], v[50:51], v[140:141] op_sel:[0,1] op_sel_hi:[1,1]\n\t" "v_pk_mul_f32 v[72:73], v[72:73], v[140:141] op_sel:[0,1] op_sel_hi:[1,1]\n\t" \
  "v_pk_mul_f32 v[74:75], v[74:75], v[140:141] op_sel:[0,1] op_sel_hi:[1,1]\n\t" "v_cvt_pk_bf16_f32 v48, v48, v49\n\t" "v_cvt_pk_bf16_f32 v49, v50, v51\n\t" "v_cvt_pk_bf16_f32 v50, v72, v73\n\t" \
  "v_cvt_pk_bf16_f32 v51, v74, v75\n\t" "s_mov_b64 exec, s[68:69]\n\t" "global_store_dwordx4 v7, v[48:51], s[66:67] offset:0 sc1\n\t" "s_mov_b64 exec, -1\n\t" \
  "ds_write2st64_b64 v6, v[48:49], v[50:51] offset0:2 offset1:26\n\t" "v_pk_fma_f32 v[52:53], v[160:161], v[142:143], v[52:53] op_sel_hi:[1,0,1] neg_lo:[1,0,0] neg_hi:[1,0,0]\n\t" \
  "v_pk_fma_f32 v[54:55], v[162:163], v[142:143], v[54:55] op_sel_hi:[1,0,1] neg_lo:[1,0,0] neg_hi:[1,0,0]\n\t" \
  "v_pk_fma_f32 v[76:77], v[164:165], v[142:143], v[76:77] op_sel_hi:[1,0,1] neg_lo:[1,0,0] neg_hi:[1,0,0]\n\t" \
  "v_pk_fma_f32 v[78:79], v[166:167], v[142:143], v[78:79] op_sel_hi:[1,0,1] neg_lo:[1,0,0] neg_hi:[1,0,0]\n\t" "v_pk_mul_f32 v[52:53], v[52:53], v[142:143] op_sel:[0,1] op_sel_hi:[1,1]\n\t" \
  "v_pk_mul_f32 v[54:55], v[54:55], v[142:143] op_sel:[0,1] op_sel_hi:[1,1]\n\t" "v_pk_mul_f32 v[76:77], v[76:77], v[142:143] op_sel:[0,1] op_sel_hi:[1,1]\n\t" \
  "v_pk_mul_f32 v[78:79], v[78:79], v[142:143] op_sel:[0,1] op_sel_hi:[1,1]\n\t" "v_cvt_pk_bf16_f32 v52, v52, v53\n\t" "v_cvt_pk_bf16_f32 v53, v54, v55\n\t" "v_cvt_pk_bf16_f32 v54, v76, v77\n\t" \
  "v_cvt_pk_bf16_f32 v55, v78, v79\n\t" "s_mov_b64 exec, s[68:69]\n\t" "global_store_dwordx4 v7, v[52:55], s[66:67] offset:1024 sc1\n\t" "s_mov_b64 exec, -1\n\t" \
  "ds_write2st64_b64 v6, v[52:53], v[54:55] offset0:3 offset1:27\n\t" "ds_read_b128 v[72:75], v5 offset:256\n\t" "ds_read_b128 v[76:79], v5 offset:640\n\t" \
  "v_pk_fma_f32 v[56:57], v[160:161], v[144:145], v[56:57] op_sel_hi:[1,0,1] neg_lo:[1,0,0] neg_hi:[1,0,0]\n\t" \
  "v_pk_fma_f32 v[58:59], v[162:163], v[144:145], v[58:59] op_sel_hi:[1,0,1] neg_lo:[1,0,0] neg_hi:[1,0,0]\n\t" \
  "v_pk_fma_f32 v[80:81], v[164:165], v[144:145], v[80:81] op_sel_hi:[1,0,1] neg_lo:[1,0,0] neg_hi:[1,0,0]\n\t" \
  "v_pk_fma_f32 v[82:83], v[166:167], v[144:145], v[82:83] op_sel_hi:[1,0,1] neg_lo:[1,0,0] neg_hi:[1,0,0]\n\t" "v_pk_mul_f32 v[56:57], v[56:57], v[144:145] op_sel:[0,1] op_sel_hi:[1,1]\n\t" \
  "v_pk_mul_f32 v[58:59], v[58:59], v[144:145] op_sel:[0,1] op_sel_hi:[1,1]\n\t" "v_pk_mul_f32 v[80:81], v[80:81], v[144:145] op_sel:[0,1] op_sel_hi:[1,1]\n\t" \
  "v_pk_mul_f32 v[82:83], v[82:83], v[144:145] op_sel:[0,1] op_sel_hi:[1,1]\n\t" "v_cvt_pk_bf16_f32 v56, v56, v57\n\t" "v_cvt_pk_bf16_f32 v57, v58, v59\n\t" "v_cvt_pk_bf16_f32 v58, v80, v81\n\t" \
  "v_cvt_pk_bf16_f32 v59, v82, v83\n\t" "s_mov_b64 exec, s[68:69]\n\t" "global_store_dwordx4 v7, v[56:59], s[66:67] offset:2048 sc1\n\t" "s_mov_b64 exec, -1\n\t" \
  "ds_write2st64_b64 v6, v[56:57], v[58:59] offset0:4 offset1:28\n\t" "v_pk_fma_f32 v[60:61], v[160:161], v[146:147], v[60:61] op_sel_hi:[1,0,1] neg_lo:[1,0,0] neg_hi:[1,0,0]\n\t" \
  "v_pk_fma_f32 v[62:63], v[162:163], v[146:147], v[62:63] op_sel_hi:[1,0,1] neg_lo:[1,0,0] neg_hi:[1,0,0]\n\t" \
  "v_pk_fma_f32 v[84:85], v[164:165], v[146:147], v[84:85] op_sel_hi:[1,0,1] neg_lo:[1,0,0] neg_hi:[1,0,0]\n\t" \
  "v_pk_fma_f32 v[86:87], v[166:167], v[146:147], v[86:87] op_sel_hi:[1,0,1] neg_lo:[1,0,0] neg_hi:[1,0,0]\n\t" "v_pk_mul_f32 v[60:61], v[60:61], v[146:147] op_sel:[0,1] op_sel_hi:[1,1]\n\t" \
  "v_pk_mul_f32 v[62:63], v[62:63], v[146:147] op_sel:[0,1] op_sel_hi:[1,1]\n\t" "v_pk_mul_f32 v[84:85], v[84:85], v[146:147] op_sel:[0,1] op_sel_hi:[1,1]\n\t" \
  "v_pk_mul_f32 v[86:87], v[86:87], v[146:147] op_sel:[0,1] op_sel_hi:[1,1]\n\t" "v_cvt_pk_bf16_f32 v60, v60, v61\n\t" "v_cvt_pk_bf16_f32 v61, v62, v63\n\t" "v_cvt_pk_bf16_f32 v62, v84, v85\n\t" \
  "v_cvt_pk_bf16_f32 v63, v86, v87\n\t" "s_mov_b64 exec, s[68:69]\n\t" "global_store_dwordx4 v7, v[60:63], s[66:67] offset:3072 sc1\n\t" "s_mov_b64 exec, -1\n\t" \
  "ds_write2st64_b64 v6, v[60:61], v[62:63] offset0:5 offset1:29\n\t" "ds_read_b128 v[80:83], v5 offset:320\n\t" "ds_read_b128 v[84:87], v5 offset:704\n\t" "s_mov_b64 exec, 1\n\t" \
  "ds_add_u32 v37, v38 offset:0\n\t" "s_mov_b64 exec, -1\n\t" "s_waitcnt lgkmcnt(14)\n\t" "v_pk_fma_f32 v[88:89], v[112:113], v[14:15], v[88:89] op_sel_hi:[1,0,1] neg_lo:[1,0,0] neg_hi:[1,0,0]\n\t" \
  "v_pk_fma_f32 v[90:91], v[114:115], v[14:15], v[90:91] op_sel_hi:[1,0,1] neg_lo:[1,0,0] neg_hi:[1,0,0]\n\t" \
  "v_pk_fma_f32 v[92:93], v[120:121], v[14:15], v[92:93] op_sel_hi:[1,0,1] neg_lo:[1,0,0] neg_hi:[1,0,0]\n\t" \
  "v_pk_fma_f32 v[94:95], v[122:123], v[14:15], v[94:95] op_sel_hi:[1,0,1] neg_lo:[1,0,0] neg_hi:[1,0,0]\n\t" "v_pk_mul_f32 v[88:89], v[88:89], v[116:117]\n\t" \
  "v_pk_mul_f32 v[90:91], v[90:91], v[118:119]\n\t" "v_pk_mul_f32 v[92:93], v[92:93], v[124:125]\n\t" "v_pk_mul_f32 v[94:95], v[94:95], v[126:127]\n\t" "v_cvt_pk_bf16_f32 v88, v88, v89\n\t" \
  "v_cvt_pk_bf16_f32 v89, v90, v91\n\t" "v_cvt_pk_bf16_f32 v92, v92, v93\n\t" "v_cvt_pk_bf16_f32 v93, v94, v95\n\t" "ds_write2st64_b64 v6, v[88:89], v[92:93] offset0:48 offset1:49\n\t" \
  "s_waitcnt lgkmcnt(10)\n\t" "v_pk_fma_f32 v[96:97], v[128:129], v[14:15], v[96:97] op_sel_hi:[1,0,1] neg_lo:[1,0,0] neg_hi:[1,0,0]\n\t" \
  "v_pk_fma_f32 v[98:99], v[130:131], v[14:15], v[98:99] op_sel_hi:[1,0,1] neg_lo:[1,0,0] neg_hi:[1,0,0]\n\t" \
  "v_pk_fma_f32 v[100:101], v[64:65], v[14:15], v[100:101] op_sel_hi:[1,0,1] neg_lo:[1,0,0] neg_hi:[1,0,0]\n\t" \
  "v_pk_fma_f32 v[102:103], v[66:67], v[14:15], v[102:103] op_sel_hi:[1,0,1] neg_lo:[1,0,0] neg_hi:[1,0,0]\n\t" "v_pk_mul_f32 v[96:97], v[96:97], v[132:133]\n\t" \
  "v_pk_mul_f32 v[98:99], v[98:99], v[134:135]\n\t" "v_pk_mul_f32 v[100:101], v[100:101], v[68:69]\n\t" "v_pk_mul_f32 v[102:103], v[102:103], v[70:71]\n\t" "v_cvt_pk_bf16_f32 v96, v96, v97\n\t" \
  "v_cvt_pk_bf16_f32 v97, v98, v99\n\t" "v_cvt_pk_bf16_f32 v100, v100, v101\n\t" "v_cvt_pk_bf16_f32 v101, v102, v103\n\t" "ds_write2st64_b64 v6, v[96:97], v[100:101] offset0:50 offset1:51\n\t" \
  "s_waitcnt lgkmcnt(3)\n\t" "v_pk_fma_f32 v[104:105], v[72:73], v[14:15], v[104:105] op_sel_hi:[1,0,1] neg_lo:[1,0,0] neg_hi:[1,0,0]\n\t" \
  "v_pk_fma_f32 v[106:107], v[74:75], v[14:15], v[106:107] op_sel_hi:[1,0,1] neg_lo:[1,0,0] neg_hi:[1,0,0]\n\t" \
  "v_pk_fma_f32 v[108:109], v[80:81], v[14:15], v[108:109] op_sel_hi:[1,0,1] neg_lo:[1,0,0] neg_hi:[1,0,0]\n\t" \
  "v_pk_fma_f32 v[110:111], v[82:83], v[14:15], v[110:111] op_sel_hi:[1,0,1] neg_lo:[1,0,0] neg_hi:[1,0,0]\n\t" "v_pk_mul_f32 v[104:105], v[104:105], v[76:77]\n\t" \
  "v_pk_mul_f32 v[106:107], v[106:107], v[78:79]\n\t" "v_pk_mul_f32 v[108:109], v[108:109], v[84:85]\n\t" "v_pk_mul_f32 v[110:111], v[110:111], v[86:87]\n\t" "v_cvt_pk_bf16_f32 v104, v104, v105\n\t" \
  "v_cvt_pk_bf16_f32 v105, v106, v107\n\t" "v_cvt_pk_bf16_f32 v108, v108, v109\n\t" "v_cvt_pk_bf16_f32 v109, v110, v111\n\t" "ds_write2st64_b64 v6, v[104:105], v[108:109] offset0:52 offset1:53\n\t" \
  "s_mov_b64 exec, 1\n\t" "ds_add_u32 v37, v38 offset:4\n\t" "s_mov_b64 exec, -1\n\t" "s_setprio 1\n\t" ".Latt_%=:\n\t" "global_load_dwordx4 v[12:15], v33, s[8:9]\n\t" \
  "global_load_dwordx4 v[16:19], v34, s[14:15]\n\t" "s_cmp_eq_u32 s34, 0\n\t" "s_cbranch_scc1 .Lk0_%=\n\t" ".Lq_%=:\n\t" "ds_read_b32 v39, v37\n\t" "ds_read_b64 v[40:41], v30 offset:0\n\t" \
  "ds_read_b64 v[42:43], v30 offset:3072\n\t" "ds_read_b64 v[44:45], v30 offset:11776\n\t" "ds_read_b64 v[46:47], v30 offset:14848\n\t" "ds_read_b64 v[48:49], v30 offset:12288\n\t" \
  "ds_read_b64 v[50:51], v30 offset:15360\n\t" "ds_read_b64 v[52:53], v31 offset:0\n\t" "ds_read_b64 v[54:55], v31 offset:3072\n\t" "ds_read_b64 v[56:57], v31 offset:11776\n\t" \
  "ds_read_b64 v[58:59], v31 offset:14848\n\t" "ds_read_b64 v[60:61], v31 offset:12288\n\t" "ds_read_b64 v[62:63], v31 offset:15360\n\t" "s_waitcnt lgkmcnt(12)\n\t" "v_readfirstlane_b32 s38, v39\n\t" \
  "s_cmp_ge_u32 s38, 4\n\t" "s_cbranch_scc0 .Lsq_%=\n\t" ".Lrq_%=:\n\t" "s_waitcnt lgkmcnt(10)\n\t" "v_cndmask_b32 v42, 0, v42, s[54:55]\n\t" "v_cndmask_b32 v43, 0, v43, s[54:55]\n\t" \
  "s_waitcnt lgkmcnt(8)\n\t" "s_nop 0\n\t" "v_mfma_f32_16x16x32_bf16 v[108:111], v[44:47], v[40:43], v[20:23]\n\t" "s_waitcnt lgkmcnt(6)\n\t" \
  "v_mfma_f32_16x16x32_bf16 v[112:115], v[48:51], v[40:43], v[24:27]\n\t" "s_waitcnt lgkmcnt(4)\n\t" "v_cndmask_b32 v52, 0, v52, s[54:55]\n\t" "v_cndmask_b32 v53, 0, v53, s[54:55]\n\t" \
  "s_waitcnt lgkmcnt(2)\n\t" "s_nop 0\n\t" "v_mfma_f32_16x16x32_bf16 v[116:119], v[56:59], v[52:55], v[20:23]\n\t" "s_waitcnt lgkmcnt(0)\n\t" \
  "v_mfma_f32_16x16x32_bf16 v[120:123], v[60:63], v[52:55], v[24:27]\n\t" "ds_read_b32 v39, v37 offset:4\n\t" "ds_read_b64 v[128:129], v30 offset:24064\n\t" \
  "ds_read_b64 v[130:131], v30 offset:24576\n\t" "ds_read_b64 v[132:133], v30 offset:27136\n\t" "ds_read_b64 v[134:135], v30 offset:27648\n\t" "ds_read_b64 v[136:137], v31 offset:24064\n\t" \
  "ds_read_b64 v[138:139], v31 offset:24576\n\t" "ds_read_b64 v[140:141], v31 offset:27136\n\t" "ds_read_b64 v[142:143], v31 offset:27648\n\t" ".Lk1_%=:\n\t" "s_nop 4\n\t" \
  "v_max_f32 v108, v108, v112\n\t" "v_max_f32 v109, v109, v113\n\t" "v_max_f32 v110, v110, v114\n\t" "v_max_f32 v111, v111, v115\n\t" "v_max_f32 v116, v116, v120\n\t" "v_max_f32 v117, v117, v121\n\t" \
  "v_max_f32 v118, v118, v122\n\t" "v_max_f32 v119, v119, v123\n\t" "v_max3_f32 v124, v108, v109, v110\n\t" "v_max3_f32 v125, v116, v117, v118\n\t" "v_max_f32 v124, v124, v111\n\t" \
  "v_max_f32 v125, v125, v119\n\t" "s_mov_b32 s38, 0x3ea52b5c\n\t" "s_nop 0\n\t" "v_permlane32_swap_b32 v124, v125\n\t" "v_max_f32 v126, v124, v125\n\t" "v_max_f32 v127, v124, v125\n\t" "s_nop 1\n\t" \
  "v_permlane16_swap_b32 v126, v127\n\t" "v_max_f32 v124, v126, v127\n\t" "v_mul_f32 v125, s38, v124\n\t" "v_mul_f32 v124, s38, v124\n\t" "s_nop 1\n\t" "v_permlane32_swap_b32 v124, v125\n\t" \
  "v_fma_f32 v108, v108, s38, -v124\n\t" "v_fma_f32 v116, v116, s38, -v125\n\t" "v_fma_f32 v109, v109, s38, -v124\n\t" "v_fma_f32 v117, v117, s38, -v125\n\t" "v_fma_f32 v110, v110, s38, -v124\n\t" \
  "v_fma_f32 v118, v118, s38, -v125\n\t" "v_fma_f32 v111, v111, s38, -v124\n\t" "v_fma_f32 v119, v119, s38, -v125\n\t" "v_exp_f32 v108, v108\n\t" "v_exp_f32 v116, v116\n\t" "v_exp_f32 v109, v109\n\t" \
  "v_exp_f32 v117, v117\n\t" "v_exp_f32 v110, v110\n\t" "v_exp_f32 v118, v118\n\t" "v_add_f32 v124, v108, v109\n\t" "v_add_f32 v125, v116, v117\n\t" "v_exp_f32 v111, v111\n\t" \
  "v_exp_f32 v119, v119\n\t" "v_add_f32 v124, v124, v110\n\t" "v_add_f32 v125, v125, v118\n\t" "v_add_f32 v124, v124, v111\n\t" "v_add_f32 v125, v125, v119\n\t" "s_cmp_lg_u32 s34, 0\n\t" "s_nop 0\n\t" \
  "v_permlane32_swap_b32 v124, v125\n\t" "v_add_f32 v126, v124, v125\n\t" "v_add_f32 v127, v124, v125\n\t" "s_nop 1\n\t" "v_permlane16_swap_b32 v126, v127\n\t" "v_add_f32 v124, v126, v127\n\t" \
  "v_rcp_f32 v126, v124\n\t" "v_rcp_f32 v124, v124\n\t" "s_nop 1\n\t" "v_permlane32_swap_b32 v124, v126\n\t" "v_pk_mul_f32 v[108:109], v[108:109], v[124:125] op_sel_hi:[1,0]\n\t" \
  "v_pk_mul_f32 v[110:111], v[110:111], v[124:125] op_sel_hi:[1,0]\n\t" "v_pk_mul_f32 v[116:117], v[116:117], v[126:127] op_sel_hi:[1,0]\n\t" \
  "v_pk_mul_f32 v[118:119], v[118:119], v[126:127] op_sel_hi:[1,0]\n\t" "v_cvt_pk_bf16_f32 v112, v108, v109\n\t" "v_cvt_pk_bf16_f32 v113, v110, v111\n\t" "v_cvt_pk_bf16_f32 v120, v116, v117\n\t" \
  "v_cvt_pk_bf16_f32 v121, v118, v119\n\t" "v_and_b32 v114, v112, v28\n\t" "v_and_b32 v115, v113, v29\n\t" "v_and_b32 v122, v120, v28\n\t" "v_and_b32 v123, v121, v29\n\t" \
  "v_xor_b32 v112, v112, v114\n\t" "v_xor_b32 v113, v113, v115\n\t" "v_xor_b32 v120, v120, v122\n\t" "v_xor_b32 v121, v121, v123\n\t" "s_cbranch_scc1 .Lk2_%=\n\t" \
  "v_mfma_f32_16x16x32_bf16 v[152:155], v[128:131], v[112:115], 0\n\t" "v_mfma_f32_16x16x32_bf16 v[156:159], v[132:135], v[112:115], 0\n\t" \
  "v_mfma_f32_16x16x32_bf16 v[152:155], v[136:139], v[120:123], v[152:155]\n\t" "v_mfma_f32_16x16x32_bf16 v[156:159], v[140:143], v[120:123], v[156:159]\n\t" "v_xor_b32 v20, 32, v35\n\t" \
  "v_xor_b32 v21, 64, v35\n\t" "v_xor_b32 v22, 96, v35\n\t" "s_nop 3\n\t" "v_cvt_pk_bf16_f32 v148, v152, v153\n\t" "v_cvt_pk_bf16_f32 v149, v154, v155\n\t" "v_cvt_pk_bf16_f32 v150, v156, v157\n\t" \
  "v_cvt_pk_bf16_f32 v151, v158, v159\n\t" ".Lk3_%=:\n\t" "s_mov_b64 exec, s[54:55]\n\t" "global_store_dwordx4 v32, v[148:151], s[60:61] offset:2560 sc1\n\t" "s_mov_b64 exec, -1\n\t" \
  "s_cmp_lg_u32 s34, 0\n\t" "s_cbranch_scc1 .Lb3a_%=\n\t" "s_cmp_ge_u32 s26, 4\n\t" "s_cbranch_scc1 .Lv21_%=\n\t" "s_waitcnt vmcnt(1)\n\t" ".Lv21_%=:\n\t" "s_setprio 1\n\t" \
  "ds_read_b64 v[152:153], v37 offset:80\n\t" "s_waitcnt lgkmcnt(0)\n\t" "v_readfirstlane_b32 s80, v152\n\t" "v_readfirstlane_b32 s81, v153\n\t" "s_sub_u32 s81, s81, s80\n\t" \
  "s_sub_u32 s80, s81, 15\n\t" "s_barrier\n\t" "s_cbranch_scc1 .Llx1_%=\n\t" "s_memrealtime s[76:77]\n\t" ".Llw1_%=:\n\t" "s_memrealtime s[78:79]\n\t" "s_waitcnt lgkmcnt(0)\n\t" \
  "s_sub_u32 s78, s78, s76\n\t" "s_cmp_lt_u32 s78, s80\n\t" "s_cbranch_scc1 .Llw1_%=\n\t" ".Llx1_%=:\n\t" "global_load_dwordx4 v[152:155], v32, s[62:63] offset:-2048 sc1\n\t" \
  "global_load_dwordx4 v[156:159], v32, s[62:63] offset:-1024 sc1\n\t" "global_load_dwordx4 v[160:163], v32, s[62:63] offset:0 sc1\n\t" ".Lb3a_%=:\n\t" "ds_read_b64_tr_b16 v[40:41], v35 offset:0\n\t" \
  "ds_read_b64_tr_b16 v[42:43], v35 offset:2048\n\t" "ds_read_b64_tr_b16 v[44:45], v20 offset:0\n\t" "ds_read_b64_tr_b16 v[46:47], v20 offset:2048\n\t" "ds_read_b64_tr_b16 v[48:49], v21 offset:0\n\t" \
  "ds_read_b64_tr_b16 v[50:51], v21 offset:2048\n\t" "ds_read_b64_tr_b16 v[52:53], v22 offset:0\n\t" "ds_read_b64_tr_b16 v[54:55], v22 offset:2048\n\t" \
  "ds_read_b64_tr_b16 v[56:57], v35 offset:4096\n\t" "ds_read_b64_tr_b16 v[58:59], v35 offset:6144\n\t" "ds_read_b64_tr_b16 v[60:61], v20 offset:4096\n\t" \
  "ds_read_b64_tr_b16 v[62:63], v20 offset:6144\n\t" "ds_read_b64_tr_b16 v[64:65], v21 offset:4096\n\t" "ds_read_b64_tr_b16 v[66:67], v21 offset:6144\n\t" \
  "ds_read_b64_tr_b16 v[68:69], v22 offset:4096\n\t" "ds_read_b64_tr_b16 v[70:71], v22 offset:6144\n\t" "ds_read_b64_tr_b16 v[72:73], v35 offset:8192\n\t" \
  "ds_read_b64_tr_b16 v[74:75], v35 offset:10240\n\t" "ds_read_b64_tr_b16 v[76:77], v20 offset:8192\n\t" "ds_read_b64_tr_b16 v[78:79], v20 offset:10240\n\t" \
  "ds_read_b64_tr_b16 v[80:81], v21 offset:8192\n\t" "ds_read_b64_tr_b16 v[82:83], v21 offset:10240\n\t" "ds_read_b64_tr_b16 v[84:85], v22 offset:8192\n\t" \
  "ds_read_b64_tr_b16 v[86:87], v22 offset:10240\n\t" "ds_read_b64_tr_b16 v[88:89], v35 offset:12288\n\t" "ds_read_b64_tr_b16 v[90:91], v35 offset:14336\n\t" \
  "ds_read_b64_tr_b16 v[92:93], v20 offset:12288\n\t" "ds_read_b64_tr_b16 v[94:95], v20 offset:14336\n\t" "ds_read_b64_tr_b16 v[96:97], v21 offset:12288\n\t" \
  "ds_read_b64_tr_b16 v[98:99], v21 offset:14336\n\t" "ds_read_b64_tr_b16 v[100:101], v22 offset:12288\n\t" "ds_read_b64_tr_b16 v[102:103], v22 offset:14336\n\t" \
  "ds_read_b64_tr_b16 v[104:105], v35 offset:16384\n\t" "ds_read_b64_tr_b16 v[106:107], v35 offset:18432\n\t" "ds_read_b64_tr_b16 v[108:109], v20 offset:16384\n\t" \
  "ds_read_b64_tr_b16 v[110:111], v20 offset:18432\n\t" "ds_read_b64_tr_b16 v[112:113], v21 offset:16384\n\t" "ds_read_b64_tr_b16 v[114:115], v21 offset:18432\n\t" \
  "ds_read_b64_tr_b16 v[116:117], v22 offset:16384\n\t" "ds_read_b64_tr_b16 v[118:119], v22 offset:18432\n\t" "s_cmp_eq_u32 s34, 0\n\t" "s_cbranch_scc1 .Lb3b_%=\n\t" "s_setprio 1\n\t" \
  "ds_read_b64 v[152:153], v37 offset:80\n\t" "s_waitcnt lgkmcnt(0)\n\t" "v_readfirstlane_b32 s80, v152\n\t" "v_readfirstlane_b32 s81, v153\n\t" "s_sub_u32 s81, s81, s80\n\t" \
  "s_sub_u32 s80, s81, 15\n\t" "s_barrier\n\t" "s_cbranch_scc1 .Llx2_%=\n\t" "s_memrealtime s[76:77]\n\t" ".Llw2_%=:\n\t" "s_memrealtime s[78:79]\n\t" "s_waitcnt lgkmcnt(0)\n\t" \
  "s_sub_u32 s78, s78, s76\n\t" "s_cmp_lt_u32 s78, s80\n\t" "s_cbranch_scc1 .Llw2_%=\n\t" ".Llx2_%=:\n\t" "global_load_dwordx4 v[152:155], v32, s[62:63] offset:-2048 sc1\n\t" \
  "global_load_dwordx4 v[156:159], v32, s[62:63] offset:-1024 sc1\n\t" "global_load_dwordx4 v[160:163], v32, s[62:63] offset:0 sc1\n\t" ".Lb3b_%=:\n\t" \
  "ds_read_b64_tr_b16 v[120:121], v35 offset:20480\n\t" "ds_read_b64_tr_b16 v[122:123], v35 offset:22528\n\t" "ds_read_b64_tr_b16 v[124:125], v20 offset:20480\n\t" \
  "ds_read_b64_tr_b16 v[126:127], v20 offset:22528\n\t" "ds_read_b64_tr_b16 v[128:129], v21 offset:20480\n\t" "ds_read_b64_tr_b16 v[130:131], v21 offset:22528\n\t" \
  "ds_read_b64_tr_b16 v[132:133], v22 offset:20480\n\t" "ds_read_b64_tr_b16 v[134:135], v22 offset:22528\n\t" "s_waitcnt vmcnt(5)\n\t" "v_permlane16_swap_b32 v12, v14\n\t" \
  "v_permlane16_swap_b32 v13, v15\n\t" "v_mov_b64 v[144:145], 0\n\t" "v_mov_b64 v[146:147], 0\n\t" "v_mov_b64 v[148:149], 0\n\t" "v_mov_b64 v[150:151], 0\n\t" "v_lshlrev_b32 v136, 16, v12\n\t" \
  "v_and_b32 v137, 0xffff0000, v12\n\t" "v_lshlrev_b32 v138, 16, v13\n\t" "v_and_b32 v139, 0xffff0000, v13\n\t" "v_lshlrev_b32 v140, 16, v14\n\t" "v_and_b32 v141, 0xffff0000, v14\n\t" \
  "v_lshlrev_b32 v142, 16, v15\n\t" "v_and_b32 v143, 0xffff0000, v15\n\t" "s_lshl_b32 s38, s26, 11\n\t" "v_and_b32 v28, 0xf0, v32\n\t" "v_add_u32 v24, s38, v32\n\t" "v_and_b32 v27, 0x7f, v34\n\t" \
  "v_xor_b32 v25, 0x800, v24\n\t" "v_lshl_add_u32 v27, v28, 3, v27\n\t" "s_lshl_b32 s38, s37, 11\n\t" "v_add_u32 v28, v28, v10\n\t" "v_add_u32 v27, s38, v27\n\t" "s_mul_i32 s38, s36, 6\n\t" \
  "s_add_u32 s38, s38, s37\n\t" "s_lshl_b32 s38, s38, 8\n\t" "v_lshl_add_u32 v26, s37, 2, v37\n\t" "v_add3_u32 v28, v28, s38, v37\n\t" "s_mul_i32 s38, s34, 768\n\t" "v_min_u32 v29, 0x2f0, v32\n\t" \
  "v_lshl_add_u32 v30, s34, 2, v37\n\t" "v_add3_u32 v29, v29, s38, v37\n\t" "s_lshl1_add_u32 s41, s29, s34\n\t" "v_min_u32 v1, 47, v8\n\t" "s_mul_i32 s38, s41, 6400\n\t" \
  "s_add_u32 s38, s38, 4829184\n\t" "v_mul_u32_u24 v2, 12, v1\n\t" "s_add_u32 s44, s24, s38\n\t" "s_addc_u32 s45, s25, 0\n\t" "s_add_u32 s42, s44, 2560\n\t" "s_addc_u32 s43, s45, 0\n\t" \
  "s_mul_i32 s38, s28, 640\n\t" "s_add_u32 s44, s44, s38\n\t" "s_addc_u32 s45, s45, 0\n\t" "s_waitcnt vmcnt(2)\n\t" "v_cmp_eq_u32 vcc, -1, v152\n\t" "s_cbranch_vccnz .Lso0_%=\n\t" ".Lro0_%=:\n\t" \
  "s_waitcnt lgkmcnt(14)\n\t" "v_mfma_f32_16x16x32_bf16 v[136:139], v[40:43], v[152:155], v[136:139]\n\t" "global_load_dwordx4 v[164:167], v32, s[62:63] offset:1024 sc1\n\t" \
  "ds_read_b64_tr_b16 v[40:41], v35 offset:24576\n\t" "ds_read_b64_tr_b16 v[42:43], v35 offset:26624\n\t" "v_mfma_f32_16x16x32_bf16 v[140:143], v[44:47], v[152:155], v[140:143]\n\t" \
  "ds_read_b64_tr_b16 v[44:45], v20 offset:24576\n\t" "ds_read_b64_tr_b16 v[46:47], v20 offset:26624\n\t" "v_mfma_f32_16x16x32_bf16 v[144:147], v[48:51], v[152:155], v[144:147]\n\t" \
  "ds_read_b64_tr_b16 v[48:49], v21 offset:24576\n\t" "ds_read_b64_tr_b16 v[50:51], v21 offset:26624\n\t" "v_mfma_f32_16x16x32_bf16 v[148:151], v[52:55], v[152:155], v[148:151]\n\t" \
  "ds_read_b64_tr_b16 v[52:53], v22 offset:24576\n\t" "ds_read_b64_tr_b16 v[54:55], v22 offset:26624\n\t" "s_waitcnt vmcnt(2)\n\t" "v_cmp_eq_u32 vcc, -1, v156\n\t" "s_cbranch_vccnz .Lso1_%=\n\t" \
  ".Lro1_%=:\n\t" "v_mfma_f32_16x16x32_bf16 v[136:139], v[56:59], v[156:159], v[136:139]\n\t" "global_load_dwordx4 v[152:155], v32, s[62:63] offset:2048 sc1\n\t" \
  "ds_read_b64_tr_b16 v[56:57], v35 offset:28672\n\t" "ds_read_b64_tr_b16 v[58:59], v35 offset:30720\n\t" "v_mfma_f32_16x16x32_bf16 v[140:143], v[60:63], v[156:159], v[140:143]\n\t" \
  "ds_read_b64_tr_b16 v[60:61], v20 offset:28672\n\t" "ds_read_b64_tr_b16 v[62:63], v20 offset:30720\n\t" "v_mfma_f32_16x16x32_bf16 v[144:147], v[64:67], v[156:159], v[144:147]\n\t" \
  "ds_read_b64_tr_b16 v[64:65], v21 offset:28672\n\t" "ds_read_b64_tr_b16 v[66:67], v21 offset:30720\n\t" "v_mfma_f32_16x16x32_bf16 v[148:151], v[68:71], v[156:159], v[148:151]\n\t" \
  "ds_read_b64_tr_b16 v[68:69], v22 offset:28672\n\t" "ds_read_b64_tr_b16 v[70:71], v22 offset:30720\n\t" "s_waitcnt vmcnt(2)\n\t" "v_cmp_eq_u32 vcc, -1, v160\n\t" "s_cbranch_vccnz .Lso2_%=\n\t" \
  ".Lro2_%=:\n\t" "v_mfma_f32_16x16x32_bf16 v[136:139], v[72:75], v[160:163], v[136:139]\n\t" "global_load_dwordx4 v[156:159], v32, s[64:65] offset:-2048 sc1\n\t" \
  "ds_read_b64_tr_b16 v[72:73], v35 offset:32768\n\t" "ds_read_b64_tr_b16 v[74:75], v35 offset:34816\n\t" "v_mfma_f32_16x16x32_bf16 v[140:143], v[76:79], v[160:163], v[140:143]\n\t" \
  "ds_read_b64_tr_b16 v[76:77], v20 offset:32768\n\t" "ds_read_b64_tr_b16 v[78:79], v20 offset:34816\n\t" "v_mfma_f32_16x16x32_bf16 v[144:147], v[80:83], v[160:163], v[144:147]\n\t" \
  "ds_read_b64_tr_b16 v[80:81], v21 offset:32768\n\t" "ds_read_b64_tr_b16 v[82:83], v21 offset:34816\n\t" "v_mfma_f32_16x16x32_bf16 v[148:151], v[84:87], v[160:163], v[148:151]\n\t" \
  "ds_read_b64_tr_b16 v[84:85], v22 offset:32768\n\t" "ds_read_b64_tr_b16 v[86:87], v22 offset:34816\n\t" "s_waitcnt vmcnt(2)\n\t" "v_cmp_eq_u32 vcc, -1, v164\n\t" "s_cbranch_vccnz .Lso3_%=\n\t" \
  ".Lro3_%=:\n\t" "v_mfma_f32_16x16x32_bf16 v[136:139], v[88:91], v[164:167], v[136:139]\n\t" "global_load_dwordx4 v[160:163], v32, s[64:65] offset:-1024 sc1\n\t" \
  "ds_read_b64_tr_b16 v[88:89], v35 offset:36864\n\t" "ds_read_b64_tr_b16 v[90:91], v35 offset:38912\n\t" "v_mfma_f32_16x16x32_bf16 v[140:143], v[92:95], v[164:167], v[140:143]\n\t" \
  "ds_read_b64_tr_b16 v[92:93], v20 offset:36864\n\t" "ds_read_b64_tr_b16 v[94:95], v20 offset:38912\n\t" "v_mfma_f32_16x16x32_bf16 v[144:147], v[96:99], v[164:167], v[144:147]\n\t" \
  "ds_read_b64_tr_b16 v[96:97], v21 offset:36864\n\t" "ds_read_b64_tr_b16 v[98:99], v21 offset:38912\n\t" "v_mfma_f32_16x16x32_bf16 v[148:151], v[100:103], v[164:167], v[148:151]\n\t" \
  "ds_read_b64_tr_b16 v[100:101], v22 offset:36864\n\t" "ds_read_b64_tr_b16 v[102:103], v22 offset:38912\n\t" "s_waitcnt vmcnt(2)\n\t" "v_cmp_eq_u32 vcc, -1, v152\n\t" "s_cbranch_vccnz .Lso4_%=\n\t" \
  ".Lro4_%=:\n\t" "v_mfma_f32_16x16x32_bf16 v[136:139], v[104:107], v[152:155], v[136:139]\n\t" "global_load_dwordx4 v[164:167], v32, s[64:65] offset:0 sc1\n\t" "s_waitcnt lgkmcnt(14)\n\t" \
  "v_mfma_f32_16x16x32_bf16 v[140:143], v[108:111], v[152:155], v[140:143]\n\t" "v_mfma_f32_16x16x32_bf16 v[144:147], v[112:115], v[152:155], v[144:147]\n\t" \
  "v_mfma_f32_16x16x32_bf16 v[148:151], v[116:119], v[152:155], v[148:151]\n\t" "s_waitcnt vmcnt(2)\n\t" "v_cmp_eq_u32 vcc, -1, v156\n\t" "s_cbranch_vccnz .Lso5_%=\n\t" ".Lro5_%=:\n\t" \
  "v_mfma_f32_16x16x32_bf16 v[136:139], v[120:123], v[156:159], v[136:139]\n\t" "global_load_dwordx4 v[152:155], v32, s[64:65] offset:1024 sc1\n\t" \
  "v_mfma_f32_16x16x32_bf16 v[140:143], v[124:127], v[156:159], v[140:143]\n\t" "v_mfma_f32_16x16x32_bf16 v[144:147], v[128:131], v[156:159], v[144:147]\n\t" \
  "v_mfma_f32_16x16x32_bf16 v[148:151], v[132:135], v[156:159], v[148:151]\n\t" "s_waitcnt vmcnt(2)\n\t" "v_cmp_eq_u32 vcc, -1, v160\n\t" "s_cbranch_vccnz .Lso6_%=\n\t" ".Lro6_%=:\n\t" \
  "v_mfma_f32_16x16x32_bf16 v[136:139], v[40:43], v[160:163], v[136:139]\n\t" "global_load_dwordx4 v[156:159], v32, s[64:65] offset:2048 sc1\n\t" \
  "v_mfma_f32_16x16x32_bf16 v[140:143], v[44:47], v[160:163], v[140:143]\n\t" "v_mfma_f32_16x16x32_bf16 v[144:147], v[48:51], v[160:163], v[144:147]\n\t" \
  "v_mfma_f32_16x16x32_bf16 v[148:151], v[52:55], v[160:163], v[148:151]\n\t" "s_waitcnt vmcnt(2)\n\t" "v_cmp_eq_u32 vcc, -1, v164\n\t" "s_cbranch_vccnz .Lso7_%=\n\t" ".Lro7_%=:\n\t" \
  "v_mfma_f32_16x16x32_bf16 v[136:139], v[56:59], v[164:167], v[136:139]\n\t" "v_mfma_f32_16x16x32_bf16 v[140:143], v[60:63], v[164:167], v[140:143]\n\t" \
  "v_mfma_f32_16x16x32_bf16 v[144:147], v[64:67], v[164:167], v[144:147]\n\t" "v_mfma_f32_16x16x32_bf16 v[148:151], v[68:71], v[164:167], v[148:151]\n\t" "s_waitcnt vmcnt(1)\n\t" \
  "v_cmp_eq_u32 vcc, -1, v152\n\t" "s_cbranch_vccnz .Lso8_%=\n\t" ".Lro8_%=:\n\t" "v_mfma_f32_16x16x32_bf16 v[136:139], v[72:75], v[152:155], v[136:139]\n\t" "s_waitcnt lgkmcnt(12)\n\t" \
  "v_mfma_f32_16x16x32_bf16 v[140:143], v[76:79], v[152:155], v[140:143]\n\t" "s_waitcnt lgkmcnt(10)\n\t" "v_mfma_f32_16x16x32_bf16 v[144:147], v[80:83], v[152:155], v[144:147]\n\t" \
  "s_waitcnt lgkmcnt(8)\n\t" "v_mfma_f32_16x16x32_bf16 v[148:151], v[84:87], v[152:155], v[148:151]\n\t" "s_waitcnt vmcnt(0)\n\t" "v_cmp_eq_u32 vcc, -1, v156\n\t" "s_cbranch_vccnz .Lso9_%=\n\t" \
  ".Lro9_%=:\n\t" "s_waitcnt lgkmcnt(2)\n\t" "v_mfma_f32_16x16x32_bf16 v[144:147], v[96:99], v[156:159], v[144:147]\n\t" "s_waitcnt lgkmcnt(0)\n\t" \
  "v_mfma_f32_16x16x32_bf16 v[148:151], v[100:103], v[156:159], v[148:151]\n\t" "v_mfma_f32_16x16x32_bf16 v[136:139], v[88:91], v[156:159], v[136:139]\n\t" \
  "v_mfma_f32_16x16x32_bf16 v[140:143], v[92:95], v[156:159], v[140:143]\n\t" "s_nop 3\n\t" "ds_write_b128 v24, v[144:147] offset:36864\n\t" "s_nop 0\n\t" \
  "ds_write_b128 v24, v[148:151] offset:37888\n\t" "s_mov_b64 exec, 1\n\t" "ds_add_u32 v26, v38 offset:48\n\t" "s_mov_b64 exec, -1\n\t" ".Lp_%=:\n\t" "ds_read_b32 v39, v26 offset:48\n\t" \
  "ds_read_b128 v[40:43], v25 offset:36864\n\t" "ds_read_b128 v[44:47], v25 offset:37888\n\t" "s_sub_u32 s39, s39, 1\n\t" "s_cmp_lt_i32 s39, 0\n\t" "s_waitcnt lgkmcnt(2)\n\t" \
  "v_readfirstlane_b32 s38, v39\n\t" "s_cselect_b32 s38, 2, s38\n\t" "s_cmp_ge_u32 s38, 2\n\t" "s_waitcnt lgkmcnt(0)\n\t" "s_cbranch_scc0 .Lp_%=\n\t" \
  "v_pk_add_f32 v[136:137], v[136:137], v[40:41]\n\t" "v_pk_add_f32 v[138:139], v[138:139], v[42:43]\n\t" "v_pk_add_f32 v[140:141], v[140:141], v[44:45]\n\t" \
  "v_pk_add_f32 v[142:143], v[142:143], v[46:47]\n\t" "v_cvt_pk_bf16_f32 v12, v136, v137\n\t" "v_cvt_pk_bf16_f32 v14, v140, v141\n\t" "v_cvt_pk_bf16_f32 v13, v138, v139\n\t" \
  "v_cvt_pk_bf16_f32 v15, v142, v143\n\t" "s_mov_b32 s38, 0x3f803f80\n\t" "v_permlane16_swap_b32 v12, v14\n\t" "v_permlane16_swap_b32 v13, v15\n\t" "v_dot2_f32_bf16 v40, v12, s38, 0\n\t" \
  "v_dot2_f32_bf16 v41, v12, v12, 0\n\t" "v_dot2_f32_bf16 v42, v12, v16, 0\n\t" "v_dot2_f32_bf16 v40, v14, s38, v40\n\t" "v_dot2_f32_bf16 v41, v14, v14, v41\n\t" \
  "v_dot2_f32_bf16 v42, v14, v18, v42\n\t" "v_dot2_f32_bf16 v40, v13, s38, v40\n\t" "v_dot2_f32_bf16 v41, v13, v13, v41\n\t" "v_dot2_f32_bf16 v42, v13, v17, v42\n\t" \
  "v_dot2_f32_bf16 v40, v15, s38, v40\n\t" "v_dot2_f32_bf16 v41, v15, v15, v41\n\t" "v_dot2_f32_bf16 v43, v15, v19, v42\n\t" "v_dot2_f32_bf16 v42, v15, v19, v42\n\t" \
  "ds_write_b128 v27, v[12:15] offset:61440\n\t" "s_nop 0\n\t" "v_permlane32_swap_b32 v40, v41\n\t" "v_permlane32_swap_b32 v42, v43\n\t" "v_add_f32 v40, v40, v41\n\t" "v_add_f32 v42, v42, v43\n\t" \
  "s_nop 1\n\t" "v_permlane16_swap_b32 v40, v42\n\t" "v_add_f32 v40, v40, v42\n\t" "ds_write_b32 v28, v40 offset:256\n\t" "s_mov_b64 exec, 1\n\t" "ds_add_rtn_u32 v39, v30, v38 offset:16\n\t" \
  "s_mov_b64 exec, -1\n\t" "ds_read_b128 v[44:47], v29 offset:256\n\t" "ds_read_b128 v[48:51], v29 offset:1792\n\t" "ds_read_b32 v52, v37 offset:40\n\t" "s_waitcnt lgkmcnt(3)\n\t" \
  "v_readfirstlane_b32 s38, v39\n\t" "s_waitcnt lgkmcnt(0)\n\t" "s_cmp_lg_u32 s38, 5\n\t" "s_setprio 0\n\t" "s_cbranch_scc1 .Lnk_%=\n\t" "s_setprio 3\n\t" \
  "v_pk_add_f32 v[44:45], v[44:45], v[48:49]\n\t" "v_add_f32 v46, v46, v50\n\t" "s_mov_b32 exec_hi, 0xffff\n\t" "global_store_dwordx3 v2, v[44:46], s[44:45] sc1\n\t" "s_mov_b32 exec_hi, -1\n\t" \
  "s_memrealtime s[76:77]\n\t" "s_mul_i32 s50, s34, 48\n\t" "v_lshl_add_u32 v4, s41, 6, v1\n\t" "v_add_u32 v1, s50, v1\n\t" "s_cmp_eq_u32 s29, 0\n\t" "s_cselect_b32 s51, 6, 7\n\t" \
  "v_cmp_le_u32 vcc, s51, v1\n\t" "s_mov_b32 s51, 155648\n\t" "v_lshl_add_u32 v1, v1, 3, s51\n\t" "v_mov_b32 v3, 0xfffff000\n\t" "s_lshl_b32 s51, s41, 7\n\t" "v_cndmask_b32 v3, 0, v3, vcc\n\t" \
  "v_lshl_add_u32 v5, v9, 3, s51\n\t" "v_lshlrev_b32 v7, 2, v8\n\t" "s_lshl_b32 s50, s50, 7\n\t" "v_and_b32 v6, 0x7c, v7\n\t" "v_add_u32 v6, s50, v6\n\t" "s_mul_i32 s50, s28, 6144\n\t" \
  "s_add_u32 s50, s50, 5302784\n\t" "s_add_u32 s46, s24, s50\n\t" "s_addc_u32 s47, s25, 0\n\t" "s_mul_i32 s50, s41, 10\n\t" "s_add_u32 s50, s50, s28\n\t" "s_lshl_b32 s50, s50, 8\n\t" \
  "s_add_u32 s50, s50, 5123584\n\t" "s_add_u32 s48, s24, s50\n\t" "s_addc_u32 s49, s25, 0\n\t" "s_mov_b32 s50, 0x3acccccd\n\t" ".Ltk_%=:\n\t" "s_memrealtime s[78:79]\n\t" "s_waitcnt lgkmcnt(0)\n\t" \
  "s_sub_u32 s78, s78, s76\n\t" "s_cmp_lt_u32 s78, 40\n\t" "s_cbranch_scc1 .Ltk_%=\n\t" ".Lkp_%=:\n\t" "global_load_dwordx3 v[60:62], v2, s[42:43] offset:-2560 sc1\n\t" \
  "global_load_dwordx3 v[64:66], v2, s[42:43] offset:-1920 sc1\n\t" "global_load_dwordx3 v[68:70], v2, s[42:43] offset:-1280 sc1\n\t" "global_load_dwordx3 v[72:74], v2, s[42:43] offset:-640 sc1\n\t" \
  "global_load_dwordx3 v[76:78], v2, s[42:43] offset:0 sc1\n\t" "global_load_dwordx3 v[80:82], v2, s[42:43] offset:640 sc1\n\t" "global_load_dwordx3 v[84:86], v2, s[42:43] offset:1280 sc1\n\t" \
  "global_load_dwordx3 v[88:90], v2, s[42:43] offset:1920 sc1\n\t" "global_load_dwordx3 v[92:94], v2, s[42:43] offset:2560 sc1\n\t" "global_load_dwordx3 v[96:98], v2, s[42:43] offset:3200 sc1\n\t" \
  "s_sub_u32 s39, s39, 1\n\t" "s_cmp_lt_i32 s39, 0\n\t" "s_waitcnt vmcnt(0)\n\t" "v_pk_add_f32 v[60:61], v[60:61], v[64:65]\n\t" "v_add_f32 v62, v62, v66\n\t" \
  "v_pk_add_f32 v[68:69], v[68:69], v[72:73]\n\t" "v_add_f32 v70, v70, v74\n\t" "v_pk_add_f32 v[76:77], v[76:77], v[80:81]\n\t" "v_add_f32 v78, v78, v82\n\t" \
  "v_pk_add_f32 v[84:85], v[84:85], v[88:89]\n\t" "v_add_f32 v86, v86, v90\n\t" "v_pk_add_f32 v[92:93], v[92:93], v[96:97]\n\t" "v_add_f32 v94, v94, v98\n\t" \
  "v_pk_add_f32 v[60:61], v[60:61], v[68:69]\n\t" "v_add_f32 v62, v62, v70\n\t" "v_pk_add_f32 v[76:77], v[76:77], v[84:85]\n\t" "v_add_f32 v78, v78, v86\n\t" \
  "v_pk_add_f32 v[60:61], v[60:61], v[92:93]\n\t" "v_add_f32 v62, v62, v94\n\t" "v_pk_add_f32 v[60:61], v[60:61], v[76:77]\n\t" "v_add_f32 v62, v62, v78\n\t" "v_mul_f32 v60, s50, v60\n\t" \
  "v_mul_f32 v63, v60, v60\n\t" "v_fma_f32 v61, -v60, v52, v61\n\t" "v_fma_f32 v62, v62, s50, -v63\n\t" "s_cbranch_scc1 .Lkq_%=\n\t" "v_cmp_u_f32 vcc, v61, v62\n\t" "s_cbranch_vccnz .Lkp_%=\n\t" \
  ".Lkq_%=:\n\t" "v_max_f32 v62, 0, v62\n\t" "v_add_f32 v63, 0x3727c5ac, v62\n\t" "v_rsq_f32 v63, v63\n\t" "v_cvt_pk_bf16_f32 v66, v62, v60\n\t" "v_mul_f32 v61, v61, v63\n\t" \
  "v_ashrrev_i32 v63, 31, v61\n\t" "v_or_b32 v63, 0x80000000, v63\n\t" "v_xor_b32 v61, v61, v63\n\t" "v_and_or_b32 v61, v61, v3, v4\n\t" "v_lshlrev_b32 v67, 16, v66\n\t" \
  "v_and_b32 v66, 0xffff0000, v66\n\t" "v_max_u32_dpp v64, v61, v61 quad_perm:[1,0,3,2] row_mask:0xf bank_mask:0xf\n\t" "v_add_f32 v67, 0x3727c5ac, v67\n\t" "v_cvt_pk_bf16_f32 v67, v67, v67\n\t" \
  "v_max_u32_dpp v64, v64, v64 quad_perm:[2,3,0,1] row_mask:0xf bank_mask:0xf\n\t" "v_lshlrev_b32 v67, 16, v67\n\t" "v_sqrt_f32 v67, v67\n\t" \
  "v_max_u32_dpp v64, v64, v64 row_half_mirror row_mask:0xf bank_mask:0xf\n\t" "s_nop 0\n\t" "v_cvt_pk_bf16_f32 v67, v67, v67\n\t" \
  "v_max_u32_dpp v64, v64, v64 row_mirror row_mask:0xf bank_mask:0xf\n\t" "v_lshlrev_b32 v67, 16, v67\n\t" "v_rcp_f32 v67, v67\n\t" \
  "v_max_u32_dpp v64, v64, v64 row_bcast:15 row_mask:0xa bank_mask:0xf\n\t" "s_nop 1\n\t" "v_max_u32_dpp v64, v64, v64 row_bcast:31 row_mask:0xc bank_mask:0xf\n\t" "v_mov_b32 v65, v64\n\t" \
  "s_mov_b32 exec_lo, 0\n\t" "s_mov_b32 exec_hi, 0xffff0000\n\t" "global_store_dwordx2 v5, v[64:65], s[46:47] sc1\n\t" "s_mov_b64 exec, -1\n\t" "v_readlane_b32 s50, v64, 63\n\t" \
  "s_and_b32 s51, s50, 63\n\t" "s_lshl_b32 s52, s51, 7\n\t" "v_add_u32 v6, s52, v6\n\t" "s_or_b32 s50, s50, 0x1000\n\t" "ds_read_b32 v68, v6 offset:61440\n\t" "v_mov_b32 v64, s50\n\t" \
  "ds_write_b64 v1, v[66:67]\n\t" "ds_write_b32 v30, v64 offset:24\n\t" "v_readlane_b32 s52, v66, s51\n\t" "v_readlane_b32 s53, v67, s51\n\t" "s_waitcnt lgkmcnt(2)\n\t" \
  "v_lshlrev_b32 v70, 16, v68\n\t" "v_and_b32 v71, 0xffff0000, v68\n\t" "v_subrev_f32 v70, s52, v70\n\t" "v_subrev_f32 v71, s52, v71\n\t" "v_cvt_pk_bf16_f32 v71, v70, v71\n\t" \
  "v_lshlrev_b32 v70, 16, v71\n\t" "v_and_b32 v71, 0xffff0000, v71\n\t" "v_mul_f32 v70, s53, v70\n\t" "v_mul_f32 v71, s53, v71\n\t" "s_mov_b32 exec_hi, 0\n\t" "v_cvt_pk_bf16_f32 v68, v70, v71\n\t" \
  "s_mov_b32 exec_hi, -1\n\t" "global_store_dword v7, v68, s[48:49] sc1\n\t" "s_setprio 0\n\t" ".Lnk_%=:\n\t" "s_lshl_b32 s40, s37, 7\n\t" "s_add_u32 s40, s40, 155648\n\t" \
  "v_lshl_add_u32 v1, v9, 3, s40\n\t" "s_lshr_b32 s76, s35, 1\n\t" "s_lshl_b32 s76, s76, 4\n\t" "v_add_u32 v113, s76, v9\n\t" "s_branch .Lw1_%=\n\t" ".Lw_%=:\n\t" "s_sleep 2\n\t" ".Lw1_%=:\n\t" \
  "ds_read_b32 v112, v30 offset:24\n\t" "ds_read_b64 v[110:111], v1\n\t" "s_sub_u32 s39, s39, 1\n\t" "s_cmp_lt_i32 s39, 0\n\t" "s_waitcnt lgkmcnt(0)\n\t" "v_readfirstlane_b32 s66, v112\n\t" \
  "s_cselect_b32 s66, 1, s66\n\t" "s_cmp_eq_u32 s66, 0\n\t" "s_cbranch_scc1 .Lw_%=\n\t" "s_and_b32 s76, s66, 63\n\t" "v_cmp_eq_u32 s[68:69], s76, v113\n\t" "v_lshlrev_b32 v120, 16, v12\n\t" \
  "v_and_b32 v121, 0xffff0000, v12\n\t" "v_lshlrev_b32 v122, 16, v13\n\t" "v_and_b32 v123, 0xffff0000, v13\n\t" "v_lshlrev_b32 v124, 16, v14\n\t" "v_and_b32 v125, 0xffff0000, v14\n\t" \
  "v_lshlrev_b32 v126, 16, v15\n\t" "v_and_b32 v127, 0xffff0000, v15\n\t" "v_pk_add_f32 v[128:129], v[120:121], v[110:111] op_sel_hi:[1,0] neg_lo:[0,1] neg_hi:[0,1]\n\t" \
  "v_pk_add_f32 v[130:131], v[122:123], v[110:111] op_sel_hi:[1,0] neg_lo:[0,1] neg_hi:[0,1]\n\t" "v_pk_add_f32 v[132:133], v[124:125], v[110:111] op_sel_hi:[1,0] neg_lo:[0,1] neg_hi:[0,1]\n\t" \
  "v_pk_add_f32 v[134:135], v[126:127], v[110:111] op_sel_hi:[1,0] neg_lo:[0,1] neg_hi:[0,1]\n\t" "v_cvt_pk_bf16_f32 v129, v128, v129\n\t" "v_cvt_pk_bf16_f32 v131, v130, v131\n\t" \
  "v_cvt_pk_bf16_f32 v133, v132, v133\n\t" "v_cvt_pk_bf16_f32 v135, v134, v135\n\t" "v_lshlrev_b32 v128, 16, v129\n\t" "v_and_b32 v129, 0xffff0000, v129\n\t" "v_lshlrev_b32 v130, 16, v131\n\t" \
  "v_and_b32 v131, 0xffff0000, v131\n\t" "v_lshlrev_b32 v132, 16, v133\n\t" "v_and_b32 v133, 0xffff0000, v133\n\t" "v_lshlrev_b32 v134, 16, v135\n\t" "v_and_b32 v135, 0xffff0000, v135\n\t" \
  "v_pk_mul_f32 v[128:129], v[128:129], v[110:111] op_sel:[0,1] op_sel_hi:[1,1]\n\t" "v_pk_mul_f32 v[130:131], v[130:131], v[110:111] op_sel:[0,1] op_sel_hi:[1,1]\n\t" \
  "v_pk_mul_f32 v[132:133], v[132:133], v[110:111] op_sel:[0,1] op_sel_hi:[1,1]\n\t" "v_pk_mul_f32 v[134:135], v[134:135], v[110:111] op_sel:[0,1] op_sel_hi:[1,1]\n\t" \
  "v_cvt_pk_bf16_f32 v129, v128, v129\n\t" "v_cvt_pk_bf16_f32 v131, v130, v131\n\t" "v_cvt_pk_bf16_f32 v133, v132, v133\n\t" "v_cvt_pk_bf16_f32 v135, v134, v135\n\t" "v_lshlrev_b32 v128, 16, v129\n\t" \
  "v_and_b32 v129, 0xffff0000, v129\n\t" "v_lshlrev_b32 v130, 16, v131\n\t" "v_and_b32 v131, 0xffff0000, v131\n\t" "v_lshlrev_b32 v132, 16, v133\n\t" "v_and_b32 v133, 0xffff0000, v133\n\t" \
  "v_lshlrev_b32 v134, 16, v135\n\t" "v_and_b32 v135, 0xffff0000, v135\n\t" "v_pk_add_f32 v[128:129], v[128:129], v[120:121]\n\t" "v_pk_add_f32 v[130:131], v[130:131], v[122:123]\n\t" \
  "v_pk_add_f32 v[132:133], v[132:133], v[124:125]\n\t" "v_pk_add_f32 v[134:135], v[134:135], v[126:127]\n\t" "v_cvt_pk_bf16_f32 v116, v128, v129\n\t" "v_cvt_pk_bf16_f32 v117, v130, v131\n\t" \
  "v_cvt_pk_bf16_f32 v118, v132, v133\n\t" "v_cvt_pk_bf16_f32 v119, v134, v135\n\t" "s_sleep 16\n\t" "s_andn2_b64 exec, s[72:73], s[68:69]\n\t" "global_store_dwordx4 v33, v[116:119], s[22:23]\n\t" \
  "s_mov_b64 exec, -1\n\t" "s_cmp_eq_u32 s26, 11\n\t" "s_cbranch_scc1 .Lrz_%=\n\t" "s_cmp_eq_u64 s[68:69], 0\n\t" "s_cbranch_scc1 .Lend_%=\n\t" ".Lb6_%=:\n\t" "s_waitcnt lgkmcnt(0)\n\t" \
  "s_barrier\n\t" "ds_read_b32 v39, v37 offset:32\n\t" "s_waitcnt lgkmcnt(0)\n\t" "v_readfirstlane_b32 s76, v39\n\t" "s_xor_b32 s76, s76, s66\n\t" "s_and_b32 s76, s76, 4095\n\t" \
  "s_cbranch_scc0 .Lend_%=\n\t" "s_and_b64 exec, s[72:73], s[68:69]\n\t" "s_cbranch_scc0 .Ln2_%=\n\t" "global_store_dwordx4 v33, v[116:119], s[22:23]\n\t" ".Ln2_%=:\n\t" "s_mov_b64 exec, -1\n\t" \
  "s_branch .Lend_%=\n\t" ".Lrz_%=:\n\t" "s_sleep 35\n\t" ".Lrt_%=:\n\t" "s_mul_i32 s40, s28, 6144\n\t" "s_add_u32 s40, s40, 5302784\n\t" "v_lshlrev_b32 v2, 7, v8\n\t" "s_add_u32 s40, s24, s40\n\t" \
  "s_addc_u32 s41, s25, 0\n\t" "v_mov_b32 v40, -1\n\t" "v_mov_b32 v41, -1\n\t" "v_mov_b32 v42, -1\n\t" "s_mov_b32 exec_hi, 0x3fff\n\t" "global_load_dword v40, v2, s[40:41] sc1\n\t" "s_sleep 2\n\t" \
  "global_load_dword v41, v2, s[40:41] sc1\n\t" "s_sleep 2\n\t" "global_load_dword v42, v2, s[40:41] sc1\n\t" "s_mov_b32 exec_hi, -1\n\t" "v_mov_b32 v43, 0xfc0\n\t" "s_mov_b32 s42, 0\n\t" \
  "s_mov_b32 s43, 0xffff0000\n\t" "v_add_u32 v3, 64, v37\n\t" "v_cndmask_b32 v3, v3, v37, s[42:43]\n\t" "s_mov_b32 s43, 0xffffc000\n\t" ".Lrl_%=:\n\t" "s_waitcnt vmcnt(2)\n\t" \
  "v_cmp_ne_u32 vcc, -1, v40\n\t" "s_or_b64 s[42:43], s[42:43], vcc\n\t" "s_cmp_eq_u64 s[42:43], -1\n\t" "v_cndmask_b32 v43, v43, v40, vcc\n\t" "s_cselect_b32 s44, 1, 0\n\t" "v_mov_b32 v45, s44\n\t" \
  "v_max_u32_dpp v44, v43, v43 quad_perm:[1,0,3,2] row_mask:0xf bank_mask:0xf\n\t" "s_nop 1\n\t" "v_max_u32_dpp v44, v44, v44 quad_perm:[2,3,0,1] row_mask:0xf bank_mask:0xf\n\t" "s_nop 1\n\t" \
  "v_max_u32_dpp v44, v44, v44 row_half_mirror row_mask:0xf bank_mask:0xf\n\t" "s_nop 1\n\t" "v_max_u32_dpp v44, v44, v44 row_mirror row_mask:0xf bank_mask:0xf\n\t" "s_nop 1\n\t" \
  "v_max_u32_dpp v44, v44, v44 row_bcast:15 row_mask:0xa bank_mask:0xf\n\t" "s_nop 1\n\t" "v_max_u32_dpp v44, v44, v44 row_bcast:31 row_mask:0xc bank_mask:0xf\n\t" \
  "ds_write_b64 v3, v[44:45] offset:32\n\t" "s_cbranch_scc1 .Lre_%=\n\t" "s_cmp_lg_u32 s75, 0\n\t" "s_cbranch_scc1 .Lrz0_%=\n\t" "s_sleep 5\n\t" ".Lrz0_%=:\n\t" "s_not_b64 exec, s[42:43]\n\t" \
  "global_load_dword v40, v2, s[40:41] sc1\n\t" "s_mov_b64 exec, -1\n\t" "s_waitcnt vmcnt(2)\n\t" "v_cmp_ne_u32 vcc, -1, v41\n\t" "s_or_b64 s[42:43], s[42:43], vcc\n\t" "s_cmp_eq_u64 s[42:43], -1\n\t" \
  "v_cndmask_b32 v43, v43, v41, vcc\n\t" "s_cselect_b32 s44, 1, 0\n\t" "v_mov_b32 v45, s44\n\t" "v_max_u32_dpp v44, v43, v43 quad_perm:[1,0,3,2] row_mask:0xf bank_mask:0xf\n\t" "s_nop 1\n\t" \
  "v_max_u32_dpp v44, v44, v44 quad_perm:[2,3,0,1] row_mask:0xf bank_mask:0xf\n\t" "s_nop 1\n\t" "v_max_u32_dpp v44, v44, v44 row_half_mirror row_mask:0xf bank_mask:0xf\n\t" "s_nop 1\n\t" \
  "v_max_u32_dpp v44, v44, v44 row_mirror row_mask:0xf bank_mask:0xf\n\t" "s_nop 1\n\t" "v_max_u32_dpp v44, v44, v44 row_bcast:15 row_mask:0xa bank_mask:0xf\n\t" "s_nop 1\n\t" \
  "v_max_u32_dpp v44, v44, v44 row_bcast:31 row_mask:0xc bank_mask:0xf\n\t" "ds_write_b64 v3, v[44:45] offset:32\n\t" "s_cbranch_scc1 .Lre_%=\n\t" "s_cmp_lg_u32 s75, 0\n\t" \
  "s_cbranch_scc1 .Lrz1_%=\n\t" "s_sleep 5\n\t" ".Lrz1_%=:\n\t" "s_not_b64 exec, s[42:43]\n\t" "global_load_dword v41, v2, s[40:41] sc1\n\t" "s_mov_b64 exec, -1\n\t" "s_waitcnt vmcnt(2)\n\t" \
  "v_cmp_ne_u32 vcc, -1, v42\n\t" "s_or_b64 s[42:43], s[42:43], vcc\n\t" "s_cmp_eq_u64 s[42:43], -1\n\t" "v_cndmask_b32 v43, v43, v42, vcc\n\t" "s_cselect_b32 s44, 1, 0\n\t" "v_mov_b32 v45, s44\n\t" \
  "v_max_u32_dpp v44, v43, v43 quad_perm:[1,0,3,2] row_mask:0xf bank_mask:0xf\n\t" "s_nop 1\n\t" "v_max_u32_dpp v44, v44, v44 quad_perm:[2,3,0,1] row_mask:0xf bank_mask:0xf\n\t" "s_nop 1\n\t" \
  "v_max_u32_dpp v44, v44, v44 row_half_mirror row_mask:0xf bank_mask:0xf\n\t" "s_nop 1\n\t" "v_max_u32_dpp v44, v44, v44 row_mirror row_mask:0xf bank_mask:0xf\n\t" "s_nop 1\n\t" \
  "v_max_u32_dpp v44, v44, v44 row_bcast:15 row_mask:0xa bank_mask:0xf\n\t" "s_nop 1\n\t" "v_max_u32_dpp v44, v44, v44 row_bcast:31 row_mask:0xc bank_mask:0xf\n\t" \
  "ds_write_b64 v3, v[44:45] offset:32\n\t" "s_cbranch_scc1 .Lre_%=\n\t" "s_cmp_lg_u32 s75, 0\n\t" "s_cbranch_scc1 .Lrz2_%=\n\t" "s_sleep 5\n\t" ".Lrz2_%=:\n\t" "s_not_b64 exec, s[42:43]\n\t" \
  "global_load_dword v42, v2, s[40:41] sc1\n\t" "s_mov_b64 exec, -1\n\t" "s_sub_u32 s39, s39, 1\n\t" "s_cmp_lt_i32 s39, 0\n\t" "s_cbranch_scc0 .Lrl_%=\n\t" ".Lre_%=:\n\t" "s_cmp_eq_u32 s75, 0\n\t" \
  "s_cbranch_scc1 .Lb6_%=\n\t" "s_waitcnt lgkmcnt(0)\n\t" "s_barrier\n\t" "s_barrier\n\t" "s_cmp_lg_u32 s29, 0\n\t" "s_cbranch_scc1 .Lend_%=\n\t" "v_mov_b32 v1, 0\n\t" "s_mov_b64 exec, 1\n\t" \
  "global_store_dwordx2 v1, v[16:17], s[24:25] sc1\n\t" "s_mov_b64 exec, -1\n\t" "s_branch .Lend_%=\n\t" ".Lsp_%=:\n\t" "v_mov_b32 v1, 0\n\t" "s_add_u32 s40, s28, 16\n\t" \
  "s_sub_u32 s41, %[bid], 240\n\t" "s_cselect_b32 s29, s40, s41\n\t" "v_lshlrev_b32 v32, 4, v8\n\t" "v_and_b32 v9, 15, v8\n\t" "v_mov_b32 v37, 156416\n\t" "v_mov_b32 v38, 1\n\t" \
  "v_mov_b32 v44, -1\n\t" "v_mov_b32 v45, -1\n\t" "v_mov_b32 v46, -1\n\t" "v_mov_b32 v47, -1\n\t" "s_mov_b32 s75, 1\n\t" "s_mov_b32 s39, 400000\n\t" "s_add_u32 s45, s29, 4\n\t" \
  "s_lshr_b32 s45, s45, 3\n\t" "s_sub_u32 s45, s29, s45\n\t" "s_mul_i32 s46, s45, 214\n\t" "s_movk_i32 s47, 213\n\t" "s_mul_i32 s40, s45, 768\n\t" "s_sub_u32 s40, s40, 11634\n\t" \
  "s_cmp_ge_u32 s29, 24\n\t" "s_cselect_b32 s46, s40, s46\n\t" "s_cselect_b32 s47, 767, s47\n\t" "s_mul_i32 s40, s29, 16\n\t" "s_add_u32 s40, s40, 5982\n\t" "s_and_b32 s48, s29, 7\n\t" \
  "s_cmp_eq_u32 s48, 3\n\t" "s_cselect_b32 s46, s40, s46\n\t" "s_cselect_b32 s47, 127, s47\n\t" "v_min_u32 v5, s47, v0\n\t" "v_add_u32 v5, s46, v5\n\t" "v_min_u32 v5, 6399, v5\n\t" \
  "v_lshlrev_b32 v5, 7, v5\n\t" "s_lshr_b32 s47, s47, 6\n\t" "s_mul_i32 s40, s45, 12\n\t" "s_add_u32 s40, s40, s26\n\t" "s_lshl_b32 s41, s40, 10\n\t" "s_add_u32 s41, s41, 2002944\n\t" \
  "s_lshl_b32 s44, s45, 7\n\t" "s_add_u32 s44, s44, 4096\n\t" "v_lshl_add_u32 v4, v9, 3, s44\n\t" "s_waitcnt lgkmcnt(0)\n\t" "global_load_dwordx2 v[40:41], v1, s[24:25] sc1\n\t" \
  "s_add_u32 s42, s24, s41\n\t" "s_addc_u32 s43, s25, 0\n\t" "v_lshl_add_u32 v3, v8, 2, v37\n\t" "s_movk_i32 s44, 0xfc0\n\t" "v_mov_b32 v2, 0\n\t" "v_writelane_b32 v2, s44, 8\n\t" \
  "s_mov_b32 exec_hi, 0\n\t" "ds_write_b32 v3, v2\n\t" "s_mov_b32 exec_hi, -1\n\t" "s_mov_b32 m0, 163072\n\t" "s_cmp_lg_u32 s48, 3\n\t" "s_cbranch_scc1 .Lpf_%=\n\t" "s_branch .Lnf_%=\n\t" \
  ".Lpf_%=:\n\t" "global_store_dwordx4 v32, v[44:47], s[42:43] sc1\n\t" "s_add_u32 s42, s42, 282624\n\t" "s_addc_u32 s43, s43, 0\n\t" "s_add_u32 s40, s40, 276\n\t" "s_cmp_lt_u32 s40, 3283\n\t" \
  "s_cbranch_scc1 .Lpf_%=\n\t" "s_waitcnt vmcnt(0)\n\t" "v_add_co_u32 v16, vcc, 1, v40\n\t" "s_cmp_lg_u32 s26, 0\n\t" "s_nop 0\n\t" "v_addc_co_u32 v17, vcc, 0, v41, vcc\n\t" "s_barrier\n\t" \
  "s_cbranch_scc1 .Lnf_%=\n\t" "global_store_dwordx2 v4, v[16:17], s[24:25] sc1\n\t" ".Lnf_%=:\n\t" "s_cmp_gt_u32 s26, s47\n\t" "s_cbranch_scc1 .Lnt_%=\n\t" "global_load_lds_dword v5, s[12:13]\n\t" \
  ".Lnt_%=:\n\t" "s_cmp_ge_u32 s29, 24\n\t" "s_cbranch_scc1 .Lend_%=\n\t" "s_cmp_ge_u32 s26, 8\n\t" "s_cbranch_scc1 .Lsq_%=x\n\t" "v_lshrrev_b32 v1, 3, v8\n\t" "v_and_b32 v2, 6, v1\n\t" \
  "v_xor_b32 v2, v2, v8\n\t" "v_and_b32 v2, 7, v2\n\t" "v_mul_u32_u24 v1, 3072, v1\n\t" "v_lshl_add_u32 v1, v2, 4, v1\n\t" "v_add_u32 v17, 23552, v1\n\t" "v_add_u32 v18, 47104, v1\n\t" \
  "v_add_u32 v19, 70656, v1\n\t" "s_cmp_ge_u32 s26, 4\n\t" "s_cselect_b64 s[42:43], s[18:19], s[16:17]\n\t" "s_mul_i32 s40, s33, 491520\n\t" "s_lshl_b32 s41, s29, 7\n\t" "s_add_u32 s40, s40, s41\n\t" \
  "s_add_u32 s42, s42, s40\n\t" "s_addc_u32 s43, s43, 0\n\t" "s_lshl_b32 s44, s26, 13\n\t" "s_add_u32 m0, s44, 0\n\t" "s_nop 0\n\t" "global_load_lds_dwordx4 v1, s[42:43] offset:0 nt\n\t" \
  "global_load_lds_dwordx4 v17, s[42:43] offset:1024 nt\n\t" "global_load_lds_dwordx4 v18, s[42:43] offset:2048 nt\n\t" "global_load_lds_dwordx4 v19, s[42:43] offset:3072 nt\n\t" \
  "s_add_u32 s42, s42, 98304\n\t" "s_addc_u32 s43, s43, 0\n\t" "s_add_u32 m0, s44, 4096\n\t" "s_nop 0\n\t" "global_load_lds_dwordx4 v1, s[42:43] offset:0 nt\n\t" \
  "global_load_lds_dwordx4 v17, s[42:43] offset:1024 nt\n\t" "global_load_lds_dwordx4 v18, s[42:43] offset:2048 nt\n\t" "global_load_lds_dwordx4 v19, s[42:43] offset:3072 nt\n\t" \
  "s_add_u32 s42, s42, 98304\n\t" "s_addc_u32 s43, s43, 0\n\t" "v_lshrrev_b32 v10, 2, v8\n\t" "v_lshlrev_b32 v3, 2, v8\n\t" "v_and_b32 v4, 0x60, v3\n\t" "v_and_b32 v3, 12, v3\n\t" \
  "v_lshl_add_u32 v2, v10, 7, v4\n\t" "v_lshl_add_u32 v2, v3, 1, v2\n\t" "v_mul_u32_u24 v4, 1280, v10\n\t" "v_lshl_add_u32 v4, v3, 1, v4\n\t" "s_mul_i32 s40, s26, 160\n\t" \
  "s_add_u32 s40, s40, 65536\n\t" "v_add_u32 v4, s40, v4\n\t" "v_and_b32 v10, 12, v10\n\t" "v_xor_b32 v12, 0, v2\n\t" "v_add_u32 v12, s44, v12\n\t" "v_xor_b32 v13, 32, v2\n\t" \
  "v_add_u32 v13, s44, v13\n\t" "v_xor_b32 v14, 64, v2\n\t" "v_add_u32 v14, s44, v14\n\t" "v_xor_b32 v15, 96, v2\n\t" "v_add_u32 v15, s44, v15\n\t" "s_waitcnt vmcnt(4)\n\t" \
  "ds_read_b64_tr_b16 v[40:41], v12 offset:0\n\t" "ds_read_b64_tr_b16 v[42:43], v12 offset:2048\n\t" "ds_read_b64_tr_b16 v[44:45], v13 offset:0\n\t" "ds_read_b64_tr_b16 v[46:47], v13 offset:2048\n\t" \
  "ds_read_b64_tr_b16 v[48:49], v14 offset:0\n\t" "ds_read_b64_tr_b16 v[50:51], v14 offset:2048\n\t" "ds_read_b64_tr_b16 v[52:53], v15 offset:0\n\t" "ds_read_b64_tr_b16 v[54:55], v15 offset:2048\n\t" \
  "s_waitcnt lgkmcnt(0)\n\t" "s_add_u32 m0, s44, 0\n\t" "s_nop 0\n\t" "global_load_lds_dwordx4 v1, s[42:43] offset:0 nt\n\t" "global_load_lds_dwordx4 v17, s[42:43] offset:1024 nt\n\t" \
  "global_load_lds_dwordx4 v18, s[42:43] offset:2048 nt\n\t" "global_load_lds_dwordx4 v19, s[42:43] offset:3072 nt\n\t" "s_add_u32 s42, s42, 98304\n\t" "s_addc_u32 s43, s43, 0\n\t" \
  "s_waitcnt vmcnt(4)\n\t" "ds_read_b64_tr_b16 v[56:57], v12 offset:4096\n\t" "ds_read_b64_tr_b16 v[58:59], v12 offset:6144\n\t" "ds_read_b64_tr_b16 v[60:61], v13 offset:4096\n\t" \
  "ds_read_b64_tr_b16 v[62:63], v13 offset:6144\n\t" "ds_read_b64_tr_b16 v[64:65], v14 offset:4096\n\t" "ds_read_b64_tr_b16 v[66:67], v14 offset:6144\n\t" \
  "ds_read_b64_tr_b16 v[68:69], v15 offset:4096\n\t" "ds_read_b64_tr_b16 v[70:71], v15 offset:6144\n\t" "s_waitcnt lgkmcnt(0)\n\t" "s_add_u32 m0, s44, 4096\n\t" "s_nop 0\n\t" \
  "global_load_lds_dwordx4 v1, s[42:43] offset:0 nt\n\t" "global_load_lds_dwordx4 v17, s[42:43] offset:1024 nt\n\t" "global_load_lds_dwordx4 v18, s[42:43] offset:2048 nt\n\t" \
  "global_load_lds_dwordx4 v19, s[42:43] offset:3072 nt\n\t" "s_add_u32 s42, s42, 98304\n\t" "s_addc_u32 s43, s43, 0\n\t" "s_waitcnt vmcnt(4)\n\t" "ds_read_b64_tr_b16 v[72:73], v12 offset:0\n\t" \
  "ds_read_b64_tr_b16 v[74:75], v12 offset:2048\n\t" "ds_read_b64_tr_b16 v[76:77], v13 offset:0\n\t" "ds_read_b64_tr_b16 v[78:79], v13 offset:2048\n\t" "ds_read_b64_tr_b16 v[80:81], v14 offset:0\n\t" \
  "ds_read_b64_tr_b16 v[82:83], v14 offset:2048\n\t" "ds_read_b64_tr_b16 v[84:85], v15 offset:0\n\t" "ds_read_b64_tr_b16 v[86:87], v15 offset:2048\n\t" "s_waitcnt lgkmcnt(0)\n\t" \
  "s_add_u32 m0, s44, 0\n\t" "s_nop 0\n\t" "global_load_lds_dwordx4 v1, s[42:43] offset:0 nt\n\t" "global_load_lds_dwordx4 v17, s[42:43] offset:1024 nt\n\t" \
  "global_load_lds_dwordx4 v18, s[42:43] offset:2048 nt\n\t" "global_load_lds_dwordx4 v19, s[42:43] offset:3072 nt\n\t" "s_barrier\n\t" "v_add_u32 v2, 40960, v4\n\t" \
  "ds_read_b64_tr_b16 v[120:121], v4 offset:0\n\t" "ds_read_b64_tr_b16 v[122:123], v4 offset:20480\n\t" "ds_read_b64_tr_b16 v[124:125], v2 offset:0\n\t" \
  "ds_read_b64_tr_b16 v[126:127], v2 offset:20480\n\t" "ds_read_b64_tr_b16 v[128:129], v4 offset:32\n\t" "ds_read_b64_tr_b16 v[130:131], v4 offset:20512\n\t" \
  "ds_read_b64_tr_b16 v[132:133], v2 offset:32\n\t" "ds_read_b64_tr_b16 v[134:135], v2 offset:20512\n\t" "ds_read_b64_tr_b16 v[136:137], v4 offset:64\n\t" \
  "ds_read_b64_tr_b16 v[138:139], v4 offset:20544\n\t" "ds_read_b64_tr_b16 v[140:141], v2 offset:64\n\t" "ds_read_b64_tr_b16 v[142:143], v2 offset:20544\n\t" \
  "ds_read_b64_tr_b16 v[144:145], v4 offset:96\n\t" "ds_read_b64_tr_b16 v[146:147], v4 offset:20576\n\t" "ds_read_b64_tr_b16 v[148:149], v2 offset:96\n\t" \
  "ds_read_b64_tr_b16 v[150:151], v2 offset:20576\n\t" "ds_read_b64_tr_b16 v[152:153], v4 offset:128\n\t" "ds_read_b64_tr_b16 v[154:155], v4 offset:20608\n\t" \
  "ds_read_b64_tr_b16 v[156:157], v2 offset:128\n\t" "ds_read_b64_tr_b16 v[158:159], v2 offset:20608\n\t" "s_waitcnt vmcnt(4)\n\t" "ds_read_b64_tr_b16 v[88:89], v12 offset:4096\n\t" \
  "ds_read_b64_tr_b16 v[90:91], v12 offset:6144\n\t" "ds_read_b64_tr_b16 v[92:93], v13 offset:4096\n\t" "ds_read_b64_tr_b16 v[94:95], v13 offset:6144\n\t" \
  "ds_read_b64_tr_b16 v[96:97], v14 offset:4096\n\t" "ds_read_b64_tr_b16 v[98:99], v14 offset:6144\n\t" "ds_read_b64_tr_b16 v[100:101], v15 offset:4096\n\t" \
  "ds_read_b64_tr_b16 v[102:103], v15 offset:6144\n\t" "s_waitcnt vmcnt(0)\n\t" "ds_read_b64_tr_b16 v[104:105], v12 offset:0\n\t" "ds_read_b64_tr_b16 v[106:107], v12 offset:2048\n\t" \
  "ds_read_b64_tr_b16 v[108:109], v13 offset:0\n\t" "ds_read_b64_tr_b16 v[110:111], v13 offset:2048\n\t" "ds_read_b64_tr_b16 v[112:113], v14 offset:0\n\t" \
  "ds_read_b64_tr_b16 v[114:115], v14 offset:2048\n\t" "ds_read_b64_tr_b16 v[116:117], v15 offset:0\n\t" "ds_read_b64_tr_b16 v[118:119], v15 offset:2048\n\t" "s_mul_i32 s40, s33, 320\n\t" \
  "s_add_u32 s40, s40, 159744\n\t" "v_lshl_add_u32 v1, v10, 1, s40\n\t" "s_lshl_b32 s40, s26, 8\n\t" "s_add_u32 s40, s40, 161024\n\t" "v_lshl_add_u32 v2, v10, 2, s40\n\t" "v_and_b32 v4, 12, v8\n\t" \
  "v_lshl_add_u32 v4, v4, 2, v10\n\t" "s_mov_b32 s40, 161024\n\t" "v_lshl_add_u32 v4, v4, 2, s40\n\t" "v_add_u32 v3, v3, v4\n\t" "s_mul_i32 s40, s26, 320\n\t" "v_lshl_add_u32 v4, v9, 6, s40\n\t" \
  "v_lshl_add_u32 v4, v10, 2, v4\n\t" "s_mul_i32 s40, s29, 2560\n\t" "s_add_u32 s40, s40, 5241344\n\t" "s_add_u32 s62, s24, s40\n\t" "s_addc_u32 s63, s25, 0\n\t" "s_mov_b32 s40, 0x10001\n\t" \
  "s_mov_b32 s41, s40\n\t" "s_lshl_b64 s[42:43], s[40:41], 1\n\t" "s_lshl_b64 s[44:45], s[40:41], 2\n\t" "s_lshl_b64 s[46:47], s[40:41], 3\n\t" "s_lshl_b64 s[48:49], s[40:41], 4\n\t" \
  "s_mov_b32 s60, 0x1f001f\n\t" "s_mov_b32 s61, s60\n\t" "s_mov_b32 s76, 0xbfb8aa3b\n\t" "s_setprio 3\n\t" "s_waitcnt lgkmcnt(0)\n\t" "s_barrier\n\t" \
  "ds_read2_b64 v[28:31], v1 offset0:0 offset1:4\n\t" "ds_read2_b64 v[32:35], v1 offset0:8 offset1:12\n\t" "ds_read2_b64 v[36:39], v1 offset0:16 offset1:20\n\t" \
  "ds_read2_b64 v[160:163], v1 offset0:24 offset1:28\n\t" "ds_read2_b64 v[164:167], v1 offset0:32 offset1:36\n\t" "s_waitcnt lgkmcnt(4)\n\t" \
  "v_mfma_f32_16x16x32_bf16 v[12:15], v[40:43], v[28:31], 0\n\t" "v_mfma_f32_16x16x32_bf16 v[16:19], v[44:47], v[28:31], 0\n\t" "v_mfma_f32_16x16x32_bf16 v[20:23], v[48:51], v[28:31], 0\n\t" \
  "v_mfma_f32_16x16x32_bf16 v[24:27], v[52:55], v[28:31], 0\n\t" "s_waitcnt lgkmcnt(3)\n\t" "v_mfma_f32_16x16x32_bf16 v[12:15], v[56:59], v[32:35], v[12:15]\n\t" \
  "v_mfma_f32_16x16x32_bf16 v[16:19], v[60:63], v[32:35], v[16:19]\n\t" "v_mfma_f32_16x16x32_bf16 v[20:23], v[64:67], v[32:35], v[20:23]\n\t" \
  "v_mfma_f32_16x16x32_bf16 v[24:27], v[68:71], v[32:35], v[24:27]\n\t" "s_waitcnt lgkmcnt(2)\n\t" "v_mfma_f32_16x16x32_bf16 v[12:15], v[72:75], v[36:39], v[12:15]\n\t" \
  "v_mfma_f32_16x16x32_bf16 v[16:19], v[76:79], v[36:39], v[16:19]\n\t" "v_mfma_f32_16x16x32_bf16 v[20:23], v[80:83], v[36:39], v[20:23]\n\t" \
  "v_mfma_f32_16x16x32_bf16 v[24:27], v[84:87], v[36:39], v[24:27]\n\t" "s_waitcnt lgkmcnt(1)\n\t" "v_mfma_f32_16x16x32_bf16 v[12:15], v[88:91], v[160:163], v[12:15]\n\t" \
  "v_mfma_f32_16x16x32_bf16 v[16:19], v[92:95], v[160:163], v[16:19]\n\t" "v_mfma_f32_16x16x32_bf16 v[20:23], v[96:99], v[160:163], v[20:23]\n\t" \
  "v_mfma_f32_16x16x32_bf16 v[24:27], v[100:103], v[160:163], v[24:27]\n\t" "s_waitcnt lgkmcnt(0)\n\t" "v_mfma_f32_16x16x32_bf16 v[12:15], v[104:107], v[164:167], v[12:15]\n\t" \
  "v_mfma_f32_16x16x32_bf16 v[16:19], v[108:111], v[164:167], v[16:19]\n\t" "v_mfma_f32_16x16x32_bf16 v[20:23], v[112:115], v[164:167], v[20:23]\n\t" \
  "v_mfma_f32_16x16x32_bf16 v[24:27], v[116:119], v[164:167], v[24:27]\n\t" "s_mov_b64 exec, s[40:41]\n\t" "s_nop 3\n\t" "ds_write_b128 v2, v[12:15] offset:0\n\t" \
  "ds_write_b128 v2, v[16:19] offset:64\n\t" "ds_write_b128 v2, v[20:23] offset:128\n\t" "ds_write_b128 v2, v[24:27] offset:192\n\t" "s_mov_b64 exec, -1\n\t" "s_waitcnt lgkmcnt(0)\n\t" "s_barrier\n\t" \
  "ds_read2st64_b32 v[40:41], v3 offset0:0 offset1:4\n\t" "ds_read2st64_b32 v[42:43], v3 offset0:1 offset1:5\n\t" "ds_read2st64_b32 v[44:45], v3 offset0:2 offset1:6\n\t" \
  "ds_read2st64_b32 v[46:47], v3 offset0:3 offset1:7\n\t" "s_waitcnt lgkmcnt(0)\n\t" "v_pk_add_f32 v[40:41], v[40:41], v[42:43]\n\t" "v_pk_add_f32 v[44:45], v[44:45], v[46:47]\n\t" \
  "v_pk_add_f32 v[40:41], v[40:41], v[44:45]\n\t" "v_cvt_pk_bf16_f32 v41, v40, v41\n\t" "v_lshlrev_b32 v40, 16, v41\n\t" "v_mul_f32 v42, s76, v40\n\t" "v_exp_f32 v42, v42\n\t" \
  "v_and_b32 v41, 0xffff0000, v41\n\t" "v_add_f32 v42, 1.0, v42\n\t" "v_rcp_f32 v42, v42\n\t" "s_nop 0\n\t" "v_mul_f32 v40, v40, v42\n\t" "v_cvt_pk_bf16_f32 v40, v40, v40\n\t" \
  "v_lshlrev_b32 v40, 16, v40\n\t" "v_mul_f32 v40, v40, v41\n\t" "s_nop 1\n\t" "v_mov_b32_dpp v41, v40 quad_perm:[1,1,3,3] row_mask:0xf bank_mask:0xf\n\t" "v_cvt_pk_bf16_f32 v40, v40, v41\n\t" \
  "s_nop 1\n\t" "v_mov_b32_dpp v44, v40 row_newbcast:0 row_mask:0xf bank_mask:0xf\n\t" "v_mov_b32_dpp v45, v40 row_newbcast:2 row_mask:0xf bank_mask:0xf\n\t" \
  "v_mov_b32_dpp v46, v40 row_newbcast:4 row_mask:0xf bank_mask:0xf\n\t" "v_mov_b32_dpp v47, v40 row_newbcast:6 row_mask:0xf bank_mask:0xf\n\t" \
  "v_mov_b32_dpp v48, v40 row_newbcast:8 row_mask:0xf bank_mask:0xf\n\t" "v_mov_b32_dpp v49, v40 row_newbcast:10 row_mask:0xf bank_mask:0xf\n\t" \
  "v_mov_b32_dpp v50, v40 row_newbcast:12 row_mask:0xf bank_mask:0xf\n\t" "v_mov_b32_dpp v51, v40 row_newbcast:14 row_mask:0xf bank_mask:0xf\n\t" \
  "v_mfma_f32_16x16x32_bf16 v[56:59], v[120:123], v[44:47], 0\n\t" "s_nop 0\n\t" "v_mfma_f32_16x16x32_bf16 v[56:59], v[124:127], v[48:51], v[56:59]\n\t" \
  "v_mfma_f32_16x16x32_bf16 v[60:63], v[128:131], v[44:47], 0\n\t" "v_mfma_f32_16x16x32_bf16 v[60:63], v[132:135], v[48:51], v[60:63]\n\t" \
  "v_mfma_f32_16x16x32_bf16 v[64:67], v[136:139], v[44:47], 0\n\t" "v_mfma_f32_16x16x32_bf16 v[64:67], v[140:143], v[48:51], v[64:67]\n\t" \
  "v_mfma_f32_16x16x32_bf16 v[68:71], v[144:147], v[44:47], 0\n\t" "v_mfma_f32_16x16x32_bf16 v[68:71], v[148:151], v[48:51], v[68:71]\n\t" \
  "v_mfma_f32_16x16x32_bf16 v[72:75], v[152:155], v[44:47], 0\n\t" "v_mfma_f32_16x16x32_bf16 v[72:75], v[156:159], v[48:51], v[72:75]\n\t" "s_nop 1\n\t" "v_cndmask_b32 v56, v56, v60, s[42:43]\n\t" \
  "v_cndmask_b32 v57, v57, v61, s[42:43]\n\t" "v_cndmask_b32 v58, v58, v62, s[42:43]\n\t" "v_cndmask_b32 v59, v59, v63, s[42:43]\n\t" "v_cndmask_b32 v56, v56, v64, s[44:45]\n\t" \
  "v_cndmask_b32 v57, v57, v65, s[44:45]\n\t" "v_cndmask_b32 v58, v58, v66, s[44:45]\n\t" "v_cndmask_b32 v59, v59, v67, s[44:45]\n\t" "v_cndmask_b32 v56, v56, v68, s[46:47]\n\t" \
  "v_cndmask_b32 v57, v57, v69, s[46:47]\n\t" "v_cndmask_b32 v58, v58, v70, s[46:47]\n\t" "v_cndmask_b32 v59, v59, v71, s[46:47]\n\t" "v_cndmask_b32 v56, v56, v72, s[48:49]\n\t" \
  "v_cndmask_b32 v57, v57, v73, s[48:49]\n\t" "v_cndmask_b32 v58, v58, v74, s[48:49]\n\t" "v_cndmask_b32 v59, v59, v75, s[48:49]\n\t" "s_mov_b64 exec, s[60:61]\n\t" \
  "global_store_dwordx4 v4, v[56:59], s[62:63] sc1\n\t" "s_mov_b64 exec, -1\n\t" "s_branch .Lend_%=\n\t" ".Lsq_%=x:\n\t" "s_mul_i32 s40, s29, 81920\n\t" "s_mul_i32 s44, s33, 20480\n\t" \
  "s_add_u32 s40, s40, s44\n\t" "s_add_u32 s44, s44, 65536\n\t" "s_add_u32 s42, s20, s40\n\t" "s_addc_u32 s43, s21, 0\n\t" "s_movk_i32 s40, 5\n\t" ".Ld2_%=:\n\t" "s_mov_b32 m0, s44\n\t" \
  "s_add_u32 s44, s44, 4096\n\t" "global_load_lds_dwordx4 v32, s[42:43] offset:0 nt\n\t" "global_load_lds_dwordx4 v32, s[42:43] offset:1024 nt\n\t" \
  "global_load_lds_dwordx4 v32, s[42:43] offset:2048 nt\n\t" "global_load_lds_dwordx4 v32, s[42:43] offset:3072 nt\n\t" "s_add_u32 s42, s42, 4096\n\t" "s_addc_u32 s43, s43, 0\n\t" \
  "s_sub_u32 s40, s40, 1\n\t" "s_cmp_lg_u32 s40, 0\n\t" "s_cbranch_scc1 .Ld2_%=\n\t" "s_waitcnt vmcnt(0) lgkmcnt(0)\n\t" "s_barrier\n\t" "v_add_co_u32 v16, vcc, 1, v40\n\t" "s_nop 1\n\t" \
  "v_addc_co_u32 v17, vcc, 0, v41, vcc\n\t" "s_mul_i32 s40, s29, 205\n\t" "s_lshr_b32 s40, s40, 11\n\t" "s_mul_i32 s40, s40, 10\n\t" "s_sub_u32 s28, s29, s40\n\t" "s_cmp_lt_u32 s26, 10\n\t" \
  "s_cbranch_scc1 .Lcb_%=\n\t" "s_cmp_eq_u32 s26, 10\n\t" "s_cbranch_scc1 .Lfe_%=\n\t" "s_movk_i32 s40, 4096\n\t" "v_min_u32 v3, 22, v8\n\t" "v_lshl_add_u32 v3, v3, 7, s40\n\t" ".Lsf_%=:\n\t" \
  "global_load_dwordx2 v[40:41], v3, s[24:25] sc1\n\t" "s_sub_u32 s39, s39, 1\n\t" "s_cmp_lt_i32 s39, 0\n\t" "s_waitcnt vmcnt(0)\n\t" "s_cbranch_scc1 .Lrt_%=\n\t" \
  "v_cmp_ne_u64 vcc, v[40:41], v[16:17]\n\t" "s_cbranch_vccnz .Lsf_%=\n\t" "s_xor_b32 s40, s28, 1\n\t" "s_mul_i32 s40, s40, 6144\n\t" "s_add_u32 s40, s40, 5302784\n\t" "v_and_b32 v3, 7, v8\n\t" \
  "v_mul_u32_u24 v3, 640, v3\n\t" "v_add_u32 v3, s40, v3\n\t" ".Lsk_%=:\n\t" "s_sleep 10\n\t" "global_load_dword v40, v3, s[24:25] sc1\n\t" "s_sub_u32 s39, s39, 1\n\t" "s_cmp_lt_i32 s39, 0\n\t" \
  "s_waitcnt vmcnt(0)\n\t" "s_cbranch_scc1 .Lrt_%=\n\t" "v_cmp_ne_u32 vcc, -1, v40\n\t" "s_cbranch_vccz .Lsk_%=\n\t" "s_branch .Lrt_%=\n\t" ".Lfe_%=:\n\t" "v_lshrrev_b32 v2, 3, v8\n\t" \
  "v_lshl_add_u32 v2, v2, 7, v32\n\t" "v_lshlrev_b32 v4, 2, v8\n\t" "v_lshrrev_b32 v3, 5, v8\n\t" "v_lshl_add_u32 v3, v3, 7, v4\n\t" "v_add_u32 v4, 160768, v4\n\t" "v_add_u32 v5, 159744, v32\n\t" \
  "s_movk_i32 s44, 63\n\t" ".Lf_%=:\n\t" "ds_read_b64 v[40:41], v37 offset:32\n\t" "s_sub_u32 s39, s39, 1\n\t" "s_cmp_lt_i32 s39, 0\n\t" "s_cselect_b32 s46, 1, 0\n\t" "s_waitcnt lgkmcnt(0)\n\t" \
  "v_readfirstlane_b32 s45, v40\n\t" "v_readfirstlane_b32 s47, v41\n\t" "s_bfe_u32 s45, s45, 0x60006\n\t" "s_or_b32 s46, s46, s47\n\t" "s_xor_b32 s47, s45, s44\n\t" "s_or_b32 s48, s47, s46\n\t" \
  "s_cbranch_scc0 .Lf_%=\n\t" "s_cmp_eq_u32 s47, 0\n\t" "s_cbranch_scc1 .Lfd_%=\n\t" "s_mul_i32 s40, s45, 2560\n\t" "s_add_u32 s40, s40, 5123584\n\t" "s_add_u32 s40, s24, s40\n\t" \
  "s_addc_u32 s41, s25, 0\n\t" "global_load_dwordx4 v[44:47], v2, s[40:41] sc1\n\t" "global_load_dword v48, v3, s[40:41] offset:2048 sc1\n\t" "s_cmp_lt_i32 s39, 0\n\t" "s_waitcnt vmcnt(0)\n\t" \
  "ds_write_b128 v5, v[44:47]\n\t" "ds_write_b32 v4, v48\n\t" "s_cbranch_scc1 .Lfd_%=\n\t" "v_max3_u32 v49, v44, v45, v46\n\t" "v_max3_u32 v49, v49, v47, v48\n\t" "s_movk_i32 s44, 99\n\t" \
  "v_cmp_eq_u32 vcc, -1, v49\n\t" "s_cbranch_vccnz .Lf_%=\n\t" "s_mov_b32 s44, s45\n\t" "s_cmp_eq_u32 s46, 0\n\t" "s_cbranch_scc1 .Lf_%=\n\t" ".Lfd_%=:\n\t" "s_waitcnt lgkmcnt(0)\n\t" "s_barrier\n\t" \
  "s_barrier\n\t" "s_branch .Lend_%=\n\t" ".Lcb_%=:\n\t" "s_cmp_ge_u32 s29, 20\n\t" "s_cbranch_scc1 .Lend_%=\n\t" "global_load_dwordx4 v[20:23], v32, s[14:15]\n\t" "v_lshlrev_b32 v6, 2, v8\n\t" \
  "global_load_dword v24, v6, s[14:15] offset:1024\n\t" "v_add_u32 v5, 159744, v32\n\t" "v_add_u32 v4, 160768, v6\n\t" "s_lshl1_add_u32 s40, s29, s33\n\t" "v_and_b32 v1, 3, v8\n\t" \
  "v_lshrrev_b32 v2, 2, v8\n\t" "v_and_b32 v3, 4, v2\n\t" "v_lshrrev_b32 v7, 4, v8\n\t" "v_and_b32 v7, 2, v7\n\t" "v_lshl_add_u32 v3, v1, 3, v3\n\t" "v_add_u32 v3, v3, v7\n\t" \
  "v_mul_u32_u24 v2, 2560, v2\n\t" "v_lshl_add_u32 v2, v1, 4, v2\n\t" "s_lshl_b32 s41, s40, 5\n\t" "s_add_u32 s42, s41, 159744\n\t" "v_add_u32 v1, s42, v3\n\t" "s_lshl_b32 s42, s40, 6\n\t" \
  "s_add_u32 s42, s42, 5241344\n\t" "s_add_u32 s44, s24, s42\n\t" "s_addc_u32 s45, s25, 0\n\t" "s_add_u32 s46, s44, 40960\n\t" "s_addc_u32 s47, s45, 0\n\t" "v_mov_b64 v[64:65], 0\n\t" \
  "v_mov_b64 v[66:67], 0\n\t" "s_waitcnt vmcnt(0)\n\t" "s_barrier\n\t" "s_barrier\n\t" "s_memrealtime s[64:65]\n\t" "ds_read_b32 v39, v37 offset:32\n\t" "ds_read_b128 v[44:47], v5\n\t" \
  "ds_read_b32 v48, v4\n\t" "ds_read_u16_d16_hi v49, v1\n\t" "s_waitcnt lgkmcnt(0)\n\t" "v_readfirstlane_b32 s42, v39\n\t" "v_dot2_f32_bf16 v50, v48, v24, 0\n\t" \
  "v_dot2_f32_bf16 v50, v44, v20, v50\n\t" "v_dot2_f32_bf16 v50, v45, v21, v50\n\t" "v_dot2_f32_bf16 v50, v46, v22, v50\n\t" "v_dot2_f32_bf16 v50, v47, v23, v50\n\t" "s_bfe_u32 s43, s42, 0x60006\n\t" \
  "s_lshr_b32 s56, s43, 1\n\t" "s_mul_i32 s56, s56, 89\n\t" "s_and_b32 s57, s43, 1\n\t" "s_mul_i32 s57, s57, 48\n\t" "s_add_u32 s56, s56, s57\n\t" "s_and_b32 s42, s42, 63\n\t" \
  "s_add_u32 s56, s56, s42\n\t" "s_sub_u32 s56, s56, 6\n\t" "v_add_f32_dpp v50, v50, v50 quad_perm:[1,0,3,2] row_mask:0xf bank_mask:0xf\n\t" "s_nop 1\n\t" \
  "v_add_f32_dpp v50, v50, v50 quad_perm:[2,3,0,1] row_mask:0xf bank_mask:0xf\n\t" "s_nop 1\n\t" "v_add_f32_dpp v50, v50, v50 row_half_mirror row_mask:0xf bank_mask:0xf\n\t" "s_nop 1\n\t" \
  "v_add_f32_dpp v50, v50, v50 row_mirror row_mask:0xf bank_mask:0xf\n\t" "s_bfe_u32 s57, s56, 0x1000a\n\t" "s_lshl1_add_u32 s56, s56, s57\n\t" "s_and_b32 s56, s56, 2047\n\t" "v_mov_b32 v51, v50\n\t" \
  "s_mul_i32 s56, s56, 1280\n\t" "s_add_u32 s56, s56, s41\n\t" "v_permlane32_swap_b32 v50, v51\n\t" "v_add_f32 v50, v50, v51\n\t" "v_mov_b32 v51, v50\n\t" "s_add_u32 s56, s22, s56\n\t" \
  "s_addc_u32 s57, s23, 0\n\t" "v_permlane16_swap_b32 v50, v51\n\t" "v_add_f32 v50, v50, v51\n\t" "v_cvt_pk_bf16_f32 v50, v50, v50\n\t" "v_lshlrev_b32 v50, 16, v50\n\t" \
  "v_mul_f32 v50, 0xbfb8aa3b, v50\n\t" "v_exp_f32 v50, v50\n\t" "s_mul_i32 s43, s43, 2560\n\t" "s_lshr_b32 s42, s40, 2\n\t" "s_lshl_b32 s42, s42, 7\n\t" "s_add_u32 s43, s43, s42\n\t" \
  "s_add_u32 s43, s43, s41\n\t" "v_add_f32 v50, 1.0, v50\n\t" "v_rcp_f32 v50, v50\n\t" "s_add_u32 s43, s43, 5123712\n\t" "s_add_u32 s58, s24, s43\n\t" "s_addc_u32 s59, s25, 0\n\t" \
  "v_cvt_pk_bf16_f32 v50, v50, v50\n\t" "v_lshlrev_b32 v50, 16, v50\n\t" "v_and_b32 v49, 0xffff0000, v49\n\t" ".Ltc_%=:\n\t" "s_memrealtime s[78:79]\n\t" "s_waitcnt lgkmcnt(0)\n\t" \
  "s_sub_u32 s78, s78, s64\n\t" "s_cmp_lt_u32 s78, 60\n\t" "s_cbranch_scc1 .Ltc_%=\n\t" ".Lcp_%=:\n\t" "global_load_short_d16_hi v51, v3, s[58:59] sc1\n\t" \
  "global_load_dwordx4 v[60:63], v2, s[44:45] sc1\n\t" "s_mov_b32 exec_hi, 0\n\t" "global_load_dwordx4 v[64:67], v2, s[46:47] sc1\n\t" "s_mov_b32 exec_hi, -1\n\t" "s_sub_u32 s39, s39, 1\n\t" \
  "s_cmp_lt_i32 s39, 0\n\t" "s_waitcnt vmcnt(0)\n\t" "v_pk_add_f32 v[60:61], v[60:61], v[64:65]\n\t" "v_pk_add_f32 v[62:63], v[62:63], v[66:67]\n\t" "v_and_b32 v51, 0xffff0000, v51\n\t" \
  "v_add_f32_dpp v60, v60, v60 row_ror:4 row_mask:0xf bank_mask:0xf\n\t" "v_add_f32_dpp v61, v61, v61 row_ror:4 row_mask:0xf bank_mask:0xf\n\t" \
  "v_add_f32_dpp v62, v62, v62 row_ror:4 row_mask:0xf bank_mask:0xf\n\t" "v_add_f32_dpp v63, v63, v63 row_ror:4 row_mask:0xf bank_mask:0xf\n\t" \
  "v_add_f32_dpp v60, v60, v60 row_ror:8 row_mask:0xf bank_mask:0xf\n\t" "v_add_f32_dpp v61, v61, v61 row_ror:8 row_mask:0xf bank_mask:0xf\n\t" \
  "v_add_f32_dpp v62, v62, v62 row_ror:8 row_mask:0xf bank_mask:0xf\n\t" "v_add_f32_dpp v63, v63, v63 row_ror:8 row_mask:0xf bank_mask:0xf\n\t" "v_permlane32_swap_b32 v60, v61\n\t" "s_nop 0\n\t" \
  "v_permlane32_swap_b32 v62, v63\n\t" "v_add_f32 v60, v60, v61\n\t" "v_add_f32 v62, v62, v63\n\t" "s_nop 1\n\t" "v_permlane16_swap_b32 v60, v62\n\t" "v_add_f32 v60, v60, v62\n\t" \
  "v_cvt_pk_bf16_f32 v60, v60, v60\n\t" "v_lshlrev_b32 v60, 16, v60\n\t" "v_mul_f32 v60, v60, v50\n\t" "v_cvt_pk_bf16_f32 v60, v60, v60\n\t" "v_lshlrev_b32 v60, 16, v60\n\t" \
  "v_add_f32 v60, v60, v49\n\t" "v_cvt_pk_bf16_f32 v60, v60, v60\n\t" "v_lshlrev_b32 v60, 16, v60\n\t" "v_add_f32 v60, v60, v51\n\t" "s_cbranch_scc1 .Lcq_%=\n\t" "v_cmp_u_f32 vcc, v60, v60\n\t" \
  "s_cbranch_vccnz .Lcp_%=\n\t" ".Lcq_%=:\n\t" "v_cvt_pk_bf16_f32 v60, v60, v60\n\t" "s_mov_b32 exec_lo, 0xf000f\n\t" "s_mov_b32 exec_hi, 0xf000f\n\t" "global_store_short v3, v60, s[56:57]\n\t" \
  "s_mov_b64 exec, -1\n\t" ".Lend_%=:\n\t"

extern "C" __global__ __launch_bounds__(768, 3) void fused_block(const u16* x, const u16* wqkv, const u16* wo, const u16* w_r,
                                                                 const u16* w_gate, const u16* w_up, const u16* w_down, u16* out,
                                                                 char* ws) {
  u32 tid = threadIdx.x;
  asm volatile(BODY
    : "+{v0}"(tid)
    : [ka] "s"(__builtin_amdgcn_kernarg_segment_ptr()), [bid] "s"(blockIdx.x)
    : "memory", "scc", "vcc", "v1", "v2", "v3", "v4", "v5", "v6", "v7", "v8", "v9", CL10(1), CL10(2), CL10(3), CL10(4), CL10(5), CL10(6),
      CL10(7), CL10(8), CL10(9), CL10(10), CL10(11), CL10(12), CL10(13), CL10(14), CL10(15), CL8(16), "s8", "s9", SL10(1), SL10(2), "s30",
      "s31", "s33", "s34", "s35", "s36", "s37", "s38", "s39", SL10(4), SL10(5), SL10(6), SL10(7), SL10(8), SL10(9));
}

extern "C" void kernel_launch(void* const* d_in, const int* in_sizes, int n_in,
                              void* d_out, int out_size, void* d_ws, size_t ws_size,
                              hipStream_t stream) {
  const u16* x = (const u16*)d_in[0];
  const u16* wqkv = (const u16*)d_in[1];
  const u16* wo = (const u16*)d_in[2];
  const u16* w_r = (const u16*)d_in[4];
  const u16* w_gate = (const u16*)d_in[5];
  const u16* w_up = (const u16*)d_in[6];
  const u16* w_down = (const u16*)d_in[7];
  (void)hipFuncSetAttribute((const void*)fused_block, hipFuncAttributeMaxDynamicSharedMemorySize, LDS_TOTAL);
  fused_block<<<256, 768, LDS_TOTAL, stream>>>(x, wqkv, wo, w_r, w_gate, w_up, w_down, (u16*)d_out, (char*)d_ws);
}
```

```cpp
#include <hip/hip_runtime.h>
#include <stdint.h>


typedef unsigned short u16;
typedef unsigned int u32;
#define LDS_TOTAL 163840
#define CL8(T) "v" #T "0", "v" #T "1", "v" #T "2", "v" #T "3", "v" #T "4", "v" #T "5", "v" #T "6", "v" #T "7"
#define CL10(T) CL8(T), "v" #T "8", "v" #T "9"
#define SL10(T) "s" #T "0", "s" #T "1", "s" #T "2", "s" #T "3", "s" #T "4", "s" #T "5", "s" #T "6", "s" #T "7", "s" #T "8", "s" #T "9"

#define BODY \
  ".Lst_%=:\n\t" "s_load_dwordx16 s[8:23], %[ka], 0x0\n\t" "s_load_dwordx2 s[24:25], %[ka], 0x40\n\t" "v_readfirstlane_b32 s26, v0\n\t" "s_lshr_b32 s76, %[bid], 3\n\t" "v_and_b32 v8, 63, v0\n\t" \
  "s_mul_i32 s77, s76, 205\n\t" "s_lshr_b32 s26, s26, 6\n\t" "s_lshr_b32 s77, s77, 10\n\t" "v_lshrrev_b32 v1, 3, v8\n\t" "s_mul_i32 s27, s77, 5\n\t" "s_and_b32 s33, s26, 3\n\t" \
  "s_sub_u32 s27, s76, s27\n\t" "s_and_b32 s78, %[bid], 1\n\t" "s_bfe_u32 s79, %[bid], 0x20001\n\t" "s_mul_i32 s28, s78, 5\n\t" "s_mul_i32 s29, s79, 6\n\t" "s_add_u32 s28, s28, s27\n\t" \
  "s_add_u32 s29, s29, s77\n\t" "s_cmp_ge_u32 s76, 30\n\t" "s_cselect_b32 s29, 24, s29\n\t" "s_cmp_ge_u32 s29, 23\n\t" "s_cbranch_scc1 .Lsp_%=\n\t" "s_lshl_b32 s77, s77, 2\n\t" \
  "s_lshl1_add_u32 s31, s27, s78\n\t" "s_lshr_b32 s77, 0x875320, s77\n\t" "s_add_u32 s31, s31, s79\n\t" "s_and_b32 s77, s77, 15\n\t" "s_mul_i32 s30, s29, 89\n\t" "s_add_u32 s31, s31, s77\n\t" \
  "s_mul_i32 s77, s31, 205\n\t" "s_lshr_b32 s77, s77, 11\n\t" "s_mul_i32 s77, s77, 10\n\t" "s_sub_u32 s31, s31, s77\n\t" "s_cmp_ge_u32 s26, 4\n\t" "s_cbranch_scc1 .Lnl_%=\n\t" "s_setprio 2\n\t" \
  "v_lshl_or_b32 v2, s33, 4, v1\n\t" "v_and_b32 v3, 2, v1\n\t" "v_mul_u32_u24 v2, 0xf00, v2\n\t" "v_xor_b32 v3, v3, v8\n\t" "s_lshl_b32 s76, s28, 7\n\t" "v_and_b32 v3, 7, v3\n\t" \
  "s_add_u32 s76, s76, 3072\n\t" "v_lshlrev_b32 v3, 4, v3\n\t" "s_mul_i32 s86, s33, 6144\n\t" "v_add3_u32 v40, v2, v3, s76\n\t" "s_add_u32 s86, s86, 3072\n\t" "v_add_u32 v41, 256, v40\n\t" \
  "v_add_u32 v42, 512, v40\n\t" "v_xor_b32 v43, 64, v40\n\t" "s_mov_b32 m0, s86\n\t" "v_add_u32 v43, 27648, v43\n\t" "s_waitcnt lgkmcnt(0)\n\t" "s_mul_i32 s76, s31, 245760\n\t" \
  "s_lshl_b32 s77, s31, 7\n\t" "s_add_u32 s78, s10, s76\n\t" "s_addc_u32 s79, s11, 0\n\t" "s_add_u32 s80, s8, s77\n\t" "s_addc_u32 s81, s9, 0\n\t" "v_add_u32 v44, 256, v43\n\t" \
  "v_add_u32 v45, 512, v43\n\t" "global_load_lds_dwordx4 v40, s[78:79] offset:-3072\n\t" "global_load_lds_dwordx4 v41, s[78:79] offset:-2048\n\t" \
  "global_load_lds_dwordx4 v42, s[78:79] offset:-1024\n\t" "global_load_lds_dwordx4 v43, s[78:79] offset:0\n\t" "global_load_lds_dwordx4 v44, s[78:79] offset:1024\n\t" \
  "global_load_lds_dwordx4 v45, s[78:79] offset:2048\n\t" "s_mul_i32 s76, s33, 24\n\t" "s_mul_i32 s87, s33, 3072\n\t" "v_add_u32 v2, s76, v1\n\t" "s_movk_i32 s77, 2048\n\t" \
  "v_lshrrev_b32 v3, 1, v2\n\t" "v_add_u32 v2, s30, v2\n\t" "v_xor_b32 v3, v3, v8\n\t" "s_add_u32 s87, s87, 26624\n\t" "v_and_b32 v3, 7, v3\n\t" "s_mov_b32 m0, s87\n\t" \
  "v_lshl_add_u32 v3, v3, 4, s77\n\t" "s_movk_i32 s77, 1280\n\t" "v_add_u32 v4, 0, v2\n\t" "v_sub_u32 v4, v4, 6 clamp\n\t" "v_lshrrev_b32 v5, 10, v4\n\t" "v_lshl_or_b32 v4, v4, 1, v5\n\t" \
  "v_and_b32 v4, 0x7ff, v4\n\t" "v_mad_u32_u24 v46, v4, s77, v3\n\t" "global_load_lds_dwordx4 v46, s[80:81] offset:-2048\n\t" "v_xor_b32 v3, 64, v3\n\t" "v_subrev_u32 v3, 1024, v3\n\t" \
  "v_add_u32 v4, 8, v2\n\t" "v_sub_u32 v4, v4, 6 clamp\n\t" "v_lshrrev_b32 v5, 10, v4\n\t" "v_lshl_or_b32 v4, v4, 1, v5\n\t" "v_and_b32 v4, 0x7ff, v4\n\t" "v_mad_u32_u24 v47, v4, s77, v3\n\t" \
  "global_load_lds_dwordx4 v47, s[80:81] offset:-1024\n\t" "v_xor_b32 v3, 64, v3\n\t" "v_subrev_u32 v3, 1024, v3\n\t" "v_add_u32 v4, 16, v2\n\t" "v_sub_u32 v4, v4, 6 clamp\n\t" \
  "v_lshrrev_b32 v5, 10, v4\n\t" "v_lshl_or_b32 v4, v4, 1, v5\n\t" "v_and_b32 v4, 0x7ff, v4\n\t" "v_mad_u32_u24 v48, v4, s77, v3\n\t" "global_load_lds_dwordx4 v48, s[80:81] offset:0\n\t" \
  ".Lnl_%=:\n\t" "s_cmp_lg_u32 s26, 11\n\t" "s_cbranch_scc1 .Lnc_%=\n\t" "s_getpc_b64 s[76:77]\n\t" ".Lpc_%=:\n\t" "s_and_b32 s78, %[bid], -8\n\t" "s_sub_u32 s76, s76, .Lpc_%=-.Lst_%=\n\t" \
  "s_subb_u32 s77, s77, 0\n\t" "v_add_u32 v1, s78, v8\n\t" "v_min_u32 v1, 138, v1\n\t" "s_mov_b32 m0, 163072\n\t" "v_lshlrev_b32 v1, 7, v1\n\t" "s_mov_b64 exec, 0xff\n\t" \
  "global_load_lds_dword v1, s[76:77]\n\t" "s_mov_b64 exec, -1\n\t" ".Lnc_%=:\n\t" "s_cmp_ge_u32 s26, 6\n\t" "s_cselect_b32 s34, 1, 0\n\t" "s_cselect_b32 s76, 6, 0\n\t" "v_and_b32 v9, 15, v8\n\t" \
  "s_sub_u32 s35, s26, s76\n\t" "v_lshrrev_b32 v10, 2, v8\n\t" "s_and_b32 s36, s26, 1\n\t" "s_lshr_b32 s37, s26, 1\n\t" "s_lshl4_add_u32 s76, s35, s30\n\t" "s_sub_u32 s76, s76, 6\n\t" \
  "v_lshlrev_b32 v32, 4, v8\n\t" "v_add_u32 v2, s76, v9\n\t" "s_mov_b32 s76, 0x1fe00\n\t" "v_and_b32 v2, 0x3ff, v2\n\t" "v_lshlrev_b32 v1, v9, s76\n\t" "v_min_u32 v2, 7, v2\n\t" \
  "v_lshlrev_b32 v35, 7, v10\n\t" "v_sub_u32 v2, v9, v2\n\t" "v_and_b32 v10, 12, v10\n\t" "v_add_u32 v2, 16, v2\n\t" "s_cmp_eq_u32 s35, 0\n\t" "s_cselect_b32 s76, 0xffff0000, -1\n\t" \
  "s_cselect_b32 s81, 0, 1024\n\t" "v_lshlrev_b32 v2, v2, -1\n\t" "v_and_b32 v1, s76, v1\n\t" "s_mov_b32 s77, 0xff80ff80\n\t" "s_cmp_eq_u32 s29, 0\n\t" "s_cselect_b32 s77, 0xffc0ffc0, s77\n\t" \
  "v_and_b32 v1, v1, v2\n\t" "s_cmp_eq_u32 s37, 0\n\t" "s_cselect_b32 s72, s77, -1\n\t" "v_not_b32 v1, v1\n\t" "s_mov_b32 s73, s72\n\t" "v_lshrrev_b32 v1, v10, v1\n\t" \
  "s_lshl4_add_u32 s76, s37, s30\n\t" "s_mov_b32 s77, 0xffff\n\t" "v_bfe_i32 v20, v1, 0, 1\n\t" "v_bfe_i32 v21, v1, 1, 1\n\t" "v_bfe_i32 v22, v1, 2, 1\n\t" "v_bfe_i32 v23, v1, 3, 1\n\t" \
  "v_add_u32 v2, s76, v9\n\t" "v_bfi_b32 v28, s77, v20, v21\n\t" "v_bfi_b32 v29, s77, v22, v23\n\t" "v_bfe_i32 v24, v1, 16, 1\n\t" "v_bfe_i32 v25, v1, 17, 1\n\t" "v_bfe_i32 v26, v1, 18, 1\n\t" \
  "v_bfe_i32 v27, v1, 19, 1\n\t" "v_sub_u32 v2, v2, 6 clamp\n\t" "v_lshrrev_b32 v3, 10, v2\n\t" "v_lshl_or_b32 v2, v2, 1, v3\n\t" "v_and_b32 v2, 0x7ff, v2\n\t" "s_lshl_b32 s76, s28, 7\n\t" \
  "s_lshl_b32 s77, s36, 6\n\t" "v_and_b32 v20, 0xff800000, v20\n\t" "v_and_b32 v21, 0xff800000, v21\n\t" "v_and_b32 v22, 0xff800000, v22\n\t" "v_and_b32 v23, 0xff800000, v23\n\t" \
  "v_and_b32 v24, 0xff800000, v24\n\t" "v_and_b32 v25, 0xff800000, v25\n\t" "v_and_b32 v26, 0xff800000, v26\n\t" "v_and_b32 v27, 0xff800000, v27\n\t" "s_add_u32 s76, s76, s77\n\t" \
  "v_lshlrev_b32 v1, 1, v8\n\t" "v_lshrrev_b32 v3, 1, v8\n\t" "v_and_b32 v1, 32, v1\n\t" "v_and_b32 v3, 16, v3\n\t" "s_movk_i32 s78, 1280\n\t" "v_or3_b32 v34, v1, v3, s76\n\t" \
  "v_lshlrev_b32 v5, 2, v8\n\t" "v_mad_u32_u24 v33, v2, s78, v34\n\t" "v_and_b32 v6, 0x60, v5\n\t" "s_mul_i32 s76, s36, 40960\n\t" "v_xor_b32 v1, s77, v6\n\t" "s_add_u32 s76, s76, 73728\n\t" \
  "v_and_b32 v5, 12, v5\n\t" "v_add3_u32 v35, v35, v1, s76\n\t" "v_mov_b32 v37, 156416\n\t" "v_lshl_add_u32 v35, v5, 1, v35\n\t" "v_mov_b32 v38, 1\n\t" "v_add_u32 v36, 1413120, v32\n\t" \
  "s_mul_i32 s78, s27, 12\n\t" "s_mov_b32 s76, 0x10321320\n\t" "s_mov_b32 s77, 0xbe0f102\n\t" "s_lshr_b64 s[76:77], s[76:77], s78\n\t" "s_and_b32 s74, s76, 0xfff\n\t" "s_mov_b32 s76, 0xedfe0f41\n\t" \
  "s_mov_b32 s77, 0xfdcedcf\n\t" "s_lshr_b64 s[76:77], s[76:77], s78\n\t" "s_pack_ll_b32_b16 s74, s74, s76\n\t" "s_mul_i32 s76, s27, 0x10001\n\t" "s_sub_u32 s76, 0xe0009, s76\n\t" \
  "s_lshl4_add_u32 s74, s74, s76\n\t" "s_mul_i32 s78, s34, 3072\n\t" "s_lshr_b32 s79, s74, 16\n\t" "v_bfe_u32 v1, s74, v10, 4\n\t" "s_lshl_b32 s76, s35, 9\n\t" "v_bfe_u32 v2, s79, v10, 4\n\t" \
  "v_and_b32 v4, 12, v8\n\t" "s_sub_u32 s76, s76, s78\n\t" "v_bfe_u32 v3, s74, v4, 4\n\t" "s_add_u32 s76, s76, 3072\n\t" "v_bfe_u32 v4, s79, v4, 4\n\t" "v_lshl_add_u32 v30, v8, 3, s76\n\t" \
  "s_sub_u32 s76, 5, s27\n\t" "v_lshl_add_u32 v31, s78, 1, v30\n\t" "s_sub_u32 s77, 15, s27\n\t" "v_cmp_gt_u32 s[40:41], s76, v1\n\t" "v_cmp_gt_u32 s[42:43], s76, v2\n\t" \
  "v_cmp_le_u32 s[44:45], s77, v1\n\t" "v_cmp_le_u32 s[46:47], s77, v2\n\t" "v_cmp_gt_u32 s[50:51], s76, v3\n\t" "v_cmp_gt_u32 s[52:53], s76, v4\n\t" "s_lshl_b32 s76, s27, 2\n\t" \
  "v_cmp_gt_u32 s[48:49], s76, v10\n\t" "s_mul_i32 s76, s29, 10\n\t" "s_add_u32 s76, s76, s28\n\t" "s_mul_i32 s76, s76, 6144\n\t" "s_add_u32 s76, s76, 585728\n\t" "s_waitcnt lgkmcnt(0)\n\t" \
  "s_add_u32 s66, s24, s76\n\t" "s_addc_u32 s67, s25, 0\n\t" "s_lshl_b32 s76, s35, 10\n\t" "s_add_u32 s76, s76, 4096\n\t" "s_add_u32 s56, s66, s76\n\t" "s_addc_u32 s57, s67, 0\n\t" \
  "s_sub_u32 s58, s56, s81\n\t" "s_subb_u32 s59, s57, 0\n\t" "s_mul_i32 s76, s29, 61440\n\t" "s_add_u32 s76, s76, 2004992\n\t" "s_mul_i32 s77, s26, 5120\n\t" "s_add_u32 s77, s77, s76\n\t" \
  "s_add_u32 s62, s24, s77\n\t" "s_addc_u32 s63, s25, 0\n\t" "s_add_u32 s64, s62, 1413120\n\t" "s_addc_u32 s65, s63, 0\n\t" "s_mul_i32 s77, s35, 10\n\t" "s_add_u32 s77, s77, s28\n\t" \
  "s_lshl_b32 s77, s77, 10\n\t" "s_add_u32 s77, s77, s76\n\t" "s_add_u32 s60, s24, s77\n\t" "s_addc_u32 s61, s25, 0\n\t" "s_mov_b64 s[76:77], 0xffff\n\t" "s_not_b64 s[54:55], s[76:77]\n\t" \
  "s_cmp_lg_u32 s34, 0\n\t" "s_cselect_b64 s[54:55], s[76:77], s[54:55]\n\t" "s_mov_b32 s39, 400000\n\t" "s_mov_b32 s75, 0\n\t" "s_cmp_lt_u32 s26, 4\n\t" "s_cbranch_scc1 .Ll_%=\n\t" \
  "s_mul_i32 s77, s27, 0x1111\n\t" "s_sub_u32 s80, 0x8765, s77\n\t" "s_sub_u32 s77, 0xdcba, s77\n\t" "s_cmp_eq_u32 s33, 1\n\t" "s_cselect_b32 s80, s74, s80\n\t" "s_cmp_eq_u32 s33, 2\n\t" \
  "s_cselect_b32 s80, s79, s80\n\t" "s_cmp_eq_u32 s33, 3\n\t" "s_cselect_b32 s80, s77, s80\n\t" "v_mul_u32_u24 v1, 768, v10\n\t" "v_bfe_u32 v5, s80, v5, 4\n\t" "v_lshlrev_b32 v2, 5, v9\n\t" \
  "v_lshlrev_b32 v5, 3, v5\n\t" "v_and_b32 v2, 0x180, v2\n\t" "v_xor_b32 v5, v5, v6\n\t" "s_cmp_ge_u32 s26, 8\n\t" "v_add3_u32 v1, v1, v5, v2\n\t" "s_cbranch_scc1 .Lg_%=\n\t" "v_mov_b32 v7, 0\n\t" \
  "s_setprio 3\n\t" "global_load_dwordx2 v[40:41], v7, s[24:25] sc1\n\t" "s_cmp_lg_u32 s33, 2\n\t" "s_cbranch_scc1 .Lre0_%=\n\t" "v_lshlrev_b32 v5, 2, v8\n\t" \
  "global_load_dwordx4 v[64:67], v32, s[14:15]\n\t" "global_load_dword v68, v5, s[14:15] offset:1024\n\t" ".Lre0_%=:\n\t" "v_lshl_add_u32 v3, v8, 2, v37\n\t" "s_mov_b32 exec_hi, 0\n\t" \
  "ds_write_b32 v3, v7\n\t" "s_mov_b32 exec_hi, -1\n\t" "s_mul_i32 s76, s29, 15360\n\t" "s_mul_i32 s77, s33, 384\n\t" "s_add_u32 s76, s76, s77\n\t" "s_add_u32 s76, s76, 8192\n\t" \
  "s_add_u32 s88, s24, s76\n\t" "s_addc_u32 s89, s25, 0\n\t" "s_add_u32 s86, s88, 3072\n\t" "s_addc_u32 s87, s89, 0\n\t" "s_mul_i32 s76, s28, 1536\n\t" "s_add_u32 s88, s88, s76\n\t" \
  "s_addc_u32 s89, s89, 0\n\t" "s_lshl2_add_u32 s76, s28, s33\n\t" "s_mul_i32 s76, s76, 5120\n\t" "s_add_u32 s76, s76, 361472\n\t" "s_add_u32 s90, s24, s76\n\t" "s_addc_u32 s91, s25, 0\n\t" \
  "s_lshl_b32 s76, s31, 9\n\t" "s_add_u32 s92, s90, s76\n\t" "s_addc_u32 s93, s91, 0\n\t" "v_mov_b32 v116, -1\n\t" "v_mov_b32 v117, -1\n\t" "v_mov_b32 v118, -1\n\t" "v_mov_b32 v119, -1\n\t" \
  "s_mul_i32 s76, s33, 1536\n\t" "s_add_u32 s76, s76, 4096\n\t" "s_add_u32 s94, s66, s76\n\t" "s_addc_u32 s95, s67, 0\n\t" "s_mul_i32 s76, s33, 3072\n\t" "v_add_u32 v2, s76, v32\n\t" \
  "s_mov_b32 s77, 0x3f803f80\n\t" "v_mov_b32 v96, s77\n\t" "v_mov_b32 v97, s77\n\t" "v_mov_b32 v98, s77\n\t" "v_mov_b32 v99, s77\n\t" "v_and_b32 v3, 7, v8\n\t" "v_lshrrev_b32 v12, 3, v8\n\t" \
  "v_lshl_add_u32 v12, v3, 3, v12\n\t" "v_lshlrev_b32 v12, 4, v12\n\t" "v_cmp_eq_u32 s[70:71], 1, v3\n\t" "v_cmp_eq_u32 s[96:97], 2, v3\n\t" "v_cmp_gt_u32 s[98:99], 3, v3\n\t" \
  "v_and_b32 v5, 31, v8\n\t" "v_lshrrev_b32 v13, 5, v8\n\t" "v_min_u32 v7, 23, v5\n\t" "v_mul_u32_u24 v3, 7680, v13\n\t" "v_lshl_add_u32 v3, v7, 4, v3\n\t" "v_mul_u32_u24 v6, 2560, v13\n\t" \
  "v_lshl_add_u32 v6, v5, 4, v6\n\t" "v_mul_u32_u24 v13, 384, v13\n\t" "v_lshl_add_u32 v13, v7, 2, v13\n\t" "s_mul_i32 s76, s33, 96\n\t" "s_add_u32 s76, s76, 155648\n\t" "v_add_u32 v13, s76, v13\n\t" \
  "v_min_u32 v4, 22, v8\n\t" "s_movk_i32 s76, 4096\n\t" "v_lshl_add_u32 v4, v4, 7, s76\n\t" "v_and_b32 v5, 16, v8\n\t" "v_lshl_add_u32 v5, v5, 1, v9\n\t" "s_lshl_b32 s76, s33, 8\n\t" \
  "s_add_u32 s76, s76, 156672\n\t" "v_lshl_add_u32 v5, v5, 2, s76\n\t" "s_mov_b32 s76, 0x3acccccd\n\t" "s_bitcmp0_b32 0x3f6c0, s29\n\t" "s_waitcnt lgkmcnt(0)\n\t" "s_barrier\n\t" \
  "ds_read_b128 v[44:47], v2 offset:24576\n\t" "ds_read_b128 v[48:51], v2 offset:25600\n\t" "ds_read_b128 v[52:55], v2 offset:26624\n\t" "s_cbranch_scc1 .Lc0_%=\n\t" \
  "ds_read_b64_tr_b16 v[72:73], v1 offset:0\n\t" "ds_read_b64_tr_b16 v[74:75], v1 offset:512\n\t" "ds_read_b64_tr_b16 v[76:77], v1 offset:1024\n\t" "ds_read_b64_tr_b16 v[78:79], v1 offset:1536\n\t" \
  "ds_read_b64_tr_b16 v[80:81], v1 offset:2048\n\t" "ds_read_b64_tr_b16 v[82:83], v1 offset:2560\n\t" "ds_read_b64_tr_b16 v[84:85], v1 offset:12288\n\t" \
  "ds_read_b64_tr_b16 v[86:87], v1 offset:12800\n\t" "ds_read_b64_tr_b16 v[88:89], v1 offset:13312\n\t" "ds_read_b64_tr_b16 v[90:91], v1 offset:13824\n\t" \
  "ds_read_b64_tr_b16 v[92:93], v1 offset:14336\n\t" "ds_read_b64_tr_b16 v[94:95], v1 offset:14848\n\t" ".Lc0_%=:\n\t" "s_waitcnt lgkmcnt(0)\n\t" "s_cbranch_scc1 .Lc1_%=\n\t" \
  "v_mfma_f32_16x16x32_bf16 v[100:103], v[96:99], v[72:75], 0\n\t" "v_mfma_f32_16x16x32_bf16 v[104:107], v[96:99], v[76:79], 0\n\t" "v_mfma_f32_16x16x32_bf16 v[108:111], v[96:99], v[80:83], 0\n\t" \
  "v_mfma_f32_16x16x32_bf16 v[100:103], v[96:99], v[84:87], v[100:103]\n\t" "v_mfma_f32_16x16x32_bf16 v[104:107], v[96:99], v[88:91], v[104:107]\n\t" \
  "v_mfma_f32_16x16x32_bf16 v[108:111], v[96:99], v[92:95], v[108:111]\n\t" ".Lc1_%=:\n\t" "s_barrier\n\t" "global_store_dwordx4 v32, v[116:119], s[94:95] sc1\n\t" "s_mov_b32 exec_hi, 0\n\t" \
  "global_store_dwordx4 v32, v[116:119], s[94:95] offset:1024 sc1\n\t" "s_mov_b32 exec_hi, -1\n\t" "v_dot2_f32_bf16 v56, v44, s77, 0\n\t" "v_dot2_f32_bf16 v57, v44, v44, 0\n\t" \
  "v_dot2_f32_bf16 v58, v48, s77, 0\n\t" "v_dot2_f32_bf16 v59, v48, v48, 0\n\t" "v_dot2_f32_bf16 v60, v52, s77, 0\n\t" "v_dot2_f32_bf16 v61, v52, v52, 0\n\t" "v_dot2_f32_bf16 v56, v45, s77, v56\n\t" \
  "v_dot2_f32_bf16 v57, v45, v45, v57\n\t" "v_dot2_f32_bf16 v58, v49, s77, v58\n\t" "v_dot2_f32_bf16 v59, v49, v49, v59\n\t" "v_dot2_f32_bf16 v60, v53, s77, v60\n\t" \
  "v_dot2_f32_bf16 v61, v53, v53, v61\n\t" "s_barrier\n\t" "v_dot2_f32_bf16 v56, v46, s77, v56\n\t" "v_dot2_f32_bf16 v57, v46, v46, v57\n\t" "v_dot2_f32_bf16 v58, v50, s77, v58\n\t" \
  "v_dot2_f32_bf16 v59, v50, v50, v59\n\t" "v_dot2_f32_bf16 v60, v54, s77, v60\n\t" "v_dot2_f32_bf16 v61, v54, v54, v61\n\t" "v_dot2_f32_bf16 v56, v47, s77, v56\n\t" \
  "v_dot2_f32_bf16 v57, v47, v47, v57\n\t" "v_dot2_f32_bf16 v58, v51, s77, v58\n\t" "v_dot2_f32_bf16 v59, v51, v51, v59\n\t" "v_dot2_f32_bf16 v60, v55, s77, v60\n\t" \
  "v_dot2_f32_bf16 v61, v55, v55, v61\n\t" "s_barrier\n\t" "s_nop 1\n\t" "v_add_f32_dpp v56, v56, v56 quad_perm:[1,0,3,2] row_mask:0xf bank_mask:0xf\n\t" \
  "v_add_f32_dpp v57, v57, v57 quad_perm:[1,0,3,2] row_mask:0xf bank_mask:0xf\n\t" "v_add_f32_dpp v58, v58, v58 quad_perm:[1,0,3,2] row_mask:0xf bank_mask:0xf\n\t" \
  "v_add_f32_dpp v59, v59, v59 quad_perm:[1,0,3,2] row_mask:0xf bank_mask:0xf\n\t" "v_add_f32_dpp v60, v60, v60 quad_perm:[1,0,3,2] row_mask:0xf bank_mask:0xf\n\t" \
  "v_add_f32_dpp v61, v61, v61 quad_perm:[1,0,3,2] row_mask:0xf bank_mask:0xf\n\t" "v_add_f32_dpp v56, v56, v56 quad_perm:[2,3,0,1] row_mask:0xf bank_mask:0xf\n\t" \
  "v_add_f32_dpp v57, v57, v57 quad_perm:[2,3,0,1] row_mask:0xf bank_mask:0xf\n\t" "v_add_f32_dpp v58, v58, v58 quad_perm:[2,3,0,1] row_mask:0xf bank_mask:0xf\n\t" \
  "v_add_f32_dpp v59, v59, v59 quad_perm:[2,3,0,1] row_mask:0xf bank_mask:0xf\n\t" "v_add_f32_dpp v60, v60, v60 quad_perm:[2,3,0,1] row_mask:0xf bank_mask:0xf\n\t" \
  "v_add_f32_dpp v61, v61, v61 quad_perm:[2,3,0,1] row_mask:0xf bank_mask:0xf\n\t" "v_add_f32_dpp v56, v56, v56 row_half_mirror row_mask:0xf bank_mask:0xf\n\t" \
  "v_add_f32_dpp v57, v57, v57 row_half_mirror row_mask:0xf bank_mask:0xf\n\t" "v_add_f32_dpp v58, v58, v58 row_half_mirror row_mask:0xf bank_mask:0xf\n\t" \
  "v_add_f32_dpp v59, v59, v59 row_half_mirror row_mask:0xf bank_mask:0xf\n\t" "v_add_f32_dpp v60, v60, v60 row_half_mirror row_mask:0xf bank_mask:0xf\n\t" \
  "v_add_f32_dpp v61, v61, v61 row_half_mirror row_mask:0xf bank_mask:0xf\n\t" "s_barrier\n\t" "s_mov_b64 vcc, 0xffff\n\t" "v_cndmask_b32 v56, v56, v58, s[70:71]\n\t" \
  "v_cndmask_b32 v57, v57, v59, s[70:71]\n\t" "v_cndmask_b32 v56, v56, v60, s[96:97]\n\t" "v_cndmask_b32 v57, v57, v61, s[96:97]\n\t" "v_mov_b32 v61, v104\n\t" "v_cndmask_b32 v60, v108, v100, vcc\n\t" \
  "s_waitcnt vmcnt(0)\n\t" "v_add_co_u32 v58, vcc, 1, v40\n\t" "s_nop 1\n\t" "v_addc_co_u32 v59, vcc, 0, v41, vcc\n\t" "s_mov_b64 exec, s[98:99]\n\t" \
  "global_store_dwordx4 v12, v[56:59], s[88:89] sc1\n\t" "s_mov_b64 exec, -1\n\t" "s_cbranch_scc1 .Lc2_%=\n\t" "s_mov_b32 exec_hi, 0\n\t" "global_store_dwordx4 v32, v[58:61], s[92:93] sc1\n\t" \
  "s_mov_b32 exec_hi, -1\n\t" ".Lc2_%=:\n\t" "s_barrier\n\t" "s_cmp_lg_u32 s33, 1\n\t" "s_cbranch_scc1 .Ltp_%=\n\t" "s_memrealtime s[96:97]\n\t" "s_mul_i32 s98, s29, 1280\n\t" \
  "s_add_u32 s98, s98, 5368320\n\t" "v_min_u32 v4, 9, v9\n\t" "v_lshl_add_u32 v4, v4, 7, s98\n\t" "s_lshl_b32 s99, s28, 7\n\t" "s_add_u32 s98, s98, s99\n\t" "v_lshl_add_u32 v7, v9, 3, s98\n\t" \
  "v_mov_b32 v57, v58\n\t" "s_waitcnt lgkmcnt(0)\n\t" "v_mov_b32 v56, s96\n\t" "s_mov_b64 exec, 0xffff\n\t" "global_store_dwordx2 v7, v[56:57], s[24:25] sc1\n\t" "s_mov_b64 exec, -1\n\t" \
  ".Ltp_%=:\n\t" "s_barrier\n\t" "s_barrier\n\t" "global_load_dwordx4 v[72:75], v3, s[86:87] offset:-3072 sc1\n\t" "global_load_dwordx4 v[76:79], v3, s[86:87] offset:-1536 sc1\n\t" \
  "global_load_dwordx4 v[80:83], v3, s[86:87] offset:0 sc1\n\t" "global_load_dwordx4 v[84:87], v3, s[86:87] offset:1536 sc1\n\t" "global_load_dwordx4 v[88:91], v3, s[86:87] offset:3072 sc1\n\t" \
  "global_load_dwordx4 v[92:95], v6, s[90:91] offset:0 sc1\n\t" "global_load_dwordx4 v[96:99], v6, s[90:91] offset:512 sc1\n\t" "global_load_dwordx4 v[100:103], v6, s[90:91] offset:1024 sc1\n\t" \
  "global_load_dwordx4 v[104:107], v6, s[90:91] offset:1536 sc1\n\t" "global_load_dwordx4 v[108:111], v6, s[90:91] offset:2048 sc1\n\t" "s_cmp_lg_u32 s33, 1\n\t" "s_cbranch_scc1 .Lfl0_%=\n\t" \
  "global_load_dwordx2 v[112:113], v4, s[24:25] sc1\n\t" ".Lfl0_%=:\n\t" "s_barrier\n\t" "s_mov_b32 s70, 0\n\t" ".Lrg_%=:\n\t" "s_waitcnt vmcnt(0)\n\t" \
  "v_pk_add_f32 v[120:121], v[72:73], v[76:77]\n\t" "v_pk_add_f32 v[126:127], v[94:95], v[98:99]\n\t" "v_pk_add_f32 v[122:123], v[80:81], v[84:85]\n\t" \
  "v_pk_add_f32 v[128:129], v[102:103], v[106:107]\n\t" "v_pk_add_f32 v[120:121], v[120:121], v[88:89]\n\t" "v_pk_add_f32 v[126:127], v[126:127], v[110:111]\n\t" \
  "v_pk_add_f32 v[124:125], v[120:121], v[122:123]\n\t" "v_pk_add_f32 v[130:131], v[126:127], v[128:129]\n\t" "v_pk_add_f32 v[120:121], v[120:121], v[122:123]\n\t" \
  "v_pk_add_f32 v[126:127], v[126:127], v[128:129]\n\t" "s_nop 0\n\t" "v_permlane32_swap_b32 v120, v124\n\t" "v_cmp_ne_u64 vcc, v[74:75], v[58:59]\n\t" "v_permlane32_swap_b32 v121, v125\n\t" \
  "v_permlane32_swap_b32 v126, v130\n\t" "v_pk_add_f32 v[120:121], v[120:121], v[124:125]\n\t" "v_permlane32_swap_b32 v127, v131\n\t" "v_mul_f32 v120, s76, v120\n\t" \
  "v_pk_add_f32 v[126:127], v[126:127], v[130:131]\n\t" "v_mul_f32 v122, v120, v120\n\t" "v_cmp_ne_u64 s[80:81], v[78:79], v[58:59]\n\t" "s_or_b64 vcc, vcc, s[80:81]\n\t" \
  "v_fma_f32 v121, v121, s76, -v122\n\t" "v_cmp_ne_u64 s[78:79], v[82:83], v[58:59]\n\t" "s_or_b64 vcc, vcc, s[78:79]\n\t" "v_max_f32 v121, 0, v121\n\t" "v_cmp_ne_u64 s[80:81], v[86:87], v[58:59]\n\t" \
  "s_or_b64 vcc, vcc, s[80:81]\n\t" "s_cmp_lg_u32 s70, 0\n\t" "s_cbranch_scc1 .Lb8_%=\n\t" "s_barrier\n\t" ".Lb8_%=:\n\t" "s_mov_b32 s70, 1\n\t" "s_cmp_lg_u32 s33, 3\n\t" "s_cbranch_scc1 .Lfl8_%=\n\t" \
  "global_load_dwordx2 v[112:113], v4, s[24:25] sc1\n\t" ".Lfl8_%=:\n\t" "v_cvt_pk_bf16_f32 v120, v121, v120\n\t" "v_cmp_ne_u64 s[78:79], v[90:91], v[58:59]\n\t" "s_or_b64 vcc, vcc, s[78:79]\n\t" \
  "v_lshlrev_b32 v121, 16, v120\n\t" "v_cmp_ne_u64 s[80:81], v[92:93], v[58:59]\n\t" "s_or_b64 vcc, vcc, s[80:81]\n\t" "v_add_f32 v121, 0x3727c5ac, v121\n\t" \
  "v_cmp_ne_u64 s[78:79], v[96:97], v[58:59]\n\t" "s_or_b64 vcc, vcc, s[78:79]\n\t" "v_cvt_pk_bf16_f32 v121, v121, v121\n\t" "v_cmp_ne_u64 s[80:81], v[100:101], v[58:59]\n\t" \
  "s_or_b64 vcc, vcc, s[80:81]\n\t" "v_lshlrev_b32 v121, 16, v121\n\t" "v_cmp_ne_u64 s[78:79], v[104:105], v[58:59]\n\t" "s_or_b64 vcc, vcc, s[78:79]\n\t" "v_sqrt_f32 v121, v121\n\t" \
  "v_cmp_ne_u64 s[80:81], v[108:109], v[58:59]\n\t" "s_or_b64 vcc, vcc, s[80:81]\n\t" "v_cvt_pk_bf16_f32 v121, v121, v121\n\t" "v_and_b32 v120, 0xffff0000, v120\n\t" "v_lshlrev_b32 v121, 16, v121\n\t" \
  "s_cbranch_vccnz .Lsg_%=\n\t" ".Lrh_%=:\n\t" "s_mov_b32 exec_lo, 0\n\t" "v_rcp_f32 v120, v121\n\t" "s_mov_b32 exec_lo, -1\n\t" "ds_write2_b32 v5, v126, v127 offset1:16\n\t" \
  "ds_write_b32 v13, v120\n\t" "s_mov_b64 exec, 1\n\t" "ds_add_u32 v37, v38 offset:12\n\t" "s_mov_b64 exec, -1\n\t" "s_setprio 0\n\t" "s_cmp_lg_u32 s33, 3\n\t" "s_cbranch_scc1 .Lnfl_%=\n\t" \
  "s_waitcnt vmcnt(0)\n\t" "v_cmp_ne_u64 vcc, v[112:113], v[58:59]\n\t" "s_cbranch_vccz .Lsfm_%=\n\t" ".Lsfl_%=:\n\t" "global_load_dwordx2 v[112:113], v4, s[24:25] sc1\n\t" "s_sub_u32 s39, s39, 1\n\t" \
  "s_cmp_lt_i32 s39, 0\n\t" "s_waitcnt vmcnt(0)\n\t" "s_cbranch_scc1 .Lsfm_%=\n\t" "v_cmp_ne_u64 vcc, v[112:113], v[58:59]\n\t" "s_cbranch_vccnz .Lsfl_%=\n\t" ".Lsfm_%=:\n\t" "s_mov_b64 exec, 1\n\t" \
  "ds_add_u32 v37, v38 offset:4\n\t" "s_mov_b64 exec, -1\n\t" ".Lnfl_%=:\n\t" "s_cmp_lg_u32 s33, 1\n\t" "s_cbranch_scc1 .Lnlg_%=\n\t" "v_subrev_u32 v112, s96, v112\n\t" \
  "v_cmp_eq_u32 vcc, v113, v58\n\t" "s_lshl_b32 s98, 7, s27\n\t" "s_lshr_b32 s98, s98, 1\n\t" "s_and_b32 s98, s98, 31\n\t" "v_cndmask_b32 v112, 60, v112, vcc\n\t" "s_sub_u32 s99, s28, s27\n\t" \
  "s_lshl_b32 s98, s98, s99\n\t" "s_or_b32 s98, s98, 0xffff0000\n\t" "s_mov_b32 s99, 0\n\t" "v_med3_i32 v112, v112, 0, 64\n\t" "v_cndmask_b32 v112, 0, v112, s[98:99]\n\t" "s_nop 1\n\t" \
  "v_max_u32_dpp v112, v112, v112 quad_perm:[1,0,3,2] row_mask:0xf bank_mask:0xf\n\t" "s_nop 1\n\t" "v_max_u32_dpp v112, v112, v112 quad_perm:[2,3,0,1] row_mask:0xf bank_mask:0xf\n\t" "s_nop 1\n\t" \
  "v_max_u32_dpp v112, v112, v112 row_half_mirror row_mask:0xf bank_mask:0xf\n\t" "s_nop 1\n\t" "v_max_u32_dpp v112, v112, v112 row_mirror row_mask:0xf bank_mask:0xf\n\t" "v_add_u32 v7, v10, v37\n\t" \
  "ds_write_b32 v7, v112 offset:80\n\t" ".Lnlg_%=:\n\t" "s_cmp_lg_u32 s33, 2\n\t" "s_cbranch_scc1 .Latt_%=\n\t" "v_dot2_f32_bf16 v1, v68, s77, 0\n\t" "v_dot2_f32_bf16 v1, v64, s77, v1\n\t" \
  "v_dot2_f32_bf16 v1, v65, s77, v1\n\t" "v_dot2_f32_bf16 v1, v66, s77, v1\n\t" "v_dot2_f32_bf16 v1, v67, s77, v1\n\t" "s_nop 2\n\t" \
  "v_add_f32_dpp v1, v1, v1 quad_perm:[1,0,3,2] row_mask:0xf bank_mask:0xf\n\t" "s_nop 1\n\t" "v_add_f32_dpp v1, v1, v1 quad_perm:[2,3,0,1] row_mask:0xf bank_mask:0xf\n\t" "s_nop 1\n\t" \
  "v_add_f32_dpp v1, v1, v1 row_half_mirror row_mask:0xf bank_mask:0xf\n\t" "s_nop 1\n\t" "v_add_f32_dpp v1, v1, v1 row_mirror row_mask:0xf bank_mask:0xf\n\t" "v_mov_b32 v2, v1\n\t" "s_nop 1\n\t" \
  "v_permlane32_swap_b32 v1, v2\n\t" "v_add_f32 v1, v1, v2\n\t" "v_mov_b32 v2, v1\n\t" "s_nop 1\n\t" "v_permlane16_swap_b32 v1, v2\n\t" "v_add_f32 v1, v1, v2\n\t" "ds_write_b32 v37, v1 offset:40\n\t" \
  "s_branch .Latt_%=\n\t" ".Ll_%=:\n\t" "s_movk_i32 s38, 1\n\t" "s_add_u32 s31, s31, 1\n\t" "s_cmp_eq_u32 s31, 10\n\t" "s_cselect_b32 s31, 0, s31\n\t" "s_mul_i32 s76, s31, 245760\n\t" \
  "s_lshl_b32 s77, s31, 7\n\t" "s_add_u32 s78, s10, s76\n\t" "s_addc_u32 s79, s11, 0\n\t" "s_add_u32 s80, s8, s77\n\t" "s_addc_u32 s81, s9, 0\n\t" "s_and_b32 s76, s38, 3\n\t" \
  "s_mul_i32 s76, s76, 36864\n\t" "s_add_u32 m0, s76, s87\n\t" "s_branch .Ll1_%=\n\t" ".Ll0_%=:\n\t" "s_add_u32 s31, s31, 1\n\t" "s_cmp_eq_u32 s31, 10\n\t" "s_cselect_b32 s31, 0, s31\n\t" \
  "s_mul_i32 s76, s31, 245760\n\t" "s_lshl_b32 s77, s31, 7\n\t" "s_add_u32 s78, s10, s76\n\t" "s_addc_u32 s79, s11, 0\n\t" "s_add_u32 s80, s8, s77\n\t" "s_addc_u32 s81, s9, 0\n\t" \
  "s_and_b32 s76, s38, 3\n\t" "s_mul_i32 s76, s76, 36864\n\t" "s_add_u32 m0, s76, s87\n\t" "s_waitcnt vmcnt(9)\n\t" "s_barrier\n\t" ".Ll1_%=:\n\t" \
  "global_load_lds_dwordx4 v46, s[80:81] offset:-2048\n\t" "global_load_lds_dwordx4 v47, s[80:81] offset:-1024\n\t" "global_load_lds_dwordx4 v48, s[80:81] offset:0\n\t" "s_add_u32 m0, s76, s86\n\t" \
  "s_add_u32 s38, s38, 1\n\t" "global_load_lds_dwordx4 v40, s[78:79] offset:-3072\n\t" "global_load_lds_dwordx4 v41, s[78:79] offset:-2048\n\t" "global_load_lds_dwordx4 v42, s[78:79] offset:-1024\n\t" \
  "global_load_lds_dwordx4 v43, s[78:79] offset:0\n\t" "global_load_lds_dwordx4 v44, s[78:79] offset:1024\n\t" "global_load_lds_dwordx4 v45, s[78:79] offset:2048\n\t" "s_cmp_lt_u32 s38, 9\n\t" \
  "s_cbranch_scc1 .Ll0_%=\n\t" "s_add_u32 s31, s31, 1\n\t" "s_cmp_eq_u32 s31, 10\n\t" "s_cselect_b32 s31, 0, s31\n\t" "s_mul_i32 s76, s31, 245760\n\t" "s_lshl_b32 s77, s31, 7\n\t" \
  "s_add_u32 s78, s10, s76\n\t" "s_addc_u32 s79, s11, 0\n\t" "s_add_u32 s80, s8, s77\n\t" "s_addc_u32 s81, s9, 0\n\t" "s_and_b32 s76, s38, 3\n\t" "s_mul_i32 s76, s76, 36864\n\t" \
  "s_add_u32 m0, s76, s87\n\t" "v_lshrrev_b32 v1, 3, v8\n\t" "v_lshrrev_b32 v4, 5, v8\n\t" "v_and_b32 v2, 6, v1\n\t" "v_and_b32 v1, 3, v1\n\t" "v_xor_b32 v2, v2, v8\n\t" \
  "v_mul_u32_u24 v1, 1280, v1\n\t" "v_and_b32 v2, 7, v2\n\t" "s_lshl_b32 s92, s28, 7\n\t" "v_lshl_add_u32 v1, v2, 4, v1\n\t" "s_and_b32 s93, s33, 1\n\t" "s_and_b32 s94, s33, 2\n\t" \
  "v_add_u32 v1, s92, v1\n\t" "s_mul_i32 s92, s93, 5\n\t" "s_add_u32 s92, s92, s94\n\t" "s_add_u32 s92, s92, 5\n\t" "v_add_u32 v2, s92, v4\n\t" "s_lshl2_add_u32 s92, s93, s94\n\t" \
  "s_movk_i32 s93, 5120\n\t" "v_add_lshl_u32 v50, v4, s92, 2\n\t" "v_mad_u32_u24 v49, v2, s93, v1\n\t" "s_lshl_b32 s92, s92, 9\n\t" "s_add_u32 s92, s92, 73728\n\t" "s_waitcnt vmcnt(9)\n\t" \
  "s_barrier\n\t" "global_load_lds_dwordx4 v46, s[80:81] offset:-2048\n\t" "global_load_lds_dwordx4 v47, s[80:81] offset:-1024\n\t" "global_load_lds_dwordx4 v48, s[80:81] offset:0\n\t" \
  "s_add_u32 m0, s76, s86\n\t" "s_nop 0\n\t" "global_load_lds_dwordx4 v40, s[78:79] offset:-3072\n\t" "global_load_lds_dwordx4 v41, s[78:79] offset:-2048\n\t" \
  "global_load_lds_dwordx4 v42, s[78:79] offset:-1024\n\t" "global_load_lds_dwordx4 v43, s[78:79] offset:0\n\t" "global_load_lds_dwordx4 v44, s[78:79] offset:1024\n\t" \
  "global_load_lds_dwordx4 v45, s[78:79] offset:2048\n\t" "s_waitcnt vmcnt(9)\n\t" "s_barrier\n\t" "s_sleep 2\n\t" "s_mov_b64 s[78:79], s[12:13]\n\t" "s_add_u32 m0, s92, 0\n\t" "s_nop 0\n\t" \
  "global_load_lds_dwordx4 v49, s[78:79]\n\t" "s_add_u32 s78, s78, 76800\n\t" "s_addc_u32 s79, s79, 0\n\t" "s_add_u32 m0, s92, 4096\n\t" "s_nop 0\n\t" "global_load_lds_dwordx4 v49, s[78:79]\n\t" \
  "s_add_u32 s78, s78, 76800\n\t" "s_addc_u32 s79, s79, 0\n\t" "s_add_u32 m0, s92, 8192\n\t" "s_nop 0\n\t" "global_load_lds_dwordx4 v49, s[78:79]\n\t" "s_add_u32 s78, s78, 76800\n\t" \
  "s_addc_u32 s79, s79, 0\n\t" "s_add_u32 m0, s92, 12288\n\t" "s_nop 0\n\t" "global_load_lds_dwordx4 v49, s[78:79]\n\t" "s_add_u32 s78, s78, 76800\n\t" "s_addc_u32 s79, s79, 0\n\t" \
  "s_add_u32 m0, s92, 16384\n\t" "s_waitcnt vmcnt(4)\n\t" "s_barrier\n\t" "global_load_lds_dwordx4 v49, s[78:79]\n\t" "s_add_u32 s78, s78, 102400\n\t" "s_addc_u32 s79, s79, 0\n\t" \
  "s_add_u32 m0, s92, 40960\n\t" "s_nop 0\n\t" "global_load_lds_dwordx4 v49, s[78:79]\n\t" "s_add_u32 s78, s78, 76800\n\t" "s_addc_u32 s79, s79, 0\n\t" "s_add_u32 m0, s92, 45056\n\t" "s_nop 0\n\t" \
  "global_load_lds_dwordx4 v49, s[78:79]\n\t" "s_add_u32 s78, s78, 76800\n\t" "s_addc_u32 s79, s79, 0\n\t" "s_add_u32 m0, s92, 49152\n\t" "s_nop 0\n\t" "global_load_lds_dwordx4 v49, s[78:79]\n\t" \
  "s_add_u32 s78, s78, 76800\n\t" "s_addc_u32 s79, s79, 0\n\t" "s_add_u32 m0, s92, 53248\n\t" "s_nop 0\n\t" "global_load_lds_dwordx4 v49, s[78:79]\n\t" "s_add_u32 s78, s78, 76800\n\t" \
  "s_addc_u32 s79, s79, 0\n\t" "s_add_u32 m0, s92, 57344\n\t" "s_nop 0\n\t" "global_load_lds_dwordx4 v49, s[78:79]\n\t" "s_setprio 0\n\t" "s_mov_b32 s94, 0xf41e3209\n\t" \
  "v_bfe_u32 v2, s94, v50, 4\n\t" "v_mad_u32_u24 v3, v2, s93, v1\n\t" "s_mov_b32 s94, 0xfe0d3218\n\t" "v_bfe_u32 v2, s94, v50, 4\n\t" "v_mad_u32_u24 v4, v2, s93, v1\n\t" \
  "s_mov_b32 s94, 0xfedc2107\n\t" "v_bfe_u32 v2, s94, v50, 4\n\t" "v_mad_u32_u24 v5, v2, s93, v1\n\t" "s_mov_b32 s94, 0xedcbf106\n\t" "v_bfe_u32 v2, s94, v50, 4\n\t" \
  "v_mad_u32_u24 v6, v2, s93, v1\n\t" "s_mov_b32 s94, 0xfdcabe05\n\t" "v_bfe_u32 v2, s94, v50, 4\n\t" "v_mad_u32_u24 v7, v2, s93, v1\n\t" "s_waitcnt vmcnt(0)\n\t" "s_mov_b64 exec, 1\n\t" \
  "ds_add_u32 v37, v38 offset:4\n\t" "s_mov_b64 exec, -1\n\t" "s_branch .Latt_%=\n\t" ".Lsg_%=:\n\t" "s_sub_u32 s39, s39, 1\n\t" "s_cmp_lt_i32 s39, 0\n\t" "s_cbranch_scc1 .Lrh_%=\n\t" \
  "global_load_dwordx4 v[72:75], v3, s[86:87] offset:-3072 sc1\n\t" "global_load_dwordx4 v[76:79], v3, s[86:87] offset:-1536 sc1\n\t" "global_load_dwordx4 v[80:83], v3, s[86:87] offset:0 sc1\n\t" \
  "global_load_dwordx4 v[84:87], v3, s[86:87] offset:1536 sc1\n\t" "global_load_dwordx4 v[88:91], v3, s[86:87] offset:3072 sc1\n\t" "global_load_dwordx4 v[92:95], v6, s[90:91] offset:0 sc1\n\t" \
  "global_load_dwordx4 v[96:99], v6, s[90:91] offset:512 sc1\n\t" "global_load_dwordx4 v[100:103], v6, s[90:91] offset:1024 sc1\n\t" "global_load_dwordx4 v[104:107], v6, s[90:91] offset:1536 sc1\n\t" \
  "global_load_dwordx4 v[108:111], v6, s[90:91] offset:2048 sc1\n\t" "s_branch .Lrg_%=\n\t" ".Lts_%=:\n\t" "ds_read_b32 v39, v37 offset:12\n\t" "ds_read_b128 v[160:163], v12 offset:1024\n\t" \
  "ds_read_b128 v[164:167], v12 offset:1088\n\t" "ds_read2_b32 v[136:137], v4 offset0:0 offset1:96\n\t" "ds_read2_b32 v[138:139], v4 offset0:16 offset1:112\n\t" \
  "ds_read2_b32 v[140:141], v4 offset0:32 offset1:128\n\t" "ds_read2_b32 v[142:143], v4 offset0:48 offset1:144\n\t" "ds_read2_b32 v[144:145], v4 offset0:64 offset1:160\n\t" \
  "ds_read2_b32 v[146:147], v4 offset0:80 offset1:176\n\t" "ds_read_b32 v14, v13 offset:1152\n\t" "s_sub_u32 s39, s39, 1\n\t" "s_cmp_lt_i32 s39, 0\n\t" "s_waitcnt lgkmcnt(0)\n\t" \
  "v_readfirstlane_b32 s38, v39\n\t" "s_cbranch_scc1 .Ltr_%=\n\t" "s_cmp_ge_u32 s38, 4\n\t" "s_cbranch_scc0 .Lts_%=\n\t" "s_branch .Ltr_%=\n\t" ".Lsq_%=:\n\t" "ds_read_b32 v39, v37 offset:0\n\t" \
  "s_sub_u32 s39, s39, 1\n\t" "s_cmp_lt_i32 s39, 0\n\t" "s_waitcnt lgkmcnt(0)\n\t" "v_readfirstlane_b32 s38, v39\n\t" "s_cbranch_scc1 .Lrq_%=\n\t" "s_cmp_ge_u32 s38, 4\n\t" \
  "s_cbranch_scc0 .Lsq_%=\n\t" "s_branch .Lq_%=\n\t" ".Lsqm_%=:\n\t" "ds_read_b32 v39, v37 offset:8\n\t" "s_sub_u32 s39, s39, 1\n\t" "s_cmp_lt_i32 s39, 0\n\t" "s_waitcnt lgkmcnt(0)\n\t" \
  "v_readfirstlane_b32 s38, v39\n\t" "s_cbranch_scc1 .Lrqm_%=\n\t" "s_cmp_ge_u32 s38, 4\n\t" "s_cbranch_scc0 .Lsqm_%=\n\t" "s_branch .Lrqm_%=\n\t" ".Lsq0_%=:\n\t" "ds_read_b32 v39, v37 offset:0\n\t" \
  "s_sub_u32 s39, s39, 1\n\t" "s_cmp_lt_i32 s39, 0\n\t" "s_waitcnt lgkmcnt(0)\n\t" "v_readfirstlane_b32 s38, v39\n\t" "s_cbranch_scc1 .Lrq0_%=\n\t" "s_cmp_ge_u32 s38, 4\n\t" \
  "s_cbranch_scc0 .Lsq0_%=\n\t" "s_branch .Lq0_%=\n\t" ".Lsv0_%=:\n\t" "ds_read_b32 v39, v37 offset:4\n\t" "s_sub_u32 s39, s39, 1\n\t" "s_cmp_lt_i32 s39, 0\n\t" "s_waitcnt lgkmcnt(0)\n\t" \
  "v_readfirstlane_b32 s38, v39\n\t" "s_cbranch_scc1 .Lrv0_%=\n\t" "s_cmp_ge_u32 s38, 9\n\t" "s_cbranch_scc0 .Lsv0_%=\n\t" "s_branch .Lv0_%=\n\t" ".Lsr_%=:\n\t" \
  "global_load_dwordx4 v[148:151], v32, s[58:59] sc1\n\t" "global_load_dwordx4 v[144:147], v32, s[56:57] sc1\n\t" "s_sub_u32 s39, s39, 1\n\t" "s_cmp_lt_i32 s39, 0\n\t" "s_waitcnt vmcnt(0)\n\t" \
  "s_cbranch_scc1 .Lrr_%=\n\t" "v_max_u32 v124, v144, v148\n\t" "v_cmp_eq_u32 vcc, -1, v124\n\t" "s_cbranch_vccnz .Lsr_%=\n\t" "s_branch .Lrr_%=\n\t" ".Lk0_%=:\n\t" "ds_read_b32 v39, v37 offset:8\n\t" \
  "s_waitcnt lgkmcnt(0)\n\t" "v_readfirstlane_b32 s38, v39\n\t" "s_cmp_ge_u32 s38, 4\n\t" "s_cbranch_scc0 .Lsqm_%=\n\t" ".Lrqm_%=:\n\t" "ds_read_b32 v39, v37 offset:80\n\t" \
  "s_memrealtime s[76:77]\n\t" "s_mul_i32 s80, s35, 5\n\t" "s_add_u32 s80, s80, 24\n\t" "s_waitcnt lgkmcnt(0)\n\t" "v_readfirstlane_b32 s38, v39\n\t" "s_add_u32 s80, s80, s38\n\t" ".Lqd_%=:\n\t" \
  "s_memrealtime s[78:79]\n\t" "s_waitcnt lgkmcnt(0)\n\t" "s_sub_u32 s78, s78, s76\n\t" "s_cmp_lt_u32 s78, s80\n\t" "s_cbranch_scc1 .Lqd_%=\n\t" "global_load_dwordx4 v[148:151], v32, s[58:59] sc1\n\t" \
  "global_load_dwordx4 v[144:147], v32, s[56:57] sc1\n\t" ".Lq0_%=:\n\t" "ds_read_b32 v39, v37\n\t" "ds_read_b64 v[40:41], v30 offset:0\n\t" "ds_read_b64 v[42:43], v30 offset:3072\n\t" \
  "ds_read_b64 v[44:45], v30 offset:11776\n\t" "ds_read_b64 v[46:47], v30 offset:14848\n\t" "ds_read_b64 v[48:49], v30 offset:12288\n\t" "ds_read_b64 v[50:51], v30 offset:15360\n\t" \
  "s_waitcnt lgkmcnt(6)\n\t" "v_readfirstlane_b32 s38, v39\n\t" "s_cmp_ge_u32 s38, 4\n\t" "s_cbranch_scc0 .Lsq0_%=\n\t" ".Lrq0_%=:\n\t" "s_waitcnt lgkmcnt(0)\n\t" \
  "v_cndmask_b32 v52, 0, v40, s[44:45]\n\t" "v_cndmask_b32 v53, 0, v41, s[44:45]\n\t" "v_cndmask_b32 v54, 0, v42, s[46:47]\n\t" "v_cndmask_b32 v55, 0, v43, s[46:47]\n\t" \
  "v_cndmask_b32 v40, 0, v40, s[40:41]\n\t" "v_cndmask_b32 v41, 0, v41, s[40:41]\n\t" "v_cndmask_b32 v42, 0, v42, s[42:43]\n\t" "v_cndmask_b32 v43, 0, v43, s[42:43]\n\t" "s_nop 1\n\t" \
  "v_mfma_f32_16x16x32_bf16 v[108:111], v[44:47], v[40:43], v[20:23]\n\t" "v_mfma_f32_16x16x32_bf16 v[112:115], v[48:51], v[40:43], v[24:27]\n\t" \
  "v_mfma_f32_16x16x32_bf16 v[116:119], v[44:47], v[52:55], v[20:23]\n\t" "v_mfma_f32_16x16x32_bf16 v[120:123], v[48:51], v[52:55], v[24:27]\n\t" ".Lv0_%=:\n\t" "ds_read_b32 v39, v37 offset:4\n\t" \
  "ds_read_b64 v[128:129], v30 offset:24064\n\t" "ds_read_b64 v[130:131], v30 offset:24576\n\t" "ds_read_b64 v[132:133], v30 offset:27136\n\t" "ds_read_b64 v[134:135], v30 offset:27648\n\t" \
  "s_waitcnt lgkmcnt(4)\n\t" "v_readfirstlane_b32 s38, v39\n\t" "s_cmp_ge_u32 s38, 9\n\t" "s_cbranch_scc0 .Lsv0_%=\n\t" ".Lrv0_%=:\n\t" "s_waitcnt lgkmcnt(0)\n\t" \
  "v_cndmask_b32 v136, v128, 0, s[50:51]\n\t" "v_cndmask_b32 v128, 0, v128, s[50:51]\n\t" "v_cndmask_b32 v137, v129, 0, s[50:51]\n\t" "v_cndmask_b32 v129, 0, v129, s[50:51]\n\t" \
  "v_cndmask_b32 v138, v130, 0, s[50:51]\n\t" "v_cndmask_b32 v130, 0, v130, s[50:51]\n\t" "v_cndmask_b32 v139, v131, 0, s[50:51]\n\t" "v_cndmask_b32 v131, 0, v131, s[50:51]\n\t" \
  "v_cndmask_b32 v140, v132, 0, s[52:53]\n\t" "v_cndmask_b32 v132, 0, v132, s[52:53]\n\t" "v_cndmask_b32 v141, v133, 0, s[52:53]\n\t" "v_cndmask_b32 v133, 0, v133, s[52:53]\n\t" \
  "v_cndmask_b32 v142, v134, 0, s[52:53]\n\t" "v_cndmask_b32 v134, 0, v134, s[52:53]\n\t" "v_cndmask_b32 v143, v135, 0, s[52:53]\n\t" "v_cndmask_b32 v135, 0, v135, s[52:53]\n\t" \
  "s_cmp_ge_u32 s26, 4\n\t" "s_cbranch_scc1 .Lnw2_%=\n\t" "s_add_u32 m0, s92, 20480\n\t" "s_nop 0\n\t" "global_load_lds_dwordx4 v3, s[12:13]\n\t" "s_add_u32 s12, s12, 81920\n\t" \
  "s_addc_u32 s13, s13, 0\n\t" "s_add_u32 m0, s92, 24576\n\t" "s_nop 0\n\t" "global_load_lds_dwordx4 v4, s[12:13]\n\t" "s_add_u32 s12, s12, 81920\n\t" "s_addc_u32 s13, s13, 0\n\t" \
  "s_add_u32 m0, s92, 28672\n\t" "s_nop 0\n\t" "global_load_lds_dwordx4 v5, s[12:13]\n\t" "s_add_u32 s12, s12, 81920\n\t" "s_addc_u32 s13, s13, 0\n\t" "s_add_u32 m0, s92, 32768\n\t" "s_nop 0\n\t" \
  "global_load_lds_dwordx4 v6, s[12:13]\n\t" "s_add_u32 s12, s12, 81920\n\t" "s_addc_u32 s13, s13, 0\n\t" "s_add_u32 m0, s92, 36864\n\t" "s_nop 0\n\t" "global_load_lds_dwordx4 v7, s[12:13]\n\t" \
  "s_add_u32 s12, s12, 81920\n\t" "s_addc_u32 s13, s13, 0\n\t" "s_add_u32 m0, s92, 61440\n\t" "s_nop 0\n\t" "global_load_lds_dwordx4 v3, s[12:13]\n\t" "s_add_u32 s12, s12, 81920\n\t" \
  "s_addc_u32 s13, s13, 0\n\t" "s_add_u32 m0, s92, 65536\n\t" "s_nop 0\n\t" "global_load_lds_dwordx4 v4, s[12:13]\n\t" "s_add_u32 s12, s12, 81920\n\t" "s_addc_u32 s13, s13, 0\n\t" \
  "s_add_u32 m0, s92, 69632\n\t" "s_nop 0\n\t" "global_load_lds_dwordx4 v5, s[12:13]\n\t" "s_add_u32 s12, s12, 81920\n\t" "s_addc_u32 s13, s13, 0\n\t" "s_add_u32 m0, s92, 73728\n\t" "s_nop 0\n\t" \
  "global_load_lds_dwordx4 v6, s[12:13]\n\t" "s_add_u32 s12, s12, 81920\n\t" "s_addc_u32 s13, s13, 0\n\t" "s_add_u32 m0, s92, 77824\n\t" "s_nop 0\n\t" "global_load_lds_dwordx4 v7, s[12:13]\n\t" \
  ".Lnw2_%=:\n\t" "s_cmp_ge_u32 s26, 4\n\t" "s_waitcnt vmcnt(10)\n\t" "s_cbranch_scc0 .Lnw3_%=\n\t" "s_waitcnt vmcnt(0)\n\t" ".Lnw3_%=:\n\t" "s_setprio 2\n\t" "v_max_u32 v124, v144, v148\n\t" \
  "v_cmp_eq_u32 vcc, -1, v124\n\t" "s_cbranch_vccnz .Lsr_%=\n\t" ".Lrr_%=:\n\t" "v_cndmask_b32 v124, 0, v144, s[48:49]\n\t" "v_cndmask_b32 v125, 0, v145, s[48:49]\n\t" \
  "v_cndmask_b32 v126, v144, 0, s[48:49]\n\t" "v_cndmask_b32 v127, v145, 0, s[48:49]\n\t" "v_mfma_f32_16x16x16_bf16 v[108:111], v[150:151], v[124:125], v[108:111]\n\t" \
  "v_mfma_f32_16x16x16_bf16 v[112:115], v[146:147], v[124:125], v[112:115]\n\t" "v_mfma_f32_16x16x16_bf16 v[116:119], v[150:151], v[126:127], v[116:119]\n\t" \
  "v_mfma_f32_16x16x16_bf16 v[120:123], v[146:147], v[126:127], v[120:123]\n\t" "s_branch .Lk1_%=\n\t" ".Lsv1_%=:\n\t" "ds_read_b32 v39, v37 offset:4\n\t" "s_sub_u32 s39, s39, 1\n\t" \
  "s_cmp_lt_i32 s39, 0\n\t" "s_waitcnt lgkmcnt(0)\n\t" "v_readfirstlane_b32 s38, v39\n\t" "s_cbranch_scc1 .Lrv1_%=\n\t" "s_cmp_ge_u32 s38, 9\n\t" "s_cbranch_scc0 .Lsv1_%=\n\t" \
  "ds_read_b64 v[128:129], v30 offset:24064\n\t" "ds_read_b64 v[130:131], v30 offset:24576\n\t" "ds_read_b64 v[132:133], v30 offset:27136\n\t" "ds_read_b64 v[134:135], v30 offset:27648\n\t" \
  "ds_read_b64 v[136:137], v31 offset:24064\n\t" "ds_read_b64 v[138:139], v31 offset:24576\n\t" "ds_read_b64 v[140:141], v31 offset:27136\n\t" "ds_read_b64 v[142:143], v31 offset:27648\n\t" \
  "s_branch .Lrv1_%=\n\t" ".Lk2_%=:\n\t" "s_waitcnt lgkmcnt(8)\n\t" "v_readfirstlane_b32 s38, v39\n\t" "s_cmp_ge_u32 s38, 9\n\t" "s_cbranch_scc0 .Lsv1_%=\n\t" ".Lrv1_%=:\n\t" \
  "s_waitcnt lgkmcnt(0)\n\t" "v_mfma_f32_16x16x32_bf16 v[152:155], v[128:131], v[112:115], 0\n\t" "v_mfma_f32_16x16x32_bf16 v[164:167], v[140:143], v[120:123], 0\n\t" \
  "v_mfma_f32_16x16x32_bf16 v[156:159], v[132:135], v[112:115], 0\n\t" "v_mfma_f32_16x16x32_bf16 v[160:163], v[136:139], v[120:123], 0\n\t" "v_xor_b32 v20, 32, v35\n\t" "v_xor_b32 v21, 64, v35\n\t" \
  "v_xor_b32 v22, 96, v35\n\t" "s_nop 1\n\t" "v_cvt_pk_bf16_f32 v144, v152, v153\n\t" "v_cvt_pk_bf16_f32 v145, v154, v155\n\t" "v_cvt_pk_bf16_f32 v146, v164, v165\n\t" \
  "v_cvt_pk_bf16_f32 v147, v166, v167\n\t" "global_store_dwordx4 v32, v[144:147], s[60:61] offset:-2048 sc1\n\t" "v_cvt_pk_bf16_f32 v148, v156, v157\n\t" "v_cvt_pk_bf16_f32 v149, v158, v159\n\t" \
  "v_cvt_pk_bf16_f32 v150, v160, v161\n\t" "v_cvt_pk_bf16_f32 v151, v162, v163\n\t" "s_branch .Lk3_%=\n\t" ".Lso0_%=:\n\t" "global_load_dwordx4 v[152:155], v32, s[62:63] offset:-2048 sc1\n\t" \
  "s_sub_u32 s39, s39, 1\n\t" "s_cmp_lt_i32 s39, 0\n\t" "s_waitcnt vmcnt(0)\n\t" "s_cbranch_scc1 .Lro0_%=\n\t" "v_cmp_eq_u32 vcc, -1, v152\n\t" "s_cbranch_vccnz .Lso0_%=\n\t" "s_branch .Lro0_%=\n\t" \
  ".Lso1_%=:\n\t" "global_load_dwordx4 v[156:159], v32, s[62:63] offset:-1024 sc1\n\t" "s_sub_u32 s39, s39, 1\n\t" "s_cmp_lt_i32 s39, 0\n\t" "s_waitcnt vmcnt(0)\n\t" "s_cbranch_scc1 .Lro1_%=\n\t" \
  "v_cmp_eq_u32 vcc, -1, v156\n\t" "s_cbranch_vccnz .Lso1_%=\n\t" "s_branch .Lro1_%=\n\t" ".Lso2_%=:\n\t" "global_load_dwordx4 v[160:163], v32, s[62:63] offset:0 sc1\n\t" "s_sub_u32 s39, s39, 1\n\t" \
  "s_cmp_lt_i32 s39, 0\n\t" "s_waitcnt vmcnt(0)\n\t" "s_cbranch_scc1 .Lro2_%=\n\t" "v_cmp_eq_u32 vcc, -1, v160\n\t" "s_cbranch_vccnz .Lso2_%=\n\t" "s_branch .Lro2_%=\n\t" ".Lso3_%=:\n\t" \
  "global_load_dwordx4 v[164:167], v32, s[62:63] offset:1024 sc1\n\t" "s_sub_u32 s39, s39, 1\n\t" "s_cmp_lt_i32 s39, 0\n\t" "s_waitcnt vmcnt(0)\n\t" "s_cbranch_scc1 .Lro3_%=\n\t" \
  "v_cmp_eq_u32 vcc, -1, v164\n\t" "s_cbranch_vccnz .Lso3_%=\n\t" "s_branch .Lro3_%=\n\t" ".Lso4_%=:\n\t" "global_load_dwordx4 v[152:155], v32, s[62:63] offset:2048 sc1\n\t" \
  "s_sub_u32 s39, s39, 1\n\t" "s_cmp_lt_i32 s39, 0\n\t" "s_waitcnt vmcnt(0)\n\t" "s_cbranch_scc1 .Lro4_%=\n\t" "v_cmp_eq_u32 vcc, -1, v152\n\t" "s_cbranch_vccnz .Lso4_%=\n\t" "s_branch .Lro4_%=\n\t" \
  ".Lso5_%=:\n\t" "global_load_dwordx4 v[156:159], v32, s[64:65] offset:-2048 sc1\n\t" "s_sub_u32 s39, s39, 1\n\t" "s_cmp_lt_i32 s39, 0\n\t" "s_waitcnt vmcnt(0)\n\t" "s_cbranch_scc1 .Lro5_%=\n\t" \
  "v_cmp_eq_u32 vcc, -1, v156\n\t" "s_cbranch_vccnz .Lso5_%=\n\t" "s_branch .Lro5_%=\n\t" ".Lso6_%=:\n\t" "global_load_dwordx4 v[160:163], v32, s[64:65] offset:-1024 sc1\n\t" \
  "s_sub_u32 s39, s39, 1\n\t" "s_cmp_lt_i32 s39, 0\n\t" "s_waitcnt vmcnt(0)\n\t" "s_cbranch_scc1 .Lro6_%=\n\t" "v_cmp_eq_u32 vcc, -1, v160\n\t" "s_cbranch_vccnz .Lso6_%=\n\t" "s_branch .Lro6_%=\n\t" \
  ".Lso7_%=:\n\t" "global_load_dwordx4 v[164:167], v32, s[64:65] offset:0 sc1\n\t" "s_sub_u32 s39, s39, 1\n\t" "s_cmp_lt_i32 s39, 0\n\t" "s_waitcnt vmcnt(0)\n\t" "s_cbranch_scc1 .Lro7_%=\n\t" \
  "v_cmp_eq_u32 vcc, -1, v164\n\t" "s_cbranch_vccnz .Lso7_%=\n\t" "s_branch .Lro7_%=\n\t" ".Lso8_%=:\n\t" "global_load_dwordx4 v[152:155], v32, s[64:65] offset:1024 sc1\n\t" \
  "s_sub_u32 s39, s39, 1\n\t" "s_cmp_lt_i32 s39, 0\n\t" "s_waitcnt vmcnt(0)\n\t" "s_cbranch_scc1 .Lro8_%=\n\t" "v_cmp_eq_u32 vcc, -1, v152\n\t" "s_cbranch_vccnz .Lso8_%=\n\t" "s_branch .Lro8_%=\n\t" \
  ".Lso9_%=:\n\t" "global_load_dwordx4 v[156:159], v32, s[64:65] offset:2048 sc1\n\t" "s_sub_u32 s39, s39, 1\n\t" "s_cmp_lt_i32 s39, 0\n\t" "s_waitcnt vmcnt(0)\n\t" "s_cbranch_scc1 .Lro9_%=\n\t" \
  "v_cmp_eq_u32 vcc, -1, v156\n\t" "s_cbranch_vccnz .Lso9_%=\n\t" "s_branch .Lro9_%=\n\t" ".Lg_%=:\n\t" "v_bfe_u32 v13, s80, v10, 4\n\t" "v_lshlrev_b32 v12, 1, v9\n\t" "s_movk_i32 s76, 24576\n\t" \
  "v_and_b32 v12, 28, v12\n\t" "v_lshl_add_u32 v2, v9, 7, s76\n\t" "v_xor_b32 v12, v12, v10\n\t" "s_mov_b32 s76, 155648\n\t" "v_lshl_add_u32 v2, v12, 2, v2\n\t" "v_lshl_add_u32 v4, v9, 2, s76\n\t" \
  "v_xor_b32 v3, 64, v2\n\t" "v_lshl_add_u32 v5, v10, 2, s76\n\t" "s_mul_i32 s76, s33, 3072\n\t" "v_lshl_add_u32 v6, v8, 3, s76\n\t" "s_sub_u32 s76, 5, s27\n\t" "s_cmp_eq_u32 s27, 0\n\t" \
  "s_cselect_b32 s76, 0, s76\n\t" "s_sub_u32 s77, 15, s27\n\t" "s_cmp_eq_u32 s27, 4\n\t" "s_cselect_b32 s77, 16, s77\n\t" "v_cmp_gt_u32 s[68:69], s76, v13\n\t" "v_cmp_le_u32 vcc, s77, v13\n\t" \
  "v_add3_u32 v13, v13, s27, -1\n\t" "s_or_b64 s[68:69], s[68:69], vcc\n\t" "v_and_b32 v13, 3, v13\n\t" "v_cndmask_b32 v12, 0, 50, vcc\n\t" "v_xor_b32 v13, v13, v12\n\t" "v_lshlrev_b32 v7, 4, v9\n\t" \
  "v_lshl_add_u32 v7, v13, 8, v7\n\t" "v_lshl_add_u32 v12, s33, 8, v5\n\t" "v_lshl_add_u32 v13, s33, 8, v4\n\t" "s_setprio 1\n\t" "s_movk_i32 s38, 9\n\t" "s_mov_b32 s77, 36864\n\t" \
  "v_mov_b64 v[40:41], 0\n\t" "v_mov_b64 v[42:43], 0\n\t" "v_mov_b64 v[44:45], 0\n\t" "v_mov_b64 v[46:47], 0\n\t" "v_mov_b64 v[48:49], 0\n\t" "v_mov_b64 v[50:51], 0\n\t" "v_mov_b64 v[52:53], 0\n\t" \
  "v_mov_b64 v[54:55], 0\n\t" "v_mov_b64 v[56:57], 0\n\t" "v_mov_b64 v[58:59], 0\n\t" "v_mov_b64 v[60:61], 0\n\t" "v_mov_b64 v[62:63], 0\n\t" "v_mov_b64 v[64:65], 0\n\t" "v_mov_b64 v[66:67], 0\n\t" \
  "v_mov_b64 v[68:69], 0\n\t" "v_mov_b64 v[70:71], 0\n\t" "v_mov_b64 v[72:73], 0\n\t" "v_mov_b64 v[74:75], 0\n\t" "v_mov_b64 v[76:77], 0\n\t" "v_mov_b64 v[78:79], 0\n\t" "v_mov_b64 v[80:81], 0\n\t" \
  "v_mov_b64 v[82:83], 0\n\t" "v_mov_b64 v[84:85], 0\n\t" "v_mov_b64 v[86:87], 0\n\t" "v_mov_b64 v[88:89], 0\n\t" "v_mov_b64 v[90:91], 0\n\t" "v_mov_b64 v[92:93], 0\n\t" "v_mov_b64 v[94:95], 0\n\t" \
  "v_mov_b64 v[96:97], 0\n\t" "v_mov_b64 v[98:99], 0\n\t" "v_mov_b64 v[100:101], 0\n\t" "v_mov_b64 v[102:103], 0\n\t" "v_mov_b64 v[104:105], 0\n\t" "v_mov_b64 v[106:107], 0\n\t" \
  "v_mov_b64 v[108:109], 0\n\t" "v_mov_b64 v[110:111], 0\n\t" "s_barrier\n\t" "ds_read_b64_tr_b16 v[136:137], v1 offset:0\n\t" "ds_read_b64_tr_b16 v[138:139], v1 offset:512\n\t" \
  "ds_read_b128 v[112:115], v2 offset:0\n\t" "ds_read_b64_tr_b16 v[140:141], v1 offset:1024\n\t" "ds_read_b64_tr_b16 v[142:143], v1 offset:1536\n\t" "ds_read_b64_tr_b16 v[144:145], v1 offset:2048\n\t" \
  "ds_read_b64_tr_b16 v[146:147], v1 offset:2560\n\t" "ds_read_b128 v[116:119], v2 offset:2048\n\t" "ds_read_b128 v[120:123], v2 offset:4096\n\t" "ds_read_b128 v[124:127], v2 offset:6144\n\t" \
  "ds_read_b128 v[128:131], v2 offset:8192\n\t" "ds_read_b128 v[132:135], v2 offset:10240\n\t" "s_branch .Lb_%=\n\t" ".La_%=:\n\t" "s_waitcnt lgkmcnt(9)\n\t" \
  "v_mfma_f32_16x16x32_bf16 v[40:43], v[148:151], v[112:115], v[40:43]\n\t" "v_xor_b32 v3, 64, v2\n\t" "s_waitcnt lgkmcnt(7)\n\t" \
  "v_mfma_f32_16x16x32_bf16 v[64:67], v[152:155], v[112:115], v[64:67]\n\t" "s_waitcnt lgkmcnt(5)\n\t" "v_mfma_f32_16x16x32_bf16 v[88:91], v[112:115], v[156:159], v[88:91]\n\t" \
  "s_waitcnt lgkmcnt(4)\n\t" "v_mfma_f32_16x16x32_bf16 v[44:47], v[148:151], v[116:119], v[44:47]\n\t" "v_mfma_f32_16x16x32_bf16 v[68:71], v[152:155], v[116:119], v[68:71]\n\t" \
  "v_mfma_f32_16x16x32_bf16 v[92:95], v[116:119], v[156:159], v[92:95]\n\t" "s_waitcnt lgkmcnt(0)\n\t" "s_barrier\n\t" "v_mfma_f32_16x16x32_bf16 v[48:51], v[148:151], v[120:123], v[48:51]\n\t" \
  "ds_read_b64_tr_b16 v[136:137], v1 offset:0\n\t" "ds_read_b64_tr_b16 v[138:139], v1 offset:512\n\t" "v_mfma_f32_16x16x32_bf16 v[72:75], v[152:155], v[120:123], v[72:75]\n\t" \
  "ds_read_b128 v[112:115], v2 offset:0\n\t" "ds_read_b64_tr_b16 v[140:141], v1 offset:1024\n\t" "v_mfma_f32_16x16x32_bf16 v[96:99], v[120:123], v[156:159], v[96:99]\n\t" \
  "ds_read_b64_tr_b16 v[142:143], v1 offset:1536\n\t" "ds_read_b64_tr_b16 v[144:145], v1 offset:2048\n\t" "v_mfma_f32_16x16x32_bf16 v[52:55], v[148:151], v[124:127], v[52:55]\n\t" \
  "ds_read_b64_tr_b16 v[146:147], v1 offset:2560\n\t" "ds_read_b128 v[116:119], v2 offset:2048\n\t" "v_mfma_f32_16x16x32_bf16 v[76:79], v[152:155], v[124:127], v[76:79]\n\t" \
  "ds_read_b128 v[120:123], v2 offset:4096\n\t" "v_mfma_f32_16x16x32_bf16 v[100:103], v[124:127], v[156:159], v[100:103]\n\t" "ds_read_b128 v[124:127], v2 offset:6144\n\t" \
  "v_mfma_f32_16x16x32_bf16 v[56:59], v[148:151], v[128:131], v[56:59]\n\t" "v_mfma_f32_16x16x32_bf16 v[80:83], v[152:155], v[128:131], v[80:83]\n\t" \
  "v_mfma_f32_16x16x32_bf16 v[104:107], v[128:131], v[156:159], v[104:107]\n\t" "ds_read_b128 v[128:131], v2 offset:8192\n\t" "v_mfma_f32_16x16x32_bf16 v[60:63], v[148:151], v[132:135], v[60:63]\n\t" \
  "v_mfma_f32_16x16x32_bf16 v[84:87], v[152:155], v[132:135], v[84:87]\n\t" "v_mfma_f32_16x16x32_bf16 v[108:111], v[132:135], v[156:159], v[108:111]\n\t" "ds_read_b128 v[132:135], v2 offset:10240\n\t" \
  ".Lb_%=:\n\t" "s_waitcnt lgkmcnt(9)\n\t" "v_mfma_f32_16x16x32_bf16 v[40:43], v[136:139], v[112:115], v[40:43]\n\t" "ds_read_b64_tr_b16 v[148:149], v1 offset:12288\n\t" "s_waitcnt lgkmcnt(8)\n\t" \
  "v_mfma_f32_16x16x32_bf16 v[64:67], v[140:143], v[112:115], v[64:67]\n\t" "ds_read_b64_tr_b16 v[150:151], v1 offset:12800\n\t" "s_waitcnt lgkmcnt(7)\n\t" \
  "v_mfma_f32_16x16x32_bf16 v[88:91], v[112:115], v[144:147], v[88:91]\n\t" "ds_read_b128 v[112:115], v3 offset:0\n\t" "s_waitcnt lgkmcnt(7)\n\t" \
  "v_mfma_f32_16x16x32_bf16 v[44:47], v[136:139], v[116:119], v[44:47]\n\t" "ds_read_b64_tr_b16 v[152:153], v1 offset:13312\n\t" \
  "v_mfma_f32_16x16x32_bf16 v[68:71], v[140:143], v[116:119], v[68:71]\n\t" "ds_read_b64_tr_b16 v[154:155], v1 offset:13824\n\t" \
  "v_mfma_f32_16x16x32_bf16 v[92:95], v[116:119], v[144:147], v[92:95]\n\t" "ds_read_b64_tr_b16 v[156:157], v1 offset:14336\n\t" "s_waitcnt lgkmcnt(9)\n\t" \
  "v_mfma_f32_16x16x32_bf16 v[48:51], v[136:139], v[120:123], v[48:51]\n\t" "ds_read_b64_tr_b16 v[158:159], v1 offset:14848\n\t" \
  "v_mfma_f32_16x16x32_bf16 v[72:75], v[140:143], v[120:123], v[72:75]\n\t" "ds_read_b128 v[116:119], v3 offset:2048\n\t" "v_mfma_f32_16x16x32_bf16 v[96:99], v[120:123], v[144:147], v[96:99]\n\t" \
  "ds_read_b128 v[120:123], v3 offset:4096\n\t" "s_waitcnt lgkmcnt(11)\n\t" "v_mfma_f32_16x16x32_bf16 v[52:55], v[136:139], v[124:127], v[52:55]\n\t" "s_bitcmp1_b32 0x44, s38\n\t" \
  "v_mfma_f32_16x16x32_bf16 v[76:79], v[140:143], v[124:127], v[76:79]\n\t" "s_cselect_b32 s76, -110592, s77\n\t" "v_add_u32 v1, s76, v1\n\t" \
  "v_mfma_f32_16x16x32_bf16 v[100:103], v[124:127], v[144:147], v[100:103]\n\t" "ds_read_b128 v[124:127], v3 offset:6144\n\t" "s_waitcnt lgkmcnt(11)\n\t" \
  "v_mfma_f32_16x16x32_bf16 v[56:59], v[136:139], v[128:131], v[56:59]\n\t" "v_add_u32 v2, s76, v2\n\t" "v_mfma_f32_16x16x32_bf16 v[80:83], v[140:143], v[128:131], v[80:83]\n\t" \
  "s_sub_u32 s38, s38, 1\n\t" "v_mfma_f32_16x16x32_bf16 v[104:107], v[128:131], v[144:147], v[104:107]\n\t" "ds_read_b128 v[128:131], v3 offset:8192\n\t" "s_waitcnt lgkmcnt(11)\n\t" \
  "v_mfma_f32_16x16x32_bf16 v[60:63], v[136:139], v[132:135], v[60:63]\n\t" "v_mfma_f32_16x16x32_bf16 v[84:87], v[140:143], v[132:135], v[84:87]\n\t" \
  "v_mfma_f32_16x16x32_bf16 v[108:111], v[132:135], v[144:147], v[108:111]\n\t" "ds_read_b128 v[132:135], v3 offset:10240\n\t" "s_cbranch_scc0 .La_%=\n\t" "s_waitcnt lgkmcnt(9)\n\t" \
  "v_mfma_f32_16x16x32_bf16 v[40:43], v[148:151], v[112:115], v[40:43]\n\t" "s_waitcnt lgkmcnt(7)\n\t" "v_mfma_f32_16x16x32_bf16 v[64:67], v[152:155], v[112:115], v[64:67]\n\t" \
  "s_waitcnt lgkmcnt(4)\n\t" "v_mfma_f32_16x16x32_bf16 v[44:47], v[148:151], v[116:119], v[44:47]\n\t" "ds_read_b32 v39, v37 offset:12\n\t" "ds_read_b128 v[160:163], v12 offset:1024\n\t" \
  "v_mfma_f32_16x16x32_bf16 v[68:71], v[152:155], v[116:119], v[68:71]\n\t" "ds_read_b128 v[164:167], v12 offset:1088\n\t" "ds_read2_b32 v[136:137], v4 offset0:0 offset1:96\n\t" \
  "s_waitcnt lgkmcnt(7)\n\t" "v_mfma_f32_16x16x32_bf16 v[48:51], v[148:151], v[120:123], v[48:51]\n\t" "ds_read2_b32 v[138:139], v4 offset0:16 offset1:112\n\t" \
  "ds_read2_b32 v[140:141], v4 offset0:32 offset1:128\n\t" "v_mfma_f32_16x16x32_bf16 v[72:75], v[152:155], v[120:123], v[72:75]\n\t" "ds_read2_b32 v[142:143], v4 offset0:48 offset1:144\n\t" \
  "ds_read2_b32 v[144:145], v4 offset0:64 offset1:160\n\t" "s_waitcnt lgkmcnt(10)\n\t" "v_mfma_f32_16x16x32_bf16 v[52:55], v[148:151], v[124:127], v[52:55]\n\t" \
  "ds_read2_b32 v[146:147], v4 offset0:80 offset1:176\n\t" "ds_read_b32 v14, v13 offset:1152\n\t" "v_mfma_f32_16x16x32_bf16 v[76:79], v[152:155], v[124:127], v[76:79]\n\t" "s_waitcnt lgkmcnt(11)\n\t" \
  "v_mfma_f32_16x16x32_bf16 v[56:59], v[148:151], v[128:131], v[56:59]\n\t" "s_waitcnt lgkmcnt(0)\n\t" "v_readfirstlane_b32 s38, v39\n\t" "s_cmp_ge_u32 s38, 4\n\t" "s_cbranch_scc0 .Lts_%=\n\t" \
  ".Ltr_%=:\n\t" "v_fma_f32 v40, -v160, v136, v40\n\t" "v_fma_f32 v41, -v161, v136, v41\n\t" "v_mfma_f32_16x16x32_bf16 v[80:83], v[152:155], v[128:131], v[80:83]\n\t" \
  "v_fma_f32 v42, -v162, v136, v42\n\t" "v_fma_f32 v43, -v163, v136, v43\n\t" "v_mfma_f32_16x16x32_bf16 v[60:63], v[148:151], v[132:135], v[60:63]\n\t" "v_fma_f32 v64, -v164, v136, v64\n\t" \
  "v_fma_f32 v65, -v165, v136, v65\n\t" "v_mfma_f32_16x16x32_bf16 v[84:87], v[152:155], v[132:135], v[84:87]\n\t" "v_fma_f32 v66, -v166, v136, v66\n\t" "v_fma_f32 v67, -v167, v136, v67\n\t" \
  "v_mfma_f32_16x16x32_bf16 v[88:91], v[112:115], v[156:159], v[88:91]\n\t" "v_mul_f32 v40, v40, v137\n\t" "v_mul_f32 v41, v41, v137\n\t" \
  "v_mfma_f32_16x16x32_bf16 v[92:95], v[116:119], v[156:159], v[92:95]\n\t" "v_mul_f32 v42, v42, v137\n\t" "v_mul_f32 v43, v43, v137\n\t" \
  "v_mfma_f32_16x16x32_bf16 v[96:99], v[120:123], v[156:159], v[96:99]\n\t" "v_mul_f32 v64, v64, v137\n\t" "v_mul_f32 v65, v65, v137\n\t" \
  "v_mfma_f32_16x16x32_bf16 v[100:103], v[124:127], v[156:159], v[100:103]\n\t" "v_mul_f32 v66, v66, v137\n\t" "v_mul_f32 v67, v67, v137\n\t" \
  "v_mfma_f32_16x16x32_bf16 v[104:107], v[128:131], v[156:159], v[104:107]\n\t" "v_cvt_pk_bf16_f32 v40, v40, v41\n\t" "v_cvt_pk_bf16_f32 v41, v42, v43\n\t" \
  "v_mfma_f32_16x16x32_bf16 v[108:111], v[132:135], v[156:159], v[108:111]\n\t" "v_cvt_pk_bf16_f32 v42, v64, v65\n\t" "v_cvt_pk_bf16_f32 v43, v66, v67\n\t" "ds_read_b128 v[112:115], v5 offset:0\n\t" \
  "ds_read_b128 v[116:119], v5 offset:384\n\t" "ds_read_b128 v[120:123], v5 offset:64\n\t" "ds_read_b128 v[124:127], v5 offset:448\n\t" "ds_read_b128 v[128:131], v5 offset:128\n\t" \
  "ds_read_b128 v[132:135], v5 offset:512\n\t" "v_fma_f32 v44, -v160, v138, v44\n\t" "v_fma_f32 v45, -v161, v138, v45\n\t" "v_fma_f32 v46, -v162, v138, v46\n\t" "v_fma_f32 v47, -v163, v138, v47\n\t" \
  "s_mov_b64 exec, s[68:69]\n\t" "global_store_dwordx4 v7, v[40:43], s[66:67] offset:-2048 sc1\n\t" "s_mov_b64 exec, -1\n\t" "ds_write2st64_b64 v6, v[40:41], v[42:43] offset0:0 offset1:24\n\t" \
  "s_mov_b64 exec, 1\n\t" "ds_add_u32 v37, v38 offset:8\n\t" "s_mov_b64 exec, -1\n\t" "v_pk_fma_f32 v[68:69], v[164:165], v[138:139], v[68:69] op_sel_hi:[1,0,1] neg_lo:[1,0,0] neg_hi:[1,0,0]\n\t" \
  "v_pk_fma_f32 v[70:71], v[166:167], v[138:139], v[70:71] op_sel_hi:[1,0,1] neg_lo:[1,0,0] neg_hi:[1,0,0]\n\t" "v_pk_mul_f32 v[44:45], v[44:45], v[138:139] op_sel:[0,1] op_sel_hi:[1,1]\n\t" \
  "v_pk_mul_f32 v[46:47], v[46:47], v[138:139] op_sel:[0,1] op_sel_hi:[1,1]\n\t" "v_pk_mul_f32 v[68:69], v[68:69], v[138:139] op_sel:[0,1] op_sel_hi:[1,1]\n\t" \
  "v_pk_mul_f32 v[70:71], v[70:71], v[138:139] op_sel:[0,1] op_sel_hi:[1,1]\n\t" "v_cvt_pk_bf16_f32 v44, v44, v45\n\t" "v_cvt_pk_bf16_f32 v45, v46, v47\n\t" "v_cvt_pk_bf16_f32 v46, v68, v69\n\t" \
  "v_cvt_pk_bf16_f32 v47, v70, v71\n\t" "s_mov_b64 exec, s[68:69]\n\t" "global_store_dwordx4 v7, v[44:47], s[66:67] offset:-1024 sc1\n\t" "s_mov_b64 exec, -1\n\t" \
  "ds_write2st64_b64 v6, v[44:45], v[46:47] offset0:1 offset1:25\n\t" "ds_read_b128 v[64:67], v5 offset:192\n\t" "ds_read_b128 v[68:71], v5 offset:576\n\t" \
  "v_pk_fma_f32 v[48:49], v[160:161], v[140:141], v[48:49] op_sel_hi:[1,0,1] neg_lo:[1,0,0] neg_hi:[1,0,0]\n\t" \
  "v_pk_fma_f32 v[50:51], v[162:163], v[140:141], v[50:51] op_sel_hi:[1,0,1] neg_lo:[1,0,0] neg_hi:[1,0,0]\n\t" \
  "v_pk_fma_f32 v[72:73], v[164:165], v[140:141], v[72:73] op_sel_hi:[1,0,1] neg_lo:[1,0,0] neg_hi:[1,0,0]\n\t" \
  "v_pk_fma_f32 v[74:75], v[166:167], v[140:141], v[74:75] op_sel_hi:[1,0,1] neg_lo:[1,0,0] neg_hi:[1,0,0]\n\t" "v_pk_mul_f32 v[48:49], v[48:49], v[140:141] op_sel:[0,1] op_sel_hi:[1,1]\n\t" \
  "v_pk_mul_f32 v[50:51], v[50:51], v[140:141] op_sel:[0,1] op_sel_hi:[1,1]\n\t" "v_pk_mul_f32 v[72:73], v[72:73], v[140:141] op_sel:[0,1] op_sel_hi:[1,1]\n\t" \
  "v_pk_mul_f32 v[74:75], v[74:75], v[140:141] op_sel:[0,1] op_sel_hi:[1,1]\n\t" "v_cvt_pk_bf16_f32 v48, v48, v49\n\t" "v_cvt_pk_bf16_f32 v49, v50, v51\n\t" "v_cvt_pk_bf16_f32 v50, v72, v73\n\t" \
  "v_cvt_pk_bf16_f32 v51, v74, v75\n\t" "s_mov_b64 exec, s[68:69]\n\t" "global_store_dwordx4 v7, v[48:51], s[66:67] offset:0 sc1\n\t" "s_mov_b64 exec, -1\n\t" \
  "ds_write2st64_b64 v6, v[48:49], v[50:51] offset0:2 offset1:26\n\t" "v_pk_fma_f32 v[52:53], v[160:161], v[142:143], v[52:53] op_sel_hi:[1,0,1] neg_lo:[1,0,0] neg_hi:[1,0,0]\n\t" \
  "v_pk_fma_f32 v[54:55], v[162:163], v[142:143], v[54:55] op_sel_hi:[1,0,1] neg_lo:[1,0,0] neg_hi:[1,0,0]\n\t" \
  "v_pk_fma_f32 v[76:77], v[164:165], v[142:143], v[76:77] op_sel_hi:[1,0,1] neg_lo:[1,0,0] neg_hi:[1,0,0]\n\t" \
  "v_pk_fma_f32 v[78:79], v[166:167], v[142:143], v[78:79] op_sel_hi:[1,0,1] neg_lo:[1,0,0] neg_hi:[1,0,0]\n\t" "v_pk_mul_f32 v[52:53], v[52:53], v[142:143] op_sel:[0,1] op_sel_hi:[1,1]\n\t" \
  "v_pk_mul_f32 v[54:55], v[54:55], v[142:143] op_sel:[0,1] op_sel_hi:[1,1]\n\t" "v_pk_mul_f32 v[76:77], v[76:77], v[142:143] op_sel:[0,1] op_sel_hi:[1,1]\n\t" \
  "v_pk_mul_f32 v[78:79], v[78:79], v[142:143] op_sel:[0,1] op_sel_hi:[1,1]\n\t" "v_cvt_pk_bf16_f32 v52, v52, v53\n\t" "v_cvt_pk_bf16_f32 v53, v54, v55\n\t" "v_cvt_pk_bf16_f32 v54, v76, v77\n\t" \
  "v_cvt_pk_bf16_f32 v55, v78, v79\n\t" "s_mov_b64 exec, s[68:69]\n\t" "global_store_dwordx4 v7, v[52:55], s[66:67] offset:1024 sc1\n\t" "s_mov_b64 exec, -1\n\t" \
  "ds_write2st64_b64 v6, v[52:53], v[54:55] offset0:3 offset1:27\n\t" "ds_read_b128 v[72:75], v5 offset:256\n\t" "ds_read_b128 v[76:79], v5 offset:640\n\t" \
  "v_pk_fma_f32 v[56:57], v[160:161], v[144:145], v[56:57] op_sel_hi:[1,0,1] neg_lo:[1,0,0] neg_hi:[1,0,0]\n\t" \
  "v_pk_fma_f32 v[58:59], v[162:163], v[144:145], v[58:59] op_sel_hi:[1,0,1] neg_lo:[1,0,0] neg_hi:[1,0,0]\n\t" \
  "v_pk_fma_f32 v[80:81], v[164:165], v[144:145], v[80:81] op_sel_hi:[1,0,1] neg_lo:[1,0,0] neg_hi:[1,0,0]\n\t" \
  "v_pk_fma_f32 v[82:83], v[166:167], v[144:145], v[82:83] op_sel_hi:[1,0,1] neg_lo:[1,0,0] neg_hi:[1,0,0]\n\t" "v_pk_mul_f32 v[56:57], v[56:57], v[144:145] op_sel:[0,1] op_sel_hi:[1,1]\n\t" \
  "v_pk_mul_f32 v[58:59], v[58:59], v[144:145] op_sel:[0,1] op_sel_hi:[1,1]\n\t" "v_pk_mul_f32 v[80:81], v[80:81], v[144:145] op_sel:[0,1] op_sel_hi:[1,1]\n\t" \
  "v_pk_mul_f32 v[82:83], v[82:83], v[144:145] op_sel:[0,1] op_sel_hi:[1,1]\n\t" "v_cvt_pk_bf16_f32 v56, v56, v57\n\t" "v_cvt_pk_bf16_f32 v57, v58, v59\n\t" "v_cvt_pk_bf16_f32 v58, v80, v81\n\t" \
  "v_cvt_pk_bf16_f32 v59, v82, v83\n\t" "s_mov_b64 exec, s[68:69]\n\t" "global_store_dwordx4 v7, v[56:59], s[66:67] offset:2048 sc1\n\t" "s_mov_b64 exec, -1\n\t" \
  "ds_write2st64_b64 v6, v[56:57], v[58:59] offset0:4 offset1:28\n\t" "v_pk_fma_f32 v[60:61], v[160:161], v[146:147], v[60:61] op_sel_hi:[1,0,1] neg_lo:[1,0,0] neg_hi:[1,0,0]\n\t" \
  "v_pk_fma_f32 v[62:63], v[162:163], v[146:147], v[62:63] op_sel_hi:[1,0,1] neg_lo:[1,0,0] neg_hi:[1,0,0]\n\t" \
  "v_pk_fma_f32 v[84:85], v[164:165], v[146:147], v[84:85] op_sel_hi:[1,0,1] neg_lo:[1,0,0] neg_hi:[1,0,0]\n\t" \
  "v_pk_fma_f32 v[86:87], v[166:167], v[146:147], v[86:87] op_sel_hi:[1,0,1] neg_lo:[1,0,0] neg_hi:[1,0,0]\n\t" "v_pk_mul_f32 v[60:61], v[60:61], v[146:147] op_sel:[0,1] op_sel_hi:[1,1]\n\t" \
  "v_pk_mul_f32 v[62:63], v[62:63], v[146:147] op_sel:[0,1] op_sel_hi:[1,1]\n\t" "v_pk_mul_f32 v[84:85], v[84:85], v[146:147] op_sel:[0,1] op_sel_hi:[1,1]\n\t" \
  "v_pk_mul_f32 v[86:87], v[86:87], v[146:147] op_sel:[0,1] op_sel_hi:[1,1]\n\t" "v_cvt_pk_bf16_f32 v60, v60, v61\n\t" "v_cvt_pk_bf16_f32 v61, v62, v63\n\t" "v_cvt_pk_bf16_f32 v62, v84, v85\n\t" \
  "v_cvt_pk_bf16_f32 v63, v86, v87\n\t" "s_mov_b64 exec, s[68:69]\n\t" "global_store_dwordx4 v7, v[60:63], s[66:67] offset:3072 sc1\n\t" "s_mov_b64 exec, -1\n\t" \
  "ds_write2st64_b64 v6, v[60:61], v[62:63] offset0:5 offset1:29\n\t" "ds_read_b128 v[80:83], v5 offset:320\n\t" "ds_read_b128 v[84:87], v5 offset:704\n\t" "s_mov_b64 exec, 1\n\t" \
  "ds_add_u32 v37, v38 offset:0\n\t" "s_mov_b64 exec, -1\n\t" "s_waitcnt lgkmcnt(14)\n\t" "v_pk_fma_f32 v[88:89], v[112:113], v[14:15], v[88:89] op_sel_hi:[1,0,1] neg_lo:[1,0,0] neg_hi:[1,0,0]\n\t" \
  "v_pk_fma_f32 v[90:91], v[114:115], v[14:15], v[90:91] op_sel_hi:[1,0,1] neg_lo:[1,0,0] neg_hi:[1,0,0]\n\t" \
  "v_pk_fma_f32 v[92:93], v[120:121], v[14:15], v[92:93] op_sel_hi:[1,0,1] neg_lo:[1,0,0] neg_hi:[1,0,0]\n\t" \
  "v_pk_fma_f32 v[94:95], v[122:123], v[14:15], v[94:95] op_sel_hi:[1,0,1] neg_lo:[1,0,0] neg_hi:[1,0,0]\n\t" "v_pk_mul_f32 v[88:89], v[88:89], v[116:117]\n\t" \
  "v_pk_mul_f32 v[90:91], v[90:91], v[118:119]\n\t" "v_pk_mul_f32 v[92:93], v[92:93], v[124:125]\n\t" "v_pk_mul_f32 v[94:95], v[94:95], v[126:127]\n\t" "v_cvt_pk_bf16_f32 v88, v88, v89\n\t" \
  "v_cvt_pk_bf16_f32 v89, v90, v91\n\t" "v_cvt_pk_bf16_f32 v92, v92, v93\n\t" "v_cvt_pk_bf16_f32 v93, v94, v95\n\t" "ds_write2st64_b64 v6, v[88:89], v[92:93] offset0:48 offset1:49\n\t" \
  "s_waitcnt lgkmcnt(10)\n\t" "v_pk_fma_f32 v[96:97], v[128:129], v[14:15], v[96:97] op_sel_hi:[1,0,1] neg_lo:[1,0,0] neg_hi:[1,0,0]\n\t" \
  "v_pk_fma_f32 v[98:99], v[130:131], v[14:15], v[98:99] op_sel_hi:[1,0,1] neg_lo:[1,0,0] neg_hi:[1,0,0]\n\t" \
  "v_pk_fma_f32 v[100:101], v[64:65], v[14:15], v[100:101] op_sel_hi:[1,0,1] neg_lo:[1,0,0] neg_hi:[1,0,0]\n\t" \
  "v_pk_fma_f32 v[102:103], v[66:67], v[14:15], v[102:103] op_sel_hi:[1,0,1] neg_lo:[1,0,0] neg_hi:[1,0,0]\n\t" "v_pk_mul_f32 v[96:97], v[96:97], v[132:133]\n\t" \
  "v_pk_mul_f32 v[98:99], v[98:99], v[134:135]\n\t" "v_pk_mul_f32 v[100:101], v[100:101], v[68:69]\n\t" "v_pk_mul_f32 v[102:103], v[102:103], v[70:71]\n\t" "v_cvt_pk_bf16_f32 v96, v96, v97\n\t" \
  "v_cvt_pk_bf16_f32 v97, v98, v99\n\t" "v_cvt_pk_bf16_f32 v100, v100, v101\n\t" "v_cvt_pk_bf16_f32 v101, v102, v103\n\t" "ds_write2st64_b64 v6, v[96:97], v[100:101] offset0:50 offset1:51\n\t" \
  "s_waitcnt lgkmcnt(3)\n\t" "v_pk_fma_f32 v[104:105], v[72:73], v[14:15], v[104:105] op_sel_hi:[1,0,1] neg_lo:[1,0,0] neg_hi:[1,0,0]\n\t" \
  "v_pk_fma_f32 v[106:107], v[74:75], v[14:15], v[106:107] op_sel_hi:[1,0,1] neg_lo:[1,0,0] neg_hi:[1,0,0]\n\t" \
  "v_pk_fma_f32 v[108:109], v[80:81], v[14:15], v[108:109] op_sel_hi:[1,0,1] neg_lo:[1,0,0] neg_hi:[1,0,0]\n\t" \
  "v_pk_fma_f32 v[110:111], v[82:83], v[14:15], v[110:111] op_sel_hi:[1,0,1] neg_lo:[1,0,0] neg_hi:[1,0,0]\n\t" "v_pk_mul_f32 v[104:105], v[104:105], v[76:77]\n\t" \
  "v_pk_mul_f32 v[106:107], v[106:107], v[78:79]\n\t" "v_pk_mul_f32 v[108:109], v[108:109], v[84:85]\n\t" "v_pk_mul_f32 v[110:111], v[110:111], v[86:87]\n\t" "v_cvt_pk_bf16_f32 v104, v104, v105\n\t" \
  "v_cvt_pk_bf16_f32 v105, v106, v107\n\t" "v_cvt_pk_bf16_f32 v108, v108, v109\n\t" "v_cvt_pk_bf16_f32 v109, v110, v111\n\t" "ds_write2st64_b64 v6, v[104:105], v[108:109] offset0:52 offset1:53\n\t" \
  "s_mov_b64 exec, 1\n\t" "ds_add_u32 v37, v38 offset:4\n\t" "s_mov_b64 exec, -1\n\t" "s_setprio 1\n\t" ".Latt_%=:\n\t" "global_load_dwordx4 v[12:15], v33, s[8:9]\n\t" \
  "global_load_dwordx4 v[16:19], v34, s[14:15]\n\t" "s_cmp_eq_u32 s34, 0\n\t" "s_cbranch_scc1 .Lk0_%=\n\t" ".Lq_%=:\n\t" "ds_read_b32 v39, v37\n\t" "ds_read_b64 v[40:41], v30 offset:0\n\t" \
  "ds_read_b64 v[42:43], v30 offset:3072\n\t" "ds_read_b64 v[44:45], v30 offset:11776\n\t" "ds_read_b64 v[46:47], v30 offset:14848\n\t" "ds_read_b64 v[48:49], v30 offset:12288\n\t" \
  "ds_read_b64 v[50:51], v30 offset:15360\n\t" "ds_read_b64 v[52:53], v31 offset:0\n\t" "ds_read_b64 v[54:55], v31 offset:3072\n\t" "ds_read_b64 v[56:57], v31 offset:11776\n\t" \
  "ds_read_b64 v[58:59], v31 offset:14848\n\t" "ds_read_b64 v[60:61], v31 offset:12288\n\t" "ds_read_b64 v[62:63], v31 offset:15360\n\t" "s_waitcnt lgkmcnt(12)\n\t" "v_readfirstlane_b32 s38, v39\n\t" \
  "s_cmp_ge_u32 s38, 4\n\t" "s_cbranch_scc0 .Lsq_%=\n\t" ".Lrq_%=:\n\t" "s_waitcnt lgkmcnt(10)\n\t" "v_cndmask_b32 v42, 0, v42, s[54:55]\n\t" "v_cndmask_b32 v43, 0, v43, s[54:55]\n\t" \
  "s_waitcnt lgkmcnt(8)\n\t" "s_nop 0\n\t" "v_mfma_f32_16x16x32_bf16 v[108:111], v[44:47], v[40:43], v[20:23]\n\t" "s_waitcnt lgkmcnt(6)\n\t" \
  "v_mfma_f32_16x16x32_bf16 v[112:115], v[48:51], v[40:43], v[24:27]\n\t" "s_waitcnt lgkmcnt(4)\n\t" "v_cndmask_b32 v52, 0, v52, s[54:55]\n\t" "v_cndmask_b32 v53, 0, v53, s[54:55]\n\t" \
  "s_waitcnt lgkmcnt(2)\n\t" "s_nop 0\n\t" "v_mfma_f32_16x16x32_bf16 v[116:119], v[56:59], v[52:55], v[20:23]\n\t" "s_waitcnt lgkmcnt(0)\n\t" \
  "v_mfma_f32_16x16x32_bf16 v[120:123], v[60:63], v[52:55], v[24:27]\n\t" "ds_read_b32 v39, v37 offset:4\n\t" "ds_read_b64 v[128:129], v30 offset:24064\n\t" \
  "ds_read_b64 v[130:131], v30 offset:24576\n\t" "ds_read_b64 v[132:133], v30 offset:27136\n\t" "ds_read_b64 v[134:135], v30 offset:27648\n\t" "ds_read_b64 v[136:137], v31 offset:24064\n\t" \
  "ds_read_b64 v[138:139], v31 offset:24576\n\t" "ds_read_b64 v[140:141], v31 offset:27136\n\t" "ds_read_b64 v[142:143], v31 offset:27648\n\t" ".Lk1_%=:\n\t" "s_nop 4\n\t" \
  "v_max_f32 v108, v108, v112\n\t" "v_max_f32 v109, v109, v113\n\t" "v_max_f32 v110, v110, v114\n\t" "v_max_f32 v111, v111, v115\n\t" "v_max_f32 v116, v116, v120\n\t" "v_max_f32 v117, v117, v121\n\t" \
  "v_max_f32 v118, v118, v122\n\t" "v_max_f32 v119, v119, v123\n\t" "v_max3_f32 v124, v108, v109, v110\n\t" "v_max3_f32 v125, v116, v117, v118\n\t" "v_max_f32 v124, v124, v111\n\t" \
  "v_max_f32 v125, v125, v119\n\t" "s_mov_b32 s38, 0x3ea52b5c\n\t" "s_nop 0\n\t" "v_permlane32_swap_b32 v124, v125\n\t" "v_max_f32 v126, v124, v125\n\t" "v_max_f32 v127, v124, v125\n\t" "s_nop 1\n\t" \
  "v_permlane16_swap_b32 v126, v127\n\t" "v_max_f32 v124, v126, v127\n\t" "v_mul_f32 v125, s38, v124\n\t" "v_mul_f32 v124, s38, v124\n\t" "s_nop 1\n\t" "v_permlane32_swap_b32 v124, v125\n\t" \
  "v_fma_f32 v108, v108, s38, -v124\n\t" "v_fma_f32 v116, v116, s38, -v125\n\t" "v_fma_f32 v109, v109, s38, -v124\n\t" "v_fma_f32 v117, v117, s38, -v125\n\t" "v_fma_f32 v110, v110, s38, -v124\n\t" \
  "v_fma_f32 v118, v118, s38, -v125\n\t" "v_fma_f32 v111, v111, s38, -v124\n\t" "v_fma_f32 v119, v119, s38, -v125\n\t" "v_exp_f32 v108, v108\n\t" "v_exp_f32 v116, v116\n\t" "v_exp_f32 v109, v109\n\t" \
  "v_exp_f32 v117, v117\n\t" "v_exp_f32 v110, v110\n\t" "v_exp_f32 v118, v118\n\t" "v_add_f32 v124, v108, v109\n\t" "v_add_f32 v125, v116, v117\n\t" "v_exp_f32 v111, v111\n\t" \
  "v_exp_f32 v119, v119\n\t" "v_add_f32 v124, v124, v110\n\t" "v_add_f32 v125, v125, v118\n\t" "v_add_f32 v124, v124, v111\n\t" "v_add_f32 v125, v125, v119\n\t" "s_cmp_lg_u32 s34, 0\n\t" "s_nop 0\n\t" \
  "v_permlane32_swap_b32 v124, v125\n\t" "v_add_f32 v126, v124, v125\n\t" "v_add_f32 v127, v124, v125\n\t" "s_nop 1\n\t" "v_permlane16_swap_b32 v126, v127\n\t" "v_add_f32 v124, v126, v127\n\t" \
  "v_rcp_f32 v126, v124\n\t" "v_rcp_f32 v124, v124\n\t" "s_nop 1\n\t" "v_permlane32_swap_b32 v124, v126\n\t" "v_pk_mul_f32 v[108:109], v[108:109], v[124:125] op_sel_hi:[1,0]\n\t" \
  "v_pk_mul_f32 v[110:111], v[110:111], v[124:125] op_sel_hi:[1,0]\n\t" "v_pk_mul_f32 v[116:117], v[116:117], v[126:127] op_sel_hi:[1,0]\n\t" \
  "v_pk_mul_f32 v[118:119], v[118:119], v[126:127] op_sel_hi:[1,0]\n\t" "v_cvt_pk_bf16_f32 v112, v108, v109\n\t" "v_cvt_pk_bf16_f32 v113, v110, v111\n\t" "v_cvt_pk_bf16_f32 v120, v116, v117\n\t" \
  "v_cvt_pk_bf16_f32 v121, v118, v119\n\t" "v_and_b32 v114, v112, v28\n\t" "v_and_b32 v115, v113, v29\n\t" "v_and_b32 v122, v120, v28\n\t" "v_and_b32 v123, v121, v29\n\t" \
  "v_xor_b32 v112, v112, v114\n\t" "v_xor_b32 v113, v113, v115\n\t" "v_xor_b32 v120, v120, v122\n\t" "v_xor_b32 v121, v121, v123\n\t" "s_cbranch_scc1 .Lk2_%=\n\t" \
  "v_mfma_f32_16x16x32_bf16 v[152:155], v[128:131], v[112:115], 0\n\t" "v_mfma_f32_16x16x32_bf16 v[156:159], v[132:135], v[112:115], 0\n\t" \
  "v_mfma_f32_16x16x32_bf16 v[152:155], v[136:139], v[120:123], v[152:155]\n\t" "v_mfma_f32_16x16x32_bf16 v[156:159], v[140:143], v[120:123], v[156:159]\n\t" "v_xor_b32 v20, 32, v35\n\t" \
  "v_xor_b32 v21, 64, v35\n\t" "v_xor_b32 v22, 96, v35\n\t" "s_nop 3\n\t" "v_cvt_pk_bf16_f32 v148, v152, v153\n\t" "v_cvt_pk_bf16_f32 v149, v154, v155\n\t" "v_cvt_pk_bf16_f32 v150, v156, v157\n\t" \
  "v_cvt_pk_bf16_f32 v151, v158, v159\n\t" ".Lk3_%=:\n\t" "s_mov_b64 exec, s[54:55]\n\t" "global_store_dwordx4 v36, v[148:151], s[60:61] offset:-2048 sc1\n\t" "s_mov_b64 exec, -1\n\t" \
  "s_cmp_lg_u32 s34, 0\n\t" "s_cbranch_scc1 .Lb3a_%=\n\t" "s_cmp_ge_u32 s26, 4\n\t" "s_cbranch_scc1 .Lv21_%=\n\t" "s_waitcnt vmcnt(1)\n\t" ".Lv21_%=:\n\t" "s_setprio 1\n\t" \
  "ds_read_b64 v[152:153], v37 offset:80\n\t" "s_waitcnt lgkmcnt(0)\n\t" "v_readfirstlane_b32 s80, v152\n\t" "v_readfirstlane_b32 s81, v153\n\t" "s_sub_u32 s81, s81, s80\n\t" \
  "s_sub_u32 s80, s81, 15\n\t" "s_barrier\n\t" "s_cbranch_scc1 .Llx1_%=\n\t" "s_memrealtime s[76:77]\n\t" ".Llw1_%=:\n\t" "s_memrealtime s[78:79]\n\t" "s_waitcnt lgkmcnt(0)\n\t" \
  "s_sub_u32 s78, s78, s76\n\t" "s_cmp_lt_u32 s78, s80\n\t" "s_cbranch_scc1 .Llw1_%=\n\t" ".Llx1_%=:\n\t" "global_load_dwordx4 v[152:155], v32, s[62:63] offset:-2048 sc1\n\t" \
  "global_load_dwordx4 v[156:159], v32, s[62:63] offset:-1024 sc1\n\t" "global_load_dwordx4 v[160:163], v32, s[62:63] offset:0 sc1\n\t" ".Lb3a_%=:\n\t" "ds_read_b64_tr_b16 v[40:41], v35 offset:0\n\t" \
  "ds_read_b64_tr_b16 v[42:43], v35 offset:2048\n\t" "ds_read_b64_tr_b16 v[44:45], v20 offset:0\n\t" "ds_read_b64_tr_b16 v[46:47], v20 offset:2048\n\t" "ds_read_b64_tr_b16 v[48:49], v21 offset:0\n\t" \
  "ds_read_b64_tr_b16 v[50:51], v21 offset:2048\n\t" "ds_read_b64_tr_b16 v[52:53], v22 offset:0\n\t" "ds_read_b64_tr_b16 v[54:55], v22 offset:2048\n\t" \
  "ds_read_b64_tr_b16 v[56:57], v35 offset:4096\n\t" "ds_read_b64_tr_b16 v[58:59], v35 offset:6144\n\t" "ds_read_b64_tr_b16 v[60:61], v20 offset:4096\n\t" \
  "ds_read_b64_tr_b16 v[62:63], v20 offset:6144\n\t" "ds_read_b64_tr_b16 v[64:65], v21 offset:4096\n\t" "ds_read_b64_tr_b16 v[66:67], v21 offset:6144\n\t" \
  "ds_read_b64_tr_b16 v[68:69], v22 offset:4096\n\t" "ds_read_b64_tr_b16 v[70:71], v22 offset:6144\n\t" "ds_read_b64_tr_b16 v[72:73], v35 offset:8192\n\t" \
  "ds_read_b64_tr_b16 v[74:75], v35 offset:10240\n\t" "ds_read_b64_tr_b16 v[76:77], v20 offset:8192\n\t" "ds_read_b64_tr_b16 v[78:79], v20 offset:10240\n\t" \
  "ds_read_b64_tr_b16 v[80:81], v21 offset:8192\n\t" "ds_read_b64_tr_b16 v[82:83], v21 offset:10240\n\t" "ds_read_b64_tr_b16 v[84:85], v22 offset:8192\n\t" \
  "ds_read_b64_tr_b16 v[86:87], v22 offset:10240\n\t" "ds_read_b64_tr_b16 v[88:89], v35 offset:12288\n\t" "ds_read_b64_tr_b16 v[90:91], v35 offset:14336\n\t" \
  "ds_read_b64_tr_b16 v[92:93], v20 offset:12288\n\t" "ds_read_b64_tr_b16 v[94:95], v20 offset:14336\n\t" "ds_read_b64_tr_b16 v[96:97], v21 offset:12288\n\t" \
  "ds_read_b64_tr_b16 v[98:99], v21 offset:14336\n\t" "ds_read_b64_tr_b16 v[100:101], v22 offset:12288\n\t" "ds_read_b64_tr_b16 v[102:103], v22 offset:14336\n\t" \
  "ds_read_b64_tr_b16 v[104:105], v35 offset:16384\n\t" "ds_read_b64_tr_b16 v[106:107], v35 offset:18432\n\t" "ds_read_b64_tr_b16 v[108:109], v20 offset:16384\n\t" \
  "ds_read_b64_tr_b16 v[110:111], v20 offset:18432\n\t" "ds_read_b64_tr_b16 v[112:113], v21 offset:16384\n\t" "ds_read_b64_tr_b16 v[114:115], v21 offset:18432\n\t" \
  "ds_read_b64_tr_b16 v[116:117], v22 offset:16384\n\t" "ds_read_b64_tr_b16 v[118:119], v22 offset:18432\n\t" "s_cmp_eq_u32 s34, 0\n\t" "s_cbranch_scc1 .Lb3b_%=\n\t" "s_setprio 1\n\t" \
  "ds_read_b64 v[152:153], v37 offset:80\n\t" "s_waitcnt lgkmcnt(0)\n\t" "v_readfirstlane_b32 s80, v152\n\t" "v_readfirstlane_b32 s81, v153\n\t" "s_sub_u32 s81, s81, s80\n\t" \
  "s_sub_u32 s80, s81, 15\n\t" "s_barrier\n\t" "s_cbranch_scc1 .Llx2_%=\n\t" "s_memrealtime s[76:77]\n\t" ".Llw2_%=:\n\t" "s_memrealtime s[78:79]\n\t" "s_waitcnt lgkmcnt(0)\n\t" \
  "s_sub_u32 s78, s78, s76\n\t" "s_cmp_lt_u32 s78, s80\n\t" "s_cbranch_scc1 .Llw2_%=\n\t" ".Llx2_%=:\n\t" "global_load_dwordx4 v[152:155], v32, s[62:63] offset:-2048 sc1\n\t" \
  "global_load_dwordx4 v[156:159], v32, s[62:63] offset:-1024 sc1\n\t" "global_load_dwordx4 v[160:163], v32, s[62:63] offset:0 sc1\n\t" ".Lb3b_%=:\n\t" \
  "ds_read_b64_tr_b16 v[120:121], v35 offset:20480\n\t" "ds_read_b64_tr_b16 v[122:123], v35 offset:22528\n\t" "ds_read_b64_tr_b16 v[124:125], v20 offset:20480\n\t" \
  "ds_read_b64_tr_b16 v[126:127], v20 offset:22528\n\t" "ds_read_b64_tr_b16 v[128:129], v21 offset:20480\n\t" "ds_read_b64_tr_b16 v[130:131], v21 offset:22528\n\t" \
  "ds_read_b64_tr_b16 v[132:133], v22 offset:20480\n\t" "ds_read_b64_tr_b16 v[134:135], v22 offset:22528\n\t" "s_waitcnt vmcnt(5)\n\t" "v_permlane16_swap_b32 v12, v14\n\t" \
  "v_permlane16_swap_b32 v13, v15\n\t" "v_mov_b64 v[144:145], 0\n\t" "v_mov_b64 v[146:147], 0\n\t" "v_mov_b64 v[148:149], 0\n\t" "v_mov_b64 v[150:151], 0\n\t" "v_lshlrev_b32 v136, 16, v12\n\t" \
  "v_and_b32 v137, 0xffff0000, v12\n\t" "v_lshlrev_b32 v138, 16, v13\n\t" "v_and_b32 v139, 0xffff0000, v13\n\t" "v_lshlrev_b32 v140, 16, v14\n\t" "v_and_b32 v141, 0xffff0000, v14\n\t" \
  "v_lshlrev_b32 v142, 16, v15\n\t" "v_and_b32 v143, 0xffff0000, v15\n\t" "s_lshl_b32 s38, s26, 11\n\t" "v_and_b32 v28, 0xf0, v32\n\t" "v_add_u32 v24, s38, v32\n\t" \
  "v_permlane16_swap_b32 v16, v18\n\t" "v_permlane16_swap_b32 v17, v19\n\t" "s_lshl_b32 s40, s36, 6\n\t" "s_add_u32 s40, s40, 0\n\t" "v_lshl_add_u32 v27, v10, 1, s40\n\t" \
  "s_mov_b32 s40, 0x3f803f80\n\t" "v_xor_b32 v25, 0x800, v24\n\t" "v_mad_u32_u24 v27, v28, 9, v27\n\t" "s_mul_i32 s38, s37, 2304\n\t" "v_add_u32 v28, v28, v10\n\t" "v_add_u32 v27, s38, v27\n\t" \
  "s_mul_i32 s38, s36, 6\n\t" "s_add_u32 s38, s38, s37\n\t" "s_lshl_b32 s38, s38, 8\n\t" "v_lshl_add_u32 v26, s37, 2, v37\n\t" "v_add3_u32 v28, v28, s38, v37\n\t" "s_mul_i32 s38, s34, 768\n\t" \
  "v_min_u32 v29, 0x2f0, v32\n\t" "v_lshl_add_u32 v30, s34, 2, v37\n\t" "v_add3_u32 v29, v29, s38, v37\n\t" "s_lshl1_add_u32 s41, s29, s34\n\t" "v_min_u32 v1, 47, v8\n\t" \
  "s_mul_i32 s38, s41, 6400\n\t" "s_add_u32 s38, s38, 4829184\n\t" "v_mul_u32_u24 v2, 12, v1\n\t" "s_add_u32 s44, s24, s38\n\t" "s_addc_u32 s45, s25, 0\n\t" "s_add_u32 s42, s44, 2560\n\t" \
  "s_addc_u32 s43, s45, 0\n\t" "s_mul_i32 s38, s28, 640\n\t" "s_add_u32 s44, s44, s38\n\t" "s_addc_u32 s45, s45, 0\n\t" "s_waitcnt vmcnt(2)\n\t" "v_cmp_eq_u32 vcc, -1, v152\n\t" \
  "s_cbranch_vccnz .Lso0_%=\n\t" ".Lro0_%=:\n\t" "s_waitcnt lgkmcnt(14)\n\t" "v_mfma_f32_16x16x32_bf16 v[136:139], v[40:43], v[152:155], v[136:139]\n\t" \
  "global_load_dwordx4 v[164:167], v32, s[62:63] offset:1024 sc1\n\t" "ds_read_b64_tr_b16 v[40:41], v35 offset:24576\n\t" "ds_read_b64_tr_b16 v[42:43], v35 offset:26624\n\t" \
  "v_mfma_f32_16x16x32_bf16 v[140:143], v[44:47], v[152:155], v[140:143]\n\t" "ds_read_b64_tr_b16 v[44:45], v20 offset:24576\n\t" "ds_read_b64_tr_b16 v[46:47], v20 offset:26624\n\t" \
  "v_mfma_f32_16x16x32_bf16 v[144:147], v[48:51], v[152:155], v[144:147]\n\t" "ds_read_b64_tr_b16 v[48:49], v21 offset:24576\n\t" "ds_read_b64_tr_b16 v[50:51], v21 offset:26624\n\t" \
  "v_mfma_f32_16x16x32_bf16 v[148:151], v[52:55], v[152:155], v[148:151]\n\t" "ds_read_b64_tr_b16 v[52:53], v22 offset:24576\n\t" "ds_read_b64_tr_b16 v[54:55], v22 offset:26624\n\t" \
  "s_waitcnt vmcnt(2)\n\t" "v_cmp_eq_u32 vcc, -1, v156\n\t" "s_cbranch_vccnz .Lso1_%=\n\t" ".Lro1_%=:\n\t" "v_mfma_f32_16x16x32_bf16 v[136:139], v[56:59], v[156:159], v[136:139]\n\t" \
  "global_load_dwordx4 v[152:155], v32, s[62:63] offset:2048 sc1\n\t" "ds_read_b64_tr_b16 v[56:57], v35 offset:28672\n\t" "ds_read_b64_tr_b16 v[58:59], v35 offset:30720\n\t" \
  "v_mfma_f32_16x16x32_bf16 v[140:143], v[60:63], v[156:159], v[140:143]\n\t" "ds_read_b64_tr_b16 v[60:61], v20 offset:28672\n\t" "ds_read_b64_tr_b16 v[62:63], v20 offset:30720\n\t" \
  "v_mfma_f32_16x16x32_bf16 v[144:147], v[64:67], v[156:159], v[144:147]\n\t" "ds_read_b64_tr_b16 v[64:65], v21 offset:28672\n\t" "ds_read_b64_tr_b16 v[66:67], v21 offset:30720\n\t" \
  "v_mfma_f32_16x16x32_bf16 v[148:151], v[68:71], v[156:159], v[148:151]\n\t" "ds_read_b64_tr_b16 v[68:69], v22 offset:28672\n\t" "ds_read_b64_tr_b16 v[70:71], v22 offset:30720\n\t" \
  "s_waitcnt vmcnt(2)\n\t" "v_cmp_eq_u32 vcc, -1, v160\n\t" "s_cbranch_vccnz .Lso2_%=\n\t" ".Lro2_%=:\n\t" "v_mfma_f32_16x16x32_bf16 v[136:139], v[72:75], v[160:163], v[136:139]\n\t" \
  "global_load_dwordx4 v[156:159], v32, s[64:65] offset:-2048 sc1\n\t" "ds_read_b64_tr_b16 v[72:73], v35 offset:32768\n\t" "ds_read_b64_tr_b16 v[74:75], v35 offset:34816\n\t" \
  "v_mfma_f32_16x16x32_bf16 v[140:143], v[76:79], v[160:163], v[140:143]\n\t" "ds_read_b64_tr_b16 v[76:77], v20 offset:32768\n\t" "ds_read_b64_tr_b16 v[78:79], v20 offset:34816\n\t" \
  "v_mfma_f32_16x16x32_bf16 v[144:147], v[80:83], v[160:163], v[144:147]\n\t" "ds_read_b64_tr_b16 v[80:81], v21 offset:32768\n\t" "ds_read_b64_tr_b16 v[82:83], v21 offset:34816\n\t" \
  "v_mfma_f32_16x16x32_bf16 v[148:151], v[84:87], v[160:163], v[148:151]\n\t" "ds_read_b64_tr_b16 v[84:85], v22 offset:32768\n\t" "ds_read_b64_tr_b16 v[86:87], v22 offset:34816\n\t" \
  "s_waitcnt vmcnt(2)\n\t" "v_cmp_eq_u32 vcc, -1, v164\n\t" "s_cbranch_vccnz .Lso3_%=\n\t" ".Lro3_%=:\n\t" "v_mfma_f32_16x16x32_bf16 v[136:139], v[88:91], v[164:167], v[136:139]\n\t" \
  "global_load_dwordx4 v[160:163], v32, s[64:65] offset:-1024 sc1\n\t" "ds_read_b64_tr_b16 v[88:89], v35 offset:36864\n\t" "ds_read_b64_tr_b16 v[90:91], v35 offset:38912\n\t" \
  "v_mfma_f32_16x16x32_bf16 v[140:143], v[92:95], v[164:167], v[140:143]\n\t" "ds_read_b64_tr_b16 v[92:93], v20 offset:36864\n\t" "ds_read_b64_tr_b16 v[94:95], v20 offset:38912\n\t" \
  "v_mfma_f32_16x16x32_bf16 v[144:147], v[96:99], v[164:167], v[144:147]\n\t" "ds_read_b64_tr_b16 v[96:97], v21 offset:36864\n\t" "ds_read_b64_tr_b16 v[98:99], v21 offset:38912\n\t" \
  "v_mfma_f32_16x16x32_bf16 v[148:151], v[100:103], v[164:167], v[148:151]\n\t" "ds_read_b64_tr_b16 v[100:101], v22 offset:36864\n\t" "ds_read_b64_tr_b16 v[102:103], v22 offset:38912\n\t" \
  "s_waitcnt vmcnt(2)\n\t" "v_cmp_eq_u32 vcc, -1, v152\n\t" "s_cbranch_vccnz .Lso4_%=\n\t" ".Lro4_%=:\n\t" "v_mfma_f32_16x16x32_bf16 v[136:139], v[104:107], v[152:155], v[136:139]\n\t" \
  "global_load_dwordx4 v[164:167], v32, s[64:65] offset:0 sc1\n\t" "s_waitcnt lgkmcnt(14)\n\t" "v_mfma_f32_16x16x32_bf16 v[140:143], v[108:111], v[152:155], v[140:143]\n\t" \
  "v_mfma_f32_16x16x32_bf16 v[144:147], v[112:115], v[152:155], v[144:147]\n\t" "v_mfma_f32_16x16x32_bf16 v[148:151], v[116:119], v[152:155], v[148:151]\n\t" "s_waitcnt vmcnt(2)\n\t" \
  "v_cmp_eq_u32 vcc, -1, v156\n\t" "s_cbranch_vccnz .Lso5_%=\n\t" ".Lro5_%=:\n\t" "v_mfma_f32_16x16x32_bf16 v[136:139], v[120:123], v[156:159], v[136:139]\n\t" \
  "global_load_dwordx4 v[152:155], v32, s[64:65] offset:1024 sc1\n\t" "v_mfma_f32_16x16x32_bf16 v[140:143], v[124:127], v[156:159], v[140:143]\n\t" \
  "v_mfma_f32_16x16x32_bf16 v[144:147], v[128:131], v[156:159], v[144:147]\n\t" "v_mfma_f32_16x16x32_bf16 v[148:151], v[132:135], v[156:159], v[148:151]\n\t" "s_waitcnt vmcnt(2)\n\t" \
  "v_cmp_eq_u32 vcc, -1, v160\n\t" "s_cbranch_vccnz .Lso6_%=\n\t" ".Lro6_%=:\n\t" "v_mfma_f32_16x16x32_bf16 v[136:139], v[40:43], v[160:163], v[136:139]\n\t" \
  "global_load_dwordx4 v[156:159], v32, s[64:65] offset:2048 sc1\n\t" "v_mfma_f32_16x16x32_bf16 v[140:143], v[44:47], v[160:163], v[140:143]\n\t" \
  "v_mfma_f32_16x16x32_bf16 v[144:147], v[48:51], v[160:163], v[144:147]\n\t" "v_mfma_f32_16x16x32_bf16 v[148:151], v[52:55], v[160:163], v[148:151]\n\t" "s_waitcnt vmcnt(2)\n\t" \
  "v_cmp_eq_u32 vcc, -1, v164\n\t" "s_cbranch_vccnz .Lso7_%=\n\t" ".Lro7_%=:\n\t" "v_mfma_f32_16x16x32_bf16 v[136:139], v[56:59], v[164:167], v[136:139]\n\t" \
  "v_mfma_f32_16x16x32_bf16 v[140:143], v[60:63], v[164:167], v[140:143]\n\t" "v_mfma_f32_16x16x32_bf16 v[144:147], v[64:67], v[164:167], v[144:147]\n\t" \
  "v_mfma_f32_16x16x32_bf16 v[148:151], v[68:71], v[164:167], v[148:151]\n\t" "s_waitcnt vmcnt(1)\n\t" "v_cmp_eq_u32 vcc, -1, v152\n\t" "s_cbranch_vccnz .Lso8_%=\n\t" ".Lro8_%=:\n\t" \
  "v_mfma_f32_16x16x32_bf16 v[136:139], v[72:75], v[152:155], v[136:139]\n\t" "s_waitcnt lgkmcnt(12)\n\t" "v_mfma_f32_16x16x32_bf16 v[140:143], v[76:79], v[152:155], v[140:143]\n\t" \
  "s_waitcnt lgkmcnt(10)\n\t" "v_mfma_f32_16x16x32_bf16 v[144:147], v[80:83], v[152:155], v[144:147]\n\t" "s_waitcnt lgkmcnt(8)\n\t" \
  "v_mfma_f32_16x16x32_bf16 v[148:151], v[84:87], v[152:155], v[148:151]\n\t" "s_waitcnt vmcnt(0)\n\t" "v_cmp_eq_u32 vcc, -1, v156\n\t" "s_cbranch_vccnz .Lso9_%=\n\t" ".Lro9_%=:\n\t" \
  "s_waitcnt lgkmcnt(2)\n\t" "v_mfma_f32_16x16x32_bf16 v[144:147], v[96:99], v[156:159], v[144:147]\n\t" "s_waitcnt lgkmcnt(0)\n\t" \
  "v_mfma_f32_16x16x32_bf16 v[148:151], v[100:103], v[156:159], v[148:151]\n\t" "v_mfma_f32_16x16x32_bf16 v[136:139], v[88:91], v[156:159], v[136:139]\n\t" \
  "v_mfma_f32_16x16x32_bf16 v[140:143], v[92:95], v[156:159], v[140:143]\n\t" "s_nop 3\n\t" "ds_write_b128 v24, v[144:147] offset:36864\n\t" "s_nop 0\n\t" \
  "ds_write_b128 v24, v[148:151] offset:37888\n\t" "s_mov_b64 exec, 1\n\t" "ds_add_u32 v26, v38 offset:48\n\t" "s_mov_b64 exec, -1\n\t" ".Lp_%=:\n\t" "ds_read_b32 v39, v26 offset:48\n\t" \
  "ds_read_b128 v[40:43], v25 offset:36864\n\t" "ds_read_b128 v[44:47], v25 offset:37888\n\t" "s_sub_u32 s39, s39, 1\n\t" "s_cmp_lt_i32 s39, 0\n\t" "s_waitcnt lgkmcnt(2)\n\t" \
  "v_readfirstlane_b32 s38, v39\n\t" "s_cselect_b32 s38, 2, s38\n\t" "s_cmp_ge_u32 s38, 2\n\t" "s_waitcnt lgkmcnt(0)\n\t" "s_cbranch_scc0 .Lp_%=\n\t" \
  "v_pk_add_f32 v[136:137], v[136:137], v[40:41]\n\t" "v_pk_add_f32 v[138:139], v[138:139], v[42:43]\n\t" "v_pk_add_f32 v[140:141], v[140:141], v[44:45]\n\t" \
  "v_pk_add_f32 v[142:143], v[142:143], v[46:47]\n\t" "v_cvt_pk_bf16_f32 v12, v136, v137\n\t" "v_cvt_pk_bf16_f32 v13, v138, v139\n\t" "v_cvt_pk_bf16_f32 v14, v140, v141\n\t" \
  "v_cvt_pk_bf16_f32 v15, v142, v143\n\t" "v_dot2_f32_bf16 v40, v12, s40, 0\n\t" "v_dot2_f32_bf16 v41, v12, v12, 0\n\t" "v_dot2_f32_bf16 v42, v12, v16, 0\n\t" "v_dot2_f32_bf16 v40, v13, s40, v40\n\t" \
  "v_dot2_f32_bf16 v41, v13, v13, v41\n\t" "v_dot2_f32_bf16 v42, v13, v17, v42\n\t" "v_dot2_f32_bf16 v40, v14, s40, v40\n\t" "v_dot2_f32_bf16 v41, v14, v14, v41\n\t" \
  "v_dot2_f32_bf16 v42, v14, v18, v42\n\t" "v_dot2_f32_bf16 v40, v15, s40, v40\n\t" "v_dot2_f32_bf16 v41, v15, v15, v41\n\t" "v_dot2_f32_bf16 v43, v15, v19, v42\n\t" \
  "v_dot2_f32_bf16 v42, v15, v19, v42\n\t" "ds_write2_b64 v27, v[12:13], v[14:15] offset1:4\n\t" "s_nop 0\n\t" "v_permlane32_swap_b32 v40, v41\n\t" "v_permlane32_swap_b32 v42, v43\n\t" \
  "v_add_f32 v40, v40, v41\n\t" "v_add_f32 v42, v42, v43\n\t" "s_nop 1\n\t" "v_permlane16_swap_b32 v40, v42\n\t" "v_add_f32 v40, v40, v42\n\t" "ds_write_b32 v28, v40 offset:256\n\t" \
  "s_mov_b64 exec, 1\n\t" "ds_add_rtn_u32 v39, v30, v38 offset:16\n\t" "s_mov_b64 exec, -1\n\t" "ds_read_b128 v[44:47], v29 offset:256\n\t" "ds_read_b128 v[48:51], v29 offset:1792\n\t" \
  "ds_read_b32 v52, v37 offset:40\n\t" "s_waitcnt lgkmcnt(3)\n\t" "v_readfirstlane_b32 s38, v39\n\t" "s_waitcnt lgkmcnt(0)\n\t" "s_cmp_lg_u32 s38, 5\n\t" "s_setprio 0\n\t" "s_cbranch_scc1 .Lnk_%=\n\t" \
  "s_setprio 3\n\t" "v_pk_add_f32 v[44:45], v[44:45], v[48:49]\n\t" "v_add_f32 v46, v46, v50\n\t" "s_mov_b32 exec_hi, 0xffff\n\t" "global_store_dwordx3 v2, v[44:46], s[44:45] sc1\n\t" \
  "s_mov_b32 exec_hi, -1\n\t" "s_memrealtime s[76:77]\n\t" "s_mul_i32 s50, s34, 48\n\t" "v_lshl_add_u32 v4, s41, 6, v1\n\t" "v_add_u32 v1, s50, v1\n\t" "s_cmp_eq_u32 s29, 0\n\t" \
  "s_cselect_b32 s51, 6, 7\n\t" "v_cmp_le_u32 vcc, s51, v1\n\t" "s_mov_b32 s51, 155648\n\t" "v_lshl_add_u32 v1, v1, 3, s51\n\t" "v_mov_b32 v3, 0xfffff000\n\t" "s_lshl_b32 s51, s41, 7\n\t" \
  "v_cndmask_b32 v3, 0, v3, vcc\n\t" "v_lshl_add_u32 v5, v9, 3, s51\n\t" "v_lshlrev_b32 v7, 2, v8\n\t" "s_mul_i32 s50, s50, 144\n\t" "v_and_b32 v6, 0x7c, v7\n\t" "v_add_u32 v6, s50, v6\n\t" \
  "s_mul_i32 s50, s28, 6144\n\t" "s_add_u32 s50, s50, 5302784\n\t" "s_add_u32 s46, s24, s50\n\t" "s_addc_u32 s47, s25, 0\n\t" "s_mul_i32 s50, s41, 10\n\t" "s_add_u32 s50, s50, s28\n\t" \
  "s_lshl_b32 s50, s50, 8\n\t" "s_add_u32 s50, s50, 5123584\n\t" "s_add_u32 s48, s24, s50\n\t" "s_addc_u32 s49, s25, 0\n\t" "s_mov_b32 s50, 0x3acccccd\n\t" ".Ltk_%=:\n\t" "s_memrealtime s[78:79]\n\t" \
  "s_waitcnt lgkmcnt(0)\n\t" "s_sub_u32 s78, s78, s76\n\t" "s_cmp_lt_u32 s78, 40\n\t" "s_cbranch_scc1 .Ltk_%=\n\t" ".Lkp_%=:\n\t" "global_load_dwordx3 v[60:62], v2, s[42:43] offset:-2560 sc1\n\t" \
  "global_load_dwordx3 v[64:66], v2, s[42:43] offset:-1920 sc1\n\t" "global_load_dwordx3 v[68:70], v2, s[42:43] offset:-1280 sc1\n\t" "global_load_dwordx3 v[72:74], v2, s[42:43] offset:-640 sc1\n\t" \
  "global_load_dwordx3 v[76:78], v2, s[42:43] offset:0 sc1\n\t" "global_load_dwordx3 v[80:82], v2, s[42:43] offset:640 sc1\n\t" "global_load_dwordx3 v[84:86], v2, s[42:43] offset:1280 sc1\n\t" \
  "global_load_dwordx3 v[88:90], v2, s[42:43] offset:1920 sc1\n\t" "global_load_dwordx3 v[92:94], v2, s[42:43] offset:2560 sc1\n\t" "global_load_dwordx3 v[96:98], v2, s[42:43] offset:3200 sc1\n\t" \
  "s_sub_u32 s39, s39, 1\n\t" "s_cmp_lt_i32 s39, 0\n\t" "s_waitcnt vmcnt(0)\n\t" "v_pk_add_f32 v[60:61], v[60:61], v[64:65]\n\t" "v_add_f32 v62, v62, v66\n\t" \
  "v_pk_add_f32 v[68:69], v[68:69], v[72:73]\n\t" "v_add_f32 v70, v70, v74\n\t" "v_pk_add_f32 v[76:77], v[76:77], v[80:81]\n\t" "v_add_f32 v78, v78, v82\n\t" \
  "v_pk_add_f32 v[84:85], v[84:85], v[88:89]\n\t" "v_add_f32 v86, v86, v90\n\t" "v_pk_add_f32 v[92:93], v[92:93], v[96:97]\n\t" "v_add_f32 v94, v94, v98\n\t" \
  "v_pk_add_f32 v[60:61], v[60:61], v[68:69]\n\t" "v_add_f32 v62, v62, v70\n\t" "v_pk_add_f32 v[76:77], v[76:77], v[84:85]\n\t" "v_add_f32 v78, v78, v86\n\t" \
  "v_pk_add_f32 v[60:61], v[60:61], v[92:93]\n\t" "v_add_f32 v62, v62, v94\n\t" "v_pk_add_f32 v[60:61], v[60:61], v[76:77]\n\t" "v_add_f32 v62, v62, v78\n\t" "v_mul_f32 v60, s50, v60\n\t" \
  "v_mul_f32 v63, v60, v60\n\t" "v_fma_f32 v61, -v60, v52, v61\n\t" "v_fma_f32 v62, v62, s50, -v63\n\t" "s_cbranch_scc1 .Lkq_%=\n\t" "v_cmp_u_f32 vcc, v61, v62\n\t" "s_cbranch_vccnz .Lkp_%=\n\t" \
  ".Lkq_%=:\n\t" "v_max_f32 v62, 0, v62\n\t" "v_add_f32 v63, 0x3727c5ac, v62\n\t" "v_rsq_f32 v63, v63\n\t" "v_cvt_pk_bf16_f32 v66, v62, v60\n\t" "v_mul_f32 v61, v61, v63\n\t" \
  "v_ashrrev_i32 v63, 31, v61\n\t" "v_or_b32 v63, 0x80000000, v63\n\t" "v_xor_b32 v61, v61, v63\n\t" "v_and_or_b32 v61, v61, v3, v4\n\t" "v_lshlrev_b32 v67, 16, v66\n\t" \
  "v_and_b32 v66, 0xffff0000, v66\n\t" "v_max_u32_dpp v64, v61, v61 quad_perm:[1,0,3,2] row_mask:0xf bank_mask:0xf\n\t" "v_add_f32 v67, 0x3727c5ac, v67\n\t" "v_cvt_pk_bf16_f32 v67, v67, v67\n\t" \
  "v_max_u32_dpp v64, v64, v64 quad_perm:[2,3,0,1] row_mask:0xf bank_mask:0xf\n\t" "v_lshlrev_b32 v67, 16, v67\n\t" "v_sqrt_f32 v67, v67\n\t" \
  "v_max_u32_dpp v64, v64, v64 row_half_mirror row_mask:0xf bank_mask:0xf\n\t" "s_nop 0\n\t" "v_cvt_pk_bf16_f32 v67, v67, v67\n\t" \
  "v_max_u32_dpp v64, v64, v64 row_mirror row_mask:0xf bank_mask:0xf\n\t" "v_lshlrev_b32 v67, 16, v67\n\t" "v_rcp_f32 v67, v67\n\t" \
  "v_max_u32_dpp v64, v64, v64 row_bcast:15 row_mask:0xa bank_mask:0xf\n\t" "s_nop 1\n\t" "v_max_u32_dpp v64, v64, v64 row_bcast:31 row_mask:0xc bank_mask:0xf\n\t" "v_mov_b32 v65, v64\n\t" \
  "s_mov_b32 exec_lo, 0\n\t" "s_mov_b32 exec_hi, 0xffff0000\n\t" "global_store_dwordx2 v5, v[64:65], s[46:47] sc1\n\t" "s_mov_b64 exec, -1\n\t" "v_readlane_b32 s50, v64, 63\n\t" \
  "s_and_b32 s51, s50, 63\n\t" "s_mul_i32 s52, s51, 144\n\t" "v_add_u32 v6, s52, v6\n\t" "s_or_b32 s50, s50, 0x1000\n\t" "ds_read_b32 v68, v6 offset:0\n\t" "v_mov_b32 v64, s50\n\t" \
  "ds_write_b64 v1, v[66:67]\n\t" "ds_write_b32 v30, v64 offset:24\n\t" "v_readlane_b32 s52, v66, s51\n\t" "v_readlane_b32 s53, v67, s51\n\t" "s_waitcnt lgkmcnt(2)\n\t" \
  "v_lshlrev_b32 v70, 16, v68\n\t" "v_and_b32 v71, 0xffff0000, v68\n\t" "v_subrev_f32 v70, s52, v70\n\t" "v_subrev_f32 v71, s52, v71\n\t" "v_cvt_pk_bf16_f32 v71, v70, v71\n\t" \
  "v_lshlrev_b32 v70, 16, v71\n\t" "v_and_b32 v71, 0xffff0000, v71\n\t" "v_mul_f32 v70, s53, v70\n\t" "v_mul_f32 v71, s53, v71\n\t" "s_mov_b32 exec_hi, 0\n\t" "v_cvt_pk_bf16_f32 v68, v70, v71\n\t" \
  "s_mov_b32 exec_hi, -1\n\t" "global_store_dword v7, v68, s[48:49] sc1\n\t" "s_setprio 0\n\t" ".Lnk_%=:\n\t" "v_lshrrev_b32 v113, 3, v8\n\t" "s_lshl_b32 s40, s26, 3\n\t" "v_add_u32 v2, s40, v113\n\t" \
  "s_mov_b32 s76, 155648\n\t" "v_lshl_add_u32 v1, v2, 3, s76\n\t" "s_cmp_eq_u32 s29, 0\n\t" "s_cselect_b32 s76, 6, 7\n\t" "v_cmp_le_u32 s[72:73], s76, v2\n\t" "v_add_u32 v2, s30, v2\n\t" \
  "v_sub_u32 v2, v2, 6 clamp\n\t" "v_lshrrev_b32 v3, 10, v2\n\t" "v_lshl_or_b32 v2, v2, 1, v3\n\t" "v_and_b32 v2, 0x7ff, v2\n\t" "v_and_b32 v3, 0x70, v32\n\t" "s_lshl_b32 s76, s28, 7\n\t" \
  "v_add_u32 v3, s76, v3\n\t" "s_movk_i32 s76, 1280\n\t" "v_mad_u32_u24 v33, v2, s76, v3\n\t" "s_mul_i32 s76, s26, 1152\n\t" "v_mad_u32_u24 v3, v113, 16, v32\n\t" "v_add_u32 v3, s76, v3\n\t" \
  "s_lshl_b32 s76, s35, 3\n\t" "v_add_u32 v113, s76, v113\n\t" "s_branch .Lw1_%=\n\t" ".Lw_%=:\n\t" "s_sleep 2\n\t" ".Lw1_%=:\n\t" "ds_read_b32 v112, v30 offset:24\n\t" \
  "ds_read_b64 v[110:111], v1\n\t" "ds_read_b128 v[12:15], v3 offset:0\n\t" "s_sub_u32 s39, s39, 1\n\t" "s_cmp_lt_i32 s39, 0\n\t" "s_waitcnt lgkmcnt(0)\n\t" "v_readfirstlane_b32 s66, v112\n\t" \
  "s_cselect_b32 s66, 1, s66\n\t" "s_cmp_eq_u32 s66, 0\n\t" "s_cbranch_scc1 .Lw_%=\n\t" "s_and_b32 s76, s66, 63\n\t" "v_cmp_eq_u32 s[68:69], s76, v113\n\t" "v_lshlrev_b32 v120, 16, v12\n\t" \
  "v_and_b32 v121, 0xffff0000, v12\n\t" "v_lshlrev_b32 v122, 16, v13\n\t" "v_and_b32 v123, 0xffff0000, v13\n\t" "v_lshlrev_b32 v124, 16, v14\n\t" "v_and_b32 v125, 0xffff0000, v14\n\t" \
  "v_lshlrev_b32 v126, 16, v15\n\t" "v_and_b32 v127, 0xffff0000, v15\n\t" "v_pk_add_f32 v[128:129], v[120:121], v[110:111] op_sel_hi:[1,0] neg_lo:[0,1] neg_hi:[0,1]\n\t" \
  "v_pk_add_f32 v[130:131], v[122:123], v[110:111] op_sel_hi:[1,0] neg_lo:[0,1] neg_hi:[0,1]\n\t" "v_pk_add_f32 v[132:133], v[124:125], v[110:111] op_sel_hi:[1,0] neg_lo:[0,1] neg_hi:[0,1]\n\t" \
  "v_pk_add_f32 v[134:135], v[126:127], v[110:111] op_sel_hi:[1,0] neg_lo:[0,1] neg_hi:[0,1]\n\t" "v_cvt_pk_bf16_f32 v129, v128, v129\n\t" "v_cvt_pk_bf16_f32 v131, v130, v131\n\t" \
  "v_cvt_pk_bf16_f32 v133, v132, v133\n\t" "v_cvt_pk_bf16_f32 v135, v134, v135\n\t" "v_lshlrev_b32 v128, 16, v129\n\t" "v_and_b32 v129, 0xffff0000, v129\n\t" "v_lshlrev_b32 v130, 16, v131\n\t" \
  "v_and_b32 v131, 0xffff0000, v131\n\t" "v_lshlrev_b32 v132, 16, v133\n\t" "v_and_b32 v133, 0xffff0000, v133\n\t" "v_lshlrev_b32 v134, 16, v135\n\t" "v_and_b32 v135, 0xffff0000, v135\n\t" \
  "v_pk_mul_f32 v[128:129], v[128:129], v[110:111] op_sel:[0,1] op_sel_hi:[1,1]\n\t" "v_pk_mul_f32 v[130:131], v[130:131], v[110:111] op_sel:[0,1] op_sel_hi:[1,1]\n\t" \
  "v_pk_mul_f32 v[132:133], v[132:133], v[110:111] op_sel:[0,1] op_sel_hi:[1,1]\n\t" "v_pk_mul_f32 v[134:135], v[134:135], v[110:111] op_sel:[0,1] op_sel_hi:[1,1]\n\t" \
  "v_cvt_pk_bf16_f32 v129, v128, v129\n\t" "v_cvt_pk_bf16_f32 v131, v130, v131\n\t" "v_cvt_pk_bf16_f32 v133, v132, v133\n\t" "v_cvt_pk_bf16_f32 v135, v134, v135\n\t" "v_lshlrev_b32 v128, 16, v129\n\t" \
  "v_and_b32 v129, 0xffff0000, v129\n\t" "v_lshlrev_b32 v130, 16, v131\n\t" "v_and_b32 v131, 0xffff0000, v131\n\t" "v_lshlrev_b32 v132, 16, v133\n\t" "v_and_b32 v133, 0xffff0000, v133\n\t" \
  "v_lshlrev_b32 v134, 16, v135\n\t" "v_and_b32 v135, 0xffff0000, v135\n\t" "v_pk_add_f32 v[128:129], v[128:129], v[120:121]\n\t" "v_pk_add_f32 v[130:131], v[130:131], v[122:123]\n\t" \
  "v_pk_add_f32 v[132:133], v[132:133], v[124:125]\n\t" "v_pk_add_f32 v[134:135], v[134:135], v[126:127]\n\t" "v_cvt_pk_bf16_f32 v116, v128, v129\n\t" "v_cvt_pk_bf16_f32 v117, v130, v131\n\t" \
  "v_cvt_pk_bf16_f32 v118, v132, v133\n\t" "v_cvt_pk_bf16_f32 v119, v134, v135\n\t" "s_sleep 16\n\t" "s_andn2_b64 exec, s[72:73], s[68:69]\n\t" "s_cbranch_scc0 .Ln1_%=\n\t" \
  "global_store_dwordx4 v33, v[116:119], s[22:23] sc0 sc1 nt\n\t" ".Ln1_%=:\n\t" "s_mov_b64 exec, -1\n\t" "s_cmp_eq_u32 s26, 11\n\t" "s_cbranch_scc1 .Lrz_%=\n\t" "s_cmp_eq_u64 s[68:69], 0\n\t" \
  "s_cbranch_scc1 .Lend_%=\n\t" ".Lb6_%=:\n\t" "s_waitcnt lgkmcnt(0)\n\t" "s_barrier\n\t" "ds_read_b32 v39, v37 offset:32\n\t" "s_waitcnt lgkmcnt(0)\n\t" "v_readfirstlane_b32 s76, v39\n\t" \
  "s_xor_b32 s76, s76, s66\n\t" "s_and_b32 s76, s76, 4095\n\t" "s_cbranch_scc0 .Lend_%=\n\t" "s_and_b64 exec, s[72:73], s[68:69]\n\t" "s_cbranch_scc0 .Ln2_%=\n\t" \
  "global_store_dwordx4 v33, v[116:119], s[22:23] sc0 sc1 nt\n\t" ".Ln2_%=:\n\t" "s_mov_b64 exec, -1\n\t" "s_branch .Lend_%=\n\t" ".Lrz_%=:\n\t" "s_sleep 35\n\t" ".Lrt_%=:\n\t" \
  "s_mul_i32 s40, s28, 6144\n\t" "s_add_u32 s40, s40, 5302784\n\t" "v_lshlrev_b32 v2, 7, v8\n\t" "s_add_u32 s40, s24, s40\n\t" "s_addc_u32 s41, s25, 0\n\t" "v_mov_b32 v40, -1\n\t" \
  "v_mov_b32 v41, -1\n\t" "v_mov_b32 v42, -1\n\t" "s_mov_b32 exec_hi, 0x3fff\n\t" "global_load_dword v40, v2, s[40:41] sc1\n\t" "s_sleep 2\n\t" "global_load_dword v41, v2, s[40:41] sc1\n\t" \
  "s_sleep 2\n\t" "global_load_dword v42, v2, s[40:41] sc1\n\t" "s_mov_b32 exec_hi, -1\n\t" "v_mov_b32 v43, 0xfc0\n\t" "s_mov_b32 s42, 0\n\t" "s_mov_b32 s43, 0xffff0000\n\t" \
  "v_add_u32 v3, 64, v37\n\t" "v_cndmask_b32 v3, v3, v37, s[42:43]\n\t" "s_mov_b32 s43, 0xffffc000\n\t" ".Lrl_%=:\n\t" "s_waitcnt vmcnt(2)\n\t" "v_cmp_ne_u32 vcc, -1, v40\n\t" \
  "s_or_b64 s[42:43], s[42:43], vcc\n\t" "s_cmp_eq_u64 s[42:43], -1\n\t" "v_cndmask_b32 v43, v43, v40, vcc\n\t" "s_cselect_b32 s44, 1, 0\n\t" "v_mov_b32 v45, s44\n\t" \
  "v_max_u32_dpp v44, v43, v43 quad_perm:[1,0,3,2] row_mask:0xf bank_mask:0xf\n\t" "s_nop 1\n\t" "v_max_u32_dpp v44, v44, v44 quad_perm:[2,3,0,1] row_mask:0xf bank_mask:0xf\n\t" "s_nop 1\n\t" \
  "v_max_u32_dpp v44, v44, v44 row_half_mirror row_mask:0xf bank_mask:0xf\n\t" "s_nop 1\n\t" "v_max_u32_dpp v44, v44, v44 row_mirror row_mask:0xf bank_mask:0xf\n\t" "s_nop 1\n\t" \
  "v_max_u32_dpp v44, v44, v44 row_bcast:15 row_mask:0xa bank_mask:0xf\n\t" "s_nop 1\n\t" "v_max_u32_dpp v44, v44, v44 row_bcast:31 row_mask:0xc bank_mask:0xf\n\t" \
  "ds_write_b64 v3, v[44:45] offset:32\n\t" "s_cbranch_scc1 .Lre_%=\n\t" "s_cmp_lg_u32 s75, 0\n\t" "s_cbranch_scc1 .Lrz0_%=\n\t" "s_sleep 5\n\t" ".Lrz0_%=:\n\t" "s_not_b64 exec, s[42:43]\n\t" \
  "global_load_dword v40, v2, s[40:41] sc1\n\t" "s_mov_b64 exec, -1\n\t" "s_waitcnt vmcnt(2)\n\t" "v_cmp_ne_u32 vcc, -1, v41\n\t" "s_or_b64 s[42:43], s[42:43], vcc\n\t" "s_cmp_eq_u64 s[42:43], -1\n\t" \
  "v_cndmask_b32 v43, v43, v41, vcc\n\t" "s_cselect_b32 s44, 1, 0\n\t" "v_mov_b32 v45, s44\n\t" "v_max_u32_dpp v44, v43, v43 quad_perm:[1,0,3,2] row_mask:0xf bank_mask:0xf\n\t" "s_nop 1\n\t" \
  "v_max_u32_dpp v44, v44, v44 quad_perm:[2,3,0,1] row_mask:0xf bank_mask:0xf\n\t" "s_nop 1\n\t" "v_max_u32_dpp v44, v44, v44 row_half_mirror row_mask:0xf bank_mask:0xf\n\t" "s_nop 1\n\t" \
  "v_max_u32_dpp v44, v44, v44 row_mirror row_mask:0xf bank_mask:0xf\n\t" "s_nop 1\n\t" "v_max_u32_dpp v44, v44, v44 row_bcast:15 row_mask:0xa bank_mask:0xf\n\t" "s_nop 1\n\t" \
  "v_max_u32_dpp v44, v44, v44 row_bcast:31 row_mask:0xc bank_mask:0xf\n\t" "ds_write_b64 v3, v[44:45] offset:32\n\t" "s_cbranch_scc1 .Lre_%=\n\t" "s_cmp_lg_u32 s75, 0\n\t" \
  "s_cbranch_scc1 .Lrz1_%=\n\t" "s_sleep 5\n\t" ".Lrz1_%=:\n\t" "s_not_b64 exec, s[42:43]\n\t" "global_load_dword v41, v2, s[40:41] sc1\n\t" "s_mov_b64 exec, -1\n\t" "s_waitcnt vmcnt(2)\n\t" \
  "v_cmp_ne_u32 vcc, -1, v42\n\t" "s_or_b64 s[42:43], s[42:43], vcc\n\t" "s_cmp_eq_u64 s[42:43], -1\n\t" "v_cndmask_b32 v43, v43, v42, vcc\n\t" "s_cselect_b32 s44, 1, 0\n\t" "v_mov_b32 v45, s44\n\t" \
  "v_max_u32_dpp v44, v43, v43 quad_perm:[1,0,3,2] row_mask:0xf bank_mask:0xf\n\t" "s_nop 1\n\t" "v_max_u32_dpp v44, v44, v44 quad_perm:[2,3,0,1] row_mask:0xf bank_mask:0xf\n\t" "s_nop 1\n\t" \
  "v_max_u32_dpp v44, v44, v44 row_half_mirror row_mask:0xf bank_mask:0xf\n\t" "s_nop 1\n\t" "v_max_u32_dpp v44, v44, v44 row_mirror row_mask:0xf bank_mask:0xf\n\t" "s_nop 1\n\t" \
  "v_max_u32_dpp v44, v44, v44 row_bcast:15 row_mask:0xa bank_mask:0xf\n\t" "s_nop 1\n\t" "v_max_u32_dpp v44, v44, v44 row_bcast:31 row_mask:0xc bank_mask:0xf\n\t" \
  "ds_write_b64 v3, v[44:45] offset:32\n\t" "s_cbranch_scc1 .Lre_%=\n\t" "s_cmp_lg_u32 s75, 0\n\t" "s_cbranch_scc1 .Lrz2_%=\n\t" "s_sleep 5\n\t" ".Lrz2_%=:\n\t" "s_not_b64 exec, s[42:43]\n\t" \
  "global_load_dword v42, v2, s[40:41] sc1\n\t" "s_mov_b64 exec, -1\n\t" "s_sub_u32 s39, s39, 1\n\t" "s_cmp_lt_i32 s39, 0\n\t" "s_cbranch_scc0 .Lrl_%=\n\t" ".Lre_%=:\n\t" "s_cmp_eq_u32 s75, 0\n\t" \
  "s_cbranch_scc1 .Lb6_%=\n\t" "s_waitcnt lgkmcnt(0)\n\t" "s_barrier\n\t" "s_barrier\n\t" "s_cmp_lg_u32 s29, 0\n\t" "s_cbranch_scc1 .Lend_%=\n\t" "v_mov_b32 v1, 0\n\t" "s_mov_b64 exec, 1\n\t" \
  "global_store_dwordx2 v1, v[16:17], s[24:25] sc1\n\t" "s_mov_b64 exec, -1\n\t" "s_branch .Lend_%=\n\t" ".Lsp_%=:\n\t" "v_mov_b32 v1, 0\n\t" "s_add_u32 s40, s28, 16\n\t" \
  "s_sub_u32 s41, %[bid], 240\n\t" "s_cselect_b32 s29, s40, s41\n\t" "v_lshlrev_b32 v32, 4, v8\n\t" "v_and_b32 v9, 15, v8\n\t" "v_mov_b32 v37, 156416\n\t" "v_mov_b32 v38, 1\n\t" \
  "v_mov_b32 v44, -1\n\t" "v_mov_b32 v45, -1\n\t" "v_mov_b32 v46, -1\n\t" "v_mov_b32 v47, -1\n\t" "s_mov_b32 s75, 1\n\t" "s_mov_b32 s39, 400000\n\t" "s_add_u32 s45, s29, 4\n\t" \
  "s_lshr_b32 s45, s45, 3\n\t" "s_sub_u32 s45, s29, s45\n\t" "s_and_b32 s48, s29, 7\n\t" "s_mul_i32 s46, s45, 232\n\t" "s_movk_i32 s47, 231\n\t" "s_mul_i32 s40, s45, 764\n\t" \
  "s_sub_u32 s40, s40, 11172\n\t" "s_cmp_ge_u32 s29, 24\n\t" "s_cselect_b32 s46, s40, s46\n\t" "s_cselect_b32 s47, 763, s47\n\t" "v_min_u32 v5, s47, v0\n\t" "v_add_u32 v5, s46, v5\n\t" \
  "v_min_u32 v5, 6399, v5\n\t" "v_lshlrev_b32 v5, 7, v5\n\t" "s_lshr_b32 s47, s47, 6\n\t" "s_mul_i32 s40, s45, 12\n\t" "s_add_u32 s40, s40, s26\n\t" "s_lshl_b32 s41, s40, 10\n\t" \
  "s_add_u32 s41, s41, 2002944\n\t" "s_lshl_b32 s44, s45, 7\n\t" "s_add_u32 s44, s44, 4096\n\t" "v_lshl_add_u32 v4, v9, 3, s44\n\t" "s_waitcnt lgkmcnt(0)\n\t" \
  "global_load_dwordx2 v[40:41], v1, s[24:25] sc1\n\t" "s_add_u32 s42, s24, s41\n\t" "s_addc_u32 s43, s25, 0\n\t" "v_lshl_add_u32 v3, v8, 2, v37\n\t" "s_movk_i32 s44, 0xfc0\n\t" "v_mov_b32 v2, 0\n\t" \
  "v_writelane_b32 v2, s44, 8\n\t" "s_mov_b32 exec_hi, 0\n\t" "ds_write_b32 v3, v2\n\t" "s_mov_b32 exec_hi, -1\n\t" "s_mul_i32 s50, s29, 81920\n\t" "s_mul_i32 s54, s33, 20480\n\t" \
  "s_add_u32 s50, s50, s54\n\t" "s_add_u32 s54, s54, 65536\n\t" "s_add_u32 s52, s20, s50\n\t" "s_addc_u32 s53, s21, 0\n\t" "s_memrealtime s[56:57]\n\t" "s_mov_b32 m0, 163072\n\t" \
  "s_cmp_eq_u32 s48, 3\n\t" "s_cbranch_scc1 .Lnf_%=\n\t" ".Lts2_%=:\n\t" "s_sleep 2\n\t" "s_memrealtime s[78:79]\n\t" "s_waitcnt lgkmcnt(0)\n\t" "s_sub_u32 s78, s78, s56\n\t" \
  "s_cmp_lt_u32 s78, 75\n\t" "s_cbranch_scc1 .Lts2_%=\n\t" ".Lpf0_%=:\n\t" "global_store_dwordx4 v32, v[44:47], s[42:43] sc1\n\t" "s_add_u32 s42, s42, 282624\n\t" "s_addc_u32 s43, s43, 0\n\t" \
  "s_add_u32 s40, s40, 276\n\t" "s_cmp_lt_u32 s40, 2760\n\t" "s_cbranch_scc1 .Lpf0_%=\n\t" ".Lpf1_%=:\n\t" "global_store_dwordx4 v32, v[44:47], s[42:43] sc1\n\t" "s_add_u32 s42, s42, 282624\n\t" \
  "s_addc_u32 s43, s43, 0\n\t" "s_add_u32 s40, s40, 276\n\t" "s_cmp_lt_u32 s40, 3283\n\t" "s_cbranch_scc1 .Lpf1_%=\n\t" "s_waitcnt vmcnt(0)\n\t" "v_add_co_u32 v16, vcc, 1, v40\n\t" \
  "s_cmp_lg_u32 s26, 0\n\t" "s_nop 0\n\t" "v_addc_co_u32 v17, vcc, 0, v41, vcc\n\t" "s_barrier\n\t" "s_cbranch_scc1 .Lnf1_%=\n\t" "global_store_dwordx2 v4, v[16:17], s[24:25] sc1\n\t" ".Lnf1_%=:\n\t" \
  "s_cmp_gt_u32 s26, s47\n\t" "s_cbranch_scc1 .Lnt_%=\n\t" "global_load_lds_dword v5, s[12:13]\n\t" ".Lnt_%=:\n\t" ".Lnf_%=:\n\t" "s_cmp_ge_u32 s29, 24\n\t" "s_cbranch_scc1 .Lend_%=\n\t" \
  "s_cmp_ge_u32 s26, 8\n\t" "s_cbranch_scc1 .Lsq_%=x\n\t" "v_lshrrev_b32 v1, 3, v8\n\t" "v_and_b32 v2, 6, v1\n\t" "v_xor_b32 v2, v2, v8\n\t" "v_and_b32 v2, 7, v2\n\t" "v_mul_u32_u24 v1, 3072, v1\n\t" \
  "v_lshl_add_u32 v1, v2, 4, v1\n\t" "v_add_u32 v17, 23552, v1\n\t" "v_add_u32 v18, 47104, v1\n\t" "v_add_u32 v19, 70656, v1\n\t" "s_cmp_ge_u32 s26, 4\n\t" \
  "s_cselect_b64 s[42:43], s[18:19], s[16:17]\n\t" "s_mul_i32 s40, s33, 491520\n\t" "s_lshl_b32 s41, s29, 7\n\t" "s_add_u32 s40, s40, s41\n\t" "s_add_u32 s42, s42, s40\n\t" \
  "s_addc_u32 s43, s43, 0\n\t" "s_lshl_b32 s44, s26, 13\n\t" "s_add_u32 m0, s44, 0\n\t" "s_nop 0\n\t" "global_load_lds_dwordx4 v1, s[42:43] offset:0 nt\n\t" \
  "global_load_lds_dwordx4 v17, s[42:43] offset:1024 nt\n\t" "global_load_lds_dwordx4 v18, s[42:43] offset:2048 nt\n\t" "global_load_lds_dwordx4 v19, s[42:43] offset:3072 nt\n\t" \
  "s_add_u32 s42, s42, 98304\n\t" "s_addc_u32 s43, s43, 0\n\t" "s_add_u32 m0, s44, 4096\n\t" "s_nop 0\n\t" "global_load_lds_dwordx4 v1, s[42:43] offset:0 nt\n\t" \
  "global_load_lds_dwordx4 v17, s[42:43] offset:1024 nt\n\t" "global_load_lds_dwordx4 v18, s[42:43] offset:2048 nt\n\t" "global_load_lds_dwordx4 v19, s[42:43] offset:3072 nt\n\t" \
  "s_add_u32 s42, s42, 98304\n\t" "s_addc_u32 s43, s43, 0\n\t" "v_lshrrev_b32 v10, 2, v8\n\t" "v_lshlrev_b32 v3, 2, v8\n\t" "v_and_b32 v4, 0x60, v3\n\t" "v_and_b32 v3, 12, v3\n\t" \
  "v_lshl_add_u32 v2, v10, 7, v4\n\t" "v_lshl_add_u32 v2, v3, 1, v2\n\t" "v_mul_u32_u24 v4, 1280, v10\n\t" "v_lshl_add_u32 v4, v3, 1, v4\n\t" "s_mul_i32 s40, s26, 160\n\t" \
  "s_add_u32 s40, s40, 65536\n\t" "v_add_u32 v4, s40, v4\n\t" "v_and_b32 v10, 12, v10\n\t" "v_xor_b32 v12, 0, v2\n\t" "v_add_u32 v12, s44, v12\n\t" "v_xor_b32 v13, 32, v2\n\t" \
  "v_add_u32 v13, s44, v13\n\t" "v_xor_b32 v14, 64, v2\n\t" "v_add_u32 v14, s44, v14\n\t" "v_xor_b32 v15, 96, v2\n\t" "v_add_u32 v15, s44, v15\n\t" "s_waitcnt vmcnt(4)\n\t" \
  "ds_read_b64_tr_b16 v[40:41], v12 offset:0\n\t" "ds_read_b64_tr_b16 v[42:43], v12 offset:2048\n\t" "ds_read_b64_tr_b16 v[44:45], v13 offset:0\n\t" "ds_read_b64_tr_b16 v[46:47], v13 offset:2048\n\t" \
  "ds_read_b64_tr_b16 v[48:49], v14 offset:0\n\t" "ds_read_b64_tr_b16 v[50:51], v14 offset:2048\n\t" "ds_read_b64_tr_b16 v[52:53], v15 offset:0\n\t" "ds_read_b64_tr_b16 v[54:55], v15 offset:2048\n\t" \
  "s_waitcnt lgkmcnt(0)\n\t" "s_add_u32 m0, s44, 0\n\t" "s_nop 0\n\t" "global_load_lds_dwordx4 v1, s[42:43] offset:0 nt\n\t" "global_load_lds_dwordx4 v17, s[42:43] offset:1024 nt\n\t" \
  "global_load_lds_dwordx4 v18, s[42:43] offset:2048 nt\n\t" "global_load_lds_dwordx4 v19, s[42:43] offset:3072 nt\n\t" "s_add_u32 s42, s42, 98304\n\t" "s_addc_u32 s43, s43, 0\n\t" \
  "s_waitcnt vmcnt(4)\n\t" "ds_read_b64_tr_b16 v[56:57], v12 offset:4096\n\t" "ds_read_b64_tr_b16 v[58:59], v12 offset:6144\n\t" "ds_read_b64_tr_b16 v[60:61], v13 offset:4096\n\t" \
  "ds_read_b64_tr_b16 v[62:63], v13 offset:6144\n\t" "ds_read_b64_tr_b16 v[64:65], v14 offset:4096\n\t" "ds_read_b64_tr_b16 v[66:67], v14 offset:6144\n\t" \
  "ds_read_b64_tr_b16 v[68:69], v15 offset:4096\n\t" "ds_read_b64_tr_b16 v[70:71], v15 offset:6144\n\t" "s_waitcnt lgkmcnt(0)\n\t" "s_add_u32 m0, s44, 4096\n\t" "s_nop 0\n\t" \
  "global_load_lds_dwordx4 v1, s[42:43] offset:0 nt\n\t" "global_load_lds_dwordx4 v17, s[42:43] offset:1024 nt\n\t" "global_load_lds_dwordx4 v18, s[42:43] offset:2048 nt\n\t" \
  "global_load_lds_dwordx4 v19, s[42:43] offset:3072 nt\n\t" "s_add_u32 s42, s42, 98304\n\t" "s_addc_u32 s43, s43, 0\n\t" "s_waitcnt vmcnt(4)\n\t" "ds_read_b64_tr_b16 v[72:73], v12 offset:0\n\t" \
  "ds_read_b64_tr_b16 v[74:75], v12 offset:2048\n\t" "ds_read_b64_tr_b16 v[76:77], v13 offset:0\n\t" "ds_read_b64_tr_b16 v[78:79], v13 offset:2048\n\t" "ds_read_b64_tr_b16 v[80:81], v14 offset:0\n\t" \
  "ds_read_b64_tr_b16 v[82:83], v14 offset:2048\n\t" "ds_read_b64_tr_b16 v[84:85], v15 offset:0\n\t" "ds_read_b64_tr_b16 v[86:87], v15 offset:2048\n\t" "s_waitcnt lgkmcnt(0)\n\t" \
  "s_add_u32 m0, s44, 0\n\t" "s_nop 0\n\t" "global_load_lds_dwordx4 v1, s[42:43] offset:0 nt\n\t" "global_load_lds_dwordx4 v17, s[42:43] offset:1024 nt\n\t" \
  "global_load_lds_dwordx4 v18, s[42:43] offset:2048 nt\n\t" "global_load_lds_dwordx4 v19, s[42:43] offset:3072 nt\n\t" "s_barrier\n\t" "v_add_u32 v2, 40960, v4\n\t" \
  "ds_read_b64_tr_b16 v[120:121], v4 offset:0\n\t" "ds_read_b64_tr_b16 v[122:123], v4 offset:20480\n\t" "ds_read_b64_tr_b16 v[124:125], v2 offset:0\n\t" \
  "ds_read_b64_tr_b16 v[126:127], v2 offset:20480\n\t" "ds_read_b64_tr_b16 v[128:129], v4 offset:32\n\t" "ds_read_b64_tr_b16 v[130:131], v4 offset:20512\n\t" \
  "ds_read_b64_tr_b16 v[132:133], v2 offset:32\n\t" "ds_read_b64_tr_b16 v[134:135], v2 offset:20512\n\t" "ds_read_b64_tr_b16 v[136:137], v4 offset:64\n\t" \
  "ds_read_b64_tr_b16 v[138:139], v4 offset:20544\n\t" "ds_read_b64_tr_b16 v[140:141], v2 offset:64\n\t" "ds_read_b64_tr_b16 v[142:143], v2 offset:20544\n\t" \
  "ds_read_b64_tr_b16 v[144:145], v4 offset:96\n\t" "ds_read_b64_tr_b16 v[146:147], v4 offset:20576\n\t" "ds_read_b64_tr_b16 v[148:149], v2 offset:96\n\t" \
  "ds_read_b64_tr_b16 v[150:151], v2 offset:20576\n\t" "ds_read_b64_tr_b16 v[152:153], v4 offset:128\n\t" "ds_read_b64_tr_b16 v[154:155], v4 offset:20608\n\t" \
  "ds_read_b64_tr_b16 v[156:157], v2 offset:128\n\t" "ds_read_b64_tr_b16 v[158:159], v2 offset:20608\n\t" "s_waitcnt vmcnt(4)\n\t" "ds_read_b64_tr_b16 v[88:89], v12 offset:4096\n\t" \
  "ds_read_b64_tr_b16 v[90:91], v12 offset:6144\n\t" "ds_read_b64_tr_b16 v[92:93], v13 offset:4096\n\t" "ds_read_b64_tr_b16 v[94:95], v13 offset:6144\n\t" \
  "ds_read_b64_tr_b16 v[96:97], v14 offset:4096\n\t" "ds_read_b64_tr_b16 v[98:99], v14 offset:6144\n\t" "ds_read_b64_tr_b16 v[100:101], v15 offset:4096\n\t" \
  "ds_read_b64_tr_b16 v[102:103], v15 offset:6144\n\t" "s_waitcnt vmcnt(0)\n\t" "ds_read_b64_tr_b16 v[104:105], v12 offset:0\n\t" "ds_read_b64_tr_b16 v[106:107], v12 offset:2048\n\t" \
  "ds_read_b64_tr_b16 v[108:109], v13 offset:0\n\t" "ds_read_b64_tr_b16 v[110:111], v13 offset:2048\n\t" "ds_read_b64_tr_b16 v[112:113], v14 offset:0\n\t" \
  "ds_read_b64_tr_b16 v[114:115], v14 offset:2048\n\t" "ds_read_b64_tr_b16 v[116:117], v15 offset:0\n\t" "ds_read_b64_tr_b16 v[118:119], v15 offset:2048\n\t" "s_mul_i32 s40, s33, 320\n\t" \
  "s_add_u32 s40, s40, 159744\n\t" "v_lshl_add_u32 v1, v10, 1, s40\n\t" "s_lshl_b32 s40, s26, 8\n\t" "s_add_u32 s40, s40, 161024\n\t" "v_lshl_add_u32 v2, v10, 2, s40\n\t" "v_and_b32 v4, 12, v8\n\t" \
  "v_lshl_add_u32 v4, v4, 2, v10\n\t" "s_mov_b32 s40, 161024\n\t" "v_lshl_add_u32 v4, v4, 2, s40\n\t" "v_add_u32 v3, v3, v4\n\t" "s_mul_i32 s40, s26, 320\n\t" "v_lshl_add_u32 v4, v9, 6, s40\n\t" \
  "v_lshl_add_u32 v4, v10, 2, v4\n\t" "s_mul_i32 s40, s29, 2560\n\t" "s_add_u32 s40, s40, 5241344\n\t" "s_add_u32 s62, s24, s40\n\t" "s_addc_u32 s63, s25, 0\n\t" "s_mov_b32 s40, 0x10001\n\t" \
  "s_mov_b32 s41, s40\n\t" "s_lshl_b64 s[42:43], s[40:41], 1\n\t" "s_lshl_b64 s[44:45], s[40:41], 2\n\t" "s_lshl_b64 s[46:47], s[40:41], 3\n\t" "s_lshl_b64 s[48:49], s[40:41], 4\n\t" \
  "s_mov_b32 s60, 0x1f001f\n\t" "s_mov_b32 s61, s60\n\t" "s_mov_b32 s76, 0xbfb8aa3b\n\t" "s_setprio 3\n\t" "s_waitcnt lgkmcnt(0)\n\t" "s_barrier\n\t" \
  "ds_read2_b64 v[28:31], v1 offset0:0 offset1:4\n\t" "ds_read2_b64 v[32:35], v1 offset0:8 offset1:12\n\t" "ds_read2_b64 v[36:39], v1 offset0:16 offset1:20\n\t" \
  "ds_read2_b64 v[160:163], v1 offset0:24 offset1:28\n\t" "ds_read2_b64 v[164:167], v1 offset0:32 offset1:36\n\t" "s_waitcnt lgkmcnt(4)\n\t" \
  "v_mfma_f32_16x16x32_bf16 v[12:15], v[40:43], v[28:31], 0\n\t" "v_mfma_f32_16x16x32_bf16 v[16:19], v[44:47], v[28:31], 0\n\t" "v_mfma_f32_16x16x32_bf16 v[20:23], v[48:51], v[28:31], 0\n\t" \
  "v_mfma_f32_16x16x32_bf16 v[24:27], v[52:55], v[28:31], 0\n\t" "s_waitcnt lgkmcnt(3)\n\t" "v_mfma_f32_16x16x32_bf16 v[12:15], v[56:59], v[32:35], v[12:15]\n\t" \
  "v_mfma_f32_16x16x32_bf16 v[16:19], v[60:63], v[32:35], v[16:19]\n\t" "v_mfma_f32_16x16x32_bf16 v[20:23], v[64:67], v[32:35], v[20:23]\n\t" \
  "v_mfma_f32_16x16x32_bf16 v[24:27], v[68:71], v[32:35], v[24:27]\n\t" "s_waitcnt lgkmcnt(2)\n\t" "v_mfma_f32_16x16x32_bf16 v[12:15], v[72:75], v[36:39], v[12:15]\n\t" \
  "v_mfma_f32_16x16x32_bf16 v[16:19], v[76:79], v[36:39], v[16:19]\n\t" "v_mfma_f32_16x16x32_bf16 v[20:23], v[80:83], v[36:39], v[20:23]\n\t" \
  "v_mfma_f32_16x16x32_bf16 v[24:27], v[84:87], v[36:39], v[24:27]\n\t" "s_waitcnt lgkmcnt(1)\n\t" "v_mfma_f32_16x16x32_bf16 v[12:15], v[88:91], v[160:163], v[12:15]\n\t" \
  "v_mfma_f32_16x16x32_bf16 v[16:19], v[92:95], v[160:163], v[16:19]\n\t" "v_mfma_f32_16x16x32_bf16 v[20:23], v[96:99], v[160:163], v[20:23]\n\t" \
  "v_mfma_f32_16x16x32_bf16 v[24:27], v[100:103], v[160:163], v[24:27]\n\t" "s_waitcnt lgkmcnt(0)\n\t" "v_mfma_f32_16x16x32_bf16 v[12:15], v[104:107], v[164:167], v[12:15]\n\t" \
  "v_mfma_f32_16x16x32_bf16 v[16:19], v[108:111], v[164:167], v[16:19]\n\t" "v_mfma_f32_16x16x32_bf16 v[20:23], v[112:115], v[164:167], v[20:23]\n\t" \
  "v_mfma_f32_16x16x32_bf16 v[24:27], v[116:119], v[164:167], v[24:27]\n\t" "s_mov_b64 exec, s[40:41]\n\t" "s_nop 3\n\t" "ds_write_b128 v2, v[12:15] offset:0\n\t" \
  "ds_write_b128 v2, v[16:19] offset:64\n\t" "ds_write_b128 v2, v[20:23] offset:128\n\t" "ds_write_b128 v2, v[24:27] offset:192\n\t" "s_mov_b64 exec, -1\n\t" "s_waitcnt lgkmcnt(0)\n\t" "s_barrier\n\t" \
  "ds_read2st64_b32 v[40:41], v3 offset0:0 offset1:4\n\t" "ds_read2st64_b32 v[42:43], v3 offset0:1 offset1:5\n\t" "ds_read2st64_b32 v[44:45], v3 offset0:2 offset1:6\n\t" \
  "ds_read2st64_b32 v[46:47], v3 offset0:3 offset1:7\n\t" "s_waitcnt lgkmcnt(0)\n\t" "v_pk_add_f32 v[40:41], v[40:41], v[42:43]\n\t" "v_pk_add_f32 v[44:45], v[44:45], v[46:47]\n\t" \
  "v_pk_add_f32 v[40:41], v[40:41], v[44:45]\n\t" "v_cvt_pk_bf16_f32 v41, v40, v41\n\t" "v_lshlrev_b32 v40, 16, v41\n\t" "v_mul_f32 v42, s76, v40\n\t" "v_exp_f32 v42, v42\n\t" \
  "v_and_b32 v41, 0xffff0000, v41\n\t" "v_add_f32 v42, 1.0, v42\n\t" "v_rcp_f32 v42, v42\n\t" "s_nop 0\n\t" "v_mul_f32 v40, v40, v42\n\t" "v_cvt_pk_bf16_f32 v40, v40, v40\n\t" \
  "v_lshlrev_b32 v40, 16, v40\n\t" "v_mul_f32 v40, v40, v41\n\t" "s_nop 1\n\t" "v_mov_b32_dpp v41, v40 quad_perm:[1,1,3,3] row_mask:0xf bank_mask:0xf\n\t" "v_cvt_pk_bf16_f32 v40, v40, v41\n\t" \
  "s_nop 1\n\t" "v_mov_b32_dpp v44, v40 row_newbcast:0 row_mask:0xf bank_mask:0xf\n\t" "v_mov_b32_dpp v45, v40 row_newbcast:2 row_mask:0xf bank_mask:0xf\n\t" \
  "v_mov_b32_dpp v46, v40 row_newbcast:4 row_mask:0xf bank_mask:0xf\n\t" "v_mov_b32_dpp v47, v40 row_newbcast:6 row_mask:0xf bank_mask:0xf\n\t" \
  "v_mov_b32_dpp v48, v40 row_newbcast:8 row_mask:0xf bank_mask:0xf\n\t" "v_mov_b32_dpp v49, v40 row_newbcast:10 row_mask:0xf bank_mask:0xf\n\t" \
  "v_mov_b32_dpp v50, v40 row_newbcast:12 row_mask:0xf bank_mask:0xf\n\t" "v_mov_b32_dpp v51, v40 row_newbcast:14 row_mask:0xf bank_mask:0xf\n\t" \
  "v_mfma_f32_16x16x32_bf16 v[56:59], v[120:123], v[44:47], 0\n\t" "s_nop 0\n\t" "v_mfma_f32_16x16x32_bf16 v[56:59], v[124:127], v[48:51], v[56:59]\n\t" \
  "v_mfma_f32_16x16x32_bf16 v[60:63], v[128:131], v[44:47], 0\n\t" "v_mfma_f32_16x16x32_bf16 v[60:63], v[132:135], v[48:51], v[60:63]\n\t" \
  "v_mfma_f32_16x16x32_bf16 v[64:67], v[136:139], v[44:47], 0\n\t" "v_mfma_f32_16x16x32_bf16 v[64:67], v[140:143], v[48:51], v[64:67]\n\t" \
  "v_mfma_f32_16x16x32_bf16 v[68:71], v[144:147], v[44:47], 0\n\t" "v_mfma_f32_16x16x32_bf16 v[68:71], v[148:151], v[48:51], v[68:71]\n\t" \
  "v_mfma_f32_16x16x32_bf16 v[72:75], v[152:155], v[44:47], 0\n\t" "v_mfma_f32_16x16x32_bf16 v[72:75], v[156:159], v[48:51], v[72:75]\n\t" "s_nop 1\n\t" "v_cndmask_b32 v56, v56, v60, s[42:43]\n\t" \
  "v_cndmask_b32 v57, v57, v61, s[42:43]\n\t" "v_cndmask_b32 v58, v58, v62, s[42:43]\n\t" "v_cndmask_b32 v59, v59, v63, s[42:43]\n\t" "v_cndmask_b32 v56, v56, v64, s[44:45]\n\t" \
  "v_cndmask_b32 v57, v57, v65, s[44:45]\n\t" "v_cndmask_b32 v58, v58, v66, s[44:45]\n\t" "v_cndmask_b32 v59, v59, v67, s[44:45]\n\t" "v_cndmask_b32 v56, v56, v68, s[46:47]\n\t" \
  "v_cndmask_b32 v57, v57, v69, s[46:47]\n\t" "v_cndmask_b32 v58, v58, v70, s[46:47]\n\t" "v_cndmask_b32 v59, v59, v71, s[46:47]\n\t" "v_cndmask_b32 v56, v56, v72, s[48:49]\n\t" \
  "v_cndmask_b32 v57, v57, v73, s[48:49]\n\t" "v_cndmask_b32 v58, v58, v74, s[48:49]\n\t" "v_cndmask_b32 v59, v59, v75, s[48:49]\n\t" "s_mov_b64 exec, s[60:61]\n\t" \
  "global_store_dwordx4 v4, v[56:59], s[62:63] sc1\n\t" "s_mov_b64 exec, -1\n\t" "s_branch .Lend_%=\n\t" ".Lsq_%=x:\n\t" "s_movk_i32 s50, 5\n\t" ".Ld3_%=:\n\t" "s_mov_b32 m0, s54\n\t" \
  "s_add_u32 s54, s54, 4096\n\t" "global_load_lds_dwordx4 v32, s[52:53] offset:0 nt\n\t" "global_load_lds_dwordx4 v32, s[52:53] offset:1024 nt\n\t" \
  "global_load_lds_dwordx4 v32, s[52:53] offset:2048 nt\n\t" "global_load_lds_dwordx4 v32, s[52:53] offset:3072 nt\n\t" "s_add_u32 s52, s52, 4096\n\t" "s_addc_u32 s53, s53, 0\n\t" \
  "s_sub_u32 s50, s50, 1\n\t" "s_cmp_lg_u32 s50, 0\n\t" "s_cbranch_scc1 .Ld3_%=\n\t" "s_waitcnt vmcnt(0) lgkmcnt(0)\n\t" "s_barrier\n\t" "v_add_co_u32 v16, vcc, 1, v40\n\t" "s_nop 1\n\t" \
  "v_addc_co_u32 v17, vcc, 0, v41, vcc\n\t" "s_mul_i32 s40, s29, 205\n\t" "s_lshr_b32 s40, s40, 11\n\t" "s_mul_i32 s40, s40, 10\n\t" "s_sub_u32 s28, s29, s40\n\t" "s_cmp_lt_u32 s26, 10\n\t" \
  "s_cbranch_scc1 .Lcb_%=\n\t" "s_cmp_eq_u32 s26, 10\n\t" "s_cbranch_scc1 .Lfe_%=\n\t" "s_movk_i32 s40, 4096\n\t" "v_min_u32 v3, 22, v8\n\t" "v_lshl_add_u32 v3, v3, 7, s40\n\t" ".Lsf_%=:\n\t" \
  "global_load_dwordx2 v[40:41], v3, s[24:25] sc1\n\t" "s_sub_u32 s39, s39, 1\n\t" "s_cmp_lt_i32 s39, 0\n\t" "s_waitcnt vmcnt(0)\n\t" "s_cbranch_scc1 .Lrt_%=\n\t" \
  "v_cmp_ne_u64 vcc, v[40:41], v[16:17]\n\t" "s_cbranch_vccnz .Lsf_%=\n\t" "s_xor_b32 s40, s28, 1\n\t" "s_mul_i32 s40, s40, 6144\n\t" "s_add_u32 s40, s40, 5302784\n\t" "v_and_b32 v3, 7, v8\n\t" \
  "v_mul_u32_u24 v3, 640, v3\n\t" "v_add_u32 v3, s40, v3\n\t" ".Lsk_%=:\n\t" "s_sleep 10\n\t" "global_load_dword v40, v3, s[24:25] sc1\n\t" "s_sub_u32 s39, s39, 1\n\t" "s_cmp_lt_i32 s39, 0\n\t" \
  "s_waitcnt vmcnt(0)\n\t" "s_cbranch_scc1 .Lrt_%=\n\t" "v_cmp_ne_u32 vcc, -1, v40\n\t" "s_cbranch_vccz .Lsk_%=\n\t" "s_branch .Lrt_%=\n\t" ".Lfe_%=:\n\t" "v_lshrrev_b32 v2, 3, v8\n\t" \
  "v_lshl_add_u32 v2, v2, 7, v32\n\t" "v_lshlrev_b32 v4, 2, v8\n\t" "v_lshrrev_b32 v3, 5, v8\n\t" "v_lshl_add_u32 v3, v3, 7, v4\n\t" "v_add_u32 v4, 160768, v4\n\t" "v_add_u32 v5, 159744, v32\n\t" \
  "s_movk_i32 s44, 63\n\t" ".Lf_%=:\n\t" "ds_read_b64 v[40:41], v37 offset:32\n\t" "s_sub_u32 s39, s39, 1\n\t" "s_cmp_lt_i32 s39, 0\n\t" "s_cselect_b32 s46, 1, 0\n\t" "s_waitcnt lgkmcnt(0)\n\t" \
  "v_readfirstlane_b32 s45, v40\n\t" "v_readfirstlane_b32 s47, v41\n\t" "s_bfe_u32 s45, s45, 0x60006\n\t" "s_or_b32 s46, s46, s47\n\t" "s_xor_b32 s47, s45, s44\n\t" "s_or_b32 s48, s47, s46\n\t" \
  "s_cbranch_scc0 .Lf_%=\n\t" "s_cmp_eq_u32 s47, 0\n\t" "s_cbranch_scc1 .Lfd_%=\n\t" "s_mul_i32 s40, s45, 2560\n\t" "s_add_u32 s40, s40, 5123584\n\t" "s_add_u32 s40, s24, s40\n\t" \
  "s_addc_u32 s41, s25, 0\n\t" "global_load_dwordx4 v[44:47], v2, s[40:41] sc1\n\t" "global_load_dword v48, v3, s[40:41] offset:2048 sc1\n\t" "s_cmp_lt_i32 s39, 0\n\t" "s_waitcnt vmcnt(0)\n\t" \
  "ds_write_b128 v5, v[44:47]\n\t" "ds_write_b32 v4, v48\n\t" "s_cbranch_scc1 .Lfd_%=\n\t" "v_max3_u32 v49, v44, v45, v46\n\t" "v_max3_u32 v49, v49, v47, v48\n\t" "s_movk_i32 s44, 99\n\t" \
  "v_cmp_eq_u32 vcc, -1, v49\n\t" "s_cbranch_vccnz .Lf_%=\n\t" "s_mov_b32 s44, s45\n\t" "s_cmp_eq_u32 s46, 0\n\t" "s_cbranch_scc1 .Lf_%=\n\t" ".Lfd_%=:\n\t" "s_waitcnt lgkmcnt(0)\n\t" "s_barrier\n\t" \
  "s_barrier\n\t" "s_branch .Lend_%=\n\t" ".Lcb_%=:\n\t" "s_cmp_ge_u32 s29, 20\n\t" "s_cbranch_scc1 .Lend_%=\n\t" "global_load_dwordx4 v[20:23], v32, s[14:15]\n\t" "v_lshlrev_b32 v6, 2, v8\n\t" \
  "global_load_dword v24, v6, s[14:15] offset:1024\n\t" "v_add_u32 v5, 159744, v32\n\t" "v_add_u32 v4, 160768, v6\n\t" "s_lshl1_add_u32 s40, s29, s33\n\t" "v_and_b32 v1, 3, v8\n\t" \
  "v_lshrrev_b32 v2, 2, v8\n\t" "v_and_b32 v3, 4, v2\n\t" "v_lshrrev_b32 v7, 4, v8\n\t" "v_and_b32 v7, 2, v7\n\t" "v_lshl_add_u32 v3, v1, 3, v3\n\t" "v_add_u32 v3, v3, v7\n\t" \
  "v_mul_u32_u24 v2, 2560, v2\n\t" "v_lshl_add_u32 v2, v1, 4, v2\n\t" "s_lshl_b32 s41, s40, 5\n\t" "s_add_u32 s42, s41, 159744\n\t" "v_add_u32 v1, s42, v3\n\t" "s_lshl_b32 s42, s40, 6\n\t" \
  "s_add_u32 s42, s42, 5241344\n\t" "s_add_u32 s44, s24, s42\n\t" "s_addc_u32 s45, s25, 0\n\t" "s_add_u32 s46, s44, 40960\n\t" "s_addc_u32 s47, s45, 0\n\t" "v_mov_b64 v[64:65], 0\n\t" \
  "v_mov_b64 v[66:67], 0\n\t" "s_waitcnt vmcnt(0)\n\t" "s_barrier\n\t" "s_barrier\n\t" "s_memrealtime s[64:65]\n\t" "ds_read_b32 v39, v37 offset:32\n\t" "ds_read_b128 v[44:47], v5\n\t" \
  "ds_read_b32 v48, v4\n\t" "ds_read_u16_d16_hi v49, v1\n\t" "s_waitcnt lgkmcnt(0)\n\t" "v_readfirstlane_b32 s42, v39\n\t" "v_dot2_f32_bf16 v50, v48, v24, 0\n\t" \
  "v_dot2_f32_bf16 v50, v44, v20, v50\n\t" "v_dot2_f32_bf16 v50, v45, v21, v50\n\t" "v_dot2_f32_bf16 v50, v46, v22, v50\n\t" "v_dot2_f32_bf16 v50, v47, v23, v50\n\t" "s_bfe_u32 s43, s42, 0x60006\n\t" \
  "s_lshr_b32 s56, s43, 1\n\t" "s_mul_i32 s56, s56, 89\n\t" "s_and_b32 s57, s43, 1\n\t" "s_mul_i32 s57, s57, 48\n\t" "s_add_u32 s56, s56, s57\n\t" "s_and_b32 s42, s42, 63\n\t" \
  "s_add_u32 s56, s56, s42\n\t" "s_sub_u32 s56, s56, 6\n\t" "v_add_f32_dpp v50, v50, v50 quad_perm:[1,0,3,2] row_mask:0xf bank_mask:0xf\n\t" "s_nop 1\n\t" \
  "v_add_f32_dpp v50, v50, v50 quad_perm:[2,3,0,1] row_mask:0xf bank_mask:0xf\n\t" "s_nop 1\n\t" "v_add_f32_dpp v50, v50, v50 row_half_mirror row_mask:0xf bank_mask:0xf\n\t" "s_nop 1\n\t" \
  "v_add_f32_dpp v50, v50, v50 row_mirror row_mask:0xf bank_mask:0xf\n\t" "s_bfe_u32 s57, s56, 0x1000a\n\t" "s_lshl1_add_u32 s56, s56, s57\n\t" "s_and_b32 s56, s56, 2047\n\t" "v_mov_b32 v51, v50\n\t" \
  "s_mul_i32 s56, s56, 1280\n\t" "s_add_u32 s56, s56, s41\n\t" "v_permlane32_swap_b32 v50, v51\n\t" "v_add_f32 v50, v50, v51\n\t" "v_mov_b32 v51, v50\n\t" "s_add_u32 s56, s22, s56\n\t" \
  "s_addc_u32 s57, s23, 0\n\t" "v_permlane16_swap_b32 v50, v51\n\t" "v_add_f32 v50, v50, v51\n\t" "v_cvt_pk_bf16_f32 v50, v50, v50\n\t" "v_lshlrev_b32 v50, 16, v50\n\t" \
  "v_mul_f32 v50, 0xbfb8aa3b, v50\n\t" "v_exp_f32 v50, v50\n\t" "s_mul_i32 s43, s43, 2560\n\t" "s_lshr_b32 s42, s40, 2\n\t" "s_lshl_b32 s42, s42, 7\n\t" "s_add_u32 s43, s43, s42\n\t" \
  "s_add_u32 s43, s43, s41\n\t" "v_add_f32 v50, 1.0, v50\n\t" "v_rcp_f32 v50, v50\n\t" "s_add_u32 s43, s43, 5123712\n\t" "s_add_u32 s58, s24, s43\n\t" "s_addc_u32 s59, s25, 0\n\t" \
  "v_cvt_pk_bf16_f32 v50, v50, v50\n\t" "v_lshlrev_b32 v50, 16, v50\n\t" "v_and_b32 v49, 0xffff0000, v49\n\t" ".Ltc_%=:\n\t" "s_memrealtime s[78:79]\n\t" "s_waitcnt lgkmcnt(0)\n\t" \
  "s_sub_u32 s78, s78, s64\n\t" "s_cmp_lt_u32 s78, 60\n\t" "s_cbranch_scc1 .Ltc_%=\n\t" ".Lcp_%=:\n\t" "global_load_short_d16_hi v51, v3, s[58:59] sc1\n\t" \
  "global_load_dwordx4 v[60:63], v2, s[44:45] sc1\n\t" "s_mov_b32 exec_hi, 0\n\t" "global_load_dwordx4 v[64:67], v2, s[46:47] sc1\n\t" "s_mov_b32 exec_hi, -1\n\t" "s_sub_u32 s39, s39, 1\n\t" \
  "s_cmp_lt_i32 s39, 0\n\t" "s_waitcnt vmcnt(0)\n\t" "v_pk_add_f32 v[60:61], v[60:61], v[64:65]\n\t" "v_pk_add_f32 v[62:63], v[62:63], v[66:67]\n\t" "v_and_b32 v51, 0xffff0000, v51\n\t" \
  "v_add_f32_dpp v60, v60, v60 row_ror:4 row_mask:0xf bank_mask:0xf\n\t" "v_add_f32_dpp v61, v61, v61 row_ror:4 row_mask:0xf bank_mask:0xf\n\t" \
  "v_add_f32_dpp v62, v62, v62 row_ror:4 row_mask:0xf bank_mask:0xf\n\t" "v_add_f32_dpp v63, v63, v63 row_ror:4 row_mask:0xf bank_mask:0xf\n\t" \
  "v_add_f32_dpp v60, v60, v60 row_ror:8 row_mask:0xf bank_mask:0xf\n\t" "v_add_f32_dpp v61, v61, v61 row_ror:8 row_mask:0xf bank_mask:0xf\n\t" \
  "v_add_f32_dpp v62, v62, v62 row_ror:8 row_mask:0xf bank_mask:0xf\n\t" "v_add_f32_dpp v63, v63, v63 row_ror:8 row_mask:0xf bank_mask:0xf\n\t" "v_permlane32_swap_b32 v60, v61\n\t" "s_nop 0\n\t" \
  "v_permlane32_swap_b32 v62, v63\n\t" "v_add_f32 v60, v60, v61\n\t" "v_add_f32 v62, v62, v63\n\t" "s_nop 1\n\t" "v_permlane16_swap_b32 v60, v62\n\t" "v_add_f32 v60, v60, v62\n\t" \
  "v_cvt_pk_bf16_f32 v60, v60, v60\n\t" "v_lshlrev_b32 v60, 16, v60\n\t" "v_mul_f32 v60, v60, v50\n\t" "v_cvt_pk_bf16_f32 v60, v60, v60\n\t" "v_lshlrev_b32 v60, 16, v60\n\t" \
  "v_add_f32 v60, v60, v49\n\t" "v_cvt_pk_bf16_f32 v60, v60, v60\n\t" "v_lshlrev_b32 v60, 16, v60\n\t" "v_add_f32 v60, v60, v51\n\t" "s_cbranch_scc1 .Lcq_%=\n\t" "v_cmp_u_f32 vcc, v60, v60\n\t" \
  "s_cbranch_vccnz .Lcp_%=\n\t" ".Lcq_%=:\n\t" "v_cvt_pk_bf16_f32 v60, v60, v60\n\t" "s_mov_b32 exec_lo, 0xf000f\n\t" "s_mov_b32 exec_hi, 0xf000f\n\t" "global_store_short v3, v60, s[56:57]\n\t" \
  "s_mov_b64 exec, -1\n\t" ".Lend_%=:\n\t"

extern "C" __global__ __launch_bounds__(768, 3) void fused_block(const u16* x, const u16* wqkv, const u16* wo, const u16* w_r,
                                                                 const u16* w_gate, const u16* w_up, const u16* w_down, u16* out,
                                                                 char* ws) {
  u32 tid = threadIdx.x;
  asm volatile(BODY
    : "+{v0}"(tid)
    : [ka] "s"(__builtin_amdgcn_kernarg_segment_ptr()), [bid] "s"(blockIdx.x)
    : "memory", "scc", "vcc", "v1", "v2", "v3", "v4", "v5", "v6", "v7", "v8", "v9", CL10(1), CL10(2), CL10(3), CL10(4), CL10(5), CL10(6),
      CL10(7), CL10(8), CL10(9), CL10(10), CL10(11), CL10(12), CL10(13), CL10(14), CL10(15), CL8(16), "s8", "s9", SL10(1), SL10(2), "s30",
      "s31", "s33", "s34", "s35", "s36", "s37", "s38", "s39", SL10(4), SL10(5), SL10(6), SL10(7), SL10(8), SL10(9));
}

extern "C" void kernel_launch(void* const* d_in, const int* in_sizes, int n_in,
                              void* d_out, int out_size, void* d_ws, size_t ws_size,
                              hipStream_t stream) {
  const u16* x = (const u16*)d_in[0];
  const u16* wqkv = (const u16*)d_in[1];
  const u16* wo = (const u16*)d_in[2];
  const u16* w_r = (const u16*)d_in[4];
  const u16* w_gate = (const u16*)d_in[5];
  const u16* w_up = (const u16*)d_in[6];
  const u16* w_down = (const u16*)d_in[7];
  (void)hipFuncSetAttribute((const void*)fused_block, hipFuncAttributeMaxDynamicSharedMemorySize, LDS_TOTAL);
  fused_block<<<256, 768, LDS_TOTAL, stream>>>(x, wqkv, wo, w_r, w_gate, w_up, w_down, (u16*)d_out, (char*)d_ws);
}
```

```cpp
#include <hip/hip_runtime.h>
#include <stdint.h>


typedef unsigned short u16;
typedef unsigned int u32;
#define LDS_TOTAL 163840
#define CL8(T) "v" #T "0", "v" #T "1", "v" #T "2", "v" #T "3", "v" #T "4", "v" #T "5", "v" #T "6", "v" #T "7"
#define CL10(T) CL8(T), "v" #T "8", "v" #T "9"
#define SL10(T) "s" #T "0", "s" #T "1", "s" #T "2", "s" #T "3", "s" #T "4", "s" #T "5", "s" #T "6", "s" #T "7", "s" #T "8", "s" #T "9"

#define BODY \
  ".Lst_%=:\n\t" "s_load_dwordx16 s[8:23], %[ka], 0x0\n\t" "s_load_dwordx2 s[24:25], %[ka], 0x40\n\t" "v_readfirstlane_b32 s26, v0\n\t" "s_lshr_b32 s76, %[bid], 3\n\t" "v_and_b32 v8, 63, v0\n\t" \
  "s_mul_i32 s77, s76, 205\n\t" "s_lshr_b32 s26, s26, 6\n\t" "s_lshr_b32 s77, s77, 10\n\t" "v_lshrrev_b32 v1, 3, v8\n\t" "s_mul_i32 s27, s77, 5\n\t" "s_and_b32 s33, s26, 3\n\t" \
  "s_sub_u32 s27, s76, s27\n\t" "s_and_b32 s78, %[bid], 1\n\t" "s_bfe_u32 s79, %[bid], 0x20001\n\t" "s_mul_i32 s28, s78, 5\n\t" "s_mul_i32 s29, s79, 6\n\t" "s_add_u32 s28, s28, s27\n\t" \
  "s_add_u32 s29, s29, s77\n\t" "s_cmp_ge_u32 s76, 30\n\t" "s_cselect_b32 s29, 24, s29\n\t" "s_cmp_ge_u32 s29, 23\n\t" "s_cbranch_scc1 .Lsp_%=\n\t" "s_lshl_b32 s77, s77, 2\n\t" \
  "s_lshl1_add_u32 s31, s27, s78\n\t" "s_lshr_b32 s77, 0x663300, s77\n\t" "s_add_u32 s31, s31, s79\n\t" "s_and_b32 s77, s77, 15\n\t" "s_mul_i32 s30, s29, 89\n\t" "s_add_u32 s31, s31, s77\n\t" \
  "s_mul_i32 s77, s31, 205\n\t" "s_lshr_b32 s77, s77, 11\n\t" "s_mul_i32 s77, s77, 10\n\t" "s_sub_u32 s31, s31, s77\n\t" "s_cmp_ge_u32 s26, 4\n\t" "s_cbranch_scc1 .Lnl_%=\n\t" "s_setprio 2\n\t" \
  "v_lshl_or_b32 v2, s33, 4, v1\n\t" "v_and_b32 v3, 2, v1\n\t" "v_mul_u32_u24 v2, 0xf00, v2\n\t" "v_xor_b32 v3, v3, v8\n\t" "s_lshl_b32 s76, s28, 7\n\t" "v_and_b32 v3, 7, v3\n\t" \
  "s_add_u32 s76, s76, 3072\n\t" "v_lshlrev_b32 v3, 4, v3\n\t" "s_mul_i32 s86, s33, 6144\n\t" "v_add3_u32 v40, v2, v3, s76\n\t" "s_add_u32 s86, s86, 3072\n\t" "v_add_u32 v41, 256, v40\n\t" \
  "v_add_u32 v42, 512, v40\n\t" "v_xor_b32 v43, 64, v40\n\t" "s_mov_b32 m0, s86\n\t" "v_add_u32 v43, 27648, v43\n\t" "s_waitcnt lgkmcnt(0)\n\t" "s_mul_i32 s76, s31, 245760\n\t" \
  "s_lshl_b32 s77, s31, 7\n\t" "s_add_u32 s78, s10, s76\n\t" "s_addc_u32 s79, s11, 0\n\t" "s_add_u32 s80, s8, s77\n\t" "s_addc_u32 s81, s9, 0\n\t" "v_add_u32 v44, 256, v43\n\t" \
  "v_add_u32 v45, 512, v43\n\t" "global_load_lds_dwordx4 v40, s[78:79] offset:-3072\n\t" "global_load_lds_dwordx4 v41, s[78:79] offset:-2048\n\t" \
  "global_load_lds_dwordx4 v42, s[78:79] offset:-1024\n\t" "global_load_lds_dwordx4 v43, s[78:79] offset:0\n\t" "global_load_lds_dwordx4 v44, s[78:79] offset:1024\n\t" \
  "global_load_lds_dwordx4 v45, s[78:79] offset:2048\n\t" "s_mul_i32 s76, s33, 24\n\t" "s_mul_i32 s87, s33, 3072\n\t" "v_add_u32 v2, s76, v1\n\t" "s_movk_i32 s77, 2048\n\t" \
  "v_lshrrev_b32 v3, 1, v2\n\t" "v_add_u32 v2, s30, v2\n\t" "v_xor_b32 v3, v3, v8\n\t" "s_add_u32 s87, s87, 26624\n\t" "v_and_b32 v3, 7, v3\n\t" "s_mov_b32 m0, s87\n\t" \
  "v_lshl_add_u32 v3, v3, 4, s77\n\t" "s_movk_i32 s77, 1280\n\t" "v_add_u32 v4, 0, v2\n\t" "v_sub_u32 v4, v4, 6 clamp\n\t" "v_lshrrev_b32 v5, 10, v4\n\t" "v_lshl_or_b32 v4, v4, 1, v5\n\t" \
  "v_and_b32 v4, 0x7ff, v4\n\t" "v_mad_u32_u24 v46, v4, s77, v3\n\t" "global_load_lds_dwordx4 v46, s[80:81] offset:-2048\n\t" "v_xor_b32 v3, 64, v3\n\t" "v_subrev_u32 v3, 1024, v3\n\t" \
  "v_add_u32 v4, 8, v2\n\t" "v_sub_u32 v4, v4, 6 clamp\n\t" "v_lshrrev_b32 v5, 10, v4\n\t" "v_lshl_or_b32 v4, v4, 1, v5\n\t" "v_and_b32 v4, 0x7ff, v4\n\t" "v_mad_u32_u24 v47, v4, s77, v3\n\t" \
  "global_load_lds_dwordx4 v47, s[80:81] offset:-1024\n\t" "v_xor_b32 v3, 64, v3\n\t" "v_subrev_u32 v3, 1024, v3\n\t" "v_add_u32 v4, 16, v2\n\t" "v_sub_u32 v4, v4, 6 clamp\n\t" \
  "v_lshrrev_b32 v5, 10, v4\n\t" "v_lshl_or_b32 v4, v4, 1, v5\n\t" "v_and_b32 v4, 0x7ff, v4\n\t" "v_mad_u32_u24 v48, v4, s77, v3\n\t" "global_load_lds_dwordx4 v48, s[80:81] offset:0\n\t" \
  ".Lnl_%=:\n\t" "s_cmp_lg_u32 s26, 11\n\t" "s_cbranch_scc1 .Lnc_%=\n\t" "s_getpc_b64 s[76:77]\n\t" ".Lpc_%=:\n\t" "s_and_b32 s78, %[bid], -8\n\t" "s_sub_u32 s76, s76, .Lpc_%=-.Lst_%=\n\t" \
  "s_subb_u32 s77, s77, 0\n\t" "v_add_u32 v1, s78, v8\n\t" "v_min_u32 v1, ((.Lend_%=-.Lst_%=)>>7)+0, v1\n\t" "s_mov_b32 m0, 163072\n\t" "v_lshlrev_b32 v1, 7, v1\n\t" "s_mov_b64 exec, 0xff\n\t" \
  "global_load_lds_dword v1, s[76:77]\n\t" "s_mov_b64 exec, -1\n\t" ".Lnc_%=:\n\t" "s_cmp_ge_u32 s26, 6\n\t" "s_cselect_b32 s34, 1, 0\n\t" "s_cselect_b32 s76, 6, 0\n\t" "v_and_b32 v9, 15, v8\n\t" \
  "s_sub_u32 s35, s26, s76\n\t" "v_lshrrev_b32 v10, 2, v8\n\t" "s_and_b32 s36, s26, 1\n\t" "s_lshr_b32 s37, s26, 1\n\t" "s_lshl4_add_u32 s76, s35, s30\n\t" "s_sub_u32 s76, s76, 6\n\t" \
  "v_lshlrev_b32 v32, 4, v8\n\t" "v_add_u32 v2, s76, v9\n\t" "s_mov_b32 s76, 0x1fe00\n\t" "v_and_b32 v2, 0x3ff, v2\n\t" "v_lshlrev_b32 v1, v9, s76\n\t" "v_min_u32 v2, 7, v2\n\t" \
  "v_lshlrev_b32 v35, 7, v10\n\t" "v_sub_u32 v2, v9, v2\n\t" "v_and_b32 v10, 12, v10\n\t" "v_add_u32 v2, 16, v2\n\t" "s_cmp_eq_u32 s35, 0\n\t" "s_cselect_b32 s76, 0xffff0000, -1\n\t" \
  "s_cselect_b32 s81, 0, 1024\n\t" "v_lshlrev_b32 v2, v2, -1\n\t" "v_and_b32 v1, s76, v1\n\t" "s_mov_b32 s77, 0xff80ff80\n\t" "s_cmp_eq_u32 s29, 0\n\t" "s_cselect_b32 s77, 0xffc0ffc0, s77\n\t" \
  "v_and_b32 v1, v1, v2\n\t" "s_cmp_eq_u32 s37, 0\n\t" "s_cselect_b32 s72, s77, -1\n\t" "v_not_b32 v1, v1\n\t" "s_mov_b32 s73, s72\n\t" "v_lshrrev_b32 v1, v10, v1\n\t" \
  "s_lshl4_add_u32 s76, s37, s30\n\t" "s_mov_b32 s77, 0xffff\n\t" "v_bfe_i32 v20, v1, 0, 1\n\t" "v_bfe_i32 v21, v1, 1, 1\n\t" "v_bfe_i32 v22, v1, 2, 1\n\t" "v_bfe_i32 v23, v1, 3, 1\n\t" \
  "v_add_u32 v2, s76, v9\n\t" "v_bfi_b32 v28, s77, v20, v21\n\t" "v_bfi_b32 v29, s77, v22, v23\n\t" "v_bfe_i32 v24, v1, 16, 1\n\t" "v_bfe_i32 v25, v1, 17, 1\n\t" "v_bfe_i32 v26, v1, 18, 1\n\t" \
  "v_bfe_i32 v27, v1, 19, 1\n\t" "v_sub_u32 v2, v2, 6 clamp\n\t" "v_lshrrev_b32 v3, 10, v2\n\t" "v_lshl_or_b32 v2, v2, 1, v3\n\t" "v_and_b32 v2, 0x7ff, v2\n\t" "s_lshl_b32 s76, s28, 7\n\t" \
  "s_lshl_b32 s77, s36, 6\n\t" "v_and_b32 v20, 0xff800000, v20\n\t" "v_and_b32 v21, 0xff800000, v21\n\t" "v_and_b32 v22, 0xff800000, v22\n\t" "v_and_b32 v23, 0xff800000, v23\n\t" \
  "v_and_b32 v24, 0xff800000, v24\n\t" "v_and_b32 v25, 0xff800000, v25\n\t" "v_and_b32 v26, 0xff800000, v26\n\t" "v_and_b32 v27, 0xff800000, v27\n\t" "s_add_u32 s76, s76, s77\n\t" \
  "v_lshlrev_b32 v1, 1, v8\n\t" "v_lshrrev_b32 v3, 1, v8\n\t" "v_and_b32 v1, 32, v1\n\t" "v_and_b32 v3, 16, v3\n\t" "s_movk_i32 s78, 1280\n\t" "v_or3_b32 v34, v1, v3, s76\n\t" \
  "v_lshlrev_b32 v5, 2, v8\n\t" "v_mad_u32_u24 v33, v2, s78, v34\n\t" "v_and_b32 v6, 0x60, v5\n\t" "s_mul_i32 s76, s36, 40960\n\t" "v_xor_b32 v1, s77, v6\n\t" "s_add_u32 s76, s76, 73728\n\t" \
  "v_and_b32 v5, 12, v5\n\t" "v_add3_u32 v35, v35, v1, s76\n\t" "v_mov_b32 v37, 156416\n\t" "v_lshl_add_u32 v35, v5, 1, v35\n\t" "v_mov_b32 v38, 1\n\t" "v_add_u32 v36, 1413120, v32\n\t" \
  "s_mul_i32 s78, s27, 12\n\t" "s_mov_b32 s76, 0x10321320\n\t" "s_mov_b32 s77, 0xbe0f102\n\t" "s_lshr_b64 s[76:77], s[76:77], s78\n\t" "s_and_b32 s74, s76, 0xfff\n\t" "s_mov_b32 s76, 0xedfe0f41\n\t" \
  "s_mov_b32 s77, 0xfdcedcf\n\t" "s_lshr_b64 s[76:77], s[76:77], s78\n\t" "s_pack_ll_b32_b16 s74, s74, s76\n\t" "s_mul_i32 s76, s27, 0x10001\n\t" "s_sub_u32 s76, 0xe0009, s76\n\t" \
  "s_lshl4_add_u32 s74, s74, s76\n\t" "s_mul_i32 s78, s34, 3072\n\t" "s_lshr_b32 s79, s74, 16\n\t" "v_bfe_u32 v1, s74, v10, 4\n\t" "s_lshl_b32 s76, s35, 9\n\t" "v_bfe_u32 v2, s79, v10, 4\n\t" \
  "v_and_b32 v4, 12, v8\n\t" "s_sub_u32 s76, s76, s78\n\t" "v_bfe_u32 v3, s74, v4, 4\n\t" "s_add_u32 s76, s76, 3072\n\t" "v_bfe_u32 v4, s79, v4, 4\n\t" "v_lshl_add_u32 v30, v8, 3, s76\n\t" \
  "s_sub_u32 s76, 5, s27\n\t" "v_lshl_add_u32 v31, s78, 1, v30\n\t" "s_sub_u32 s77, 15, s27\n\t" "v_cmp_gt_u32 s[40:41], s76, v1\n\t" "v_cmp_gt_u32 s[42:43], s76, v2\n\t" \
  "v_cmp_le_u32 s[44:45], s77, v1\n\t" "v_cmp_le_u32 s[46:47], s77, v2\n\t" "v_cmp_gt_u32 s[50:51], s76, v3\n\t" "v_cmp_gt_u32 s[52:53], s76, v4\n\t" "s_lshl_b32 s76, s27, 2\n\t" \
  "v_cmp_gt_u32 s[48:49], s76, v10\n\t" "s_mul_i32 s76, s29, 10\n\t" "s_add_u32 s76, s76, s28\n\t" "s_mul_i32 s76, s76, 6144\n\t" "s_add_u32 s76, s76, 585728\n\t" "s_waitcnt lgkmcnt(0)\n\t" \
  "s_add_u32 s66, s24, s76\n\t" "s_addc_u32 s67, s25, 0\n\t" "s_lshl_b32 s76, s35, 10\n\t" "s_add_u32 s76, s76, 4096\n\t" "s_add_u32 s56, s66, s76\n\t" "s_addc_u32 s57, s67, 0\n\t" \
  "s_sub_u32 s58, s56, s81\n\t" "s_subb_u32 s59, s57, 0\n\t" "s_mul_i32 s76, s29, 61440\n\t" "s_add_u32 s76, s76, 2004992\n\t" "s_mul_i32 s77, s26, 5120\n\t" "s_add_u32 s77, s77, s76\n\t" \
  "s_add_u32 s62, s24, s77\n\t" "s_addc_u32 s63, s25, 0\n\t" "s_add_u32 s64, s62, 1413120\n\t" "s_addc_u32 s65, s63, 0\n\t" "s_mul_i32 s77, s35, 10\n\t" "s_add_u32 s77, s77, s28\n\t" \
  "s_lshl_b32 s77, s77, 10\n\t" "s_add_u32 s77, s77, s76\n\t" "s_add_u32 s60, s24, s77\n\t" "s_addc_u32 s61, s25, 0\n\t" "s_mov_b64 s[76:77], 0xffff\n\t" "s_not_b64 s[54:55], s[76:77]\n\t" \
  "s_cmp_lg_u32 s34, 0\n\t" "s_cselect_b64 s[54:55], s[76:77], s[54:55]\n\t" "s_mov_b32 s39, 400000\n\t" "s_mov_b32 s75, 0\n\t" "s_cmp_lt_u32 s26, 4\n\t" "s_cbranch_scc1 .Ll_%=\n\t" \
  "s_mul_i32 s77, s27, 0x1111\n\t" "s_sub_u32 s80, 0x8765, s77\n\t" "s_sub_u32 s77, 0xdcba, s77\n\t" "s_cmp_eq_u32 s33, 1\n\t" "s_cselect_b32 s80, s74, s80\n\t" "s_cmp_eq_u32 s33, 2\n\t" \
  "s_cselect_b32 s80, s79, s80\n\t" "s_cmp_eq_u32 s33, 3\n\t" "s_cselect_b32 s80, s77, s80\n\t" "v_mul_u32_u24 v1, 768, v10\n\t" "v_bfe_u32 v5, s80, v5, 4\n\t" "v_lshlrev_b32 v2, 5, v9\n\t" \
  "v_lshlrev_b32 v5, 3, v5\n\t" "v_and_b32 v2, 0x180, v2\n\t" "v_xor_b32 v5, v5, v6\n\t" "s_cmp_ge_u32 s26, 8\n\t" "v_add3_u32 v1, v1, v5, v2\n\t" "s_cbranch_scc1 .Lg_%=\n\t" "v_mov_b32 v7, 0\n\t" \
  "s_setprio 3\n\t" "global_load_dwordx2 v[40:41], v7, s[24:25] sc1\n\t" "s_cmp_lg_u32 s33, 2\n\t" "s_cbranch_scc1 .Lre0_%=\n\t" "v_lshlrev_b32 v5, 2, v8\n\t" \
  "global_load_dwordx4 v[64:67], v32, s[14:15]\n\t" "global_load_dword v68, v5, s[14:15] offset:1024\n\t" ".Lre0_%=:\n\t" "v_lshl_add_u32 v3, v8, 2, v37\n\t" "s_mov_b32 exec_hi, 0\n\t" \
  "ds_write_b32 v3, v7\n\t" "s_mov_b32 exec_hi, -1\n\t" "s_mul_i32 s76, s29, 15360\n\t" "s_mul_i32 s77, s33, 384\n\t" "s_add_u32 s76, s76, s77\n\t" "s_add_u32 s76, s76, 8192\n\t" \
  "s_add_u32 s88, s24, s76\n\t" "s_addc_u32 s89, s25, 0\n\t" "s_add_u32 s86, s88, 3072\n\t" "s_addc_u32 s87, s89, 0\n\t" "s_mul_i32 s76, s28, 1536\n\t" "s_add_u32 s88, s88, s76\n\t" \
  "s_addc_u32 s89, s89, 0\n\t" "s_lshl2_add_u32 s76, s28, s33\n\t" "s_mul_i32 s76, s76, 5120\n\t" "s_add_u32 s76, s76, 361472\n\t" "s_add_u32 s90, s24, s76\n\t" "s_addc_u32 s91, s25, 0\n\t" \
  "s_lshl_b32 s76, s31, 9\n\t" "s_add_u32 s92, s90, s76\n\t" "s_addc_u32 s93, s91, 0\n\t" "v_mov_b32 v116, -1\n\t" "v_mov_b32 v117, -1\n\t" "v_mov_b32 v118, -1\n\t" "v_mov_b32 v119, -1\n\t" \
  "s_mul_i32 s76, s33, 1536\n\t" "s_add_u32 s76, s76, 4096\n\t" "s_add_u32 s94, s66, s76\n\t" "s_addc_u32 s95, s67, 0\n\t" "s_lshr_b32 s76, s33, 1\n\t" "s_add_u32 s76, s76, 2\n\t" \
  "s_sub_u32 s76, s76, s35\n\t" "s_mul_i32 s76, s76, 10240\n\t" "s_and_b32 s77, s33, 1\n\t" "s_lshl_b32 s77, s77, 9\n\t" "s_add_u32 s76, s76, s77\n\t" "s_sub_u32 s76, s76, 2048\n\t" \
  "s_ashr_i32 s77, s76, 31\n\t" "s_add_u32 s68, s60, s76\n\t" "s_addc_u32 s69, s61, s77\n\t" "s_mul_i32 s76, s33, 3072\n\t" "v_add_u32 v2, s76, v32\n\t" "s_mov_b32 s77, 0x3f803f80\n\t" \
  "v_mov_b32 v96, s77\n\t" "v_mov_b32 v97, s77\n\t" "v_mov_b32 v98, s77\n\t" "v_mov_b32 v99, s77\n\t" "v_and_b32 v3, 7, v8\n\t" "v_lshrrev_b32 v12, 3, v8\n\t" "v_lshl_add_u32 v12, v3, 3, v12\n\t" \
  "v_lshlrev_b32 v12, 4, v12\n\t" "v_cmp_eq_u32 s[70:71], 1, v3\n\t" "v_cmp_eq_u32 s[96:97], 2, v3\n\t" "v_cmp_gt_u32 s[98:99], 3, v3\n\t" "v_and_b32 v5, 31, v8\n\t" "v_lshrrev_b32 v13, 5, v8\n\t" \
  "v_min_u32 v7, 23, v5\n\t" "v_mul_u32_u24 v3, 7680, v13\n\t" "v_lshl_add_u32 v3, v7, 4, v3\n\t" "v_mul_u32_u24 v6, 2560, v13\n\t" "v_lshl_add_u32 v6, v5, 4, v6\n\t" "v_mul_u32_u24 v13, 384, v13\n\t" \
  "v_lshl_add_u32 v13, v7, 2, v13\n\t" "s_mul_i32 s76, s33, 96\n\t" "s_add_u32 s76, s76, 155648\n\t" "v_add_u32 v13, s76, v13\n\t" "v_min_u32 v4, 22, v8\n\t" "s_movk_i32 s76, 4096\n\t" \
  "v_lshl_add_u32 v4, v4, 7, s76\n\t" "v_and_b32 v5, 16, v8\n\t" "v_lshl_add_u32 v5, v5, 1, v9\n\t" "s_lshl_b32 s76, s33, 8\n\t" "s_add_u32 s76, s76, 156672\n\t" "v_lshl_add_u32 v5, v5, 2, s76\n\t" \
  "s_mov_b32 s76, 0x3acccccd\n\t" "s_bitcmp0_b32 0x555541, s29\n\t" "s_waitcnt lgkmcnt(0)\n\t" "s_barrier\n\t" "ds_read_b128 v[44:47], v2 offset:24576\n\t" "ds_read_b128 v[48:51], v2 offset:25600\n\t" \
  "ds_read_b128 v[52:55], v2 offset:26624\n\t" "s_cbranch_scc1 .Lc0_%=\n\t" "ds_read_b64_tr_b16 v[72:73], v1 offset:0\n\t" "ds_read_b64_tr_b16 v[74:75], v1 offset:512\n\t" \
  "ds_read_b64_tr_b16 v[76:77], v1 offset:1024\n\t" "ds_read_b64_tr_b16 v[78:79], v1 offset:1536\n\t" "ds_read_b64_tr_b16 v[80:81], v1 offset:2048\n\t" \
  "ds_read_b64_tr_b16 v[82:83], v1 offset:2560\n\t" "ds_read_b64_tr_b16 v[84:85], v1 offset:12288\n\t" "ds_read_b64_tr_b16 v[86:87], v1 offset:12800\n\t" \
  "ds_read_b64_tr_b16 v[88:89], v1 offset:13312\n\t" "ds_read_b64_tr_b16 v[90:91], v1 offset:13824\n\t" "ds_read_b64_tr_b16 v[92:93], v1 offset:14336\n\t" \
  "ds_read_b64_tr_b16 v[94:95], v1 offset:14848\n\t" ".Lc0_%=:\n\t" "s_waitcnt lgkmcnt(0)\n\t" "s_cbranch_scc1 .Lc1_%=\n\t" "v_mfma_f32_16x16x32_bf16 v[100:103], v[96:99], v[72:75], 0\n\t" \
  "v_mfma_f32_16x16x32_bf16 v[104:107], v[96:99], v[76:79], 0\n\t" "v_mfma_f32_16x16x32_bf16 v[108:111], v[96:99], v[80:83], 0\n\t" \
  "v_mfma_f32_16x16x32_bf16 v[100:103], v[96:99], v[84:87], v[100:103]\n\t" "v_mfma_f32_16x16x32_bf16 v[104:107], v[96:99], v[88:91], v[104:107]\n\t" \
  "v_mfma_f32_16x16x32_bf16 v[108:111], v[96:99], v[92:95], v[108:111]\n\t" ".Lc1_%=:\n\t" "s_barrier\n\t" "global_store_dwordx4 v32, v[116:119], s[94:95] sc1\n\t" "s_mov_b32 exec_hi, 0\n\t" \
  "global_store_dwordx4 v32, v[116:119], s[94:95] offset:1024 sc1\n\t" "s_mov_b32 exec_hi, -1\n\t" "global_store_dwordx4 v32, v[116:119], s[60:61] offset:-2048 sc1\n\t" "s_mov_b32 exec_hi, 0\n\t" \
  "global_store_dwordx4 v32, v[116:119], s[68:69] sc1\n\t" "s_mov_b32 exec_hi, -1\n\t" "v_dot2_f32_bf16 v56, v44, s77, 0\n\t" "v_dot2_f32_bf16 v57, v44, v44, 0\n\t" \
  "v_dot2_f32_bf16 v58, v48, s77, 0\n\t" "v_dot2_f32_bf16 v59, v48, v48, 0\n\t" "v_dot2_f32_bf16 v60, v52, s77, 0\n\t" "v_dot2_f32_bf16 v61, v52, v52, 0\n\t" "v_dot2_f32_bf16 v56, v45, s77, v56\n\t" \
  "v_dot2_f32_bf16 v57, v45, v45, v57\n\t" "v_dot2_f32_bf16 v58, v49, s77, v58\n\t" "v_dot2_f32_bf16 v59, v49, v49, v59\n\t" "v_dot2_f32_bf16 v60, v53, s77, v60\n\t" \
  "v_dot2_f32_bf16 v61, v53, v53, v61\n\t" "s_barrier\n\t" "v_dot2_f32_bf16 v56, v46, s77, v56\n\t" "v_dot2_f32_bf16 v57, v46, v46, v57\n\t" "v_dot2_f32_bf16 v58, v50, s77, v58\n\t" \
  "v_dot2_f32_bf16 v59, v50, v50, v59\n\t" "v_dot2_f32_bf16 v60, v54, s77, v60\n\t" "v_dot2_f32_bf16 v61, v54, v54, v61\n\t" "v_dot2_f32_bf16 v56, v47, s77, v56\n\t" \
  "v_dot2_f32_bf16 v57, v47, v47, v57\n\t" "v_dot2_f32_bf16 v58, v51, s77, v58\n\t" "v_dot2_f32_bf16 v59, v51, v51, v59\n\t" "v_dot2_f32_bf16 v60, v55, s77, v60\n\t" \
  "v_dot2_f32_bf16 v61, v55, v55, v61\n\t" "s_barrier\n\t" "s_nop 1\n\t" "v_add_f32_dpp v56, v56, v56 quad_perm:[1,0,3,2] row_mask:0xf bank_mask:0xf\n\t" \
  "v_add_f32_dpp v57, v57, v57 quad_perm:[1,0,3,2] row_mask:0xf bank_mask:0xf\n\t" "v_add_f32_dpp v58, v58, v58 quad_perm:[1,0,3,2] row_mask:0xf bank_mask:0xf\n\t" \
  "v_add_f32_dpp v59, v59, v59 quad_perm:[1,0,3,2] row_mask:0xf bank_mask:0xf\n\t" "v_add_f32_dpp v60, v60, v60 quad_perm:[1,0,3,2] row_mask:0xf bank_mask:0xf\n\t" \
  "v_add_f32_dpp v61, v61, v61 quad_perm:[1,0,3,2] row_mask:0xf bank_mask:0xf\n\t" "v_add_f32_dpp v56, v56, v56 quad_perm:[2,3,0,1] row_mask:0xf bank_mask:0xf\n\t" \
  "v_add_f32_dpp v57, v57, v57 quad_perm:[2,3,0,1] row_mask:0xf bank_mask:0xf\n\t" "v_add_f32_dpp v58, v58, v58 quad_perm:[2,3,0,1] row_mask:0xf bank_mask:0xf\n\t" \
  "v_add_f32_dpp v59, v59, v59 quad_perm:[2,3,0,1] row_mask:0xf bank_mask:0xf\n\t" "v_add_f32_dpp v60, v60, v60 quad_perm:[2,3,0,1] row_mask:0xf bank_mask:0xf\n\t" \
  "v_add_f32_dpp v61, v61, v61 quad_perm:[2,3,0,1] row_mask:0xf bank_mask:0xf\n\t" "v_add_f32_dpp v56, v56, v56 row_half_mirror row_mask:0xf bank_mask:0xf\n\t" \
  "v_add_f32_dpp v57, v57, v57 row_half_mirror row_mask:0xf bank_mask:0xf\n\t" "v_add_f32_dpp v58, v58, v58 row_half_mirror row_mask:0xf bank_mask:0xf\n\t" \
  "v_add_f32_dpp v59, v59, v59 row_half_mirror row_mask:0xf bank_mask:0xf\n\t" "v_add_f32_dpp v60, v60, v60 row_half_mirror row_mask:0xf bank_mask:0xf\n\t" \
  "v_add_f32_dpp v61, v61, v61 row_half_mirror row_mask:0xf bank_mask:0xf\n\t" "s_barrier\n\t" "s_mov_b64 vcc, 0xffff\n\t" "v_cndmask_b32 v56, v56, v58, s[70:71]\n\t" \
  "v_cndmask_b32 v57, v57, v59, s[70:71]\n\t" "v_cndmask_b32 v56, v56, v60, s[96:97]\n\t" "v_cndmask_b32 v57, v57, v61, s[96:97]\n\t" "v_mov_b32 v61, v104\n\t" "v_cndmask_b32 v60, v108, v100, vcc\n\t" \
  "s_waitcnt vmcnt(0)\n\t" "v_add_co_u32 v58, vcc, 1, v40\n\t" "s_nop 1\n\t" "v_addc_co_u32 v59, vcc, 0, v41, vcc\n\t" "s_mov_b64 exec, s[98:99]\n\t" \
  "global_store_dwordx4 v12, v[56:59], s[88:89] sc1\n\t" "s_mov_b64 exec, -1\n\t" "s_cbranch_scc1 .Lc2_%=\n\t" "s_mov_b32 exec_hi, 0\n\t" "global_store_dwordx4 v32, v[58:61], s[92:93] sc1\n\t" \
  "s_mov_b32 exec_hi, -1\n\t" ".Lc2_%=:\n\t" "s_barrier\n\t" "s_cmp_lg_u32 s33, 1\n\t" "s_cbranch_scc1 .Ltp_%=\n\t" "s_memrealtime s[96:97]\n\t" "s_mul_i32 s98, s29, 1280\n\t" \
  "s_add_u32 s98, s98, 5368320\n\t" "v_min_u32 v4, 9, v9\n\t" "v_lshl_add_u32 v4, v4, 7, s98\n\t" "s_lshl_b32 s99, s28, 7\n\t" "s_add_u32 s98, s98, s99\n\t" "v_lshl_add_u32 v7, v9, 3, s98\n\t" \
  "v_mov_b32 v57, v58\n\t" "s_waitcnt lgkmcnt(0)\n\t" "v_mov_b32 v56, s96\n\t" "s_mov_b64 exec, 0xffff\n\t" "global_store_dwordx2 v7, v[56:57], s[24:25] sc1\n\t" "s_mov_b64 exec, -1\n\t" \
  ".Ltp_%=:\n\t" "s_barrier\n\t" "s_barrier\n\t" "global_load_dwordx4 v[72:75], v3, s[86:87] offset:-3072 sc1\n\t" "global_load_dwordx4 v[76:79], v3, s[86:87] offset:-1536 sc1\n\t" \
  "global_load_dwordx4 v[80:83], v3, s[86:87] offset:0 sc1\n\t" "global_load_dwordx4 v[84:87], v3, s[86:87] offset:1536 sc1\n\t" "global_load_dwordx4 v[88:91], v3, s[86:87] offset:3072 sc1\n\t" \
  "global_load_dwordx4 v[92:95], v6, s[90:91] offset:0 sc1\n\t" "global_load_dwordx4 v[96:99], v6, s[90:91] offset:512 sc1\n\t" "global_load_dwordx4 v[100:103], v6, s[90:91] offset:1024 sc1\n\t" \
  "global_load_dwordx4 v[104:107], v6, s[90:91] offset:1536 sc1\n\t" "global_load_dwordx4 v[108:111], v6, s[90:91] offset:2048 sc1\n\t" "s_cmp_lg_u32 s33, 1\n\t" "s_cbranch_scc1 .Lfl0_%=\n\t" \
  "global_load_dwordx2 v[112:113], v4, s[24:25] sc1\n\t" ".Lfl0_%=:\n\t" "s_barrier\n\t" "s_mov_b32 s70, 0\n\t" ".Lrg_%=:\n\t" "s_waitcnt vmcnt(0)\n\t" \
  "v_pk_add_f32 v[120:121], v[72:73], v[76:77]\n\t" "v_pk_add_f32 v[126:127], v[94:95], v[98:99]\n\t" "v_pk_add_f32 v[122:123], v[80:81], v[84:85]\n\t" \
  "v_pk_add_f32 v[128:129], v[102:103], v[106:107]\n\t" "v_pk_add_f32 v[120:121], v[120:121], v[88:89]\n\t" "v_pk_add_f32 v[126:127], v[126:127], v[110:111]\n\t" \
  "v_pk_add_f32 v[124:125], v[120:121], v[122:123]\n\t" "v_pk_add_f32 v[130:131], v[126:127], v[128:129]\n\t" "v_pk_add_f32 v[120:121], v[120:121], v[122:123]\n\t" \
  "v_pk_add_f32 v[126:127], v[126:127], v[128:129]\n\t" "s_nop 0\n\t" "v_permlane32_swap_b32 v120, v124\n\t" "v_cmp_ne_u64 vcc, v[74:75], v[58:59]\n\t" "v_permlane32_swap_b32 v121, v125\n\t" \
  "v_permlane32_swap_b32 v126, v130\n\t" "v_pk_add_f32 v[120:121], v[120:121], v[124:125]\n\t" "v_permlane32_swap_b32 v127, v131\n\t" "v_mul_f32 v120, s76, v120\n\t" \
  "v_pk_add_f32 v[126:127], v[126:127], v[130:131]\n\t" "v_mul_f32 v122, v120, v120\n\t" "v_cmp_ne_u64 s[80:81], v[78:79], v[58:59]\n\t" "s_or_b64 vcc, vcc, s[80:81]\n\t" \
  "v_fma_f32 v121, v121, s76, -v122\n\t" "v_cmp_ne_u64 s[78:79], v[82:83], v[58:59]\n\t" "s_or_b64 vcc, vcc, s[78:79]\n\t" "v_max_f32 v121, 0, v121\n\t" "v_cmp_ne_u64 s[80:81], v[86:87], v[58:59]\n\t" \
  "s_or_b64 vcc, vcc, s[80:81]\n\t" "s_cmp_lg_u32 s70, 0\n\t" "s_cbranch_scc1 .Lb8_%=\n\t" "s_barrier\n\t" ".Lb8_%=:\n\t" "s_mov_b32 s70, 1\n\t" "s_cmp_lg_u32 s33, 3\n\t" "s_cbranch_scc1 .Lfl8_%=\n\t" \
  "global_load_dwordx2 v[112:113], v4, s[24:25] sc1\n\t" ".Lfl8_%=:\n\t" "v_cvt_pk_bf16_f32 v120, v121, v120\n\t" "v_cmp_ne_u64 s[78:79], v[90:91], v[58:59]\n\t" "s_or_b64 vcc, vcc, s[78:79]\n\t" \
  "v_lshlrev_b32 v121, 16, v120\n\t" "v_cmp_ne_u64 s[80:81], v[92:93], v[58:59]\n\t" "s_or_b64 vcc, vcc, s[80:81]\n\t" "v_add_f32 v121, 0x3727c5ac, v121\n\t" \
  "v_cmp_ne_u64 s[78:79], v[96:97], v[58:59]\n\t" "s_or_b64 vcc, vcc, s[78:79]\n\t" "v_cvt_pk_bf16_f32 v121, v121, v121\n\t" "v_cmp_ne_u64 s[80:81], v[100:101], v[58:59]\n\t" \
  "s_or_b64 vcc, vcc, s[80:81]\n\t" "v_lshlrev_b32 v121, 16, v121\n\t" "v_cmp_ne_u64 s[78:79], v[104:105], v[58:59]\n\t" "s_or_b64 vcc, vcc, s[78:79]\n\t" "v_sqrt_f32 v121, v121\n\t" \
  "v_cmp_ne_u64 s[80:81], v[108:109], v[58:59]\n\t" "s_or_b64 vcc, vcc, s[80:81]\n\t" "v_cvt_pk_bf16_f32 v121, v121, v121\n\t" "v_and_b32 v120, 0xffff0000, v120\n\t" "v_lshlrev_b32 v121, 16, v121\n\t" \
  "s_cbranch_vccnz .Lsg_%=\n\t" ".Lrh_%=:\n\t" "s_mov_b32 exec_lo, 0\n\t" "v_rcp_f32 v120, v121\n\t" "s_mov_b32 exec_lo, -1\n\t" "ds_write2_b32 v5, v126, v127 offset1:16\n\t" \
  "ds_write_b32 v13, v120\n\t" "s_mov_b64 exec, 1\n\t" "ds_add_u32 v37, v38 offset:12\n\t" "s_mov_b64 exec, -1\n\t" "s_setprio 0\n\t" "s_cmp_lg_u32 s33, 3\n\t" "s_cbranch_scc1 .Lnfl_%=\n\t" \
  "s_waitcnt vmcnt(0)\n\t" "v_cmp_ne_u64 vcc, v[112:113], v[58:59]\n\t" "s_cbranch_vccz .Lsfm_%=\n\t" ".Lsfl_%=:\n\t" "global_load_dwordx2 v[112:113], v4, s[24:25] sc1\n\t" "s_sub_u32 s39, s39, 1\n\t" \
  "s_cmp_lt_i32 s39, 0\n\t" "s_waitcnt vmcnt(0)\n\t" "s_cbranch_scc1 .Lsfm_%=\n\t" "v_cmp_ne_u64 vcc, v[112:113], v[58:59]\n\t" "s_cbranch_vccnz .Lsfl_%=\n\t" ".Lsfm_%=:\n\t" "s_mov_b64 exec, 1\n\t" \
  "ds_add_u32 v37, v38 offset:4\n\t" "s_mov_b64 exec, -1\n\t" ".Lnfl_%=:\n\t" "s_cmp_lg_u32 s33, 1\n\t" "s_cbranch_scc1 .Lnlg_%=\n\t" "v_subrev_u32 v112, s96, v112\n\t" \
  "v_cmp_eq_u32 vcc, v113, v58\n\t" "s_lshl_b32 s98, 7, s27\n\t" "s_lshr_b32 s98, s98, 1\n\t" "s_and_b32 s98, s98, 31\n\t" "v_cndmask_b32 v112, 60, v112, vcc\n\t" "s_sub_u32 s99, s28, s27\n\t" \
  "s_lshl_b32 s98, s98, s99\n\t" "s_or_b32 s98, s98, 0xffff0000\n\t" "s_mov_b32 s99, 0\n\t" "v_med3_i32 v112, v112, 0, 64\n\t" "v_cndmask_b32 v112, 0, v112, s[98:99]\n\t" "s_nop 1\n\t" \
  "v_max_u32_dpp v112, v112, v112 quad_perm:[1,0,3,2] row_mask:0xf bank_mask:0xf\n\t" "s_nop 1\n\t" "v_max_u32_dpp v112, v112, v112 quad_perm:[2,3,0,1] row_mask:0xf bank_mask:0xf\n\t" "s_nop 1\n\t" \
  "v_max_u32_dpp v112, v112, v112 row_half_mirror row_mask:0xf bank_mask:0xf\n\t" "s_nop 1\n\t" "v_max_u32_dpp v112, v112, v112 row_mirror row_mask:0xf bank_mask:0xf\n\t" "v_add_u32 v7, v10, v37\n\t" \
  "ds_write_b32 v7, v112 offset:80\n\t" ".Lnlg_%=:\n\t" "s_cmp_lg_u32 s33, 2\n\t" "s_cbranch_scc1 .Latt_%=\n\t" "v_dot2_f32_bf16 v1, v68, s77, 0\n\t" "v_dot2_f32_bf16 v1, v64, s77, v1\n\t" \
  "v_dot2_f32_bf16 v1, v65, s77, v1\n\t" "v_dot2_f32_bf16 v1, v66, s77, v1\n\t" "v_dot2_f32_bf16 v1, v67, s77, v1\n\t" "s_nop 2\n\t" \
  "v_add_f32_dpp v1, v1, v1 quad_perm:[1,0,3,2] row_mask:0xf bank_mask:0xf\n\t" "s_nop 1\n\t" "v_add_f32_dpp v1, v1, v1 quad_perm:[2,3,0,1] row_mask:0xf bank_mask:0xf\n\t" "s_nop 1\n\t" \
  "v_add_f32_dpp v1, v1, v1 row_half_mirror row_mask:0xf bank_mask:0xf\n\t" "s_nop 1\n\t" "v_add_f32_dpp v1, v1, v1 row_mirror row_mask:0xf bank_mask:0xf\n\t" "v_mov_b32 v2, v1\n\t" "s_nop 1\n\t" \
  "v_permlane32_swap_b32 v1, v2\n\t" "v_add_f32 v1, v1, v2\n\t" "v_mov_b32 v2, v1\n\t" "s_nop 1\n\t" "v_permlane16_swap_b32 v1, v2\n\t" "v_add_f32 v1, v1, v2\n\t" "ds_write_b32 v37, v1 offset:40\n\t" \
  "s_branch .Latt_%=\n\t" ".Ll_%=:\n\t" "s_movk_i32 s38, 1\n\t" "s_add_u32 s31, s31, 1\n\t" "s_cmp_eq_u32 s31, 10\n\t" "s_cselect_b32 s31, 0, s31\n\t" "s_mul_i32 s76, s31, 245760\n\t" \
  "s_lshl_b32 s77, s31, 7\n\t" "s_add_u32 s78, s10, s76\n\t" "s_addc_u32 s79, s11, 0\n\t" "s_add_u32 s80, s8, s77\n\t" "s_addc_u32 s81, s9, 0\n\t" "s_and_b32 s76, s38, 3\n\t" \
  "s_mul_i32 s76, s76, 36864\n\t" "s_add_u32 m0, s76, s87\n\t" "s_branch .Ll1_%=\n\t" ".Ll0_%=:\n\t" "s_add_u32 s31, s31, 1\n\t" "s_cmp_eq_u32 s31, 10\n\t" "s_cselect_b32 s31, 0, s31\n\t" \
  "s_mul_i32 s76, s31, 245760\n\t" "s_lshl_b32 s77, s31, 7\n\t" "s_add_u32 s78, s10, s76\n\t" "s_addc_u32 s79, s11, 0\n\t" "s_add_u32 s80, s8, s77\n\t" "s_addc_u32 s81, s9, 0\n\t" \
  "s_and_b32 s76, s38, 3\n\t" "s_mul_i32 s76, s76, 36864\n\t" "s_add_u32 m0, s76, s87\n\t" "s_waitcnt vmcnt(9)\n\t" "s_barrier\n\t" ".Ll1_%=:\n\t" \
  "global_load_lds_dwordx4 v46, s[80:81] offset:-2048\n\t" "global_load_lds_dwordx4 v47, s[80:81] offset:-1024\n\t" "global_load_lds_dwordx4 v48, s[80:81] offset:0\n\t" "s_add_u32 m0, s76, s86\n\t" \
  "s_add_u32 s38, s38, 1\n\t" "global_load_lds_dwordx4 v40, s[78:79] offset:-3072\n\t" "global_load_lds_dwordx4 v41, s[78:79] offset:-2048\n\t" "global_load_lds_dwordx4 v42, s[78:79] offset:-1024\n\t" \
  "global_load_lds_dwordx4 v43, s[78:79] offset:0\n\t" "global_load_lds_dwordx4 v44, s[78:79] offset:1024\n\t" "global_load_lds_dwordx4 v45, s[78:79] offset:2048\n\t" "s_cmp_lt_u32 s38, 9\n\t" \
  "s_cbranch_scc1 .Ll0_%=\n\t" "s_add_u32 s31, s31, 1\n\t" "s_cmp_eq_u32 s31, 10\n\t" "s_cselect_b32 s31, 0, s31\n\t" "s_mul_i32 s76, s31, 245760\n\t" "s_lshl_b32 s77, s31, 7\n\t" \
  "s_add_u32 s78, s10, s76\n\t" "s_addc_u32 s79, s11, 0\n\t" "s_add_u32 s80, s8, s77\n\t" "s_addc_u32 s81, s9, 0\n\t" "s_and_b32 s76, s38, 3\n\t" "s_mul_i32 s76, s76, 36864\n\t" \
  "s_add_u32 m0, s76, s87\n\t" "v_lshrrev_b32 v1, 3, v8\n\t" "v_lshrrev_b32 v4, 5, v8\n\t" "v_and_b32 v2, 6, v1\n\t" "v_and_b32 v1, 3, v1\n\t" "v_xor_b32 v2, v2, v8\n\t" \
  "v_mul_u32_u24 v1, 1280, v1\n\t" "v_and_b32 v2, 7, v2\n\t" "s_lshl_b32 s92, s28, 7\n\t" "v_lshl_add_u32 v1, v2, 4, v1\n\t" "s_and_b32 s93, s33, 1\n\t" "s_and_b32 s94, s33, 2\n\t" \
  "v_add_u32 v1, s92, v1\n\t" "s_mul_i32 s92, s93, 5\n\t" "s_add_u32 s92, s92, s94\n\t" "s_add_u32 s92, s92, 5\n\t" "v_add_u32 v2, s92, v4\n\t" "s_lshl2_add_u32 s92, s93, s94\n\t" \
  "s_movk_i32 s93, 5120\n\t" "v_add_lshl_u32 v50, v4, s92, 2\n\t" "v_mad_u32_u24 v49, v2, s93, v1\n\t" "s_lshl_b32 s92, s92, 9\n\t" "s_add_u32 s92, s92, 73728\n\t" "s_waitcnt vmcnt(9)\n\t" \
  "s_barrier\n\t" "global_load_lds_dwordx4 v46, s[80:81] offset:-2048\n\t" "global_load_lds_dwordx4 v47, s[80:81] offset:-1024\n\t" "global_load_lds_dwordx4 v48, s[80:81] offset:0\n\t" \
  "s_add_u32 m0, s76, s86\n\t" "s_nop 0\n\t" "global_load_lds_dwordx4 v40, s[78:79] offset:-3072\n\t" "global_load_lds_dwordx4 v41, s[78:79] offset:-2048\n\t" \
  "global_load_lds_dwordx4 v42, s[78:79] offset:-1024\n\t" "global_load_lds_dwordx4 v43, s[78:79] offset:0\n\t" "global_load_lds_dwordx4 v44, s[78:79] offset:1024\n\t" \
  "global_load_lds_dwordx4 v45, s[78:79] offset:2048\n\t" "s_waitcnt vmcnt(9)\n\t" "s_barrier\n\t" "s_sleep 2\n\t" "s_mov_b64 s[78:79], s[12:13]\n\t" "s_add_u32 m0, s92, 0\n\t" "s_nop 0\n\t" \
  "global_load_lds_dwordx4 v49, s[78:79]\n\t" "s_add_u32 s78, s78, 76800\n\t" "s_addc_u32 s79, s79, 0\n\t" "s_add_u32 m0, s92, 4096\n\t" "s_nop 0\n\t" "global_load_lds_dwordx4 v49, s[78:79]\n\t" \
  "s_add_u32 s78, s78, 76800\n\t" "s_addc_u32 s79, s79, 0\n\t" "s_add_u32 m0, s92, 8192\n\t" "s_nop 0\n\t" "global_load_lds_dwordx4 v49, s[78:79]\n\t" "s_add_u32 s78, s78, 76800\n\t" \
  "s_addc_u32 s79, s79, 0\n\t" "s_add_u32 m0, s92, 12288\n\t" "s_nop 0\n\t" "global_load_lds_dwordx4 v49, s[78:79]\n\t" "s_add_u32 s78, s78, 76800\n\t" "s_addc_u32 s79, s79, 0\n\t" \
  "s_add_u32 m0, s92, 16384\n\t" "s_waitcnt vmcnt(4)\n\t" "s_barrier\n\t" "global_load_lds_dwordx4 v49, s[78:79]\n\t" "s_add_u32 s78, s78, 102400\n\t" "s_addc_u32 s79, s79, 0\n\t" \
  "s_add_u32 m0, s92, 40960\n\t" "s_nop 0\n\t" "global_load_lds_dwordx4 v49, s[78:79]\n\t" "s_add_u32 s78, s78, 76800\n\t" "s_addc_u32 s79, s79, 0\n\t" "s_add_u32 m0, s92, 45056\n\t" "s_nop 0\n\t" \
  "global_load_lds_dwordx4 v49, s[78:79]\n\t" "s_add_u32 s78, s78, 76800\n\t" "s_addc_u32 s79, s79, 0\n\t" "s_add_u32 m0, s92, 49152\n\t" "s_nop 0\n\t" "global_load_lds_dwordx4 v49, s[78:79]\n\t" \
  "s_add_u32 s78, s78, 76800\n\t" "s_addc_u32 s79, s79, 0\n\t" "s_add_u32 m0, s92, 53248\n\t" "s_nop 0\n\t" "global_load_lds_dwordx4 v49, s[78:79]\n\t" "s_add_u32 s78, s78, 76800\n\t" \
  "s_addc_u32 s79, s79, 0\n\t" "s_add_u32 m0, s92, 57344\n\t" "s_nop 0\n\t" "global_load_lds_dwordx4 v49, s[78:79]\n\t" "s_setprio 0\n\t" "s_mov_b32 s94, 0xf41e3209\n\t" \
  "v_bfe_u32 v2, s94, v50, 4\n\t" "v_mad_u32_u24 v3, v2, s93, v1\n\t" "s_mov_b32 s94, 0xfe0d3218\n\t" "v_bfe_u32 v2, s94, v50, 4\n\t" "v_mad_u32_u24 v4, v2, s93, v1\n\t" \
  "s_mov_b32 s94, 0xfedc2107\n\t" "v_bfe_u32 v2, s94, v50, 4\n\t" "v_mad_u32_u24 v5, v2, s93, v1\n\t" "s_mov_b32 s94, 0xedcbf106\n\t" "v_bfe_u32 v2, s94, v50, 4\n\t" \
  "v_mad_u32_u24 v6, v2, s93, v1\n\t" "s_mov_b32 s94, 0xfdcabe05\n\t" "v_bfe_u32 v2, s94, v50, 4\n\t" "v_mad_u32_u24 v7, v2, s93, v1\n\t" "s_waitcnt vmcnt(0)\n\t" "s_mov_b64 exec, 1\n\t" \
  "ds_add_u32 v37, v38 offset:4\n\t" "s_mov_b64 exec, -1\n\t" "s_branch .Latt_%=\n\t" ".Lsg_%=:\n\t" "s_sub_u32 s39, s39, 1\n\t" "s_cmp_lt_i32 s39, 0\n\t" "s_cbranch_scc1 .Lrh_%=\n\t" \
  "global_load_dwordx4 v[72:75], v3, s[86:87] offset:-3072 sc1\n\t" "global_load_dwordx4 v[76:79], v3, s[86:87] offset:-1536 sc1\n\t" "global_load_dwordx4 v[80:83], v3, s[86:87] offset:0 sc1\n\t" \
  "global_load_dwordx4 v[84:87], v3, s[86:87] offset:1536 sc1\n\t" "global_load_dwordx4 v[88:91], v3, s[86:87] offset:3072 sc1\n\t" "global_load_dwordx4 v[92:95], v6, s[90:91] offset:0 sc1\n\t" \
  "global_load_dwordx4 v[96:99], v6, s[90:91] offset:512 sc1\n\t" "global_load_dwordx4 v[100:103], v6, s[90:91] offset:1024 sc1\n\t" "global_load_dwordx4 v[104:107], v6, s[90:91] offset:1536 sc1\n\t" \
  "global_load_dwordx4 v[108:111], v6, s[90:91] offset:2048 sc1\n\t" "s_branch .Lrg_%=\n\t" ".Lts_%=:\n\t" "ds_read_b32 v39, v37 offset:12\n\t" "ds_read_b128 v[160:163], v12 offset:1024\n\t" \
  "ds_read_b128 v[164:167], v12 offset:1088\n\t" "ds_read2_b32 v[136:137], v4 offset0:0 offset1:96\n\t" "ds_read2_b32 v[138:139], v4 offset0:16 offset1:112\n\t" \
  "ds_read2_b32 v[140:141], v4 offset0:32 offset1:128\n\t" "ds_read2_b32 v[142:143], v4 offset0:48 offset1:144\n\t" "ds_read2_b32 v[144:145], v4 offset0:64 offset1:160\n\t" \
  "ds_read2_b32 v[146:147], v4 offset0:80 offset1:176\n\t" "ds_read_b32 v14, v13 offset:1152\n\t" "s_sub_u32 s39, s39, 1\n\t" "s_cmp_lt_i32 s39, 0\n\t" "s_waitcnt lgkmcnt(0)\n\t" \
  "v_readfirstlane_b32 s38, v39\n\t" "s_cbranch_scc1 .Ltr_%=\n\t" "s_cmp_ge_u32 s38, 4\n\t" "s_cbranch_scc0 .Lts_%=\n\t" "s_branch .Ltr_%=\n\t" ".Lsq_%=:\n\t" "ds_read_b32 v39, v37 offset:0\n\t" \
  "s_sub_u32 s39, s39, 1\n\t" "s_cmp_lt_i32 s39, 0\n\t" "s_waitcnt lgkmcnt(0)\n\t" "v_readfirstlane_b32 s38, v39\n\t" "s_cbranch_scc1 .Lrq_%=\n\t" "s_cmp_ge_u32 s38, 4\n\t" \
  "s_cbranch_scc0 .Lsq_%=\n\t" "s_branch .Lq_%=\n\t" ".Lsqm_%=:\n\t" "ds_read_b32 v39, v37 offset:8\n\t" "s_sub_u32 s39, s39, 1\n\t" "s_cmp_lt_i32 s39, 0\n\t" "s_waitcnt lgkmcnt(0)\n\t" \
  "v_readfirstlane_b32 s38, v39\n\t" "s_cbranch_scc1 .Lrqm_%=\n\t" "s_cmp_ge_u32 s38, 4\n\t" "s_cbranch_scc0 .Lsqm_%=\n\t" "s_branch .Lrqm_%=\n\t" ".Lsq0_%=:\n\t" "ds_read_b32 v39, v37 offset:0\n\t" \
  "s_sub_u32 s39, s39, 1\n\t" "s_cmp_lt_i32 s39, 0\n\t" "s_waitcnt lgkmcnt(0)\n\t" "v_readfirstlane_b32 s38, v39\n\t" "s_cbranch_scc1 .Lrq0_%=\n\t" "s_cmp_ge_u32 s38, 4\n\t" \
  "s_cbranch_scc0 .Lsq0_%=\n\t" "s_branch .Lq0_%=\n\t" ".Lsv0_%=:\n\t" "ds_read_b32 v39, v37 offset:4\n\t" "s_sub_u32 s39, s39, 1\n\t" "s_cmp_lt_i32 s39, 0\n\t" "s_waitcnt lgkmcnt(0)\n\t" \
  "v_readfirstlane_b32 s38, v39\n\t" "s_cbranch_scc1 .Lrv0_%=\n\t" "s_cmp_ge_u32 s38, 9\n\t" "s_cbranch_scc0 .Lsv0_%=\n\t" "s_branch .Lv0_%=\n\t" ".Lsr_%=:\n\t" \
  "global_load_dwordx4 v[148:151], v32, s[58:59] sc1\n\t" "global_load_dwordx4 v[144:147], v32, s[56:57] sc1\n\t" "s_sub_u32 s39, s39, 1\n\t" "s_cmp_lt_i32 s39, 0\n\t" "s_waitcnt vmcnt(0)\n\t" \
  "s_cbranch_scc1 .Lrr_%=\n\t" "v_max_u32 v124, v144, v148\n\t" "v_cmp_eq_u32 vcc, -1, v124\n\t" "s_cbranch_vccnz .Lsr_%=\n\t" "s_branch .Lrr_%=\n\t" ".Lk0_%=:\n\t" "v_xor_b32 v0, 32, v35\n\t" \
  "v_xor_b32 v1, 64, v35\n\t" "v_xor_b32 v2, 96, v35\n\t" "ds_read_b32 v39, v37 offset:8\n\t" "s_waitcnt lgkmcnt(0)\n\t" "v_readfirstlane_b32 s38, v39\n\t" "s_cmp_ge_u32 s38, 4\n\t" \
  "s_cbranch_scc0 .Lsqm_%=\n\t" ".Lrqm_%=:\n\t" "ds_read_b32 v39, v37 offset:80\n\t" "s_memrealtime s[76:77]\n\t" "s_mul_i32 s80, s35, 5\n\t" "s_add_u32 s80, s80, 24\n\t" "s_waitcnt lgkmcnt(0)\n\t" \
  "v_readfirstlane_b32 s38, v39\n\t" "s_add_u32 s80, s80, s38\n\t" ".Lqd_%=:\n\t" "s_memrealtime s[78:79]\n\t" "s_waitcnt lgkmcnt(0)\n\t" "s_sub_u32 s78, s78, s76\n\t" "s_cmp_lt_u32 s78, s80\n\t" \
  "s_cbranch_scc1 .Lqd_%=\n\t" "global_load_dwordx4 v[148:151], v32, s[58:59] sc1\n\t" "global_load_dwordx4 v[144:147], v32, s[56:57] sc1\n\t" ".Lq0_%=:\n\t" "ds_read_b32 v39, v37\n\t" \
  "ds_read_b64 v[40:41], v30 offset:0\n\t" "ds_read_b64 v[42:43], v30 offset:3072\n\t" "ds_read_b64 v[44:45], v30 offset:11776\n\t" "ds_read_b64 v[46:47], v30 offset:14848\n\t" \
  "ds_read_b64 v[48:49], v30 offset:12288\n\t" "ds_read_b64 v[50:51], v30 offset:15360\n\t" "s_waitcnt lgkmcnt(6)\n\t" "v_readfirstlane_b32 s38, v39\n\t" "s_cmp_ge_u32 s38, 4\n\t" \
  "s_cbranch_scc0 .Lsq0_%=\n\t" ".Lrq0_%=:\n\t" "s_waitcnt lgkmcnt(0)\n\t" "v_cndmask_b32 v52, 0, v40, s[44:45]\n\t" "v_cndmask_b32 v53, 0, v41, s[44:45]\n\t" "v_cndmask_b32 v54, 0, v42, s[46:47]\n\t" \
  "v_cndmask_b32 v55, 0, v43, s[46:47]\n\t" "v_cndmask_b32 v40, 0, v40, s[40:41]\n\t" "v_cndmask_b32 v41, 0, v41, s[40:41]\n\t" "v_cndmask_b32 v42, 0, v42, s[42:43]\n\t" \
  "v_cndmask_b32 v43, 0, v43, s[42:43]\n\t" "s_nop 1\n\t" "v_mfma_f32_16x16x32_bf16 v[108:111], v[44:47], v[40:43], v[20:23]\n\t" \
  "v_mfma_f32_16x16x32_bf16 v[112:115], v[48:51], v[40:43], v[24:27]\n\t" "v_mfma_f32_16x16x32_bf16 v[116:119], v[44:47], v[52:55], v[20:23]\n\t" \
  "v_mfma_f32_16x16x32_bf16 v[120:123], v[48:51], v[52:55], v[24:27]\n\t" ".Lv0_%=:\n\t" "ds_read_b32 v39, v37 offset:4\n\t" "ds_read_b64 v[128:129], v30 offset:24064\n\t" \
  "ds_read_b64 v[130:131], v30 offset:24576\n\t" "ds_read_b64 v[132:133], v30 offset:27136\n\t" "ds_read_b64 v[134:135], v30 offset:27648\n\t" "s_waitcnt lgkmcnt(4)\n\t" \
  "v_readfirstlane_b32 s38, v39\n\t" "s_cmp_ge_u32 s38, 9\n\t" "s_cbranch_scc0 .Lsv0_%=\n\t" ".Lrv0_%=:\n\t" "s_waitcnt lgkmcnt(0)\n\t" "v_cndmask_b32 v136, v128, 0, s[50:51]\n\t" \
  "v_cndmask_b32 v128, 0, v128, s[50:51]\n\t" "v_cndmask_b32 v137, v129, 0, s[50:51]\n\t" "v_cndmask_b32 v129, 0, v129, s[50:51]\n\t" "v_cndmask_b32 v138, v130, 0, s[50:51]\n\t" \
  "v_cndmask_b32 v130, 0, v130, s[50:51]\n\t" "v_cndmask_b32 v139, v131, 0, s[50:51]\n\t" "v_cndmask_b32 v131, 0, v131, s[50:51]\n\t" "v_cndmask_b32 v140, v132, 0, s[52:53]\n\t" \
  "v_cndmask_b32 v132, 0, v132, s[52:53]\n\t" "v_cndmask_b32 v141, v133, 0, s[52:53]\n\t" "v_cndmask_b32 v133, 0, v133, s[52:53]\n\t" "v_cndmask_b32 v142, v134, 0, s[52:53]\n\t" \
  "v_cndmask_b32 v134, 0, v134, s[52:53]\n\t" "v_cndmask_b32 v143, v135, 0, s[52:53]\n\t" "v_cndmask_b32 v135, 0, v135, s[52:53]\n\t" "s_cmp_ge_u32 s26, 4\n\t" "s_cbranch_scc1 .Lnw2_%=\n\t" \
  "s_add_u32 m0, s92, 20480\n\t" "s_nop 0\n\t" "global_load_lds_dwordx4 v3, s[12:13]\n\t" "s_add_u32 s12, s12, 81920\n\t" "s_addc_u32 s13, s13, 0\n\t" "s_add_u32 m0, s92, 24576\n\t" "s_nop 0\n\t" \
  "global_load_lds_dwordx4 v4, s[12:13]\n\t" "s_add_u32 s12, s12, 81920\n\t" "s_addc_u32 s13, s13, 0\n\t" "s_add_u32 m0, s92, 28672\n\t" "s_nop 0\n\t" "global_load_lds_dwordx4 v5, s[12:13]\n\t" \
  "s_add_u32 s12, s12, 81920\n\t" "s_addc_u32 s13, s13, 0\n\t" "s_add_u32 m0, s92, 32768\n\t" "s_nop 0\n\t" "global_load_lds_dwordx4 v6, s[12:13]\n\t" "s_add_u32 s12, s12, 81920\n\t" \
  "s_addc_u32 s13, s13, 0\n\t" "s_add_u32 m0, s92, 36864\n\t" "s_nop 0\n\t" "global_load_lds_dwordx4 v7, s[12:13]\n\t" "s_add_u32 s12, s12, 81920\n\t" "s_addc_u32 s13, s13, 0\n\t" \
  "s_add_u32 m0, s92, 61440\n\t" "s_nop 0\n\t" "global_load_lds_dwordx4 v3, s[12:13]\n\t" "s_add_u32 s12, s12, 81920\n\t" "s_addc_u32 s13, s13, 0\n\t" "s_add_u32 m0, s92, 65536\n\t" "s_nop 0\n\t" \
  "global_load_lds_dwordx4 v4, s[12:13]\n\t" "s_add_u32 s12, s12, 81920\n\t" "s_addc_u32 s13, s13, 0\n\t" "s_add_u32 m0, s92, 69632\n\t" "s_nop 0\n\t" "global_load_lds_dwordx4 v5, s[12:13]\n\t" \
  "s_add_u32 s12, s12, 81920\n\t" "s_addc_u32 s13, s13, 0\n\t" "s_add_u32 m0, s92, 73728\n\t" "s_nop 0\n\t" "global_load_lds_dwordx4 v6, s[12:13]\n\t" "s_add_u32 s12, s12, 81920\n\t" \
  "s_addc_u32 s13, s13, 0\n\t" "s_add_u32 m0, s92, 77824\n\t" "s_nop 0\n\t" "global_load_lds_dwordx4 v7, s[12:13]\n\t" ".Lnw2_%=:\n\t" "s_cmp_ge_u32 s26, 4\n\t" "s_waitcnt vmcnt(10)\n\t" \
  "s_cbranch_scc0 .Lnw3_%=\n\t" "s_waitcnt vmcnt(0)\n\t" ".Lnw3_%=:\n\t" "s_setprio 2\n\t" "v_max_u32 v124, v144, v148\n\t" "v_cmp_eq_u32 vcc, -1, v124\n\t" "s_cbranch_vccnz .Lsr_%=\n\t" \
  ".Lrr_%=:\n\t" "v_cndmask_b32 v124, 0, v144, s[48:49]\n\t" "v_cndmask_b32 v125, 0, v145, s[48:49]\n\t" "v_cndmask_b32 v126, v144, 0, s[48:49]\n\t" "v_cndmask_b32 v127, v145, 0, s[48:49]\n\t" \
  "v_mfma_f32_16x16x16_bf16 v[108:111], v[150:151], v[124:125], v[108:111]\n\t" "v_mfma_f32_16x16x16_bf16 v[112:115], v[146:147], v[124:125], v[112:115]\n\t" \
  "v_mfma_f32_16x16x16_bf16 v[116:119], v[150:151], v[126:127], v[116:119]\n\t" "v_mfma_f32_16x16x16_bf16 v[120:123], v[146:147], v[126:127], v[120:123]\n\t" "s_branch .Lk1_%=\n\t" ".Lsv1_%=:\n\t" \
  "ds_read_b32 v39, v37 offset:4\n\t" "s_sub_u32 s39, s39, 1\n\t" "s_cmp_lt_i32 s39, 0\n\t" "s_waitcnt lgkmcnt(0)\n\t" "v_readfirstlane_b32 s38, v39\n\t" "s_cbranch_scc1 .Lrv1_%=\n\t" \
  "s_cmp_ge_u32 s38, 9\n\t" "s_cbranch_scc0 .Lsv1_%=\n\t" "ds_read_b64 v[128:129], v30 offset:24064\n\t" "ds_read_b64 v[130:131], v30 offset:24576\n\t" "ds_read_b64 v[132:133], v30 offset:27136\n\t" \
  "ds_read_b64 v[134:135], v30 offset:27648\n\t" "ds_read_b64 v[136:137], v31 offset:24064\n\t" "ds_read_b64 v[138:139], v31 offset:24576\n\t" "ds_read_b64 v[140:141], v31 offset:27136\n\t" \
  "ds_read_b64 v[142:143], v31 offset:27648\n\t" "s_branch .Lrv1_%=\n\t" ".Lk2_%=:\n\t" "s_waitcnt lgkmcnt(8)\n\t" "v_readfirstlane_b32 s38, v39\n\t" "s_cmp_ge_u32 s38, 9\n\t" \
  "s_cbranch_scc0 .Lsv1_%=\n\t" ".Lrv1_%=:\n\t" "s_waitcnt lgkmcnt(0)\n\t" "v_mfma_f32_16x16x32_bf16 v[152:155], v[128:131], v[112:115], 0\n\t" \
  "v_mfma_f32_16x16x32_bf16 v[164:167], v[140:143], v[120:123], 0\n\t" "v_mfma_f32_16x16x32_bf16 v[156:159], v[132:135], v[112:115], 0\n\t" \
  "v_mfma_f32_16x16x32_bf16 v[160:163], v[136:139], v[120:123], 0\n\t" "v_xor_b32 v0, 32, v35\n\t" "v_xor_b32 v1, 64, v35\n\t" "v_xor_b32 v2, 96, v35\n\t" "s_nop 1\n\t" \
  "v_cvt_pk_bf16_f32 v144, v152, v153\n\t" "v_cvt_pk_bf16_f32 v145, v154, v155\n\t" "v_cvt_pk_bf16_f32 v146, v164, v165\n\t" "v_cvt_pk_bf16_f32 v147, v166, v167\n\t" \
  "global_store_dwordx4 v32, v[144:147], s[60:61] offset:-2048 sc1\n\t" "v_cvt_pk_bf16_f32 v148, v156, v157\n\t" "v_cvt_pk_bf16_f32 v149, v158, v159\n\t" "v_cvt_pk_bf16_f32 v150, v160, v161\n\t" \
  "v_cvt_pk_bf16_f32 v151, v162, v163\n\t" "s_branch .Lk3_%=\n\t" ".Lso0_%=:\n\t" "global_load_dwordx4 v[152:155], v32, s[62:63] offset:-2048 sc1\n\t" "s_sub_u32 s39, s39, 1\n\t" \
  "s_cmp_lt_i32 s39, 0\n\t" "s_waitcnt vmcnt(0)\n\t" "s_cbranch_scc1 .Lro0_%=\n\t" "v_cmp_eq_u32 vcc, -1, v152\n\t" "s_cbranch_vccnz .Lso0_%=\n\t" "s_branch .Lro0_%=\n\t" ".Lso1_%=:\n\t" \
  "global_load_dwordx4 v[156:159], v32, s[62:63] offset:-1024 sc1\n\t" "s_sub_u32 s39, s39, 1\n\t" "s_cmp_lt_i32 s39, 0\n\t" "s_waitcnt vmcnt(0)\n\t" "s_cbranch_scc1 .Lro1_%=\n\t" \
  "v_cmp_eq_u32 vcc, -1, v156\n\t" "s_cbranch_vccnz .Lso1_%=\n\t" "s_branch .Lro1_%=\n\t" ".Lso2_%=:\n\t" "global_load_dwordx4 v[160:163], v32, s[62:63] offset:0 sc1\n\t" "s_sub_u32 s39, s39, 1\n\t" \
  "s_cmp_lt_i32 s39, 0\n\t" "s_waitcnt vmcnt(0)\n\t" "s_cbranch_scc1 .Lro2_%=\n\t" "v_cmp_eq_u32 vcc, -1, v160\n\t" "s_cbranch_vccnz .Lso2_%=\n\t" "s_branch .Lro2_%=\n\t" ".Lso3_%=:\n\t" \
  "global_load_dwordx4 v[164:167], v32, s[62:63] offset:1024 sc1\n\t" "s_sub_u32 s39, s39, 1\n\t" "s_cmp_lt_i32 s39, 0\n\t" "s_waitcnt vmcnt(0)\n\t" "s_cbranch_scc1 .Lro3_%=\n\t" \
  "v_cmp_eq_u32 vcc, -1, v164\n\t" "s_cbranch_vccnz .Lso3_%=\n\t" "s_branch .Lro3_%=\n\t" ".Lso4_%=:\n\t" "global_load_dwordx4 v[152:155], v32, s[62:63] offset:2048 sc1\n\t" \
  "s_sub_u32 s39, s39, 1\n\t" "s_cmp_lt_i32 s39, 0\n\t" "s_waitcnt vmcnt(0)\n\t" "s_cbranch_scc1 .Lro4_%=\n\t" "v_cmp_eq_u32 vcc, -1, v152\n\t" "s_cbranch_vccnz .Lso4_%=\n\t" "s_branch .Lro4_%=\n\t" \
  ".Lso5_%=:\n\t" "global_load_dwordx4 v[156:159], v32, s[64:65] offset:-2048 sc1\n\t" "s_sub_u32 s39, s39, 1\n\t" "s_cmp_lt_i32 s39, 0\n\t" "s_waitcnt vmcnt(0)\n\t" "s_cbranch_scc1 .Lro5_%=\n\t" \
  "v_cmp_eq_u32 vcc, -1, v156\n\t" "s_cbranch_vccnz .Lso5_%=\n\t" "s_branch .Lro5_%=\n\t" ".Lso6_%=:\n\t" "global_load_dwordx4 v[160:163], v32, s[64:65] offset:-1024 sc1\n\t" \
  "s_sub_u32 s39, s39, 1\n\t" "s_cmp_lt_i32 s39, 0\n\t" "s_waitcnt vmcnt(0)\n\t" "s_cbranch_scc1 .Lro6_%=\n\t" "v_cmp_eq_u32 vcc, -1, v160\n\t" "s_cbranch_vccnz .Lso6_%=\n\t" "s_branch .Lro6_%=\n\t" \
  ".Lso7_%=:\n\t" "global_load_dwordx4 v[164:167], v32, s[64:65] offset:0 sc1\n\t" "s_sub_u32 s39, s39, 1\n\t" "s_cmp_lt_i32 s39, 0\n\t" "s_waitcnt vmcnt(0)\n\t" "s_cbranch_scc1 .Lro7_%=\n\t" \
  "v_cmp_eq_u32 vcc, -1, v164\n\t" "s_cbranch_vccnz .Lso7_%=\n\t" "s_branch .Lro7_%=\n\t" ".Lso8_%=:\n\t" "global_load_dwordx4 v[152:155], v32, s[64:65] offset:1024 sc1\n\t" \
  "s_sub_u32 s39, s39, 1\n\t" "s_cmp_lt_i32 s39, 0\n\t" "s_waitcnt vmcnt(0)\n\t" "s_cbranch_scc1 .Lro8_%=\n\t" "v_cmp_eq_u32 vcc, -1, v152\n\t" "s_cbranch_vccnz .Lso8_%=\n\t" "s_branch .Lro8_%=\n\t" \
  ".Lso9_%=:\n\t" "global_load_dwordx4 v[156:159], v32, s[64:65] offset:2048 sc1\n\t" "s_sub_u32 s39, s39, 1\n\t" "s_cmp_lt_i32 s39, 0\n\t" "s_waitcnt vmcnt(0)\n\t" "s_cbranch_scc1 .Lro9_%=\n\t" \
  "v_cmp_eq_u32 vcc, -1, v156\n\t" "s_cbranch_vccnz .Lso9_%=\n\t" "s_branch .Lro9_%=\n\t" ".Lg_%=:\n\t" "v_bfe_u32 v13, s80, v10, 4\n\t" "v_lshlrev_b32 v12, 1, v9\n\t" "s_movk_i32 s76, 24576\n\t" \
  "v_and_b32 v12, 28, v12\n\t" "v_lshl_add_u32 v2, v9, 7, s76\n\t" "v_xor_b32 v12, v12, v10\n\t" "s_mov_b32 s76, 155648\n\t" "v_lshl_add_u32 v2, v12, 2, v2\n\t" "v_lshl_add_u32 v4, v9, 2, s76\n\t" \
  "v_xor_b32 v3, 64, v2\n\t" "v_lshl_add_u32 v5, v10, 2, s76\n\t" "s_mul_i32 s76, s33, 3072\n\t" "v_lshl_add_u32 v6, v8, 3, s76\n\t" "s_sub_u32 s76, 5, s27\n\t" "s_cmp_eq_u32 s27, 0\n\t" \
  "s_cselect_b32 s76, 0, s76\n\t" "s_sub_u32 s77, 15, s27\n\t" "s_cmp_eq_u32 s27, 4\n\t" "s_cselect_b32 s77, 16, s77\n\t" "v_cmp_gt_u32 s[68:69], s76, v13\n\t" "v_cmp_le_u32 vcc, s77, v13\n\t" \
  "v_add3_u32 v13, v13, s27, -1\n\t" "s_or_b64 s[68:69], s[68:69], vcc\n\t" "v_and_b32 v13, 3, v13\n\t" "v_cndmask_b32 v12, 0, 50, vcc\n\t" "v_xor_b32 v13, v13, v12\n\t" "v_lshlrev_b32 v7, 4, v9\n\t" \
  "v_lshl_add_u32 v7, v13, 8, v7\n\t" "v_lshl_add_u32 v12, s33, 8, v5\n\t" "v_lshl_add_u32 v13, s33, 8, v4\n\t" "s_setprio 1\n\t" "s_movk_i32 s38, 9\n\t" "s_mov_b32 s77, 36864\n\t" \
  "v_mov_b64 v[40:41], 0\n\t" "v_mov_b64 v[42:43], 0\n\t" "v_mov_b64 v[44:45], 0\n\t" "v_mov_b64 v[46:47], 0\n\t" "v_mov_b64 v[48:49], 0\n\t" "v_mov_b64 v[50:51], 0\n\t" "v_mov_b64 v[52:53], 0\n\t" \
  "v_mov_b64 v[54:55], 0\n\t" "v_mov_b64 v[56:57], 0\n\t" "v_mov_b64 v[58:59], 0\n\t" "v_mov_b64 v[60:61], 0\n\t" "v_mov_b64 v[62:63], 0\n\t" "v_mov_b64 v[64:65], 0\n\t" "v_mov_b64 v[66:67], 0\n\t" \
  "v_mov_b64 v[68:69], 0\n\t" "v_mov_b64 v[70:71], 0\n\t" "v_mov_b64 v[72:73], 0\n\t" "v_mov_b64 v[74:75], 0\n\t" "v_mov_b64 v[76:77], 0\n\t" "v_mov_b64 v[78:79], 0\n\t" "v_mov_b64 v[80:81], 0\n\t" \
  "v_mov_b64 v[82:83], 0\n\t" "v_mov_b64 v[84:85], 0\n\t" "v_mov_b64 v[86:87], 0\n\t" "v_mov_b64 v[88:89], 0\n\t" "v_mov_b64 v[90:91], 0\n\t" "v_mov_b64 v[92:93], 0\n\t" "v_mov_b64 v[94:95], 0\n\t" \
  "v_mov_b64 v[96:97], 0\n\t" "v_mov_b64 v[98:99], 0\n\t" "v_mov_b64 v[100:101], 0\n\t" "v_mov_b64 v[102:103], 0\n\t" "v_mov_b64 v[104:105], 0\n\t" "v_mov_b64 v[106:107], 0\n\t" \
  "v_mov_b64 v[108:109], 0\n\t" "v_mov_b64 v[110:111], 0\n\t" "s_barrier\n\t" "ds_read_b64_tr_b16 v[136:137], v1 offset:0\n\t" "ds_read_b64_tr_b16 v[138:139], v1 offset:512\n\t" \
  "ds_read_b128 v[112:115], v2 offset:0\n\t" "ds_read_b64_tr_b16 v[140:141], v1 offset:1024\n\t" "ds_read_b64_tr_b16 v[142:143], v1 offset:1536\n\t" "ds_read_b64_tr_b16 v[144:145], v1 offset:2048\n\t" \
  "ds_read_b64_tr_b16 v[146:147], v1 offset:2560\n\t" "ds_read_b128 v[116:119], v2 offset:2048\n\t" "ds_read_b128 v[120:123], v2 offset:4096\n\t" "ds_read_b128 v[124:127], v2 offset:6144\n\t" \
  "ds_read_b128 v[128:131], v2 offset:8192\n\t" "ds_read_b128 v[132:135], v2 offset:10240\n\t" "s_branch .Lb_%=\n\t" ".p2align 6\n\t" ".La_%=:\n\t" "s_waitcnt lgkmcnt(9)\n\t" \
  "v_mfma_f32_16x16x32_bf16 v[40:43], v[148:151], v[112:115], v[40:43]\n\t" "v_xor_b32 v3, 64, v2\n\t" "s_waitcnt lgkmcnt(7)\n\t" \
  "v_mfma_f32_16x16x32_bf16 v[64:67], v[152:155], v[112:115], v[64:67]\n\t" "s_waitcnt lgkmcnt(5)\n\t" "v_mfma_f32_16x16x32_bf16 v[88:91], v[112:115], v[156:159], v[88:91]\n\t" \
  "s_waitcnt lgkmcnt(4)\n\t" "v_mfma_f32_16x16x32_bf16 v[44:47], v[148:151], v[116:119], v[44:47]\n\t" "v_mfma_f32_16x16x32_bf16 v[68:71], v[152:155], v[116:119], v[68:71]\n\t" \
  "v_mfma_f32_16x16x32_bf16 v[92:95], v[116:119], v[156:159], v[92:95]\n\t" "s_waitcnt lgkmcnt(0)\n\t" "s_barrier\n\t" "v_mfma_f32_16x16x32_bf16 v[48:51], v[148:151], v[120:123], v[48:51]\n\t" \
  "ds_read_b64_tr_b16 v[136:137], v1 offset:0\n\t" "ds_read_b64_tr_b16 v[138:139], v1 offset:512\n\t" "v_mfma_f32_16x16x32_bf16 v[72:75], v[152:155], v[120:123], v[72:75]\n\t" \
  "ds_read_b128 v[112:115], v2 offset:0\n\t" "ds_read_b64_tr_b16 v[140:141], v1 offset:1024\n\t" "v_mfma_f32_16x16x32_bf16 v[96:99], v[120:123], v[156:159], v[96:99]\n\t" \
  "ds_read_b64_tr_b16 v[142:143], v1 offset:1536\n\t" "ds_read_b64_tr_b16 v[144:145], v1 offset:2048\n\t" "v_mfma_f32_16x16x32_bf16 v[52:55], v[148:151], v[124:127], v[52:55]\n\t" \
  "ds_read_b64_tr_b16 v[146:147], v1 offset:2560\n\t" "ds_read_b128 v[116:119], v2 offset:2048\n\t" "v_mfma_f32_16x16x32_bf16 v[76:79], v[152:155], v[124:127], v[76:79]\n\t" \
  "ds_read_b128 v[120:123], v2 offset:4096\n\t" "v_mfma_f32_16x16x32_bf16 v[100:103], v[124:127], v[156:159], v[100:103]\n\t" "ds_read_b128 v[124:127], v2 offset:6144\n\t" \
  "v_mfma_f32_16x16x32_bf16 v[56:59], v[148:151], v[128:131], v[56:59]\n\t" "v_mfma_f32_16x16x32_bf16 v[80:83], v[152:155], v[128:131], v[80:83]\n\t" \
  "v_mfma_f32_16x16x32_bf16 v[104:107], v[128:131], v[156:159], v[104:107]\n\t" "ds_read_b128 v[128:131], v2 offset:8192\n\t" "v_mfma_f32_16x16x32_bf16 v[60:63], v[148:151], v[132:135], v[60:63]\n\t" \
  "v_mfma_f32_16x16x32_bf16 v[84:87], v[152:155], v[132:135], v[84:87]\n\t" "v_mfma_f32_16x16x32_bf16 v[108:111], v[132:135], v[156:159], v[108:111]\n\t" "ds_read_b128 v[132:135], v2 offset:10240\n\t" \
  ".Lb_%=:\n\t" "s_waitcnt lgkmcnt(9)\n\t" "v_mfma_f32_16x16x32_bf16 v[40:43], v[136:139], v[112:115], v[40:43]\n\t" "ds_read_b64_tr_b16 v[148:149], v1 offset:12288\n\t" "s_waitcnt lgkmcnt(8)\n\t" \
  "v_mfma_f32_16x16x32_bf16 v[64:67], v[140:143], v[112:115], v[64:67]\n\t" "ds_read_b64_tr_b16 v[150:151], v1 offset:12800\n\t" "s_waitcnt lgkmcnt(7)\n\t" \
  "v_mfma_f32_16x16x32_bf16 v[88:91], v[112:115], v[144:147], v[88:91]\n\t" "ds_read_b128 v[112:115], v3 offset:0\n\t" "s_waitcnt lgkmcnt(7)\n\t" \
  "v_mfma_f32_16x16x32_bf16 v[44:47], v[136:139], v[116:119], v[44:47]\n\t" "ds_read_b64_tr_b16 v[152:153], v1 offset:13312\n\t" \
  "v_mfma_f32_16x16x32_bf16 v[68:71], v[140:143], v[116:119], v[68:71]\n\t" "ds_read_b64_tr_b16 v[154:155], v1 offset:13824\n\t" \
  "v_mfma_f32_16x16x32_bf16 v[92:95], v[116:119], v[144:147], v[92:95]\n\t" "ds_read_b64_tr_b16 v[156:157], v1 offset:14336\n\t" "s_waitcnt lgkmcnt(9)\n\t" \
  "v_mfma_f32_16x16x32_bf16 v[48:51], v[136:139], v[120:123], v[48:51]\n\t" "ds_read_b64_tr_b16 v[158:159], v1 offset:14848\n\t" \
  "v_mfma_f32_16x16x32_bf16 v[72:75], v[140:143], v[120:123], v[72:75]\n\t" "ds_read_b128 v[116:119], v3 offset:2048\n\t" "v_mfma_f32_16x16x32_bf16 v[96:99], v[120:123], v[144:147], v[96:99]\n\t" \
  "ds_read_b128 v[120:123], v3 offset:4096\n\t" "s_waitcnt lgkmcnt(11)\n\t" "v_mfma_f32_16x16x32_bf16 v[52:55], v[136:139], v[124:127], v[52:55]\n\t" "s_bitcmp1_b32 0x44, s38\n\t" \
  "v_mfma_f32_16x16x32_bf16 v[76:79], v[140:143], v[124:127], v[76:79]\n\t" "s_cselect_b32 s76, -110592, s77\n\t" "v_add_u32 v1, s76, v1\n\t" \
  "v_mfma_f32_16x16x32_bf16 v[100:103], v[124:127], v[144:147], v[100:103]\n\t" "ds_read_b128 v[124:127], v3 offset:6144\n\t" "s_waitcnt lgkmcnt(11)\n\t" \
  "v_mfma_f32_16x16x32_bf16 v[56:59], v[136:139], v[128:131], v[56:59]\n\t" "v_add_u32 v2, s76, v2\n\t" "v_mfma_f32_16x16x32_bf16 v[80:83], v[140:143], v[128:131], v[80:83]\n\t" \
  "s_sub_u32 s38, s38, 1\n\t" "v_mfma_f32_16x16x32_bf16 v[104:107], v[128:131], v[144:147], v[104:107]\n\t" "ds_read_b128 v[128:131], v3 offset:8192\n\t" "s_waitcnt lgkmcnt(11)\n\t" \
  "v_mfma_f32_16x16x32_bf16 v[60:63], v[136:139], v[132:135], v[60:63]\n\t" "v_mfma_f32_16x16x32_bf16 v[84:87], v[140:143], v[132:135], v[84:87]\n\t" \
  "v_mfma_f32_16x16x32_bf16 v[108:111], v[132:135], v[144:147], v[108:111]\n\t" "ds_read_b128 v[132:135], v3 offset:10240\n\t" "s_cbranch_scc0 .La_%=\n\t" "s_waitcnt lgkmcnt(9)\n\t" \
  "v_mfma_f32_16x16x32_bf16 v[40:43], v[148:151], v[112:115], v[40:43]\n\t" "s_waitcnt lgkmcnt(7)\n\t" "v_mfma_f32_16x16x32_bf16 v[64:67], v[152:155], v[112:115], v[64:67]\n\t" \
  "s_waitcnt lgkmcnt(4)\n\t" "v_mfma_f32_16x16x32_bf16 v[44:47], v[148:151], v[116:119], v[44:47]\n\t" "ds_read_b32 v39, v37 offset:12\n\t" "ds_read_b128 v[160:163], v12 offset:1024\n\t" \
  "v_mfma_f32_16x16x32_bf16 v[68:71], v[152:155], v[116:119], v[68:71]\n\t" "ds_read_b128 v[164:167], v12 offset:1088\n\t" "ds_read2_b32 v[136:137], v4 offset0:0 offset1:96\n\t" \
  "s_waitcnt lgkmcnt(7)\n\t" "v_mfma_f32_16x16x32_bf16 v[48:51], v[148:151], v[120:123], v[48:51]\n\t" "ds_read2_b32 v[138:139], v4 offset0:16 offset1:112\n\t" \
  "ds_read2_b32 v[140:141], v4 offset0:32 offset1:128\n\t" "v_mfma_f32_16x16x32_bf16 v[72:75], v[152:155], v[120:123], v[72:75]\n\t" "ds_read2_b32 v[142:143], v4 offset0:48 offset1:144\n\t" \
  "ds_read2_b32 v[144:145], v4 offset0:64 offset1:160\n\t" "s_waitcnt lgkmcnt(10)\n\t" "v_mfma_f32_16x16x32_bf16 v[52:55], v[148:151], v[124:127], v[52:55]\n\t" \
  "ds_read2_b32 v[146:147], v4 offset0:80 offset1:176\n\t" "ds_read_b32 v14, v13 offset:1152\n\t" "v_mfma_f32_16x16x32_bf16 v[76:79], v[152:155], v[124:127], v[76:79]\n\t" "s_waitcnt lgkmcnt(11)\n\t" \
  "v_mfma_f32_16x16x32_bf16 v[56:59], v[148:151], v[128:131], v[56:59]\n\t" "s_waitcnt lgkmcnt(0)\n\t" "v_readfirstlane_b32 s38, v39\n\t" "s_cmp_ge_u32 s38, 4\n\t" "s_cbranch_scc0 .Lts_%=\n\t" \
  ".Ltr_%=:\n\t" "v_fma_f32 v40, -v160, v136, v40\n\t" "v_fma_f32 v41, -v161, v136, v41\n\t" "v_mfma_f32_16x16x32_bf16 v[80:83], v[152:155], v[128:131], v[80:83]\n\t" \
  "v_fma_f32 v42, -v162, v136, v42\n\t" "v_fma_f32 v43, -v163, v136, v43\n\t" "v_mfma_f32_16x16x32_bf16 v[60:63], v[148:151], v[132:135], v[60:63]\n\t" "v_fma_f32 v64, -v164, v136, v64\n\t" \
  "v_fma_f32 v65, -v165, v136, v65\n\t" "v_mfma_f32_16x16x32_bf16 v[84:87], v[152:155], v[132:135], v[84:87]\n\t" "v_fma_f32 v66, -v166, v136, v66\n\t" "v_fma_f32 v67, -v167, v136, v67\n\t" \
  "v_mfma_f32_16x16x32_bf16 v[88:91], v[112:115], v[156:159], v[88:91]\n\t" "v_mul_f32 v40, v40, v137\n\t" "v_mul_f32 v41, v41, v137\n\t" \
  "v_mfma_f32_16x16x32_bf16 v[92:95], v[116:119], v[156:159], v[92:95]\n\t" "v_mul_f32 v42, v42, v137\n\t" "v_mul_f32 v43, v43, v137\n\t" \
  "v_mfma_f32_16x16x32_bf16 v[96:99], v[120:123], v[156:159], v[96:99]\n\t" "v_mul_f32 v64, v64, v137\n\t" "v_mul_f32 v65, v65, v137\n\t" \
  "v_mfma_f32_16x16x32_bf16 v[100:103], v[124:127], v[156:159], v[100:103]\n\t" "v_mul_f32 v66, v66, v137\n\t" "v_mul_f32 v67, v67, v137\n\t" \
  "v_mfma_f32_16x16x32_bf16 v[104:107], v[128:131], v[156:159], v[104:107]\n\t" "v_cvt_pk_bf16_f32 v40, v40, v41\n\t" "v_cvt_pk_bf16_f32 v41, v42, v43\n\t" \
  "v_mfma_f32_16x16x32_bf16 v[108:111], v[132:135], v[156:159], v[108:111]\n\t" "v_cvt_pk_bf16_f32 v42, v64, v65\n\t" "v_cvt_pk_bf16_f32 v43, v66, v67\n\t" "ds_read_b128 v[112:115], v5 offset:0\n\t" \
  "ds_read_b128 v[116:119], v5 offset:384\n\t" "ds_read_b128 v[120:123], v5 offset:64\n\t" "ds_read_b128 v[124:127], v5 offset:448\n\t" "ds_read_b128 v[128:131], v5 offset:128\n\t" \
  "ds_read_b128 v[132:135], v5 offset:512\n\t" "v_fma_f32 v44, -v160, v138, v44\n\t" "v_fma_f32 v45, -v161, v138, v45\n\t" "v_fma_f32 v46, -v162, v138, v46\n\t" "v_fma_f32 v47, -v163, v138, v47\n\t" \
  "s_mov_b64 exec, s[68:69]\n\t" "global_store_dwordx4 v7, v[40:43], s[66:67] offset:-2048 sc1\n\t" "s_mov_b64 exec, -1\n\t" "ds_write2st64_b64 v6, v[40:41], v[42:43] offset0:0 offset1:24\n\t" \
  "s_mov_b64 exec, 1\n\t" "ds_add_u32 v37, v38 offset:8\n\t" "s_mov_b64 exec, -1\n\t" "v_pk_fma_f32 v[68:69], v[164:165], v[138:139], v[68:69] op_sel_hi:[1,0,1] neg_lo:[1,0,0] neg_hi:[1,0,0]\n\t" \
  "v_pk_fma_f32 v[70:71], v[166:167], v[138:139], v[70:71] op_sel_hi:[1,0,1] neg_lo:[1,0,0] neg_hi:[1,0,0]\n\t" "v_pk_mul_f32 v[44:45], v[44:45], v[138:139] op_sel:[0,1] op_sel_hi:[1,1]\n\t" \
  "v_pk_mul_f32 v[46:47], v[46:47], v[138:139] op_sel:[0,1] op_sel_hi:[1,1]\n\t" "v_pk_mul_f32 v[68:69], v[68:69], v[138:139] op_sel:[0,1] op_sel_hi:[1,1]\n\t" \
  "v_pk_mul_f32 v[70:71], v[70:71], v[138:139] op_sel:[0,1] op_sel_hi:[1,1]\n\t" "v_cvt_pk_bf16_f32 v44, v44, v45\n\t" "v_cvt_pk_bf16_f32 v45, v46, v47\n\t" "v_cvt_pk_bf16_f32 v46, v68, v69\n\t" \
  "v_cvt_pk_bf16_f32 v47, v70, v71\n\t" "s_mov_b64 exec, s[68:69]\n\t" "global_store_dwordx4 v7, v[44:47], s[66:67] offset:-1024 sc1\n\t" "s_mov_b64 exec, -1\n\t" \
  "ds_write2st64_b64 v6, v[44:45], v[46:47] offset0:1 offset1:25\n\t" "ds_read_b128 v[64:67], v5 offset:192\n\t" "ds_read_b128 v[68:71], v5 offset:576\n\t" \
  "v_pk_fma_f32 v[48:49], v[160:161], v[140:141], v[48:49] op_sel_hi:[1,0,1] neg_lo:[1,0,0] neg_hi:[1,0,0]\n\t" \
  "v_pk_fma_f32 v[50:51], v[162:163], v[140:141], v[50:51] op_sel_hi:[1,0,1] neg_lo:[1,0,0] neg_hi:[1,0,0]\n\t" \
  "v_pk_fma_f32 v[72:73], v[164:165], v[140:141], v[72:73] op_sel_hi:[1,0,1] neg_lo:[1,0,0] neg_hi:[1,0,0]\n\t" \
  "v_pk_fma_f32 v[74:75], v[166:167], v[140:141], v[74:75] op_sel_hi:[1,0,1] neg_lo:[1,0,0] neg_hi:[1,0,0]\n\t" "v_pk_mul_f32 v[48:49], v[48:49], v[140:141] op_sel:[0,1] op_sel_hi:[1,1]\n\t" \
  "v_pk_mul_f32 v[50:51], v[50:51], v[140:141] op_sel:[0,1] op_sel_hi:[1,1]\n\t" "v_pk_mul_f32 v[72:73], v[72:73], v[140:141] op_sel:[0,1] op_sel_hi:[1,1]\n\t" \
  "v_pk_mul_f32 v[74:75], v[74:75], v[140:141] op_sel:[0,1] op_sel_hi:[1,1]\n\t" "v_cvt_pk_bf16_f32 v48, v48, v49\n\t" "v_cvt_pk_bf16_f32 v49, v50, v51\n\t" "v_cvt_pk_bf16_f32 v50, v72, v73\n\t" \
  "v_cvt_pk_bf16_f32 v51, v74, v75\n\t" "s_mov_b64 exec, s[68:69]\n\t" "global_store_dwordx4 v7, v[48:51], s[66:67] offset:0 sc1\n\t" "s_mov_b64 exec, -1\n\t" \
  "ds_write2st64_b64 v6, v[48:49], v[50:51] offset0:2 offset1:26\n\t" "v_pk_fma_f32 v[52:53], v[160:161], v[142:143], v[52:53] op_sel_hi:[1,0,1] neg_lo:[1,0,0] neg_hi:[1,0,0]\n\t" \
  "v_pk_fma_f32 v[54:55], v[162:163], v[142:143], v[54:55] op_sel_hi:[1,0,1] neg_lo:[1,0,0] neg_hi:[1,0,0]\n\t" \
  "v_pk_fma_f32 v[76:77], v[164:165], v[142:143], v[76:77] op_sel_hi:[1,0,1] neg_lo:[1,0,0] neg_hi:[1,0,0]\n\t" \
  "v_pk_fma_f32 v[78:79], v[166:167], v[142:143], v[78:79] op_sel_hi:[1,0,1] neg_lo:[1,0,0] neg_hi:[1,0,0]\n\t" "v_pk_mul_f32 v[52:53], v[52:53], v[142:143] op_sel:[0,1] op_sel_hi:[1,1]\n\t" \
  "v_pk_mul_f32 v[54:55], v[54:55], v[142:143] op_sel:[0,1] op_sel_hi:[1,1]\n\t" "v_pk_mul_f32 v[76:77], v[76:77], v[142:143] op_sel:[0,1] op_sel_hi:[1,1]\n\t" \
  "v_pk_mul_f32 v[78:79], v[78:79], v[142:143] op_sel:[0,1] op_sel_hi:[1,1]\n\t" "v_cvt_pk_bf16_f32 v52, v52, v53\n\t" "v_cvt_pk_bf16_f32 v53, v54, v55\n\t" "v_cvt_pk_bf16_f32 v54, v76, v77\n\t" \
  "v_cvt_pk_bf16_f32 v55, v78, v79\n\t" "s_mov_b64 exec, s[68:69]\n\t" "global_store_dwordx4 v7, v[52:55], s[66:67] offset:1024 sc1\n\t" "s_mov_b64 exec, -1\n\t" \
  "ds_write2st64_b64 v6, v[52:53], v[54:55] offset0:3 offset1:27\n\t" "ds_read_b128 v[72:75], v5 offset:256\n\t" "ds_read_b128 v[76:79], v5 offset:640\n\t" \
  "v_pk_fma_f32 v[56:57], v[160:161], v[144:145], v[56:57] op_sel_hi:[1,0,1] neg_lo:[1,0,0] neg_hi:[1,0,0]\n\t" \
  "v_pk_fma_f32 v[58:59], v[162:163], v[144:145], v[58:59] op_sel_hi:[1,0,1] neg_lo:[1,0,0] neg_hi:[1,0,0]\n\t" \
  "v_pk_fma_f32 v[80:81], v[164:165], v[144:145], v[80:81] op_sel_hi:[1,0,1] neg_lo:[1,0,0] neg_hi:[1,0,0]\n\t" \
  "v_pk_fma_f32 v[82:83], v[166:167], v[144:145], v[82:83] op_sel_hi:[1,0,1] neg_lo:[1,0,0] neg_hi:[1,0,0]\n\t" "v_pk_mul_f32 v[56:57], v[56:57], v[144:145] op_sel:[0,1] op_sel_hi:[1,1]\n\t" \
  "v_pk_mul_f32 v[58:59], v[58:59], v[144:145] op_sel:[0,1] op_sel_hi:[1,1]\n\t" "v_pk_mul_f32 v[80:81], v[80:81], v[144:145] op_sel:[0,1] op_sel_hi:[1,1]\n\t" \
  "v_pk_mul_f32 v[82:83], v[82:83], v[144:145] op_sel:[0,1] op_sel_hi:[1,1]\n\t" "v_cvt_pk_bf16_f32 v56, v56, v57\n\t" "v_cvt_pk_bf16_f32 v57, v58, v59\n\t" "v_cvt_pk_bf16_f32 v58, v80, v81\n\t" \
  "v_cvt_pk_bf16_f32 v59, v82, v83\n\t" "s_mov_b64 exec, s[68:69]\n\t" "global_store_dwordx4 v7, v[56:59], s[66:67] offset:2048 sc1\n\t" "s_mov_b64 exec, -1\n\t" \
  "ds_write2st64_b64 v6, v[56:57], v[58:59] offset0:4 offset1:28\n\t" "v_pk_fma_f32 v[60:61], v[160:161], v[146:147], v[60:61] op_sel_hi:[1,0,1] neg_lo:[1,0,0] neg_hi:[1,0,0]\n\t" \
  "v_pk_fma_f32 v[62:63], v[162:163], v[146:147], v[62:63] op_sel_hi:[1,0,1] neg_lo:[1,0,0] neg_hi:[1,0,0]\n\t" \
  "v_pk_fma_f32 v[84:85], v[164:165], v[146:147], v[84:85] op_sel_hi:[1,0,1] neg_lo:[1,0,0] neg_hi:[1,0,0]\n\t" \
  "v_pk_fma_f32 v[86:87], v[166:167], v[146:147], v[86:87] op_sel_hi:[1,0,1] neg_lo:[1,0,0] neg_hi:[1,0,0]\n\t" "v_pk_mul_f32 v[60:61], v[60:61], v[146:147] op_sel:[0,1] op_sel_hi:[1,1]\n\t" \
  "v_pk_mul_f32 v[62:63], v[62:63], v[146:147] op_sel:[0,1] op_sel_hi:[1,1]\n\t" "v_pk_mul_f32 v[84:85], v[84:85], v[146:147] op_sel:[0,1] op_sel_hi:[1,1]\n\t" \
  "v_pk_mul_f32 v[86:87], v[86:87], v[146:147] op_sel:[0,1] op_sel_hi:[1,1]\n\t" "v_cvt_pk_bf16_f32 v60, v60, v61\n\t" "v_cvt_pk_bf16_f32 v61, v62, v63\n\t" "v_cvt_pk_bf16_f32 v62, v84, v85\n\t" \
  "v_cvt_pk_bf16_f32 v63, v86, v87\n\t" "s_mov_b64 exec, s[68:69]\n\t" "global_store_dwordx4 v7, v[60:63], s[66:67] offset:3072 sc1\n\t" "s_mov_b64 exec, -1\n\t" \
  "ds_write2st64_b64 v6, v[60:61], v[62:63] offset0:5 offset1:29\n\t" "ds_read_b128 v[80:83], v5 offset:320\n\t" "ds_read_b128 v[84:87], v5 offset:704\n\t" "s_mov_b64 exec, 1\n\t" \
  "ds_add_u32 v37, v38 offset:0\n\t" "s_mov_b64 exec, -1\n\t" "s_waitcnt lgkmcnt(14)\n\t" "v_pk_fma_f32 v[88:89], v[112:113], v[14:15], v[88:89] op_sel_hi:[1,0,1] neg_lo:[1,0,0] neg_hi:[1,0,0]\n\t" \
  "v_pk_fma_f32 v[90:91], v[114:115], v[14:15], v[90:91] op_sel_hi:[1,0,1] neg_lo:[1,0,0] neg_hi:[1,0,0]\n\t" \
  "v_pk_fma_f32 v[92:93], v[120:121], v[14:15], v[92:93] op_sel_hi:[1,0,1] neg_lo:[1,0,0] neg_hi:[1,0,0]\n\t" \
  "v_pk_fma_f32 v[94:95], v[122:123], v[14:15], v[94:95] op_sel_hi:[1,0,1] neg_lo:[1,0,0] neg_hi:[1,0,0]\n\t" "v_pk_mul_f32 v[88:89], v[88:89], v[116:117]\n\t" \
  "v_pk_mul_f32 v[90:91], v[90:91], v[118:119]\n\t" "v_pk_mul_f32 v[92:93], v[92:93], v[124:125]\n\t" "v_pk_mul_f32 v[94:95], v[94:95], v[126:127]\n\t" "v_cvt_pk_bf16_f32 v88, v88, v89\n\t" \
  "v_cvt_pk_bf16_f32 v89, v90, v91\n\t" "v_cvt_pk_bf16_f32 v92, v92, v93\n\t" "v_cvt_pk_bf16_f32 v93, v94, v95\n\t" "ds_write2st64_b64 v6, v[88:89], v[92:93] offset0:48 offset1:49\n\t" \
  "s_waitcnt lgkmcnt(10)\n\t" "v_pk_fma_f32 v[96:97], v[128:129], v[14:15], v[96:97] op_sel_hi:[1,0,1] neg_lo:[1,0,0] neg_hi:[1,0,0]\n\t" \
  "v_pk_fma_f32 v[98:99], v[130:131], v[14:15], v[98:99] op_sel_hi:[1,0,1] neg_lo:[1,0,0] neg_hi:[1,0,0]\n\t" \
  "v_pk_fma_f32 v[100:101], v[64:65], v[14:15], v[100:101] op_sel_hi:[1,0,1] neg_lo:[1,0,0] neg_hi:[1,0,0]\n\t" \
  "v_pk_fma_f32 v[102:103], v[66:67], v[14:15], v[102:103] op_sel_hi:[1,0,1] neg_lo:[1,0,0] neg_hi:[1,0,0]\n\t" "v_pk_mul_f32 v[96:97], v[96:97], v[132:133]\n\t" \
  "v_pk_mul_f32 v[98:99], v[98:99], v[134:135]\n\t" "v_pk_mul_f32 v[100:101], v[100:101], v[68:69]\n\t" "v_pk_mul_f32 v[102:103], v[102:103], v[70:71]\n\t" "v_cvt_pk_bf16_f32 v96, v96, v97\n\t" \
  "v_cvt_pk_bf16_f32 v97, v98, v99\n\t" "v_cvt_pk_bf16_f32 v100, v100, v101\n\t" "v_cvt_pk_bf16_f32 v101, v102, v103\n\t" "ds_write2st64_b64 v6, v[96:97], v[100:101] offset0:50 offset1:51\n\t" \
  "s_waitcnt lgkmcnt(3)\n\t" "v_pk_fma_f32 v[104:105], v[72:73], v[14:15], v[104:105] op_sel_hi:[1,0,1] neg_lo:[1,0,0] neg_hi:[1,0,0]\n\t" \
  "v_pk_fma_f32 v[106:107], v[74:75], v[14:15], v[106:107] op_sel_hi:[1,0,1] neg_lo:[1,0,0] neg_hi:[1,0,0]\n\t" \
  "v_pk_fma_f32 v[108:109], v[80:81], v[14:15], v[108:109] op_sel_hi:[1,0,1] neg_lo:[1,0,0] neg_hi:[1,0,0]\n\t" \
  "v_pk_fma_f32 v[110:111], v[82:83], v[14:15], v[110:111] op_sel_hi:[1,0,1] neg_lo:[1,0,0] neg_hi:[1,0,0]\n\t" "v_pk_mul_f32 v[104:105], v[104:105], v[76:77]\n\t" \
  "v_pk_mul_f32 v[106:107], v[106:107], v[78:79]\n\t" "v_pk_mul_f32 v[108:109], v[108:109], v[84:85]\n\t" "v_pk_mul_f32 v[110:111], v[110:111], v[86:87]\n\t" "v_cvt_pk_bf16_f32 v104, v104, v105\n\t" \
  "v_cvt_pk_bf16_f32 v105, v106, v107\n\t" "v_cvt_pk_bf16_f32 v108, v108, v109\n\t" "v_cvt_pk_bf16_f32 v109, v110, v111\n\t" "ds_write2st64_b64 v6, v[104:105], v[108:109] offset0:52 offset1:53\n\t" \
  "s_mov_b64 exec, 1\n\t" "ds_add_u32 v37, v38 offset:4\n\t" "s_mov_b64 exec, -1\n\t" "s_setprio 1\n\t" ".Latt_%=:\n\t" "global_load_dwordx4 v[12:15], v33, s[8:9]\n\t" \
  "global_load_dwordx4 v[16:19], v34, s[14:15]\n\t" "s_cmp_eq_u32 s34, 0\n\t" "s_cbranch_scc1 .Lk0_%=\n\t" ".Lq_%=:\n\t" "ds_read_b32 v39, v37\n\t" "ds_read_b64 v[40:41], v30 offset:0\n\t" \
  "ds_read_b64 v[42:43], v30 offset:3072\n\t" "ds_read_b64 v[44:45], v30 offset:11776\n\t" "ds_read_b64 v[46:47], v30 offset:14848\n\t" "ds_read_b64 v[48:49], v30 offset:12288\n\t" \
  "ds_read_b64 v[50:51], v30 offset:15360\n\t" "ds_read_b64 v[52:53], v31 offset:0\n\t" "ds_read_b64 v[54:55], v31 offset:3072\n\t" "ds_read_b64 v[56:57], v31 offset:11776\n\t" \
  "ds_read_b64 v[58:59], v31 offset:14848\n\t" "ds_read_b64 v[60:61], v31 offset:12288\n\t" "ds_read_b64 v[62:63], v31 offset:15360\n\t" "s_waitcnt lgkmcnt(12)\n\t" "v_readfirstlane_b32 s38, v39\n\t" \
  "s_cmp_ge_u32 s38, 4\n\t" "s_cbranch_scc0 .Lsq_%=\n\t" ".Lrq_%=:\n\t" "s_waitcnt lgkmcnt(10)\n\t" "v_cndmask_b32 v42, 0, v42, s[54:55]\n\t" "v_cndmask_b32 v43, 0, v43, s[54:55]\n\t" \
  "s_waitcnt lgkmcnt(8)\n\t" "s_nop 0\n\t" "v_mfma_f32_16x16x32_bf16 v[108:111], v[44:47], v[40:43], v[20:23]\n\t" "s_waitcnt lgkmcnt(6)\n\t" \
  "v_mfma_f32_16x16x32_bf16 v[112:115], v[48:51], v[40:43], v[24:27]\n\t" "s_waitcnt lgkmcnt(4)\n\t" "v_cndmask_b32 v52, 0, v52, s[54:55]\n\t" "v_cndmask_b32 v53, 0, v53, s[54:55]\n\t" \
  "s_waitcnt lgkmcnt(2)\n\t" "s_nop 0\n\t" "v_mfma_f32_16x16x32_bf16 v[116:119], v[56:59], v[52:55], v[20:23]\n\t" "s_waitcnt lgkmcnt(0)\n\t" \
  "v_mfma_f32_16x16x32_bf16 v[120:123], v[60:63], v[52:55], v[24:27]\n\t" "ds_read_b32 v39, v37 offset:4\n\t" "ds_read_b64 v[128:129], v30 offset:24064\n\t" \
  "ds_read_b64 v[130:131], v30 offset:24576\n\t" "ds_read_b64 v[132:133], v30 offset:27136\n\t" "ds_read_b64 v[134:135], v30 offset:27648\n\t" "ds_read_b64 v[136:137], v31 offset:24064\n\t" \
  "ds_read_b64 v[138:139], v31 offset:24576\n\t" "ds_read_b64 v[140:141], v31 offset:27136\n\t" "ds_read_b64 v[142:143], v31 offset:27648\n\t" ".Lk1_%=:\n\t" "s_nop 4\n\t" \
  "v_max_f32 v108, v108, v112\n\t" "v_max_f32 v109, v109, v113\n\t" "v_max_f32 v110, v110, v114\n\t" "v_max_f32 v111, v111, v115\n\t" "v_max_f32 v116, v116, v120\n\t" "v_max_f32 v117, v117, v121\n\t" \
  "v_max_f32 v118, v118, v122\n\t" "v_max_f32 v119, v119, v123\n\t" "v_max3_f32 v124, v108, v109, v110\n\t" "v_max3_f32 v125, v116, v117, v118\n\t" "v_max_f32 v124, v124, v111\n\t" \
  "v_max_f32 v125, v125, v119\n\t" "s_mov_b32 s38, 0x3ea52b5c\n\t" "s_nop 0\n\t" "v_permlane32_swap_b32 v124, v125\n\t" "v_max_f32 v126, v124, v125\n\t" "v_max_f32 v127, v124, v125\n\t" "s_nop 1\n\t" \
  "v_permlane16_swap_b32 v126, v127\n\t" "v_max_f32 v124, v126, v127\n\t" "v_mul_f32 v125, s38, v124\n\t" "v_mul_f32 v124, s38, v124\n\t" "s_nop 1\n\t" "v_permlane32_swap_b32 v124, v125\n\t" \
  "v_fma_f32 v108, v108, s38, -v124\n\t" "v_fma_f32 v116, v116, s38, -v125\n\t" "v_fma_f32 v109, v109, s38, -v124\n\t" "v_fma_f32 v117, v117, s38, -v125\n\t" "v_fma_f32 v110, v110, s38, -v124\n\t" \
  "v_fma_f32 v118, v118, s38, -v125\n\t" "v_fma_f32 v111, v111, s38, -v124\n\t" "v_fma_f32 v119, v119, s38, -v125\n\t" "v_exp_f32 v108, v108\n\t" "v_exp_f32 v116, v116\n\t" "v_exp_f32 v109, v109\n\t" \
  "v_exp_f32 v117, v117\n\t" "v_exp_f32 v110, v110\n\t" "v_exp_f32 v118, v118\n\t" "v_add_f32 v124, v108, v109\n\t" "v_add_f32 v125, v116, v117\n\t" "v_exp_f32 v111, v111\n\t" \
  "v_exp_f32 v119, v119\n\t" "v_add_f32 v124, v124, v110\n\t" "v_add_f32 v125, v125, v118\n\t" "v_add_f32 v124, v124, v111\n\t" "v_add_f32 v125, v125, v119\n\t" "s_cmp_lg_u32 s34, 0\n\t" "s_nop 0\n\t" \
  "v_permlane32_swap_b32 v124, v125\n\t" "v_add_f32 v126, v124, v125\n\t" "v_add_f32 v127, v124, v125\n\t" "s_nop 1\n\t" "v_permlane16_swap_b32 v126, v127\n\t" "v_add_f32 v124, v126, v127\n\t" \
  "v_rcp_f32 v126, v124\n\t" "v_rcp_f32 v124, v124\n\t" "s_nop 1\n\t" "v_permlane32_swap_b32 v124, v126\n\t" "v_pk_mul_f32 v[108:109], v[108:109], v[124:125] op_sel_hi:[1,0]\n\t" \
  "v_pk_mul_f32 v[110:111], v[110:111], v[124:125] op_sel_hi:[1,0]\n\t" "v_pk_mul_f32 v[116:117], v[116:117], v[126:127] op_sel_hi:[1,0]\n\t" \
  "v_pk_mul_f32 v[118:119], v[118:119], v[126:127] op_sel_hi:[1,0]\n\t" "v_cvt_pk_bf16_f32 v112, v108, v109\n\t" "v_cvt_pk_bf16_f32 v113, v110, v111\n\t" "v_cvt_pk_bf16_f32 v120, v116, v117\n\t" \
  "v_cvt_pk_bf16_f32 v121, v118, v119\n\t" "v_and_b32 v114, v112, v28\n\t" "v_and_b32 v115, v113, v29\n\t" "v_and_b32 v122, v120, v28\n\t" "v_and_b32 v123, v121, v29\n\t" \
  "v_xor_b32 v112, v112, v114\n\t" "v_xor_b32 v113, v113, v115\n\t" "v_xor_b32 v120, v120, v122\n\t" "v_xor_b32 v121, v121, v123\n\t" "s_cbranch_scc1 .Lk2_%=\n\t" \
  "v_mfma_f32_16x16x32_bf16 v[152:155], v[128:131], v[112:115], 0\n\t" "v_mfma_f32_16x16x32_bf16 v[156:159], v[132:135], v[112:115], 0\n\t" \
  "v_mfma_f32_16x16x32_bf16 v[152:155], v[136:139], v[120:123], v[152:155]\n\t" "v_mfma_f32_16x16x32_bf16 v[156:159], v[140:143], v[120:123], v[156:159]\n\t" "s_nop 6\n\t" \
  "v_cvt_pk_bf16_f32 v148, v152, v153\n\t" "v_cvt_pk_bf16_f32 v149, v154, v155\n\t" "v_cvt_pk_bf16_f32 v150, v156, v157\n\t" "v_cvt_pk_bf16_f32 v151, v158, v159\n\t" ".Lk3_%=:\n\t" \
  "s_mov_b64 exec, s[54:55]\n\t" "global_store_dwordx4 v36, v[148:151], s[60:61] offset:-2048 sc1\n\t" "s_mov_b64 exec, -1\n\t" "s_cmp_ge_u32 s26, 6\n\t" "s_cbranch_scc1 .Lb3a_%=\n\t" \
  "s_cmp_ge_u32 s26, 4\n\t" "s_cbranch_scc1 .Lv21_%=\n\t" "s_waitcnt vmcnt(1)\n\t" ".Lv21_%=:\n\t" "s_setprio 1\n\t" "ds_read_b64 v[152:153], v37 offset:80\n\t" "s_waitcnt lgkmcnt(0)\n\t" \
  "v_readfirstlane_b32 s80, v152\n\t" "v_readfirstlane_b32 s81, v153\n\t" "s_sub_u32 s81, s81, s80\n\t" "s_sub_u32 s80, s81, 15\n\t" "s_barrier\n\t" "s_cbranch_scc1 .Llx1_%=\n\t" \
  "s_memrealtime s[76:77]\n\t" ".Llw1_%=:\n\t" "s_memrealtime s[78:79]\n\t" "s_waitcnt lgkmcnt(0)\n\t" "s_sub_u32 s78, s78, s76\n\t" "s_cmp_lt_u32 s78, s80\n\t" "s_cbranch_scc1 .Llw1_%=\n\t" \
  ".Llx1_%=:\n\t" "global_load_dwordx4 v[152:155], v32, s[62:63] offset:-2048 sc1\n\t" "global_load_dwordx4 v[156:159], v32, s[62:63] offset:-1024 sc1\n\t" \
  "global_load_dwordx4 v[160:163], v32, s[62:63] offset:0 sc1\n\t" ".Lb3a_%=:\n\t" "ds_read_b64_tr_b16 v[40:41], v35 offset:0\n\t" "ds_read_b64_tr_b16 v[42:43], v35 offset:2048\n\t" \
  "ds_read_b64_tr_b16 v[44:45], v0 offset:0\n\t" "ds_read_b64_tr_b16 v[46:47], v0 offset:2048\n\t" "ds_read_b64_tr_b16 v[48:49], v1 offset:0\n\t" "ds_read_b64_tr_b16 v[50:51], v1 offset:2048\n\t" \
  "ds_read_b64_tr_b16 v[52:53], v2 offset:0\n\t" "ds_read_b64_tr_b16 v[54:55], v2 offset:2048\n\t" "ds_read_b64_tr_b16 v[56:57], v35 offset:4096\n\t" "ds_read_b64_tr_b16 v[58:59], v35 offset:6144\n\t" \
  "ds_read_b64_tr_b16 v[60:61], v0 offset:4096\n\t" "ds_read_b64_tr_b16 v[62:63], v0 offset:6144\n\t" "ds_read_b64_tr_b16 v[64:65], v1 offset:4096\n\t" \
  "ds_read_b64_tr_b16 v[66:67], v1 offset:6144\n\t" "ds_read_b64_tr_b16 v[68:69], v2 offset:4096\n\t" "ds_read_b64_tr_b16 v[70:71], v2 offset:6144\n\t" \
  "ds_read_b64_tr_b16 v[72:73], v35 offset:8192\n\t" "ds_read_b64_tr_b16 v[74:75], v35 offset:10240\n\t" "ds_read_b64_tr_b16 v[76:77], v0 offset:8192\n\t" \
  "ds_read_b64_tr_b16 v[78:79], v0 offset:10240\n\t" "ds_read_b64_tr_b16 v[80:81], v1 offset:8192\n\t" "ds_read_b64_tr_b16 v[82:83], v1 offset:10240\n\t" \
  "ds_read_b64_tr_b16 v[84:85], v2 offset:8192\n\t" "ds_read_b64_tr_b16 v[86:87], v2 offset:10240\n\t" "ds_read_b64_tr_b16 v[88:89], v35 offset:12288\n\t" \
  "ds_read_b64_tr_b16 v[90:91], v35 offset:14336\n\t" "ds_read_b64_tr_b16 v[92:93], v0 offset:12288\n\t" "ds_read_b64_tr_b16 v[94:95], v0 offset:14336\n\t" \
  "ds_read_b64_tr_b16 v[96:97], v1 offset:12288\n\t" "ds_read_b64_tr_b16 v[98:99], v1 offset:14336\n\t" "ds_read_b64_tr_b16 v[100:101], v2 offset:12288\n\t" \
  "ds_read_b64_tr_b16 v[102:103], v2 offset:14336\n\t" "ds_read_b64_tr_b16 v[104:105], v35 offset:16384\n\t" "ds_read_b64_tr_b16 v[106:107], v35 offset:18432\n\t" \
  "ds_read_b64_tr_b16 v[108:109], v0 offset:16384\n\t" "ds_read_b64_tr_b16 v[110:111], v0 offset:18432\n\t" "ds_read_b64_tr_b16 v[112:113], v1 offset:16384\n\t" \
  "ds_read_b64_tr_b16 v[114:115], v1 offset:18432\n\t" "ds_read_b64_tr_b16 v[116:117], v2 offset:16384\n\t" "ds_read_b64_tr_b16 v[118:119], v2 offset:18432\n\t" "s_cmp_lt_u32 s26, 6\n\t" \
  "s_cbranch_scc1 .Lb3b_%=\n\t" "s_setprio 1\n\t" "ds_read_b64 v[152:153], v37 offset:80\n\t" "s_waitcnt lgkmcnt(0)\n\t" "v_readfirstlane_b32 s80, v152\n\t" "v_readfirstlane_b32 s81, v153\n\t" \
  "s_sub_u32 s81, s81, s80\n\t" "s_sub_u32 s80, s81, 15\n\t" "s_barrier\n\t" "s_cbranch_scc1 .Llx2_%=\n\t" "s_memrealtime s[76:77]\n\t" ".Llw2_%=:\n\t" "s_memrealtime s[78:79]\n\t" \
  "s_waitcnt lgkmcnt(0)\n\t" "s_sub_u32 s78, s78, s76\n\t" "s_cmp_lt_u32 s78, s80\n\t" "s_cbranch_scc1 .Llw2_%=\n\t" ".Llx2_%=:\n\t" \
  "global_load_dwordx4 v[152:155], v32, s[62:63] offset:-2048 sc1\n\t" "global_load_dwordx4 v[156:159], v32, s[62:63] offset:-1024 sc1\n\t" \
  "global_load_dwordx4 v[160:163], v32, s[62:63] offset:0 sc1\n\t" ".Lb3b_%=:\n\t" "ds_read_b64_tr_b16 v[120:121], v35 offset:20480\n\t" "ds_read_b64_tr_b16 v[122:123], v35 offset:22528\n\t" \
  "ds_read_b64_tr_b16 v[124:125], v0 offset:20480\n\t" "ds_read_b64_tr_b16 v[126:127], v0 offset:22528\n\t" "ds_read_b64_tr_b16 v[128:129], v1 offset:20480\n\t" \
  "ds_read_b64_tr_b16 v[130:131], v1 offset:22528\n\t" "ds_read_b64_tr_b16 v[132:133], v2 offset:20480\n\t" "ds_read_b64_tr_b16 v[134:135], v2 offset:22528\n\t" "s_waitcnt vmcnt(5)\n\t" \
  "v_permlane16_swap_b32 v12, v14\n\t" "v_permlane16_swap_b32 v13, v15\n\t" "v_mov_b64 v[144:145], 0\n\t" "v_mov_b64 v[146:147], 0\n\t" "v_mov_b64 v[148:149], 0\n\t" "v_mov_b64 v[150:151], 0\n\t" \
  "v_lshlrev_b32 v136, 16, v12\n\t" "v_and_b32 v137, 0xffff0000, v12\n\t" "v_lshlrev_b32 v138, 16, v13\n\t" "v_and_b32 v139, 0xffff0000, v13\n\t" "v_lshlrev_b32 v140, 16, v14\n\t" \
  "v_and_b32 v141, 0xffff0000, v14\n\t" "v_lshlrev_b32 v142, 16, v15\n\t" "v_and_b32 v143, 0xffff0000, v15\n\t" "s_lshl_b32 s38, s26, 11\n\t" "v_and_b32 v28, 0xf0, v32\n\t" \
  "v_add_u32 v24, s38, v32\n\t" "v_permlane16_swap_b32 v16, v18\n\t" "v_permlane16_swap_b32 v17, v19\n\t" "s_lshl_b32 s40, s36, 6\n\t" "s_add_u32 s40, s40, 0\n\t" "v_lshl_add_u32 v27, v10, 1, s40\n\t" \
  "s_mov_b32 s40, 0x3f803f80\n\t" "v_xor_b32 v25, 0x800, v24\n\t" "v_mad_u32_u24 v27, v28, 9, v27\n\t" "s_mul_i32 s38, s37, 2304\n\t" "v_add_u32 v28, v28, v10\n\t" "v_add_u32 v27, s38, v27\n\t" \
  "s_mul_i32 s38, s36, 6\n\t" "s_add_u32 s38, s38, s37\n\t" "s_lshl_b32 s38, s38, 8\n\t" "v_lshl_add_u32 v26, s37, 2, v37\n\t" "v_add3_u32 v28, v28, s38, v37\n\t" "s_mul_i32 s38, s34, 768\n\t" \
  "v_min_u32 v29, 0x2f0, v32\n\t" "v_lshl_add_u32 v30, s34, 2, v37\n\t" "v_add3_u32 v29, v29, s38, v37\n\t" "s_lshl1_add_u32 s41, s29, s34\n\t" "v_min_u32 v20, 47, v8\n\t" \
  "s_mul_i32 s38, s41, 6400\n\t" "s_add_u32 s38, s38, 4829184\n\t" "v_mul_u32_u24 v21, 12, v20\n\t" "s_add_u32 s44, s24, s38\n\t" "s_addc_u32 s45, s25, 0\n\t" "s_add_u32 s42, s44, 2560\n\t" \
  "s_addc_u32 s43, s45, 0\n\t" "s_mul_i32 s38, s28, 640\n\t" "s_add_u32 s44, s44, s38\n\t" "s_addc_u32 s45, s45, 0\n\t" "s_waitcnt vmcnt(2)\n\t" "v_cmp_eq_u32 vcc, -1, v152\n\t" \
  "s_cbranch_vccnz .Lso0_%=\n\t" ".Lro0_%=:\n\t" "s_waitcnt lgkmcnt(14)\n\t" "v_mfma_f32_16x16x32_bf16 v[136:139], v[40:43], v[152:155], v[136:139]\n\t" \
  "global_load_dwordx4 v[164:167], v32, s[62:63] offset:1024 sc1\n\t" "ds_read_b64_tr_b16 v[40:41], v35 offset:24576\n\t" "ds_read_b64_tr_b16 v[42:43], v35 offset:26624\n\t" \
  "v_mfma_f32_16x16x32_bf16 v[140:143], v[44:47], v[152:155], v[140:143]\n\t" "ds_read_b64_tr_b16 v[44:45], v0 offset:24576\n\t" "ds_read_b64_tr_b16 v[46:47], v0 offset:26624\n\t" \
  "v_mfma_f32_16x16x32_bf16 v[144:147], v[48:51], v[152:155], v[144:147]\n\t" "ds_read_b64_tr_b16 v[48:49], v1 offset:24576\n\t" "ds_read_b64_tr_b16 v[50:51], v1 offset:26624\n\t" \
  "v_mfma_f32_16x16x32_bf16 v[148:151], v[52:55], v[152:155], v[148:151]\n\t" "ds_read_b64_tr_b16 v[52:53], v2 offset:24576\n\t" "ds_read_b64_tr_b16 v[54:55], v2 offset:26624\n\t" \
  "s_waitcnt vmcnt(2)\n\t" "v_cmp_eq_u32 vcc, -1, v156\n\t" "s_cbranch_vccnz .Lso1_%=\n\t" ".Lro1_%=:\n\t" "v_mfma_f32_16x16x32_bf16 v[136:139], v[56:59], v[156:159], v[136:139]\n\t" \
  "global_load_dwordx4 v[152:155], v32, s[62:63] offset:2048 sc1\n\t" "ds_read_b64_tr_b16 v[56:57], v35 offset:28672\n\t" "ds_read_b64_tr_b16 v[58:59], v35 offset:30720\n\t" \
  "v_mfma_f32_16x16x32_bf16 v[140:143], v[60:63], v[156:159], v[140:143]\n\t" "ds_read_b64_tr_b16 v[60:61], v0 offset:28672\n\t" "ds_read_b64_tr_b16 v[62:63], v0 offset:30720\n\t" \
  "v_mfma_f32_16x16x32_bf16 v[144:147], v[64:67], v[156:159], v[144:147]\n\t" "ds_read_b64_tr_b16 v[64:65], v1 offset:28672\n\t" "ds_read_b64_tr_b16 v[66:67], v1 offset:30720\n\t" \
  "v_mfma_f32_16x16x32_bf16 v[148:151], v[68:71], v[156:159], v[148:151]\n\t" "ds_read_b64_tr_b16 v[68:69], v2 offset:28672\n\t" "ds_read_b64_tr_b16 v[70:71], v2 offset:30720\n\t" \
  "s_waitcnt vmcnt(2)\n\t" "v_cmp_eq_u32 vcc, -1, v160\n\t" "s_cbranch_vccnz .Lso2_%=\n\t" ".Lro2_%=:\n\t" "v_mfma_f32_16x16x32_bf16 v[136:139], v[72:75], v[160:163], v[136:139]\n\t" \
  "global_load_dwordx4 v[156:159], v32, s[64:65] offset:-2048 sc1\n\t" "ds_read_b64_tr_b16 v[72:73], v35 offset:32768\n\t" "ds_read_b64_tr_b16 v[74:75], v35 offset:34816\n\t" \
  "v_mfma_f32_16x16x32_bf16 v[140:143], v[76:79], v[160:163], v[140:143]\n\t" "ds_read_b64_tr_b16 v[76:77], v0 offset:32768\n\t" "ds_read_b64_tr_b16 v[78:79], v0 offset:34816\n\t" \
  "v_mfma_f32_16x16x32_bf16 v[144:147], v[80:83], v[160:163], v[144:147]\n\t" "ds_read_b64_tr_b16 v[80:81], v1 offset:32768\n\t" "ds_read_b64_tr_b16 v[82:83], v1 offset:34816\n\t" \
  "v_mfma_f32_16x16x32_bf16 v[148:151], v[84:87], v[160:163], v[148:151]\n\t" "ds_read_b64_tr_b16 v[84:85], v2 offset:32768\n\t" "ds_read_b64_tr_b16 v[86:87], v2 offset:34816\n\t" \
  "s_waitcnt vmcnt(2)\n\t" "v_cmp_eq_u32 vcc, -1, v164\n\t" "s_cbranch_vccnz .Lso3_%=\n\t" ".Lro3_%=:\n\t" "v_mfma_f32_16x16x32_bf16 v[136:139], v[88:91], v[164:167], v[136:139]\n\t" \
  "global_load_dwordx4 v[160:163], v32, s[64:65] offset:-1024 sc1\n\t" "ds_read_b64_tr_b16 v[88:89], v35 offset:36864\n\t" "ds_read_b64_tr_b16 v[90:91], v35 offset:38912\n\t" \
  "v_mfma_f32_16x16x32_bf16 v[140:143], v[92:95], v[164:167], v[140:143]\n\t" "ds_read_b64_tr_b16 v[92:93], v0 offset:36864\n\t" "ds_read_b64_tr_b16 v[94:95], v0 offset:38912\n\t" \
  "v_mfma_f32_16x16x32_bf16 v[144:147], v[96:99], v[164:167], v[144:147]\n\t" "ds_read_b64_tr_b16 v[96:97], v1 offset:36864\n\t" "ds_read_b64_tr_b16 v[98:99], v1 offset:38912\n\t" \
  "v_mfma_f32_16x16x32_bf16 v[148:151], v[100:103], v[164:167], v[148:151]\n\t" "ds_read_b64_tr_b16 v[100:101], v2 offset:36864\n\t" "ds_read_b64_tr_b16 v[102:103], v2 offset:38912\n\t" \
  "s_waitcnt vmcnt(2)\n\t" "v_cmp_eq_u32 vcc, -1, v152\n\t" "s_cbranch_vccnz .Lso4_%=\n\t" ".Lro4_%=:\n\t" "v_mfma_f32_16x16x32_bf16 v[136:139], v[104:107], v[152:155], v[136:139]\n\t" \
  "global_load_dwordx4 v[164:167], v32, s[64:65] offset:0 sc1\n\t" "s_waitcnt lgkmcnt(14)\n\t" "v_mfma_f32_16x16x32_bf16 v[140:143], v[108:111], v[152:155], v[140:143]\n\t" \
  "v_mfma_f32_16x16x32_bf16 v[144:147], v[112:115], v[152:155], v[144:147]\n\t" "v_mfma_f32_16x16x32_bf16 v[148:151], v[116:119], v[152:155], v[148:151]\n\t" "s_waitcnt vmcnt(2)\n\t" \
  "v_cmp_eq_u32 vcc, -1, v156\n\t" "s_cbranch_vccnz .Lso5_%=\n\t" ".Lro5_%=:\n\t" "v_mfma_f32_16x16x32_bf16 v[136:139], v[120:123], v[156:159], v[136:139]\n\t" \
  "global_load_dwordx4 v[152:155], v32, s[64:65] offset:1024 sc1\n\t" "v_mfma_f32_16x16x32_bf16 v[140:143], v[124:127], v[156:159], v[140:143]\n\t" \
  "v_mfma_f32_16x16x32_bf16 v[144:147], v[128:131], v[156:159], v[144:147]\n\t" "v_mfma_f32_16x16x32_bf16 v[148:151], v[132:135], v[156:159], v[148:151]\n\t" "s_waitcnt vmcnt(2)\n\t" \
  "v_cmp_eq_u32 vcc, -1, v160\n\t" "s_cbranch_vccnz .Lso6_%=\n\t" ".Lro6_%=:\n\t" "v_mfma_f32_16x16x32_bf16 v[136:139], v[40:43], v[160:163], v[136:139]\n\t" \
  "global_load_dwordx4 v[156:159], v32, s[64:65] offset:2048 sc1\n\t" "v_mfma_f32_16x16x32_bf16 v[140:143], v[44:47], v[160:163], v[140:143]\n\t" \
  "v_mfma_f32_16x16x32_bf16 v[144:147], v[48:51], v[160:163], v[144:147]\n\t" "v_mfma_f32_16x16x32_bf16 v[148:151], v[52:55], v[160:163], v[148:151]\n\t" "s_waitcnt vmcnt(2)\n\t" \
  "v_cmp_eq_u32 vcc, -1, v164\n\t" "s_cbranch_vccnz .Lso7_%=\n\t" ".Lro7_%=:\n\t" "v_mfma_f32_16x16x32_bf16 v[136:139], v[56:59], v[164:167], v[136:139]\n\t" \
  "v_mfma_f32_16x16x32_bf16 v[140:143], v[60:63], v[164:167], v[140:143]\n\t" "v_mfma_f32_16x16x32_bf16 v[144:147], v[64:67], v[164:167], v[144:147]\n\t" \
  "v_mfma_f32_16x16x32_bf16 v[148:151], v[68:71], v[164:167], v[148:151]\n\t" "s_waitcnt vmcnt(1)\n\t" "v_cmp_eq_u32 vcc, -1, v152\n\t" "s_cbranch_vccnz .Lso8_%=\n\t" ".Lro8_%=:\n\t" \
  "v_mfma_f32_16x16x32_bf16 v[136:139], v[72:75], v[152:155], v[136:139]\n\t" "s_waitcnt lgkmcnt(12)\n\t" "v_mfma_f32_16x16x32_bf16 v[140:143], v[76:79], v[152:155], v[140:143]\n\t" \
  "s_waitcnt lgkmcnt(10)\n\t" "v_mfma_f32_16x16x32_bf16 v[144:147], v[80:83], v[152:155], v[144:147]\n\t" "s_waitcnt lgkmcnt(8)\n\t" \
  "v_mfma_f32_16x16x32_bf16 v[148:151], v[84:87], v[152:155], v[148:151]\n\t" "s_waitcnt vmcnt(0)\n\t" "v_cmp_eq_u32 vcc, -1, v156\n\t" "s_cbranch_vccnz .Lso9_%=\n\t" ".Lro9_%=:\n\t" \
  "s_waitcnt lgkmcnt(2)\n\t" "v_mfma_f32_16x16x32_bf16 v[144:147], v[96:99], v[156:159], v[144:147]\n\t" "s_waitcnt lgkmcnt(0)\n\t" \
  "v_mfma_f32_16x16x32_bf16 v[148:151], v[100:103], v[156:159], v[148:151]\n\t" "v_mfma_f32_16x16x32_bf16 v[136:139], v[88:91], v[156:159], v[136:139]\n\t" \
  "v_mfma_f32_16x16x32_bf16 v[140:143], v[92:95], v[156:159], v[140:143]\n\t" "s_nop 3\n\t" "ds_write_b128 v24, v[144:147] offset:36864\n\t" "s_nop 0\n\t" \
  "ds_write_b128 v24, v[148:151] offset:37888\n\t" "s_mov_b64 exec, 1\n\t" "ds_add_u32 v26, v38 offset:48\n\t" "s_mov_b64 exec, -1\n\t" ".Lp_%=:\n\t" "ds_read_b32 v39, v26 offset:48\n\t" \
  "ds_read_b128 v[40:43], v25 offset:36864\n\t" "ds_read_b128 v[44:47], v25 offset:37888\n\t" "s_sub_u32 s39, s39, 1\n\t" "s_cmp_lt_i32 s39, 0\n\t" "s_waitcnt lgkmcnt(2)\n\t" \
  "v_readfirstlane_b32 s38, v39\n\t" "s_cselect_b32 s38, 2, s38\n\t" "s_cmp_ge_u32 s38, 2\n\t" "s_waitcnt lgkmcnt(0)\n\t" "s_cbranch_scc0 .Lp_%=\n\t" \
  "v_pk_add_f32 v[136:137], v[136:137], v[40:41]\n\t" "v_pk_add_f32 v[138:139], v[138:139], v[42:43]\n\t" "v_pk_add_f32 v[140:141], v[140:141], v[44:45]\n\t" \
  "v_pk_add_f32 v[142:143], v[142:143], v[46:47]\n\t" "v_cvt_pk_bf16_f32 v12, v136, v137\n\t" "v_cvt_pk_bf16_f32 v13, v138, v139\n\t" "v_cvt_pk_bf16_f32 v14, v140, v141\n\t" \
  "v_cvt_pk_bf16_f32 v15, v142, v143\n\t" "v_dot2_f32_bf16 v40, v12, s40, 0\n\t" "v_dot2_f32_bf16 v41, v12, v12, 0\n\t" "v_dot2_f32_bf16 v42, v12, v16, 0\n\t" "v_dot2_f32_bf16 v40, v13, s40, v40\n\t" \
  "v_dot2_f32_bf16 v41, v13, v13, v41\n\t" "v_dot2_f32_bf16 v42, v13, v17, v42\n\t" "v_dot2_f32_bf16 v40, v14, s40, v40\n\t" "v_dot2_f32_bf16 v41, v14, v14, v41\n\t" \
  "v_dot2_f32_bf16 v42, v14, v18, v42\n\t" "v_dot2_f32_bf16 v40, v15, s40, v40\n\t" "v_dot2_f32_bf16 v41, v15, v15, v41\n\t" "v_dot2_f32_bf16 v43, v15, v19, v42\n\t" \
  "v_dot2_f32_bf16 v42, v15, v19, v42\n\t" "ds_write2_b64 v27, v[12:13], v[14:15] offset1:4\n\t" "s_nop 0\n\t" "v_permlane32_swap_b32 v40, v41\n\t" "v_permlane32_swap_b32 v42, v43\n\t" \
  "v_add_f32 v40, v40, v41\n\t" "v_add_f32 v42, v42, v43\n\t" "s_nop 1\n\t" "v_permlane16_swap_b32 v40, v42\n\t" "v_add_f32 v40, v40, v42\n\t" "ds_write_b32 v28, v40 offset:256\n\t" \
  "s_mov_b64 exec, 1\n\t" "ds_add_rtn_u32 v39, v30, v38 offset:16\n\t" "s_mov_b64 exec, -1\n\t" "ds_read_b128 v[44:47], v29 offset:256\n\t" "ds_read_b128 v[48:51], v29 offset:1792\n\t" \
  "ds_read_b32 v52, v37 offset:40\n\t" "s_waitcnt lgkmcnt(3)\n\t" "v_readfirstlane_b32 s38, v39\n\t" "s_waitcnt lgkmcnt(0)\n\t" "s_cmp_lg_u32 s38, 5\n\t" "s_setprio 0\n\t" "s_cbranch_scc1 .Lnk_%=\n\t" \
  "s_setprio 3\n\t" "v_pk_add_f32 v[44:45], v[44:45], v[48:49]\n\t" "v_add_f32 v46, v46, v50\n\t" "s_mov_b32 exec_hi, 0xffff\n\t" "global_store_dwordx3 v21, v[44:46], s[44:45] sc1\n\t" \
  "s_mov_b32 exec_hi, -1\n\t" "s_memrealtime s[76:77]\n\t" "s_mul_i32 s50, s34, 48\n\t" "v_lshl_add_u32 v4, s41, 6, v20\n\t" "v_add_u32 v1, s50, v20\n\t" "s_cmp_eq_u32 s29, 0\n\t" \
  "s_cselect_b32 s51, 6, 7\n\t" "v_cmp_le_u32 vcc, s51, v1\n\t" "s_mov_b32 s51, 155648\n\t" "v_lshl_add_u32 v1, v1, 3, s51\n\t" "v_mov_b32 v3, 0xfffff000\n\t" "s_lshl_b32 s51, s41, 7\n\t" \
  "v_cndmask_b32 v3, 0, v3, vcc\n\t" "v_lshl_add_u32 v5, v9, 3, s51\n\t" "v_lshlrev_b32 v7, 2, v8\n\t" "s_mul_i32 s50, s50, 144\n\t" "v_and_b32 v6, 0x7c, v7\n\t" "v_add_u32 v6, s50, v6\n\t" \
  "s_mul_i32 s50, s28, 6144\n\t" "s_add_u32 s50, s50, 5302784\n\t" "s_add_u32 s46, s24, s50\n\t" "s_addc_u32 s47, s25, 0\n\t" "s_mul_i32 s50, s41, 10\n\t" "s_add_u32 s50, s50, s28\n\t" \
  "s_lshl_b32 s50, s50, 8\n\t" "s_add_u32 s50, s50, 5123584\n\t" "s_add_u32 s48, s24, s50\n\t" "s_addc_u32 s49, s25, 0\n\t" "s_mov_b32 s50, 0x3acccccd\n\t" ".Ltk_%=:\n\t" "s_memrealtime s[78:79]\n\t" \
  "s_waitcnt lgkmcnt(0)\n\t" "s_sub_u32 s78, s78, s76\n\t" "s_cmp_lt_u32 s78, 40\n\t" "s_cbranch_scc1 .Ltk_%=\n\t" ".Lkp_%=:\n\t" "global_load_dwordx3 v[60:62], v21, s[42:43] offset:-2560 sc1\n\t" \
  "global_load_dwordx3 v[64:66], v21, s[42:43] offset:-1920 sc1\n\t" "global_load_dwordx3 v[68:70], v21, s[42:43] offset:-1280 sc1\n\t" \
  "global_load_dwordx3 v[72:74], v21, s[42:43] offset:-640 sc1\n\t" "global_load_dwordx3 v[76:78], v21, s[42:43] offset:0 sc1\n\t" "global_load_dwordx3 v[80:82], v21, s[42:43] offset:640 sc1\n\t" \
  "global_load_dwordx3 v[84:86], v21, s[42:43] offset:1280 sc1\n\t" "global_load_dwordx3 v[88:90], v21, s[42:43] offset:1920 sc1\n\t" "global_load_dwordx3 v[92:94], v21, s[42:43] offset:2560 sc1\n\t" \
  "global_load_dwordx3 v[96:98], v21, s[42:43] offset:3200 sc1\n\t" "s_sub_u32 s39, s39, 1\n\t" "s_cmp_lt_i32 s39, 0\n\t" "s_waitcnt vmcnt(0)\n\t" "v_pk_add_f32 v[60:61], v[60:61], v[64:65]\n\t" \
  "v_add_f32 v62, v62, v66\n\t" "v_pk_add_f32 v[68:69], v[68:69], v[72:73]\n\t" "v_add_f32 v70, v70, v74\n\t" "v_pk_add_f32 v[76:77], v[76:77], v[80:81]\n\t" "v_add_f32 v78, v78, v82\n\t" \
  "v_pk_add_f32 v[84:85], v[84:85], v[88:89]\n\t" "v_add_f32 v86, v86, v90\n\t" "v_pk_add_f32 v[92:93], v[92:93], v[96:97]\n\t" "v_add_f32 v94, v94, v98\n\t" \
  "v_pk_add_f32 v[60:61], v[60:61], v[68:69]\n\t" "v_add_f32 v62, v62, v70\n\t" "v_pk_add_f32 v[76:77], v[76:77], v[84:85]\n\t" "v_add_f32 v78, v78, v86\n\t" \
  "v_pk_add_f32 v[60:61], v[60:61], v[92:93]\n\t" "v_add_f32 v62, v62, v94\n\t" "v_pk_add_f32 v[60:61], v[60:61], v[76:77]\n\t" "v_add_f32 v62, v62, v78\n\t" "v_mul_f32 v60, s50, v60\n\t" \
  "v_mul_f32 v63, v60, v60\n\t" "v_fma_f32 v61, -v60, v52, v61\n\t" "v_fma_f32 v62, v62, s50, -v63\n\t" "s_cbranch_scc1 .Lkq_%=\n\t" "v_cmp_u_f32 vcc, v61, v62\n\t" "s_cbranch_vccnz .Lkp_%=\n\t" \
  ".Lkq_%=:\n\t" "v_max_f32 v62, 0, v62\n\t" "v_add_f32 v63, 0x3727c5ac, v62\n\t" "v_rsq_f32 v63, v63\n\t" "v_cvt_pk_bf16_f32 v66, v62, v60\n\t" "v_mul_f32 v61, v61, v63\n\t" \
  "v_ashrrev_i32 v63, 31, v61\n\t" "v_or_b32 v63, 0x80000000, v63\n\t" "v_xor_b32 v61, v61, v63\n\t" "v_and_or_b32 v61, v61, v3, v4\n\t" "v_lshlrev_b32 v67, 16, v66\n\t" \
  "v_and_b32 v66, 0xffff0000, v66\n\t" "v_max_u32_dpp v64, v61, v61 quad_perm:[1,0,3,2] row_mask:0xf bank_mask:0xf\n\t" "v_add_f32 v67, 0x3727c5ac, v67\n\t" "v_cvt_pk_bf16_f32 v67, v67, v67\n\t" \
  "v_max_u32_dpp v64, v64, v64 quad_perm:[2,3,0,1] row_mask:0xf bank_mask:0xf\n\t" "v_lshlrev_b32 v67, 16, v67\n\t" "v_sqrt_f32 v67, v67\n\t" \
  "v_max_u32_dpp v64, v64, v64 row_half_mirror row_mask:0xf bank_mask:0xf\n\t" "s_nop 0\n\t" "v_cvt_pk_bf16_f32 v67, v67, v67\n\t" \
  "v_max_u32_dpp v64, v64, v64 row_mirror row_mask:0xf bank_mask:0xf\n\t" "v_lshlrev_b32 v67, 16, v67\n\t" "v_rcp_f32 v67, v67\n\t" \
  "v_max_u32_dpp v64, v64, v64 row_bcast:15 row_mask:0xa bank_mask:0xf\n\t" "s_nop 1\n\t" "v_max_u32_dpp v64, v64, v64 row_bcast:31 row_mask:0xc bank_mask:0xf\n\t" "v_mov_b32 v65, v64\n\t" \
  "s_mov_b32 exec_lo, 0\n\t" "s_mov_b32 exec_hi, 0xffff0000\n\t" "global_store_dwordx2 v5, v[64:65], s[46:47] sc1\n\t" "s_mov_b64 exec, -1\n\t" "v_readlane_b32 s50, v64, 63\n\t" \
  "s_and_b32 s51, s50, 63\n\t" "s_mul_i32 s52, s51, 144\n\t" "v_add_u32 v6, s52, v6\n\t" "s_or_b32 s50, s50, 0x1000\n\t" "ds_read_b32 v68, v6 offset:0\n\t" "v_mov_b32 v64, s50\n\t" \
  "ds_write_b64 v1, v[66:67]\n\t" "ds_write_b32 v30, v64 offset:24\n\t" "v_readlane_b32 s52, v66, s51\n\t" "v_readlane_b32 s53, v67, s51\n\t" "s_waitcnt lgkmcnt(2)\n\t" \
  "v_lshlrev_b32 v70, 16, v68\n\t" "v_and_b32 v71, 0xffff0000, v68\n\t" "v_subrev_f32 v70, s52, v70\n\t" "v_subrev_f32 v71, s52, v71\n\t" "v_cvt_pk_bf16_f32 v71, v70, v71\n\t" \
  "v_lshlrev_b32 v70, 16, v71\n\t" "v_and_b32 v71, 0xffff0000, v71\n\t" "v_mul_f32 v70, s53, v70\n\t" "v_mul_f32 v71, s53, v71\n\t" "s_mov_b32 exec_hi, 0\n\t" "v_cvt_pk_bf16_f32 v68, v70, v71\n\t" \
  "s_mov_b32 exec_hi, -1\n\t" "global_store_dword v7, v68, s[48:49] sc1\n\t" "s_setprio 0\n\t" ".Lnk_%=:\n\t" "v_lshrrev_b32 v113, 3, v8\n\t" "s_lshl_b32 s40, s26, 3\n\t" "v_add_u32 v2, s40, v113\n\t" \
  "s_mov_b32 s76, 155648\n\t" "v_lshl_add_u32 v1, v2, 3, s76\n\t" "s_cmp_eq_u32 s29, 0\n\t" "s_cselect_b32 s76, 6, 7\n\t" "v_cmp_le_u32 s[72:73], s76, v2\n\t" "v_add_u32 v2, s30, v2\n\t" \
  "v_sub_u32 v2, v2, 6 clamp\n\t" "v_lshrrev_b32 v3, 10, v2\n\t" "v_lshl_or_b32 v2, v2, 1, v3\n\t" "v_and_b32 v2, 0x7ff, v2\n\t" "v_and_b32 v3, 0x70, v32\n\t" "s_lshl_b32 s76, s28, 7\n\t" \
  "v_add_u32 v3, s76, v3\n\t" "s_movk_i32 s76, 1280\n\t" "v_mad_u32_u24 v33, v2, s76, v3\n\t" "s_mul_i32 s76, s26, 1152\n\t" "v_mad_u32_u24 v3, v113, 16, v32\n\t" "v_add_u32 v3, s76, v3\n\t" \
  "s_lshl_b32 s76, s35, 3\n\t" "v_add_u32 v113, s76, v113\n\t" "s_branch .Lw1_%=\n\t" ".Lw_%=:\n\t" "s_sleep 2\n\t" ".Lw1_%=:\n\t" "ds_read_b32 v112, v30 offset:24\n\t" \
  "ds_read_b64 v[110:111], v1\n\t" "ds_read_b128 v[12:15], v3 offset:0\n\t" "s_sub_u32 s39, s39, 1\n\t" "s_cmp_lt_i32 s39, 0\n\t" "s_waitcnt lgkmcnt(0)\n\t" "v_readfirstlane_b32 s66, v112\n\t" \
  "s_cselect_b32 s66, 1, s66\n\t" "s_cmp_eq_u32 s66, 0\n\t" "s_cbranch_scc1 .Lw_%=\n\t" "s_and_b32 s76, s66, 63\n\t" "v_cmp_eq_u32 s[68:69], s76, v113\n\t" "v_lshlrev_b32 v120, 16, v12\n\t" \
  "v_and_b32 v121, 0xffff0000, v12\n\t" "v_lshlrev_b32 v122, 16, v13\n\t" "v_and_b32 v123, 0xffff0000, v13\n\t" "v_lshlrev_b32 v124, 16, v14\n\t" "v_and_b32 v125, 0xffff0000, v14\n\t" \
  "v_lshlrev_b32 v126, 16, v15\n\t" "v_and_b32 v127, 0xffff0000, v15\n\t" "v_pk_add_f32 v[128:129], v[120:121], v[110:111] op_sel_hi:[1,0] neg_lo:[0,1] neg_hi:[0,1]\n\t" \
  "v_pk_add_f32 v[130:131], v[122:123], v[110:111] op_sel_hi:[1,0] neg_lo:[0,1] neg_hi:[0,1]\n\t" "v_pk_add_f32 v[132:133], v[124:125], v[110:111] op_sel_hi:[1,0] neg_lo:[0,1] neg_hi:[0,1]\n\t" \
  "v_pk_add_f32 v[134:135], v[126:127], v[110:111] op_sel_hi:[1,0] neg_lo:[0,1] neg_hi:[0,1]\n\t" "v_cvt_pk_bf16_f32 v129, v128, v129\n\t" "v_cvt_pk_bf16_f32 v131, v130, v131\n\t" \
  "v_cvt_pk_bf16_f32 v133, v132, v133\n\t" "v_cvt_pk_bf16_f32 v135, v134, v135\n\t" "v_lshlrev_b32 v128, 16, v129\n\t" "v_and_b32 v129, 0xffff0000, v129\n\t" "v_lshlrev_b32 v130, 16, v131\n\t" \
  "v_and_b32 v131, 0xffff0000, v131\n\t" "v_lshlrev_b32 v132, 16, v133\n\t" "v_and_b32 v133, 0xffff0000, v133\n\t" "v_lshlrev_b32 v134, 16, v135\n\t" "v_and_b32 v135, 0xffff0000, v135\n\t" \
  "v_pk_mul_f32 v[128:129], v[128:129], v[110:111] op_sel:[0,1] op_sel_hi:[1,1]\n\t" "v_pk_mul_f32 v[130:131], v[130:131], v[110:111] op_sel:[0,1] op_sel_hi:[1,1]\n\t" \
  "v_pk_mul_f32 v[132:133], v[132:133], v[110:111] op_sel:[0,1] op_sel_hi:[1,1]\n\t" "v_pk_mul_f32 v[134:135], v[134:135], v[110:111] op_sel:[0,1] op_sel_hi:[1,1]\n\t" \
  "v_cvt_pk_bf16_f32 v129, v128, v129\n\t" "v_cvt_pk_bf16_f32 v131, v130, v131\n\t" "v_cvt_pk_bf16_f32 v133, v132, v133\n\t" "v_cvt_pk_bf16_f32 v135, v134, v135\n\t" "v_lshlrev_b32 v128, 16, v129\n\t" \
  "v_and_b32 v129, 0xffff0000, v129\n\t" "v_lshlrev_b32 v130, 16, v131\n\t" "v_and_b32 v131, 0xffff0000, v131\n\t" "v_lshlrev_b32 v132, 16, v133\n\t" "v_and_b32 v133, 0xffff0000, v133\n\t" \
  "v_lshlrev_b32 v134, 16, v135\n\t" "v_and_b32 v135, 0xffff0000, v135\n\t" "v_pk_add_f32 v[128:129], v[128:129], v[120:121]\n\t" "v_pk_add_f32 v[130:131], v[130:131], v[122:123]\n\t" \
  "v_pk_add_f32 v[132:133], v[132:133], v[124:125]\n\t" "v_pk_add_f32 v[134:135], v[134:135], v[126:127]\n\t" "v_cvt_pk_bf16_f32 v116, v128, v129\n\t" "v_cvt_pk_bf16_f32 v117, v130, v131\n\t" \
  "v_cvt_pk_bf16_f32 v118, v132, v133\n\t" "v_cvt_pk_bf16_f32 v119, v134, v135\n\t" "s_sleep 16\n\t" "s_andn2_b64 exec, s[72:73], s[68:69]\n\t" "s_cbranch_scc0 .Ln1_%=\n\t" \
  "global_store_dwordx4 v33, v[116:119], s[22:23] sc0 sc1 nt\n\t" ".Ln1_%=:\n\t" "s_mov_b64 exec, -1\n\t" "s_cmp_eq_u32 s26, 11\n\t" "s_cbranch_scc1 .Lrz_%=\n\t" "s_cmp_eq_u64 s[68:69], 0\n\t" \
  "s_cbranch_scc1 .Lend_%=\n\t" ".Lb6_%=:\n\t" "s_waitcnt lgkmcnt(0)\n\t" "s_barrier\n\t" "ds_read_b32 v39, v37 offset:32\n\t" "s_waitcnt lgkmcnt(0)\n\t" "v_readfirstlane_b32 s76, v39\n\t" \
  "s_xor_b32 s76, s76, s66\n\t" "s_and_b32 s76, s76, 4095\n\t" "s_cbranch_scc0 .Lend_%=\n\t" "s_and_b64 exec, s[72:73], s[68:69]\n\t" "s_cbranch_scc0 .Ln2_%=\n\t" \
  "global_store_dwordx4 v33, v[116:119], s[22:23] sc0 sc1 nt\n\t" ".Ln2_%=:\n\t" "s_mov_b64 exec, -1\n\t" "s_branch .Lend_%=\n\t" ".Lrz_%=:\n\t" "s_sleep 35\n\t" ".Lrt_%=:\n\t" \
  "s_mul_i32 s40, s28, 6144\n\t" "s_add_u32 s40, s40, 5302784\n\t" "v_lshlrev_b32 v2, 7, v8\n\t" "s_add_u32 s40, s24, s40\n\t" "s_addc_u32 s41, s25, 0\n\t" "v_mov_b32 v40, -1\n\t" \
  "v_mov_b32 v41, -1\n\t" "v_mov_b32 v42, -1\n\t" "s_mov_b32 exec_hi, 0x3fff\n\t" "global_load_dword v40, v2, s[40:41] sc1\n\t" "s_sleep 2\n\t" "global_load_dword v41, v2, s[40:41] sc1\n\t" \
  "s_sleep 2\n\t" "global_load_dword v42, v2, s[40:41] sc1\n\t" "s_mov_b32 exec_hi, -1\n\t" "v_mov_b32 v43, 0xfc0\n\t" "s_mov_b32 s42, 0\n\t" "s_mov_b32 s43, 0xffff0000\n\t" \
  "v_add_u32 v3, 64, v37\n\t" "v_cndmask_b32 v3, v3, v37, s[42:43]\n\t" "s_mov_b32 s43, 0xffffc000\n\t" ".Lrl_%=:\n\t" "s_waitcnt vmcnt(2)\n\t" "v_cmp_ne_u32 vcc, -1, v40\n\t" \
  "s_or_b64 s[42:43], s[42:43], vcc\n\t" "s_cmp_eq_u64 s[42:43], -1\n\t" "v_cndmask_b32 v43, v43, v40, vcc\n\t" "s_cselect_b32 s44, 1, 0\n\t" "v_mov_b32 v45, s44\n\t" \
  "v_max_u32_dpp v44, v43, v43 quad_perm:[1,0,3,2] row_mask:0xf bank_mask:0xf\n\t" "s_nop 1\n\t" "v_max_u32_dpp v44, v44, v44 quad_perm:[2,3,0,1] row_mask:0xf bank_mask:0xf\n\t" "s_nop 1\n\t" \
  "v_max_u32_dpp v44, v44, v44 row_half_mirror row_mask:0xf bank_mask:0xf\n\t" "s_nop 1\n\t" "v_max_u32_dpp v44, v44, v44 row_mirror row_mask:0xf bank_mask:0xf\n\t" "s_nop 1\n\t" \
  "v_max_u32_dpp v44, v44, v44 row_bcast:15 row_mask:0xa bank_mask:0xf\n\t" "s_nop 1\n\t" "v_max_u32_dpp v44, v44, v44 row_bcast:31 row_mask:0xc bank_mask:0xf\n\t" \
  "ds_write_b64 v3, v[44:45] offset:32\n\t" "s_cbranch_scc1 .Lre_%=\n\t" "s_cmp_lg_u32 s75, 0\n\t" "s_cbranch_scc1 .Lrz0_%=\n\t" "s_sleep 5\n\t" ".Lrz0_%=:\n\t" "s_not_b64 exec, s[42:43]\n\t" \
  "global_load_dword v40, v2, s[40:41] sc1\n\t" "s_mov_b64 exec, -1\n\t" "s_waitcnt vmcnt(2)\n\t" "v_cmp_ne_u32 vcc, -1, v41\n\t" "s_or_b64 s[42:43], s[42:43], vcc\n\t" "s_cmp_eq_u64 s[42:43], -1\n\t" \
  "v_cndmask_b32 v43, v43, v41, vcc\n\t" "s_cselect_b32 s44, 1, 0\n\t" "v_mov_b32 v45, s44\n\t" "v_max_u32_dpp v44, v43, v43 quad_perm:[1,0,3,2] row_mask:0xf bank_mask:0xf\n\t" "s_nop 1\n\t" \
  "v_max_u32_dpp v44, v44, v44 quad_perm:[2,3,0,1] row_mask:0xf bank_mask:0xf\n\t" "s_nop 1\n\t" "v_max_u32_dpp v44, v44, v44 row_half_mirror row_mask:0xf bank_mask:0xf\n\t" "s_nop 1\n\t" \
  "v_max_u32_dpp v44, v44, v44 row_mirror row_mask:0xf bank_mask:0xf\n\t" "s_nop 1\n\t" "v_max_u32_dpp v44, v44, v44 row_bcast:15 row_mask:0xa bank_mask:0xf\n\t" "s_nop 1\n\t" \
  "v_max_u32_dpp v44, v44, v44 row_bcast:31 row_mask:0xc bank_mask:0xf\n\t" "ds_write_b64 v3, v[44:45] offset:32\n\t" "s_cbranch_scc1 .Lre_%=\n\t" "s_cmp_lg_u32 s75, 0\n\t" \
  "s_cbranch_scc1 .Lrz1_%=\n\t" "s_sleep 5\n\t" ".Lrz1_%=:\n\t" "s_not_b64 exec, s[42:43]\n\t" "global_load_dword v41, v2, s[40:41] sc1\n\t" "s_mov_b64 exec, -1\n\t" "s_waitcnt vmcnt(2)\n\t" \
  "v_cmp_ne_u32 vcc, -1, v42\n\t" "s_or_b64 s[42:43], s[42:43], vcc\n\t" "s_cmp_eq_u64 s[42:43], -1\n\t" "v_cndmask_b32 v43, v43, v42, vcc\n\t" "s_cselect_b32 s44, 1, 0\n\t" "v_mov_b32 v45, s44\n\t" \
  "v_max_u32_dpp v44, v43, v43 quad_perm:[1,0,3,2] row_mask:0xf bank_mask:0xf\n\t" "s_nop 1\n\t" "v_max_u32_dpp v44, v44, v44 quad_perm:[2,3,0,1] row_mask:0xf bank_mask:0xf\n\t" "s_nop 1\n\t" \
  "v_max_u32_dpp v44, v44, v44 row_half_mirror row_mask:0xf bank_mask:0xf\n\t" "s_nop 1\n\t" "v_max_u32_dpp v44, v44, v44 row_mirror row_mask:0xf bank_mask:0xf\n\t" "s_nop 1\n\t" \
  "v_max_u32_dpp v44, v44, v44 row_bcast:15 row_mask:0xa bank_mask:0xf\n\t" "s_nop 1\n\t" "v_max_u32_dpp v44, v44, v44 row_bcast:31 row_mask:0xc bank_mask:0xf\n\t" \
  "ds_write_b64 v3, v[44:45] offset:32\n\t" "s_cbranch_scc1 .Lre_%=\n\t" "s_cmp_lg_u32 s75, 0\n\t" "s_cbranch_scc1 .Lrz2_%=\n\t" "s_sleep 5\n\t" ".Lrz2_%=:\n\t" "s_not_b64 exec, s[42:43]\n\t" \
  "global_load_dword v42, v2, s[40:41] sc1\n\t" "s_mov_b64 exec, -1\n\t" "s_sub_u32 s39, s39, 1\n\t" "s_cmp_lt_i32 s39, 0\n\t" "s_cbranch_scc0 .Lrl_%=\n\t" ".Lre_%=:\n\t" "s_cmp_eq_u32 s75, 0\n\t" \
  "s_cbranch_scc1 .Lb6_%=\n\t" "s_waitcnt lgkmcnt(0)\n\t" "s_barrier\n\t" "s_barrier\n\t" "s_cmp_lg_u32 s29, 0\n\t" "s_cbranch_scc1 .Lend_%=\n\t" "v_mov_b32 v1, 0\n\t" "s_mov_b64 exec, 1\n\t" \
  "global_store_dwordx2 v1, v[16:17], s[24:25] sc1\n\t" "s_mov_b64 exec, -1\n\t" "s_branch .Lend_%=\n\t" ".Lsp_%=:\n\t" "v_mov_b32 v1, 0\n\t" "s_add_u32 s40, s28, 16\n\t" \
  "s_sub_u32 s41, %[bid], 240\n\t" "s_cselect_b32 s29, s40, s41\n\t" "v_lshlrev_b32 v32, 4, v8\n\t" "v_and_b32 v9, 15, v8\n\t" "v_mov_b32 v37, 156416\n\t" "v_mov_b32 v38, 1\n\t" \
  "v_mov_b32 v44, -1\n\t" "v_mov_b32 v45, -1\n\t" "v_mov_b32 v46, -1\n\t" "v_mov_b32 v47, -1\n\t" "s_mov_b32 s75, 1\n\t" "s_mov_b32 s39, 400000\n\t" "s_add_u32 s45, s29, 4\n\t" \
  "s_lshr_b32 s45, s45, 3\n\t" "s_sub_u32 s45, s29, s45\n\t" "s_and_b32 s48, s29, 7\n\t" "s_mul_i32 s46, s45, 232\n\t" "s_movk_i32 s47, 231\n\t" "s_mul_i32 s40, s45, 764\n\t" \
  "s_sub_u32 s40, s40, 11172\n\t" "s_cmp_ge_u32 s29, 24\n\t" "s_cselect_b32 s46, s40, s46\n\t" "s_cselect_b32 s47, 763, s47\n\t" "v_min_u32 v5, s47, v0\n\t" "v_add_u32 v5, s46, v5\n\t" \
  "v_min_u32 v5, 6399, v5\n\t" "v_lshlrev_b32 v5, 7, v5\n\t" "s_lshr_b32 s47, s47, 6\n\t" "s_mul_i32 s40, s45, 12\n\t" "s_add_u32 s40, s40, s26\n\t" "s_lshl_b32 s41, s40, 10\n\t" \
  "s_add_u32 s41, s41, 3416064\n\t" "s_lshl_b32 s44, s45, 7\n\t" "s_add_u32 s44, s44, 4096\n\t" "v_lshl_add_u32 v4, v9, 3, s44\n\t" "s_waitcnt lgkmcnt(0)\n\t" \
  "global_load_dwordx2 v[40:41], v1, s[24:25] sc1\n\t" "s_add_u32 s42, s24, s41\n\t" "s_addc_u32 s43, s25, 0\n\t" "v_lshl_add_u32 v3, v8, 2, v37\n\t" "s_movk_i32 s44, 0xfc0\n\t" "v_mov_b32 v2, 0\n\t" \
  "v_writelane_b32 v2, s44, 8\n\t" "ds_write_b32 v3, v2\n\t" "s_mul_i32 s50, s29, 81920\n\t" "s_mul_i32 s54, s33, 20480\n\t" "s_add_u32 s50, s50, s54\n\t" "s_add_u32 s54, s54, 65536\n\t" \
  "s_add_u32 s52, s20, s50\n\t" "s_addc_u32 s53, s21, 0\n\t" "s_memrealtime s[56:57]\n\t" "s_mov_b32 m0, 163072\n\t" "s_cmp_eq_u32 s48, 3\n\t" "s_cbranch_scc1 .Lnf_%=\n\t" ".Lts2_%=:\n\t" \
  "s_sleep 2\n\t" "s_memrealtime s[78:79]\n\t" "s_waitcnt lgkmcnt(0)\n\t" "s_sub_u32 s78, s78, s56\n\t" "s_cmp_lt_u32 s78, 255\n\t" "s_cbranch_scc1 .Lts2_%=\n\t" ".Lpf0_%=:\n\t" \
  "global_store_dwordx4 v32, v[44:47], s[42:43] sc1\n\t" "s_add_u32 s42, s42, 282624\n\t" "s_addc_u32 s43, s43, 0\n\t" "s_add_u32 s40, s40, 276\n\t" "s_cmp_lt_u32 s40, 1380\n\t" \
  "s_cbranch_scc1 .Lpf0_%=\n\t" ".Lpf1_%=:\n\t" "global_store_dwordx4 v32, v[44:47], s[42:43] sc1\n\t" "s_add_u32 s42, s42, 282624\n\t" "s_addc_u32 s43, s43, 0\n\t" "s_add_u32 s40, s40, 276\n\t" \
  "s_cmp_lt_u32 s40, 1903\n\t" "s_cbranch_scc1 .Lpf1_%=\n\t" "s_waitcnt vmcnt(0)\n\t" "v_add_co_u32 v16, vcc, 1, v40\n\t" "s_cmp_lg_u32 s26, 0\n\t" "s_nop 0\n\t" \
  "v_addc_co_u32 v17, vcc, 0, v41, vcc\n\t" "s_barrier\n\t" "s_cbranch_scc1 .Lnf1_%=\n\t" "global_store_dwordx2 v4, v[16:17], s[24:25] sc1\n\t" ".Lnf1_%=:\n\t" "s_cmp_gt_u32 s26, s47\n\t" \
  "s_cbranch_scc1 .Lnt_%=\n\t" "global_load_lds_dword v5, s[12:13]\n\t" ".Lnt_%=:\n\t" ".Lnf_%=:\n\t" "s_cmp_ge_u32 s29, 24\n\t" "s_cbranch_scc1 .Lend_%=\n\t" "s_cmp_ge_u32 s26, 8\n\t" \
  "s_cbranch_scc1 .Lsq_%=x\n\t" "v_lshrrev_b32 v1, 3, v8\n\t" "v_and_b32 v2, 6, v1\n\t" "v_xor_b32 v2, v2, v8\n\t" "v_and_b32 v2, 7, v2\n\t" "v_mul_u32_u24 v1, 3072, v1\n\t" \
  "v_lshl_add_u32 v1, v2, 4, v1\n\t" "v_add_u32 v17, 23552, v1\n\t" "v_add_u32 v18, 47104, v1\n\t" "v_add_u32 v19, 70656, v1\n\t" "s_cmp_ge_u32 s26, 4\n\t" \
  "s_cselect_b64 s[42:43], s[18:19], s[16:17]\n\t" "s_mul_i32 s40, s33, 491520\n\t" "s_lshl_b32 s41, s29, 7\n\t" "s_add_u32 s40, s40, s41\n\t" "s_add_u32 s42, s42, s40\n\t" \
  "s_addc_u32 s43, s43, 0\n\t" "s_lshl_b32 s44, s26, 13\n\t" "s_add_u32 m0, s44, 0\n\t" "s_nop 0\n\t" "global_load_lds_dwordx4 v1, s[42:43] offset:0 nt\n\t" \
  "global_load_lds_dwordx4 v17, s[42:43] offset:1024 nt\n\t" "global_load_lds_dwordx4 v18, s[42:43] offset:2048 nt\n\t" "global_load_lds_dwordx4 v19, s[42:43] offset:3072 nt\n\t" \
  "s_add_u32 s42, s42, 98304\n\t" "s_addc_u32 s43, s43, 0\n\t" "s_add_u32 m0, s44, 4096\n\t" "s_nop 0\n\t" "global_load_lds_dwordx4 v1, s[42:43] offset:0 nt\n\t" \
  "global_load_lds_dwordx4 v17, s[42:43] offset:1024 nt\n\t" "global_load_lds_dwordx4 v18, s[42:43] offset:2048 nt\n\t" "global_load_lds_dwordx4 v19, s[42:43] offset:3072 nt\n\t" \
  "s_add_u32 s42, s42, 98304\n\t" "s_addc_u32 s43, s43, 0\n\t" "v_lshrrev_b32 v10, 2, v8\n\t" "v_lshlrev_b32 v3, 2, v8\n\t" "v_and_b32 v4, 0x60, v3\n\t" "v_and_b32 v3, 12, v3\n\t" \
  "v_lshl_add_u32 v2, v10, 7, v4\n\t" "v_lshl_add_u32 v2, v3, 1, v2\n\t" "v_mul_u32_u24 v4, 1280, v10\n\t" "v_lshl_add_u32 v4, v3, 1, v4\n\t" "s_mul_i32 s40, s26, 160\n\t" \
  "s_add_u32 s40, s40, 65536\n\t" "v_add_u32 v4, s40, v4\n\t" "v_and_b32 v10, 12, v10\n\t" "v_xor_b32 v12, 0, v2\n\t" "v_add_u32 v12, s44, v12\n\t" "v_xor_b32 v13, 32, v2\n\t" \
  "v_add_u32 v13, s44, v13\n\t" "v_xor_b32 v14, 64, v2\n\t" "v_add_u32 v14, s44, v14\n\t" "v_xor_b32 v15, 96, v2\n\t" "v_add_u32 v15, s44, v15\n\t" "s_waitcnt vmcnt(4)\n\t" \
  "ds_read_b64_tr_b16 v[40:41], v12 offset:0\n\t" "ds_read_b64_tr_b16 v[42:43], v12 offset:2048\n\t" "ds_read_b64_tr_b16 v[44:45], v13 offset:0\n\t" "ds_read_b64_tr_b16 v[46:47], v13 offset:2048\n\t" \
  "ds_read_b64_tr_b16 v[48:49], v14 offset:0\n\t" "ds_read_b64_tr_b16 v[50:51], v14 offset:2048\n\t" "ds_read_b64_tr_b16 v[52:53], v15 offset:0\n\t" "ds_read_b64_tr_b16 v[54:55], v15 offset:2048\n\t" \
  "s_waitcnt lgkmcnt(0)\n\t" "s_add_u32 m0, s44, 0\n\t" "s_nop 0\n\t" "global_load_lds_dwordx4 v1, s[42:43] offset:0 nt\n\t" "global_load_lds_dwordx4 v17, s[42:43] offset:1024 nt\n\t" \
  "global_load_lds_dwordx4 v18, s[42:43] offset:2048 nt\n\t" "global_load_lds_dwordx4 v19, s[42:43] offset:3072 nt\n\t" "s_add_u32 s42, s42, 98304\n\t" "s_addc_u32 s43, s43, 0\n\t" \
  "s_waitcnt vmcnt(4)\n\t" "ds_read_b64_tr_b16 v[56:57], v12 offset:4096\n\t" "ds_read_b64_tr_b16 v[58:59], v12 offset:6144\n\t" "ds_read_b64_tr_b16 v[60:61], v13 offset:4096\n\t" \
  "ds_read_b64_tr_b16 v[62:63], v13 offset:6144\n\t" "ds_read_b64_tr_b16 v[64:65], v14 offset:4096\n\t" "ds_read_b64_tr_b16 v[66:67], v14 offset:6144\n\t" \
  "ds_read_b64_tr_b16 v[68:69], v15 offset:4096\n\t" "ds_read_b64_tr_b16 v[70:71], v15 offset:6144\n\t" "s_waitcnt lgkmcnt(0)\n\t" "s_add_u32 m0, s44, 4096\n\t" "s_nop 0\n\t" \
  "global_load_lds_dwordx4 v1, s[42:43] offset:0 nt\n\t" "global_load_lds_dwordx4 v17, s[42:43] offset:1024 nt\n\t" "global_load_lds_dwordx4 v18, s[42:43] offset:2048 nt\n\t" \
  "global_load_lds_dwordx4 v19, s[42:43] offset:3072 nt\n\t" "s_add_u32 s42, s42, 98304\n\t" "s_addc_u32 s43, s43, 0\n\t" "s_waitcnt vmcnt(4)\n\t" "ds_read_b64_tr_b16 v[72:73], v12 offset:0\n\t" \
  "ds_read_b64_tr_b16 v[74:75], v12 offset:2048\n\t" "ds_read_b64_tr_b16 v[76:77], v13 offset:0\n\t" "ds_read_b64_tr_b16 v[78:79], v13 offset:2048\n\t" "ds_read_b64_tr_b16 v[80:81], v14 offset:0\n\t" \
  "ds_read_b64_tr_b16 v[82:83], v14 offset:2048\n\t" "ds_read_b64_tr_b16 v[84:85], v15 offset:0\n\t" "ds_read_b64_tr_b16 v[86:87], v15 offset:2048\n\t" "s_waitcnt lgkmcnt(0)\n\t" \
  "s_add_u32 m0, s44, 0\n\t" "s_nop 0\n\t" "global_load_lds_dwordx4 v1, s[42:43] offset:0 nt\n\t" "global_load_lds_dwordx4 v17, s[42:43] offset:1024 nt\n\t" \
  "global_load_lds_dwordx4 v18, s[42:43] offset:2048 nt\n\t" "global_load_lds_dwordx4 v19, s[42:43] offset:3072 nt\n\t" "s_barrier\n\t" "v_add_u32 v2, 40960, v4\n\t" \
  "ds_read_b64_tr_b16 v[120:121], v4 offset:0\n\t" "ds_read_b64_tr_b16 v[122:123], v4 offset:20480\n\t" "ds_read_b64_tr_b16 v[124:125], v2 offset:0\n\t" \
  "ds_read_b64_tr_b16 v[126:127], v2 offset:20480\n\t" "ds_read_b64_tr_b16 v[128:129], v4 offset:32\n\t" "ds_read_b64_tr_b16 v[130:131], v4 offset:20512\n\t" \
  "ds_read_b64_tr_b16 v[132:133], v2 offset:32\n\t" "ds_read_b64_tr_b16 v[134:135], v2 offset:20512\n\t" "ds_read_b64_tr_b16 v[136:137], v4 offset:64\n\t" \
  "ds_read_b64_tr_b16 v[138:139], v4 offset:20544\n\t" "ds_read_b64_tr_b16 v[140:141], v2 offset:64\n\t" "ds_read_b64_tr_b16 v[142:143], v2 offset:20544\n\t" \
  "ds_read_b64_tr_b16 v[144:145], v4 offset:96\n\t" "ds_read_b64_tr_b16 v[146:147], v4 offset:20576\n\t" "ds_read_b64_tr_b16 v[148:149], v2 offset:96\n\t" \
  "ds_read_b64_tr_b16 v[150:151], v2 offset:20576\n\t" "ds_read_b64_tr_b16 v[152:153], v4 offset:128\n\t" "ds_read_b64_tr_b16 v[154:155], v4 offset:20608\n\t" \
  "ds_read_b64_tr_b16 v[156:157], v2 offset:128\n\t" "ds_read_b64_tr_b16 v[158:159], v2 offset:20608\n\t" "s_waitcnt vmcnt(4)\n\t" "ds_read_b64_tr_b16 v[88:89], v12 offset:4096\n\t" \
  "ds_read_b64_tr_b16 v[90:91], v12 offset:6144\n\t" "ds_read_b64_tr_b16 v[92:93], v13 offset:4096\n\t" "ds_read_b64_tr_b16 v[94:95], v13 offset:6144\n\t" \
  "ds_read_b64_tr_b16 v[96:97], v14 offset:4096\n\t" "ds_read_b64_tr_b16 v[98:99], v14 offset:6144\n\t" "ds_read_b64_tr_b16 v[100:101], v15 offset:4096\n\t" \
  "ds_read_b64_tr_b16 v[102:103], v15 offset:6144\n\t" "s_waitcnt vmcnt(0)\n\t" "ds_read_b64_tr_b16 v[104:105], v12 offset:0\n\t" "ds_read_b64_tr_b16 v[106:107], v12 offset:2048\n\t" \
  "ds_read_b64_tr_b16 v[108:109], v13 offset:0\n\t" "ds_read_b64_tr_b16 v[110:111], v13 offset:2048\n\t" "ds_read_b64_tr_b16 v[112:113], v14 offset:0\n\t" \
  "ds_read_b64_tr_b16 v[114:115], v14 offset:2048\n\t" "ds_read_b64_tr_b16 v[116:117], v15 offset:0\n\t" "ds_read_b64_tr_b16 v[118:119], v15 offset:2048\n\t" "s_mul_i32 s40, s33, 320\n\t" \
  "s_add_u32 s40, s40, 159744\n\t" "v_lshl_add_u32 v1, v10, 1, s40\n\t" "s_lshl_b32 s40, s26, 8\n\t" "s_add_u32 s40, s40, 161024\n\t" "v_lshl_add_u32 v2, v10, 2, s40\n\t" "v_and_b32 v4, 12, v8\n\t" \
  "v_lshl_add_u32 v4, v4, 2, v10\n\t" "s_mov_b32 s40, 161024\n\t" "v_lshl_add_u32 v4, v4, 2, s40\n\t" "v_add_u32 v3, v3, v4\n\t" "s_mul_i32 s40, s26, 320\n\t" "v_lshl_add_u32 v4, v9, 6, s40\n\t" \
  "v_lshl_add_u32 v4, v10, 2, v4\n\t" "s_mul_i32 s40, s29, 2560\n\t" "s_add_u32 s40, s40, 5241344\n\t" "s_add_u32 s62, s24, s40\n\t" "s_addc_u32 s63, s25, 0\n\t" "s_mov_b32 s40, 0x10001\n\t" \
  "s_mov_b32 s41, s40\n\t" "s_lshl_b64 s[42:43], s[40:41], 1\n\t" "s_lshl_b64 s[44:45], s[40:41], 2\n\t" "s_lshl_b64 s[46:47], s[40:41], 3\n\t" "s_lshl_b64 s[48:49], s[40:41], 4\n\t" \
  "s_mov_b32 s60, 0x1f001f\n\t" "s_mov_b32 s61, s60\n\t" "s_mov_b32 s76, 0xbfb8aa3b\n\t" "s_setprio 3\n\t" "s_waitcnt lgkmcnt(0)\n\t" "s_barrier\n\t" \
  "ds_read2_b64 v[28:31], v1 offset0:0 offset1:4\n\t" "ds_read2_b64 v[32:35], v1 offset0:8 offset1:12\n\t" "ds_read2_b64 v[36:39], v1 offset0:16 offset1:20\n\t" \
  "ds_read2_b64 v[160:163], v1 offset0:24 offset1:28\n\t" "ds_read2_b64 v[164:167], v1 offset0:32 offset1:36\n\t" "s_waitcnt lgkmcnt(4)\n\t" \
  "v_mfma_f32_16x16x32_bf16 v[12:15], v[40:43], v[28:31], 0\n\t" "v_mfma_f32_16x16x32_bf16 v[16:19], v[44:47], v[28:31], 0\n\t" "v_mfma_f32_16x16x32_bf16 v[20:23], v[48:51], v[28:31], 0\n\t" \
  "v_mfma_f32_16x16x32_bf16 v[24:27], v[52:55], v[28:31], 0\n\t" "s_waitcnt lgkmcnt(3)\n\t" "v_mfma_f32_16x16x32_bf16 v[12:15], v[56:59], v[32:35], v[12:15]\n\t" \
  "v_mfma_f32_16x16x32_bf16 v[16:19], v[60:63], v[32:35], v[16:19]\n\t" "v_mfma_f32_16x16x32_bf16 v[20:23], v[64:67], v[32:35], v[20:23]\n\t" \
  "v_mfma_f32_16x16x32_bf16 v[24:27], v[68:71], v[32:35], v[24:27]\n\t" "s_waitcnt lgkmcnt(2)\n\t" "v_mfma_f32_16x16x32_bf16 v[12:15], v[72:75], v[36:39], v[12:15]\n\t" \
  "v_mfma_f32_16x16x32_bf16 v[16:19], v[76:79], v[36:39], v[16:19]\n\t" "v_mfma_f32_16x16x32_bf16 v[20:23], v[80:83], v[36:39], v[20:23]\n\t" \
  "v_mfma_f32_16x16x32_bf16 v[24:27], v[84:87], v[36:39], v[24:27]\n\t" "s_waitcnt lgkmcnt(1)\n\t" "v_mfma_f32_16x16x32_bf16 v[12:15], v[88:91], v[160:163], v[12:15]\n\t" \
  "v_mfma_f32_16x16x32_bf16 v[16:19], v[92:95], v[160:163], v[16:19]\n\t" "v_mfma_f32_16x16x32_bf16 v[20:23], v[96:99], v[160:163], v[20:23]\n\t" \
  "v_mfma_f32_16x16x32_bf16 v[24:27], v[100:103], v[160:163], v[24:27]\n\t" "s_waitcnt lgkmcnt(0)\n\t" "v_mfma_f32_16x16x32_bf16 v[12:15], v[104:107], v[164:167], v[12:15]\n\t" \
  "v_mfma_f32_16x16x32_bf16 v[16:19], v[108:111], v[164:167], v[16:19]\n\t" "v_mfma_f32_16x16x32_bf16 v[20:23], v[112:115], v[164:167], v[20:23]\n\t" \
  "v_mfma_f32_16x16x32_bf16 v[24:27], v[116:119], v[164:167], v[24:27]\n\t" "s_mov_b64 exec, s[40:41]\n\t" "s_nop 3\n\t" "ds_write_b128 v2, v[12:15] offset:0\n\t" \
  "ds_write_b128 v2, v[16:19] offset:64\n\t" "ds_write_b128 v2, v[20:23] offset:128\n\t" "ds_write_b128 v2, v[24:27] offset:192\n\t" "s_mov_b64 exec, -1\n\t" "s_waitcnt lgkmcnt(0)\n\t" "s_barrier\n\t" \
  "ds_read2st64_b32 v[40:41], v3 offset0:0 offset1:4\n\t" "ds_read2st64_b32 v[42:43], v3 offset0:1 offset1:5\n\t" "ds_read2st64_b32 v[44:45], v3 offset0:2 offset1:6\n\t" \
  "ds_read2st64_b32 v[46:47], v3 offset0:3 offset1:7\n\t" "s_waitcnt lgkmcnt(0)\n\t" "v_pk_add_f32 v[40:41], v[40:41], v[42:43]\n\t" "v_pk_add_f32 v[44:45], v[44:45], v[46:47]\n\t" \
  "v_pk_add_f32 v[40:41], v[40:41], v[44:45]\n\t" "v_cvt_pk_bf16_f32 v41, v40, v41\n\t" "v_lshlrev_b32 v40, 16, v41\n\t" "v_mul_f32 v42, s76, v40\n\t" "v_exp_f32 v42, v42\n\t" \
  "v_and_b32 v41, 0xffff0000, v41\n\t" "v_add_f32 v42, 1.0, v42\n\t" "v_rcp_f32 v42, v42\n\t" "s_nop 0\n\t" "v_mul_f32 v40, v40, v42\n\t" "v_cvt_pk_bf16_f32 v40, v40, v40\n\t" \
  "v_lshlrev_b32 v40, 16, v40\n\t" "v_mul_f32 v40, v40, v41\n\t" "s_nop 1\n\t" "v_mov_b32_dpp v41, v40 quad_perm:[1,1,3,3] row_mask:0xf bank_mask:0xf\n\t" "v_cvt_pk_bf16_f32 v40, v40, v41\n\t" \
  "s_nop 1\n\t" "v_mov_b32_dpp v44, v40 row_newbcast:0 row_mask:0xf bank_mask:0xf\n\t" "v_mov_b32_dpp v45, v40 row_newbcast:2 row_mask:0xf bank_mask:0xf\n\t" \
  "v_mov_b32_dpp v46, v40 row_newbcast:4 row_mask:0xf bank_mask:0xf\n\t" "v_mov_b32_dpp v47, v40 row_newbcast:6 row_mask:0xf bank_mask:0xf\n\t" \
  "v_mov_b32_dpp v48, v40 row_newbcast:8 row_mask:0xf bank_mask:0xf\n\t" "v_mov_b32_dpp v49, v40 row_newbcast:10 row_mask:0xf bank_mask:0xf\n\t" \
  "v_mov_b32_dpp v50, v40 row_newbcast:12 row_mask:0xf bank_mask:0xf\n\t" "v_mov_b32_dpp v51, v40 row_newbcast:14 row_mask:0xf bank_mask:0xf\n\t" \
  "v_mfma_f32_16x16x32_bf16 v[56:59], v[120:123], v[44:47], 0\n\t" "s_nop 0\n\t" "v_mfma_f32_16x16x32_bf16 v[56:59], v[124:127], v[48:51], v[56:59]\n\t" \
  "v_mfma_f32_16x16x32_bf16 v[60:63], v[128:131], v[44:47], 0\n\t" "v_mfma_f32_16x16x32_bf16 v[60:63], v[132:135], v[48:51], v[60:63]\n\t" \
  "v_mfma_f32_16x16x32_bf16 v[64:67], v[136:139], v[44:47], 0\n\t" "v_mfma_f32_16x16x32_bf16 v[64:67], v[140:143], v[48:51], v[64:67]\n\t" \
  "v_mfma_f32_16x16x32_bf16 v[68:71], v[144:147], v[44:47], 0\n\t" "v_mfma_f32_16x16x32_bf16 v[68:71], v[148:151], v[48:51], v[68:71]\n\t" \
  "v_mfma_f32_16x16x32_bf16 v[72:75], v[152:155], v[44:47], 0\n\t" "v_mfma_f32_16x16x32_bf16 v[72:75], v[156:159], v[48:51], v[72:75]\n\t" "s_nop 1\n\t" "v_cndmask_b32 v56, v56, v60, s[42:43]\n\t" \
  "v_cndmask_b32 v57, v57, v61, s[42:43]\n\t" "v_cndmask_b32 v58, v58, v62, s[42:43]\n\t" "v_cndmask_b32 v59, v59, v63, s[42:43]\n\t" "v_cndmask_b32 v56, v56, v64, s[44:45]\n\t" \
  "v_cndmask_b32 v57, v57, v65, s[44:45]\n\t" "v_cndmask_b32 v58, v58, v66, s[44:45]\n\t" "v_cndmask_b32 v59, v59, v67, s[44:45]\n\t" "v_cndmask_b32 v56, v56, v68, s[46:47]\n\t" \
  "v_cndmask_b32 v57, v57, v69, s[46:47]\n\t" "v_cndmask_b32 v58, v58, v70, s[46:47]\n\t" "v_cndmask_b32 v59, v59, v71, s[46:47]\n\t" "v_cndmask_b32 v56, v56, v72, s[48:49]\n\t" \
  "v_cndmask_b32 v57, v57, v73, s[48:49]\n\t" "v_cndmask_b32 v58, v58, v74, s[48:49]\n\t" "v_cndmask_b32 v59, v59, v75, s[48:49]\n\t" "s_mov_b64 exec, s[60:61]\n\t" \
  "global_store_dwordx4 v4, v[56:59], s[62:63] sc1\n\t" "s_mov_b64 exec, -1\n\t" "s_branch .Lend_%=\n\t" ".Lsq_%=x:\n\t" "s_movk_i32 s50, 5\n\t" ".Ld3_%=:\n\t" "s_mov_b32 m0, s54\n\t" \
  "s_add_u32 s54, s54, 4096\n\t" "global_load_lds_dwordx4 v32, s[52:53] offset:0 nt\n\t" "global_load_lds_dwordx4 v32, s[52:53] offset:1024 nt\n\t" \
  "global_load_lds_dwordx4 v32, s[52:53] offset:2048 nt\n\t" "global_load_lds_dwordx4 v32, s[52:53] offset:3072 nt\n\t" "s_add_u32 s52, s52, 4096\n\t" "s_addc_u32 s53, s53, 0\n\t" \
  "s_sub_u32 s50, s50, 1\n\t" "s_cmp_lg_u32 s50, 0\n\t" "s_cbranch_scc1 .Ld3_%=\n\t" "s_waitcnt vmcnt(0) lgkmcnt(0)\n\t" "s_barrier\n\t" "v_add_co_u32 v16, vcc, 1, v40\n\t" "s_nop 1\n\t" \
  "v_addc_co_u32 v17, vcc, 0, v41, vcc\n\t" "s_mul_i32 s40, s29, 205\n\t" "s_lshr_b32 s40, s40, 11\n\t" "s_mul_i32 s40, s40, 10\n\t" "s_sub_u32 s28, s29, s40\n\t" "s_cmp_lt_u32 s26, 10\n\t" \
  "s_cbranch_scc1 .Lcb_%=\n\t" "s_setprio 3\n\t" "s_cmp_eq_u32 s26, 10\n\t" "s_cbranch_scc1 .Lfe_%=\n\t" "s_movk_i32 s40, 4096\n\t" "v_min_u32 v3, 22, v8\n\t" "v_lshl_add_u32 v3, v3, 7, s40\n\t" \
  ".Lsf_%=:\n\t" "global_load_dwordx2 v[40:41], v3, s[24:25] sc1\n\t" "s_sub_u32 s39, s39, 1\n\t" "s_cmp_lt_i32 s39, 0\n\t" "s_waitcnt vmcnt(0)\n\t" "s_cbranch_scc1 .Lrt_%=\n\t" \
  "v_cmp_ne_u64 vcc, v[40:41], v[16:17]\n\t" "s_cbranch_vccnz .Lsf_%=\n\t" "s_xor_b32 s40, s28, 1\n\t" "s_mul_i32 s40, s40, 6144\n\t" "s_add_u32 s40, s40, 5302784\n\t" "v_and_b32 v3, 7, v8\n\t" \
  "v_mul_u32_u24 v3, 640, v3\n\t" "v_add_u32 v3, s40, v3\n\t" ".Lsk_%=:\n\t" "s_sleep 6\n\t" "global_load_dword v40, v3, s[24:25] sc1\n\t" "s_sub_u32 s39, s39, 1\n\t" "s_cmp_lt_i32 s39, 0\n\t" \
  "s_waitcnt vmcnt(0)\n\t" "s_cbranch_scc1 .Lrt_%=\n\t" "v_cmp_ne_u32 vcc, -1, v40\n\t" "s_cbranch_vccz .Lsk_%=\n\t" "s_branch .Lrt_%=\n\t" ".Lfe_%=:\n\t" "v_lshrrev_b32 v2, 3, v8\n\t" \
  "v_lshl_add_u32 v2, v2, 7, v32\n\t" "v_lshlrev_b32 v4, 2, v8\n\t" "v_lshrrev_b32 v3, 5, v8\n\t" "v_lshl_add_u32 v3, v3, 7, v4\n\t" "v_add_u32 v4, 160768, v4\n\t" "v_add_u32 v5, 159744, v32\n\t" \
  "s_movk_i32 s44, 63\n\t" ".Lf_%=:\n\t" "ds_read_b64 v[40:41], v37 offset:32\n\t" "s_sub_u32 s39, s39, 1\n\t" "s_cmp_lt_i32 s39, 0\n\t" "s_cselect_b32 s46, 1, 0\n\t" "s_waitcnt lgkmcnt(0)\n\t" \
  "v_readfirstlane_b32 s45, v40\n\t" "v_readfirstlane_b32 s47, v41\n\t" "s_bfe_u32 s45, s45, 0x60006\n\t" "s_or_b32 s46, s46, s47\n\t" "s_xor_b32 s47, s45, s44\n\t" "s_or_b32 s48, s47, s46\n\t" \
  "s_cbranch_scc0 .Lf_%=\n\t" "s_cmp_eq_u32 s47, 0\n\t" "s_cbranch_scc1 .Lfd_%=\n\t" "s_mul_i32 s40, s45, 2560\n\t" "s_add_u32 s40, s40, 5123584\n\t" "s_add_u32 s40, s24, s40\n\t" \
  "s_addc_u32 s41, s25, 0\n\t" "global_load_dwordx4 v[44:47], v2, s[40:41] sc1\n\t" "global_load_dword v48, v3, s[40:41] offset:2048 sc1\n\t" "s_add_u32 s49, s45, 1\n\t" "v_mov_b32 v50, s49\n\t" \
  "v_mov_b32 v51, s46\n\t" "s_cmp_lt_i32 s39, 0\n\t" "s_waitcnt vmcnt(0)\n\t" "ds_write_b128 v5, v[44:47]\n\t" "ds_write_b32 v4, v48\n\t" "s_cbranch_scc1 .Lfd_%=\n\t" \
  "v_max3_u32 v49, v44, v45, v46\n\t" "v_max3_u32 v49, v49, v47, v48\n\t" "s_movk_i32 s44, 99\n\t" "v_cmp_eq_u32 vcc, -1, v49\n\t" "s_cbranch_vccnz .Lf_%=\n\t" "s_mov_b32 s44, s45\n\t" \
  "s_cmp_eq_u32 s46, 0\n\t" "s_cbranch_scc1 .Lf_%=\n\t" ".Lfd_%=:\n\t" "s_waitcnt lgkmcnt(0)\n\t" "s_barrier\n\t" "s_barrier\n\t" "s_branch .Lend_%=\n\t" ".Lcb_%=:\n\t" "s_cmp_ge_u32 s29, 20\n\t" \
  "s_cbranch_scc1 .Lend_%=\n\t" "global_load_dwordx4 v[20:23], v32, s[14:15]\n\t" "v_lshlrev_b32 v6, 2, v8\n\t" "global_load_dword v24, v6, s[14:15] offset:1024\n\t" "v_add_u32 v5, 159744, v32\n\t" \
  "v_add_u32 v4, 160768, v6\n\t" "s_lshl1_add_u32 s40, s29, s33\n\t" "v_and_b32 v1, 3, v8\n\t" "v_lshrrev_b32 v2, 2, v8\n\t" "v_and_b32 v3, 4, v2\n\t" "v_lshrrev_b32 v7, 4, v8\n\t" \
  "v_and_b32 v7, 2, v7\n\t" "v_lshl_add_u32 v3, v1, 3, v3\n\t" "v_add_u32 v3, v3, v7\n\t" "v_mul_u32_u24 v2, 2560, v2\n\t" "v_lshl_add_u32 v2, v1, 4, v2\n\t" "s_lshl_b32 s41, s40, 5\n\t" \
  "s_add_u32 s42, s41, 159744\n\t" "v_add_u32 v1, s42, v3\n\t" "s_lshl_b32 s42, s40, 6\n\t" "s_add_u32 s42, s42, 5241344\n\t" "s_add_u32 s44, s24, s42\n\t" "s_addc_u32 s45, s25, 0\n\t" \
  "s_add_u32 s46, s44, 40960\n\t" "s_addc_u32 s47, s45, 0\n\t" "v_mov_b64 v[64:65], 0\n\t" "v_mov_b64 v[66:67], 0\n\t" "s_waitcnt vmcnt(0)\n\t" "s_barrier\n\t" "s_barrier\n\t" \
  "s_memrealtime s[64:65]\n\t" "ds_read_b32 v39, v37 offset:32\n\t" "ds_read_b128 v[44:47], v5\n\t" "ds_read_b32 v48, v4\n\t" "ds_read_u16_d16_hi v49, v1\n\t" "s_waitcnt lgkmcnt(0)\n\t" \
  "v_readfirstlane_b32 s42, v39\n\t" "v_dot2_f32_bf16 v50, v48, v24, 0\n\t" "v_dot2_f32_bf16 v50, v44, v20, v50\n\t" "v_dot2_f32_bf16 v50, v45, v21, v50\n\t" "v_dot2_f32_bf16 v50, v46, v22, v50\n\t" \
  "v_dot2_f32_bf16 v50, v47, v23, v50\n\t" "s_bfe_u32 s43, s42, 0x60006\n\t" "s_lshr_b32 s56, s43, 1\n\t" "s_mul_i32 s56, s56, 89\n\t" "s_and_b32 s57, s43, 1\n\t" "s_mul_i32 s57, s57, 48\n\t" \
  "s_add_u32 s56, s56, s57\n\t" "s_and_b32 s42, s42, 63\n\t" "s_add_u32 s56, s56, s42\n\t" "s_sub_u32 s56, s56, 6\n\t" "v_add_f32_dpp v50, v50, v50 quad_perm:[1,0,3,2] row_mask:0xf bank_mask:0xf\n\t" \
  "s_nop 1\n\t" "v_add_f32_dpp v50, v50, v50 quad_perm:[2,3,0,1] row_mask:0xf bank_mask:0xf\n\t" "s_nop 1\n\t" "v_add_f32_dpp v50, v50, v50 row_half_mirror row_mask:0xf bank_mask:0xf\n\t" \
  "s_nop 1\n\t" "v_add_f32_dpp v50, v50, v50 row_mirror row_mask:0xf bank_mask:0xf\n\t" "s_bfe_u32 s57, s56, 0x1000a\n\t" "s_lshl1_add_u32 s56, s56, s57\n\t" "s_and_b32 s56, s56, 2047\n\t" \
  "v_mov_b32 v51, v50\n\t" "s_mul_i32 s56, s56, 1280\n\t" "s_add_u32 s56, s56, s41\n\t" "v_permlane32_swap_b32 v50, v51\n\t" "v_add_f32 v50, v50, v51\n\t" "v_mov_b32 v51, v50\n\t" \
  "s_add_u32 s56, s22, s56\n\t" "s_addc_u32 s57, s23, 0\n\t" "v_permlane16_swap_b32 v50, v51\n\t" "v_add_f32 v50, v50, v51\n\t" "v_cvt_pk_bf16_f32 v50, v50, v50\n\t" "v_lshlrev_b32 v50, 16, v50\n\t" \
  "v_mul_f32 v50, 0xbfb8aa3b, v50\n\t" "v_exp_f32 v50, v50\n\t" "s_mul_i32 s43, s43, 2560\n\t" "s_lshr_b32 s42, s40, 2\n\t" "s_lshl_b32 s42, s42, 7\n\t" "s_add_u32 s43, s43, s42\n\t" \
  "s_add_u32 s43, s43, s41\n\t" "v_add_f32 v50, 1.0, v50\n\t" "v_rcp_f32 v50, v50\n\t" "s_add_u32 s43, s43, 5123712\n\t" "s_add_u32 s58, s24, s43\n\t" "s_addc_u32 s59, s25, 0\n\t" \
  "v_cvt_pk_bf16_f32 v50, v50, v50\n\t" "v_lshlrev_b32 v50, 16, v50\n\t" "v_and_b32 v49, 0xffff0000, v49\n\t" ".Ltc_%=:\n\t" "s_memrealtime s[78:79]\n\t" "s_waitcnt lgkmcnt(0)\n\t" \
  "s_sub_u32 s78, s78, s64\n\t" "s_cmp_lt_u32 s78, 60\n\t" "s_cbranch_scc1 .Ltc_%=\n\t" ".Lcp_%=:\n\t" "global_load_short_d16_hi v51, v3, s[58:59] sc1\n\t" \
  "global_load_dwordx4 v[60:63], v2, s[44:45] sc1\n\t" "s_mov_b32 exec_hi, 0\n\t" "global_load_dwordx4 v[64:67], v2, s[46:47] sc1\n\t" "s_mov_b32 exec_hi, -1\n\t" "s_sub_u32 s39, s39, 1\n\t" \
  "s_cmp_lt_i32 s39, 0\n\t" "s_waitcnt vmcnt(0)\n\t" "v_pk_add_f32 v[60:61], v[60:61], v[64:65]\n\t" "v_pk_add_f32 v[62:63], v[62:63], v[66:67]\n\t" "v_and_b32 v51, 0xffff0000, v51\n\t" \
  "v_add_f32_dpp v60, v60, v60 row_ror:4 row_mask:0xf bank_mask:0xf\n\t" "v_add_f32_dpp v61, v61, v61 row_ror:4 row_mask:0xf bank_mask:0xf\n\t" \
  "v_add_f32_dpp v62, v62, v62 row_ror:4 row_mask:0xf bank_mask:0xf\n\t" "v_add_f32_dpp v63, v63, v63 row_ror:4 row_mask:0xf bank_mask:0xf\n\t" \
  "v_add_f32_dpp v60, v60, v60 row_ror:8 row_mask:0xf bank_mask:0xf\n\t" "v_add_f32_dpp v61, v61, v61 row_ror:8 row_mask:0xf bank_mask:0xf\n\t" \
  "v_add_f32_dpp v62, v62, v62 row_ror:8 row_mask:0xf bank_mask:0xf\n\t" "v_add_f32_dpp v63, v63, v63 row_ror:8 row_mask:0xf bank_mask:0xf\n\t" "v_permlane32_swap_b32 v60, v61\n\t" "s_nop 0\n\t" \
  "v_permlane32_swap_b32 v62, v63\n\t" "v_add_f32 v60, v60, v61\n\t" "v_add_f32 v62, v62, v63\n\t" "s_nop 1\n\t" "v_permlane16_swap_b32 v60, v62\n\t" "v_add_f32 v60, v60, v62\n\t" \
  "v_cvt_pk_bf16_f32 v60, v60, v60\n\t" "v_lshlrev_b32 v60, 16, v60\n\t" "v_mul_f32 v60, v60, v50\n\t" "v_cvt_pk_bf16_f32 v60, v60, v60\n\t" "v_lshlrev_b32 v60, 16, v60\n\t" \
  "v_add_f32 v60, v60, v49\n\t" "v_cvt_pk_bf16_f32 v60, v60, v60\n\t" "v_lshlrev_b32 v60, 16, v60\n\t" "v_add_f32 v60, v60, v51\n\t" "s_cbranch_scc1 .Lcq_%=\n\t" "v_cmp_u_f32 vcc, v60, v60\n\t" \
  "s_cbranch_vccnz .Lcp_%=\n\t" ".Lcq_%=:\n\t" "v_cvt_pk_bf16_f32 v60, v60, v60\n\t" "s_mov_b32 exec_lo, 0xf000f\n\t" "s_mov_b32 exec_hi, 0xf000f\n\t" "global_store_short v3, v60, s[56:57]\n\t" \
  "s_mov_b64 exec, -1\n\t" ".Lend_%=:\n\t"

extern "C" __global__ __launch_bounds__(768, 3) void fused_block(const u16* x, const u16* wqkv, const u16* wo, const u16* w_r,
                                                                 const u16* w_gate, const u16* w_up, const u16* w_down, u16* out,
                                                                 char* ws) {
  u32 tid = threadIdx.x;
  asm volatile(BODY
    : "+{v0}"(tid)
    : [ka] "s"(__builtin_amdgcn_kernarg_segment_ptr()), [bid] "s"(blockIdx.x)
    : "memory", "scc", "vcc", "v1", "v2", "v3", "v4", "v5", "v6", "v7", "v8", "v9", CL10(1), CL10(2), CL10(3), CL10(4), CL10(5), CL10(6),
      CL10(7), CL10(8), CL10(9), CL10(10), CL10(11), CL10(12), CL10(13), CL10(14), CL10(15), CL8(16), "s8", "s9", SL10(1), SL10(2), "s30",
      "s31", "s33", "s34", "s35", "s36", "s37", "s38", "s39", SL10(4), SL10(5), SL10(6), SL10(7), SL10(8), SL10(9));
}

extern "C" void kernel_launch(void* const* d_in, const int* in_sizes, int n_in,
                              void* d_out, int out_size, void* d_ws, size_t ws_size,
                              hipStream_t stream) {
  const u16* x = (const u16*)d_in[0];
  const u16* wqkv = (const u16*)d_in[1];
  const u16* wo = (const u16*)d_in[2];
  const u16* w_r = (const u16*)d_in[4];
  const u16* w_gate = (const u16*)d_in[5];
  const u16* w_up = (const u16*)d_in[6];
  const u16* w_down = (const u16*)d_in[7];
  (void)hipFuncSetAttribute((const void*)fused_block, hipFuncAttributeMaxDynamicSharedMemorySize, LDS_TOTAL);
  fused_block<<<256, 768, LDS_TOTAL, stream>>>(x, wqkv, wo, w_r, w_gate, w_up, w_down, (u16*)d_out, (char*)d_ws);
}
```

```cpp
#include <hip/hip_runtime.h>
#include <stdint.h>


typedef unsigned short u16;
typedef unsigned int u32;
#define LDS_TOTAL 163840
#define CL8(T) "v" #T "0", "v" #T "1", "v" #T "2", "v" #T "3", "v" #T "4", "v" #T "5", "v" #T "6", "v" #T "7"
#define CL10(T) CL8(T), "v" #T "8", "v" #T "9"
#define SL10(T) "s" #T "0", "s" #T "1", "s" #T "2", "s" #T "3", "s" #T "4", "s" #T "5", "s" #T "6", "s" #T "7", "s" #T "8", "s" #T "9"

#define BODY \
  ".Lst_%=:\n\t" "s_load_dwordx16 s[8:23], %[ka], 0x0\n\t" "s_load_dwordx2 s[24:25], %[ka], 0x40\n\t" "v_readfirstlane_b32 s26, v0\n\t" "s_lshr_b32 s76, %[bid], 3\n\t" "v_and_b32 v8, 63, v0\n\t" \
  "s_mul_i32 s77, s76, 205\n\t" "s_lshr_b32 s26, s26, 6\n\t" "s_lshr_b32 s77, s77, 10\n\t" "v_lshrrev_b32 v1, 3, v8\n\t" "s_mul_i32 s27, s77, 5\n\t" "s_and_b32 s33, s26, 3\n\t" \
  "s_sub_u32 s27, s76, s27\n\t" "s_and_b32 s78, %[bid], 1\n\t" "s_bfe_u32 s79, %[bid], 0x20001\n\t" "s_xor_b32 s79, s79, 3\n\t" "s_mul_i32 s28, s78, 5\n\t" "s_mul_i32 s29, s79, 6\n\t" \
  "s_add_u32 s28, s28, s27\n\t" "s_add_u32 s29, s29, s77\n\t" "s_cmp_ge_u32 s76, 30\n\t" "s_cselect_b32 s29, 24, s29\n\t" "s_cmp_ge_u32 s29, 23\n\t" "s_cbranch_scc1 .Lsp_%=\n\t" \
  "s_lshl_b32 s77, s77, 2\n\t" "s_lshl1_add_u32 s31, s27, s78\n\t" "s_lshr_b32 s77, 0x663300, s77\n\t" "s_add_u32 s31, s31, s79\n\t" "s_and_b32 s77, s77, 15\n\t" "s_mul_i32 s30, s29, 89\n\t" \
  "s_add_u32 s31, s31, s77\n\t" "s_mul_i32 s77, s31, 205\n\t" "s_lshr_b32 s77, s77, 11\n\t" "s_mul_i32 s77, s77, 10\n\t" "s_sub_u32 s31, s31, s77\n\t" "s_cmp_ge_u32 s26, 4\n\t" \
  "s_cbranch_scc1 .Lnl_%=\n\t" "s_setprio 2\n\t" "v_lshl_or_b32 v2, s33, 4, v1\n\t" "v_and_b32 v3, 2, v1\n\t" "v_mul_u32_u24 v2, 0xf00, v2\n\t" "v_xor_b32 v3, v3, v8\n\t" "s_lshl_b32 s76, s28, 7\n\t" \
  "v_and_b32 v3, 7, v3\n\t" "s_add_u32 s76, s76, 3072\n\t" "v_lshlrev_b32 v3, 4, v3\n\t" "s_mul_i32 s86, s33, 6144\n\t" "v_add3_u32 v40, v2, v3, s76\n\t" "s_add_u32 s86, s86, 3072\n\t" \
  "v_add_u32 v41, 256, v40\n\t" "v_add_u32 v42, 512, v40\n\t" "v_xor_b32 v43, 64, v40\n\t" "s_mov_b32 m0, s86\n\t" "v_add_u32 v43, 27648, v43\n\t" "s_waitcnt lgkmcnt(0)\n\t" \
  "s_mul_i32 s76, s31, 245760\n\t" "s_lshl_b32 s77, s31, 7\n\t" "s_add_u32 s78, s10, s76\n\t" "s_addc_u32 s79, s11, 0\n\t" "s_add_u32 s80, s8, s77\n\t" "s_addc_u32 s81, s9, 0\n\t" \
  "v_add_u32 v44, 256, v43\n\t" "v_add_u32 v45, 512, v43\n\t" "global_load_lds_dwordx4 v40, s[78:79] offset:-3072\n\t" "global_load_lds_dwordx4 v41, s[78:79] offset:-2048\n\t" \
  "global_load_lds_dwordx4 v42, s[78:79] offset:-1024\n\t" "global_load_lds_dwordx4 v43, s[78:79] offset:0\n\t" "global_load_lds_dwordx4 v44, s[78:79] offset:1024\n\t" \
  "global_load_lds_dwordx4 v45, s[78:79] offset:2048\n\t" "s_mul_i32 s76, s33, 24\n\t" "s_mul_i32 s87, s33, 3072\n\t" "v_add_u32 v2, s76, v1\n\t" "s_movk_i32 s77, 2048\n\t" \
  "v_lshrrev_b32 v3, 1, v2\n\t" "v_add_u32 v2, s30, v2\n\t" "v_xor_b32 v3, v3, v8\n\t" "s_add_u32 s87, s87, 26624\n\t" "v_and_b32 v3, 7, v3\n\t" "s_mov_b32 m0, s87\n\t" \
  "v_lshl_add_u32 v3, v3, 4, s77\n\t" "s_movk_i32 s77, 1280\n\t" "v_add_u32 v4, 0, v2\n\t" "v_sub_u32 v4, v4, 6 clamp\n\t" "v_lshrrev_b32 v5, 10, v4\n\t" "v_lshl_or_b32 v4, v4, 1, v5\n\t" \
  "v_and_b32 v4, 0x7ff, v4\n\t" "v_mad_u32_u24 v46, v4, s77, v3\n\t" "global_load_lds_dwordx4 v46, s[80:81] offset:-2048\n\t" "v_xor_b32 v3, 64, v3\n\t" "v_subrev_u32 v3, 1024, v3\n\t" \
  "v_add_u32 v4, 8, v2\n\t" "v_sub_u32 v4, v4, 6 clamp\n\t" "v_lshrrev_b32 v5, 10, v4\n\t" "v_lshl_or_b32 v4, v4, 1, v5\n\t" "v_and_b32 v4, 0x7ff, v4\n\t" "v_mad_u32_u24 v47, v4, s77, v3\n\t" \
  "global_load_lds_dwordx4 v47, s[80:81] offset:-1024\n\t" "v_xor_b32 v3, 64, v3\n\t" "v_subrev_u32 v3, 1024, v3\n\t" "v_add_u32 v4, 16, v2\n\t" "v_sub_u32 v4, v4, 6 clamp\n\t" \
  "v_lshrrev_b32 v5, 10, v4\n\t" "v_lshl_or_b32 v4, v4, 1, v5\n\t" "v_and_b32 v4, 0x7ff, v4\n\t" "v_mad_u32_u24 v48, v4, s77, v3\n\t" "global_load_lds_dwordx4 v48, s[80:81] offset:0\n\t" \
  ".Lnl_%=:\n\t" "s_cmp_lg_u32 s26, 11\n\t" "s_cbranch_scc1 .Lnc_%=\n\t" "s_getpc_b64 s[76:77]\n\t" ".Lpc_%=:\n\t" "s_and_b32 s78, %[bid], -8\n\t" "s_sub_u32 s76, s76, .Lpc_%=-.Lst_%=\n\t" \
  "s_subb_u32 s77, s77, 0\n\t" "v_add_u32 v1, s78, v8\n\t" "v_min_u32 v1, ((.Lend_%=-.Lst_%=)>>7)+0, v1\n\t" "s_mov_b32 m0, 163072\n\t" "v_lshlrev_b32 v1, 7, v1\n\t" "s_mov_b64 exec, 0xff\n\t" \
  "global_load_lds_dword v1, s[76:77]\n\t" "s_mov_b64 exec, -1\n\t" ".Lnc_%=:\n\t" "s_cmp_ge_u32 s26, 6\n\t" "s_cselect_b32 s34, 1, 0\n\t" "s_cselect_b32 s76, 6, 0\n\t" "v_and_b32 v9, 15, v8\n\t" \
  "s_sub_u32 s35, s26, s76\n\t" "v_lshrrev_b32 v10, 2, v8\n\t" "s_and_b32 s36, s26, 1\n\t" "s_lshr_b32 s37, s26, 1\n\t" "s_lshl4_add_u32 s76, s35, s30\n\t" "s_sub_u32 s76, s76, 6\n\t" \
  "v_lshlrev_b32 v32, 4, v8\n\t" "v_add_u32 v2, s76, v9\n\t" "s_mov_b32 s76, 0x1fe00\n\t" "v_and_b32 v2, 0x3ff, v2\n\t" "v_lshlrev_b32 v1, v9, s76\n\t" "v_min_u32 v2, 7, v2\n\t" \
  "v_lshlrev_b32 v35, 7, v10\n\t" "v_sub_u32 v2, v9, v2\n\t" "v_and_b32 v10, 12, v10\n\t" "v_add_u32 v2, 16, v2\n\t" "s_cmp_eq_u32 s35, 0\n\t" "s_cselect_b32 s76, 0xffff0000, -1\n\t" \
  "s_cselect_b32 s81, 0, 1024\n\t" "v_lshlrev_b32 v2, v2, -1\n\t" "v_and_b32 v1, s76, v1\n\t" "s_mov_b32 s77, 0xff80ff80\n\t" "s_cmp_eq_u32 s29, 0\n\t" "s_cselect_b32 s77, 0xffc0ffc0, s77\n\t" \
  "v_and_b32 v1, v1, v2\n\t" "s_cmp_eq_u32 s37, 0\n\t" "s_cselect_b32 s72, s77, -1\n\t" "v_not_b32 v1, v1\n\t" "s_mov_b32 s73, s72\n\t" "v_lshrrev_b32 v1, v10, v1\n\t" \
  "s_lshl4_add_u32 s76, s37, s30\n\t" "s_mov_b32 s77, 0xffff\n\t" "v_bfe_i32 v20, v1, 0, 1\n\t" "v_bfe_i32 v21, v1, 1, 1\n\t" "v_bfe_i32 v22, v1, 2, 1\n\t" "v_bfe_i32 v23, v1, 3, 1\n\t" \
  "v_add_u32 v2, s76, v9\n\t" "v_bfi_b32 v28, s77, v20, v21\n\t" "v_bfi_b32 v29, s77, v22, v23\n\t" "v_bfe_i32 v24, v1, 16, 1\n\t" "v_bfe_i32 v25, v1, 17, 1\n\t" "v_bfe_i32 v26, v1, 18, 1\n\t" \
  "v_bfe_i32 v27, v1, 19, 1\n\t" "v_sub_u32 v2, v2, 6 clamp\n\t" "v_lshrrev_b32 v3, 10, v2\n\t" "v_lshl_or_b32 v2, v2, 1, v3\n\t" "v_and_b32 v2, 0x7ff, v2\n\t" "s_lshl_b32 s76, s28, 7\n\t" \
  "s_lshl_b32 s77, s36, 6\n\t" "v_and_b32 v20, 0xff800000, v20\n\t" "v_and_b32 v21, 0xff800000, v21\n\t" "v_and_b32 v22, 0xff800000, v22\n\t" "v_and_b32 v23, 0xff800000, v23\n\t" \
  "v_and_b32 v24, 0xff800000, v24\n\t" "v_and_b32 v25, 0xff800000, v25\n\t" "v_and_b32 v26, 0xff800000, v26\n\t" "v_and_b32 v27, 0xff800000, v27\n\t" "s_add_u32 s76, s76, s77\n\t" \
  "v_lshlrev_b32 v1, 1, v8\n\t" "v_lshrrev_b32 v3, 1, v8\n\t" "v_and_b32 v1, 32, v1\n\t" "v_and_b32 v3, 16, v3\n\t" "s_movk_i32 s78, 1280\n\t" "v_or3_b32 v34, v1, v3, s76\n\t" \
  "v_lshlrev_b32 v5, 2, v8\n\t" "v_mad_u32_u24 v33, v2, s78, v34\n\t" "v_and_b32 v6, 0x60, v5\n\t" "s_mul_i32 s76, s36, 40960\n\t" "v_xor_b32 v1, s77, v6\n\t" "s_add_u32 s76, s76, 73728\n\t" \
  "v_and_b32 v5, 12, v5\n\t" "v_add3_u32 v35, v35, v1, s76\n\t" "v_mov_b32 v37, 156416\n\t" "v_lshl_add_u32 v35, v5, 1, v35\n\t" "v_mov_b32 v38, 1\n\t" "v_add_u32 v36, 1413120, v32\n\t" \
  "s_mul_i32 s78, s27, 12\n\t" "s_mov_b32 s76, 0x10321320\n\t" "s_mov_b32 s77, 0xbe0f102\n\t" "s_lshr_b64 s[76:77], s[76:77], s78\n\t" "s_and_b32 s74, s76, 0xfff\n\t" "s_mov_b32 s76, 0xedfe0f41\n\t" \
  "s_mov_b32 s77, 0xfdcedcf\n\t" "s_lshr_b64 s[76:77], s[76:77], s78\n\t" "s_pack_ll_b32_b16 s74, s74, s76\n\t" "s_mul_i32 s76, s27, 0x10001\n\t" "s_sub_u32 s76, 0xe0009, s76\n\t" \
  "s_lshl4_add_u32 s74, s74, s76\n\t" "s_mul_i32 s78, s34, 3072\n\t" "s_lshr_b32 s79, s74, 16\n\t" "v_bfe_u32 v1, s74, v10, 4\n\t" "s_lshl_b32 s76, s35, 9\n\t" "v_bfe_u32 v2, s79, v10, 4\n\t" \
  "v_and_b32 v4, 12, v8\n\t" "s_sub_u32 s76, s76, s78\n\t" "v_bfe_u32 v3, s74, v4, 4\n\t" "s_add_u32 s76, s76, 3072\n\t" "v_bfe_u32 v4, s79, v4, 4\n\t" "v_lshl_add_u32 v30, v8, 3, s76\n\t" \
  "s_sub_u32 s76, 5, s27\n\t" "v_lshl_add_u32 v31, s78, 1, v30\n\t" "s_sub_u32 s77, 15, s27\n\t" "v_cmp_gt_u32 s[40:41], s76, v1\n\t" "v_cmp_gt_u32 s[42:43], s76, v2\n\t" \
  "v_cmp_le_u32 s[44:45], s77, v1\n\t" "v_cmp_le_u32 s[46:47], s77, v2\n\t" "v_cmp_gt_u32 s[50:51], s76, v3\n\t" "v_cmp_gt_u32 s[52:53], s76, v4\n\t" "s_lshl_b32 s76, s27, 2\n\t" \
  "v_cmp_gt_u32 s[48:49], s76, v10\n\t" "s_mul_i32 s76, s29, 10\n\t" "s_add_u32 s76, s76, s28\n\t" "s_mul_i32 s76, s76, 6144\n\t" "s_add_u32 s76, s76, 585728\n\t" "s_waitcnt lgkmcnt(0)\n\t" \
  "s_add_u32 s66, s24, s76\n\t" "s_addc_u32 s67, s25, 0\n\t" "s_lshl_b32 s76, s35, 10\n\t" "s_add_u32 s76, s76, 4096\n\t" "s_add_u32 s56, s66, s76\n\t" "s_addc_u32 s57, s67, 0\n\t" \
  "s_sub_u32 s58, s56, s81\n\t" "s_subb_u32 s59, s57, 0\n\t" "s_mul_i32 s76, s29, 61440\n\t" "s_add_u32 s76, s76, 2004992\n\t" "s_mul_i32 s77, s26, 5120\n\t" "s_add_u32 s77, s77, s76\n\t" \
  "s_add_u32 s62, s24, s77\n\t" "s_addc_u32 s63, s25, 0\n\t" "s_add_u32 s64, s62, 1413120\n\t" "s_addc_u32 s65, s63, 0\n\t" "s_mul_i32 s77, s35, 10\n\t" "s_add_u32 s77, s77, s28\n\t" \
  "s_lshl_b32 s77, s77, 10\n\t" "s_add_u32 s77, s77, s76\n\t" "s_add_u32 s60, s24, s77\n\t" "s_addc_u32 s61, s25, 0\n\t" "s_mov_b64 s[76:77], 0xffff\n\t" "s_not_b64 s[54:55], s[76:77]\n\t" \
  "s_cmp_lg_u32 s34, 0\n\t" "s_cselect_b64 s[54:55], s[76:77], s[54:55]\n\t" "s_mov_b32 s39, 400000\n\t" "s_mov_b32 s75, 0\n\t" "s_cmp_lt_u32 s26, 4\n\t" "s_cbranch_scc1 .Ll_%=\n\t" \
  "s_mul_i32 s77, s27, 0x1111\n\t" "s_sub_u32 s80, 0x8765, s77\n\t" "s_sub_u32 s77, 0xdcba, s77\n\t" "s_cmp_eq_u32 s33, 1\n\t" "s_cselect_b32 s80, s74, s80\n\t" "s_cmp_eq_u32 s33, 2\n\t" \
  "s_cselect_b32 s80, s79, s80\n\t" "s_cmp_eq_u32 s33, 3\n\t" "s_cselect_b32 s80, s77, s80\n\t" "v_mul_u32_u24 v1, 768, v10\n\t" "v_bfe_u32 v5, s80, v5, 4\n\t" "v_lshlrev_b32 v2, 5, v9\n\t" \
  "v_lshlrev_b32 v5, 3, v5\n\t" "v_and_b32 v2, 0x180, v2\n\t" "v_xor_b32 v5, v5, v6\n\t" "s_cmp_ge_u32 s26, 8\n\t" "v_add3_u32 v1, v1, v5, v2\n\t" "s_cbranch_scc1 .Lg_%=\n\t" "v_mov_b32 v7, 0\n\t" \
  "s_setprio 3\n\t" "global_load_dwordx2 v[40:41], v7, s[24:25] sc1\n\t" "s_cmp_lg_u32 s33, 2\n\t" "s_cbranch_scc1 .Lre0_%=\n\t" "v_lshlrev_b32 v5, 2, v8\n\t" \
  "global_load_dwordx4 v[64:67], v32, s[14:15]\n\t" "global_load_dword v68, v5, s[14:15] offset:1024\n\t" ".Lre0_%=:\n\t" "v_lshl_add_u32 v3, v8, 2, v37\n\t" "s_mov_b32 exec_hi, 0\n\t" \
  "ds_write_b32 v3, v7\n\t" "s_mov_b32 exec_hi, -1\n\t" "s_lshl2_add_u32 s76, s29, s33\n\t" "s_mul_i32 s76, s76, 2560\n\t" "s_add_u32 s76, s76, 8192\n\t" "s_add_u32 s86, s24, s76\n\t" \
  "s_addc_u32 s87, s25, 0\n\t" "s_lshl_b32 s76, s28, 8\n\t" "s_add_u32 s88, s86, s76\n\t" "s_addc_u32 s89, s87, 0\n\t" "s_lshl2_add_u32 s76, s28, s33\n\t" "s_mul_i32 s76, s76, 5120\n\t" \
  "s_add_u32 s76, s76, 361472\n\t" "s_add_u32 s90, s24, s76\n\t" "s_addc_u32 s91, s25, 0\n\t" "s_lshl_b32 s76, s31, 8\n\t" "s_add_u32 s92, s90, s76\n\t" "s_addc_u32 s93, s91, 0\n\t" \
  "v_mov_b32 v116, -1\n\t" "v_mov_b32 v117, -1\n\t" "v_mov_b32 v118, -1\n\t" "v_mov_b32 v119, -1\n\t" "s_mul_i32 s76, s33, 1536\n\t" "s_add_u32 s76, s76, 4096\n\t" "s_add_u32 s94, s66, s76\n\t" \
  "s_addc_u32 s95, s67, 0\n\t" "s_lshr_b32 s76, s33, 1\n\t" "s_add_u32 s76, s76, 2\n\t" "s_sub_u32 s76, s76, s35\n\t" "s_mul_i32 s76, s76, 10240\n\t" "s_and_b32 s77, s33, 1\n\t" \
  "s_lshl_b32 s77, s77, 9\n\t" "s_add_u32 s76, s76, s77\n\t" "s_sub_u32 s76, s76, 2048\n\t" "s_ashr_i32 s77, s76, 31\n\t" "s_add_u32 s68, s60, s76\n\t" "s_addc_u32 s69, s61, s77\n\t" \
  "s_mul_i32 s76, s33, 3072\n\t" "v_add_u32 v2, s76, v32\n\t" "s_mov_b32 s77, 0x3f803f80\n\t" "v_mov_b32 v96, s77\n\t" "v_mov_b32 v97, s77\n\t" "v_mov_b32 v98, s77\n\t" "v_mov_b32 v99, s77\n\t" \
  "v_lshrrev_b32 v12, 2, v8\n\t" "v_and_or_b32 v12, v8, 1, v12\n\t" "v_lshlrev_b32 v12, 4, v12\n\t" "s_mov_b32 s98, 0x03030303\n\t" "s_mov_b32 s99, s98\n\t" "s_mov_b32 s70, 0xaaaaaaaa\n\t" \
  "s_mov_b32 s71, s70\n\t" "v_and_b32 v13, 16, v8\n\t" "v_lshrrev_b32 v7, 2, v8\n\t" "v_and_or_b32 v13, v7, 8, v13\n\t" "v_min_u32 v13, 16, v13\n\t" "v_lshrrev_b32 v7, 1, v9\n\t" \
  "v_add_u32 v13, v13, v7\n\t" "v_and_b32 v7, 1, v8\n\t" "v_mul_u32_u24 v7, 384, v7\n\t" "v_lshl_add_u32 v13, v13, 2, v7\n\t" "s_mul_i32 s76, s33, 96\n\t" "s_add_u32 s76, s76, 155648\n\t" \
  "v_add_u32 v13, s76, v13\n\t" "v_min_u32 v4, 22, v8\n\t" "s_movk_i32 s76, 4096\n\t" "v_lshl_add_u32 v4, v4, 7, s76\n\t" "v_and_b32 v5, 16, v8\n\t" "v_lshl_add_u32 v5, v5, 1, v9\n\t" \
  "v_lshrrev_b32 v7, 1, v8\n\t" "v_and_or_b32 v5, v7, 16, v5\n\t" "s_lshl_b32 s76, s33, 8\n\t" "s_add_u32 s76, s76, 156672\n\t" "v_lshl_add_u32 v5, v5, 2, s76\n\t" "s_mov_b32 s76, 0x3acccccd\n\t" \
  "s_bitcmp0_b32 0x555541, s29\n\t" "s_waitcnt lgkmcnt(0)\n\t" "s_barrier\n\t" "ds_read_b128 v[44:47], v2 offset:24576\n\t" "ds_read_b128 v[48:51], v2 offset:25600\n\t" \
  "ds_read_b128 v[52:55], v2 offset:26624\n\t" "s_cbranch_scc1 .Lc0_%=\n\t" "ds_read_b64_tr_b16 v[72:73], v1 offset:0\n\t" "ds_read_b64_tr_b16 v[74:75], v1 offset:512\n\t" \
  "ds_read_b64_tr_b16 v[76:77], v1 offset:1024\n\t" "ds_read_b64_tr_b16 v[78:79], v1 offset:1536\n\t" "ds_read_b64_tr_b16 v[80:81], v1 offset:2048\n\t" \
  "ds_read_b64_tr_b16 v[82:83], v1 offset:2560\n\t" "ds_read_b64_tr_b16 v[84:85], v1 offset:12288\n\t" "ds_read_b64_tr_b16 v[86:87], v1 offset:12800\n\t" \
  "ds_read_b64_tr_b16 v[88:89], v1 offset:13312\n\t" "ds_read_b64_tr_b16 v[90:91], v1 offset:13824\n\t" "ds_read_b64_tr_b16 v[92:93], v1 offset:14336\n\t" \
  "ds_read_b64_tr_b16 v[94:95], v1 offset:14848\n\t" ".Lc0_%=:\n\t" "s_waitcnt lgkmcnt(0)\n\t" "s_cbranch_scc1 .Lc1_%=\n\t" "v_mfma_f32_16x16x32_bf16 v[100:103], v[96:99], v[72:75], 0\n\t" \
  "v_mfma_f32_16x16x32_bf16 v[104:107], v[96:99], v[76:79], 0\n\t" "v_mfma_f32_16x16x32_bf16 v[108:111], v[96:99], v[80:83], 0\n\t" \
  "v_mfma_f32_16x16x32_bf16 v[100:103], v[96:99], v[84:87], v[100:103]\n\t" "v_mfma_f32_16x16x32_bf16 v[104:107], v[96:99], v[88:91], v[104:107]\n\t" \
  "v_mfma_f32_16x16x32_bf16 v[108:111], v[96:99], v[92:95], v[108:111]\n\t" ".Lc1_%=:\n\t" "s_barrier\n\t" "global_store_dwordx4 v32, v[116:119], s[94:95] sc1\n\t" "s_mov_b32 exec_hi, 0\n\t" \
  "global_store_dwordx4 v32, v[116:119], s[94:95] offset:1024 sc1\n\t" "s_mov_b32 exec_hi, -1\n\t" "global_store_dwordx4 v32, v[116:119], s[60:61] offset:-2048 sc1\n\t" "s_mov_b32 exec_hi, 0\n\t" \
  "global_store_dwordx4 v32, v[116:119], s[68:69] sc1\n\t" "s_mov_b32 exec_hi, -1\n\t" "v_dot2_f32_bf16 v60, v44, s77, 0\n\t" "v_dot2_f32_bf16 v120, v44, v44, 0\n\t" \
  "v_dot2_f32_bf16 v61, v48, s77, 0\n\t" "v_dot2_f32_bf16 v121, v48, v48, 0\n\t" "v_dot2_f32_bf16 v62, v52, s77, 0\n\t" "v_dot2_f32_bf16 v122, v52, v52, 0\n\t" "v_dot2_f32_bf16 v60, v45, s77, v60\n\t" \
  "v_dot2_f32_bf16 v120, v45, v45, v120\n\t" "v_dot2_f32_bf16 v61, v49, s77, v61\n\t" "v_dot2_f32_bf16 v121, v49, v49, v121\n\t" "v_dot2_f32_bf16 v62, v53, s77, v62\n\t" \
  "v_dot2_f32_bf16 v122, v53, v53, v122\n\t" "s_barrier\n\t" "v_dot2_f32_bf16 v60, v46, s77, v60\n\t" "v_dot2_f32_bf16 v120, v46, v46, v120\n\t" "v_dot2_f32_bf16 v61, v50, s77, v61\n\t" \
  "v_dot2_f32_bf16 v121, v50, v50, v121\n\t" "v_dot2_f32_bf16 v62, v54, s77, v62\n\t" "v_dot2_f32_bf16 v122, v54, v54, v122\n\t" "v_dot2_f32_bf16 v60, v47, s77, v60\n\t" \
  "v_dot2_f32_bf16 v120, v47, v47, v120\n\t" "v_dot2_f32_bf16 v61, v51, s77, v61\n\t" "v_dot2_f32_bf16 v121, v51, v51, v121\n\t" "v_dot2_f32_bf16 v62, v55, s77, v62\n\t" \
  "v_dot2_f32_bf16 v122, v55, v55, v122\n\t" "s_barrier\n\t" "s_nop 1\n\t" "v_add_f32_dpp v60, v60, v60 quad_perm:[1,0,3,2] row_mask:0xf bank_mask:0xf\n\t" \
  "v_add_f32_dpp v120, v120, v120 quad_perm:[1,0,3,2] row_mask:0xf bank_mask:0xf\n\t" "v_add_f32_dpp v61, v61, v61 quad_perm:[1,0,3,2] row_mask:0xf bank_mask:0xf\n\t" \
  "v_add_f32_dpp v121, v121, v121 quad_perm:[1,0,3,2] row_mask:0xf bank_mask:0xf\n\t" "v_add_f32_dpp v62, v62, v62 quad_perm:[1,0,3,2] row_mask:0xf bank_mask:0xf\n\t" \
  "v_add_f32_dpp v122, v122, v122 quad_perm:[1,0,3,2] row_mask:0xf bank_mask:0xf\n\t" "v_add_f32_dpp v60, v60, v60 quad_perm:[2,3,0,1] row_mask:0xf bank_mask:0xf\n\t" \
  "v_add_f32_dpp v120, v120, v120 quad_perm:[2,3,0,1] row_mask:0xf bank_mask:0xf\n\t" "v_add_f32_dpp v61, v61, v61 quad_perm:[2,3,0,1] row_mask:0xf bank_mask:0xf\n\t" \
  "v_add_f32_dpp v121, v121, v121 quad_perm:[2,3,0,1] row_mask:0xf bank_mask:0xf\n\t" "v_add_f32_dpp v62, v62, v62 quad_perm:[2,3,0,1] row_mask:0xf bank_mask:0xf\n\t" \
  "v_add_f32_dpp v122, v122, v122 quad_perm:[2,3,0,1] row_mask:0xf bank_mask:0xf\n\t" "v_add_f32_dpp v60, v60, v60 row_half_mirror row_mask:0xf bank_mask:0xf\n\t" \
  "v_add_f32_dpp v120, v120, v120 row_half_mirror row_mask:0xf bank_mask:0xf\n\t" "v_add_f32_dpp v61, v61, v61 row_half_mirror row_mask:0xf bank_mask:0xf\n\t" \
  "v_add_f32_dpp v121, v121, v121 row_half_mirror row_mask:0xf bank_mask:0xf\n\t" "v_add_f32_dpp v62, v62, v62 row_half_mirror row_mask:0xf bank_mask:0xf\n\t" \
  "v_add_f32_dpp v122, v122, v122 row_half_mirror row_mask:0xf bank_mask:0xf\n\t" "s_barrier\n\t" "v_cndmask_b32 v60, v60, v120, s[70:71]\n\t" "v_cndmask_b32 v61, v61, v121, s[70:71]\n\t" \
  "v_cndmask_b32 v62, v62, v122, s[70:71]\n\t" "v_mov_b32 v101, v104\n\t" "v_mov_b32 v102, v108\n\t" "s_waitcnt vmcnt(0)\n\t" "v_add_co_u32 v58, vcc, 1, v40\n\t" "v_add_u32 v63, 1, v40\n\t" \
  "v_add_u32 v103, 1, v40\n\t" "v_addc_co_u32 v59, vcc, 0, v41, vcc\n\t" "s_mov_b64 exec, s[98:99]\n\t" "global_store_dwordx4 v12, v[60:63], s[88:89] sc1\n\t" "s_mov_b64 exec, -1\n\t" \
  "s_cbranch_scc1 .Lc2_%=\n\t" "s_mov_b64 exec, 0xffff\n\t" "global_store_dwordx4 v32, v[100:103], s[92:93] sc1\n\t" "s_mov_b64 exec, -1\n\t" ".Lc2_%=:\n\t" "v_mov_b64 v[84:85], 0\n\t" \
  "v_mov_b32 v86, 0\n\t" "v_mov_b32 v87, v58\n\t" "v_mov_b64 v[104:105], 0\n\t" "v_mov_b32 v106, 0\n\t" "v_mov_b32 v107, v58\n\t" "s_barrier\n\t" "s_cmp_lg_u32 s33, 1\n\t" "s_cbranch_scc1 .Ltp_%=\n\t" \
  "s_memrealtime s[96:97]\n\t" "s_mul_i32 s98, s29, 1280\n\t" "s_add_u32 s98, s98, 5368320\n\t" "v_min_u32 v4, 9, v9\n\t" "v_lshl_add_u32 v4, v4, 7, s98\n\t" "s_lshl_b32 s99, s28, 7\n\t" \
  "s_add_u32 s98, s98, s99\n\t" "v_lshl_add_u32 v7, v9, 3, s98\n\t" "v_mov_b32 v57, v58\n\t" "s_waitcnt lgkmcnt(0)\n\t" "v_mov_b32 v56, s96\n\t" "s_mov_b64 exec, 0xffff\n\t" \
  "global_store_dwordx2 v7, v[56:57], s[24:25] sc1\n\t" "s_mov_b64 exec, -1\n\t" ".Ltp_%=:\n\t" "s_barrier\n\t" "s_barrier\n\t" "global_load_dwordx4 v[72:75], v32, s[86:87] offset:0 sc1\n\t" \
  "global_load_dwordx4 v[76:79], v32, s[86:87] offset:1024 sc1\n\t" "global_load_dwordx4 v[92:95], v32, s[90:91] offset:0 sc1\n\t" "global_load_dwordx4 v[96:99], v32, s[90:91] offset:1024 sc1\n\t" \
  "s_mov_b32 exec_hi, 0\n\t" "global_load_dwordx4 v[84:87], v32, s[86:87] offset:2048 sc1\n\t" "global_load_dwordx4 v[104:107], v32, s[90:91] offset:2048 sc1\n\t" "s_mov_b32 exec_hi, -1\n\t" \
  "s_cmp_lg_u32 s33, 1\n\t" "s_cbranch_scc1 .Lfl0_%=\n\t" "global_load_dwordx2 v[112:113], v4, s[24:25] sc1\n\t" ".Lfl0_%=:\n\t" "s_barrier\n\t" "s_mov_b32 s98, 0\n\t" ".Lrg_%=:\n\t" \
  "s_waitcnt vmcnt(0)\n\t" "v_pk_add_f32 v[120:121], v[72:73], v[76:77]\n\t" "v_pk_add_f32 v[126:127], v[92:93], v[96:97]\n\t" "v_add_f32 v122, v74, v78\n\t" "v_add_f32 v128, v94, v98\n\t" \
  "v_pk_add_f32 v[120:121], v[120:121], v[84:85]\n\t" "v_pk_add_f32 v[126:127], v[126:127], v[104:105]\n\t" "v_add_f32 v123, v122, v86\n\t" "v_add_f32 v129, v128, v106\n\t" \
  "v_add_f32 v122, v122, v86\n\t" "v_add_f32 v128, v128, v106\n\t" "v_permlane32_swap_b32 v120, v121\n\t" "v_permlane32_swap_b32 v126, v127\n\t" "v_permlane32_swap_b32 v122, v123\n\t" \
  "v_permlane32_swap_b32 v128, v129\n\t" "v_add_f32 v120, v120, v121\n\t" "v_add_f32 v126, v126, v127\n\t" "v_add_f32 v122, v122, v123\n\t" "v_add_f32 v128, v128, v129\n\t" "s_nop 0\n\t" \
  "v_permlane16_swap_b32 v120, v122\n\t" "v_permlane16_swap_b32 v126, v128\n\t" "v_add_f32 v120, v120, v122\n\t" "v_add_f32 v126, v126, v128\n\t" "v_mul_f32 v120, s76, v120\n\t" \
  "v_cmp_ne_u32 vcc, v75, v58\n\t" "s_cmp_lg_u32 s98, 0\n\t" "s_cbranch_scc1 .Lb8_%=\n\t" "s_barrier\n\t" ".Lb8_%=:\n\t" "s_mov_b32 s98, 1\n\t" "s_cmp_lg_u32 s33, 3\n\t" "s_cbranch_scc1 .Lfl8_%=\n\t" \
  "global_load_dwordx2 v[112:113], v4, s[24:25] sc1\n\t" ".Lfl8_%=:\n\t" "v_mov_b32_dpp v122, v120 quad_perm:[0,0,2,2] row_mask:0xf bank_mask:0xf\n\t" "v_cmp_ne_u32 s[80:81], v79, v58\n\t" \
  "s_or_b64 vcc, vcc, s[80:81]\n\t" "v_fma_f32 v121, -v122, v122, v120\n\t" "v_cmp_ne_u32 s[78:79], v87, v58\n\t" "s_or_b64 vcc, vcc, s[78:79]\n\t" "v_max_f32 v121, 0, v121\n\t" \
  "v_cmp_ne_u32 s[80:81], v95, v58\n\t" "s_or_b64 vcc, vcc, s[80:81]\n\t" "v_cvt_pk_bf16_f32 v120, v121, v120\n\t" "v_cmp_ne_u32 s[78:79], v99, v58\n\t" "s_or_b64 vcc, vcc, s[78:79]\n\t" \
  "v_lshlrev_b32 v121, 16, v120\n\t" "v_cmp_ne_u32 s[80:81], v107, v58\n\t" "s_or_b64 vcc, vcc, s[80:81]\n\t" "v_add_f32 v121, 0x3727c5ac, v121\n\t" "v_cvt_pk_bf16_f32 v121, v121, v121\n\t" \
  "v_lshlrev_b32 v121, 16, v121\n\t" "v_sqrt_f32 v121, v121\n\t" "v_and_b32 v120, 0xffff0000, v120\n\t" "v_cvt_pk_bf16_f32 v121, v121, v121\n\t" "v_lshlrev_b32 v121, 16, v121\n\t" \
  "s_cbranch_vccnz .Lsg_%=\n\t" ".Lrh_%=:\n\t" "s_mov_b64 exec, s[70:71]\n\t" "v_rcp_f32 v120, v121\n\t" "s_mov_b64 exec, -1\n\t" "ds_write_b32 v5, v126\n\t" "ds_write_b32 v13, v120\n\t" \
  "s_mov_b64 exec, 1\n\t" "ds_add_u32 v37, v38 offset:12\n\t" "s_mov_b64 exec, -1\n\t" "s_setprio 0\n\t" "s_cmp_lg_u32 s33, 3\n\t" "s_cbranch_scc1 .Lnfl_%=\n\t" "s_waitcnt vmcnt(0)\n\t" \
  "v_cmp_ne_u64 vcc, v[112:113], v[58:59]\n\t" "s_cbranch_vccz .Lsfm_%=\n\t" ".Lsfl_%=:\n\t" "global_load_dwordx2 v[112:113], v4, s[24:25] sc1\n\t" "s_sub_u32 s39, s39, 1\n\t" \
  "s_cmp_lt_i32 s39, 0\n\t" "s_waitcnt vmcnt(0)\n\t" "s_cbranch_scc1 .Lsfm_%=\n\t" "v_cmp_ne_u64 vcc, v[112:113], v[58:59]\n\t" "s_cbranch_vccnz .Lsfl_%=\n\t" ".Lsfm_%=:\n\t" "s_mov_b64 exec, 1\n\t" \
  "ds_add_u32 v37, v38 offset:4\n\t" "s_mov_b64 exec, -1\n\t" ".Lnfl_%=:\n\t" "s_cmp_lg_u32 s33, 1\n\t" "s_cbranch_scc1 .Lnlg_%=\n\t" "v_subrev_u32 v112, s96, v112\n\t" \
  "v_cmp_eq_u32 vcc, v113, v58\n\t" "s_lshl_b32 s98, 7, s27\n\t" "s_lshr_b32 s98, s98, 1\n\t" "s_and_b32 s98, s98, 31\n\t" "v_cndmask_b32 v112, 60, v112, vcc\n\t" "s_sub_u32 s99, s28, s27\n\t" \
  "s_lshl_b32 s98, s98, s99\n\t" "s_or_b32 s98, s98, 0xffff0000\n\t" "s_mov_b32 s99, 0\n\t" "v_med3_i32 v112, v112, 0, 64\n\t" "v_cndmask_b32 v112, 0, v112, s[98:99]\n\t" "s_nop 1\n\t" \
  "v_max_u32_dpp v112, v112, v112 quad_perm:[1,0,3,2] row_mask:0xf bank_mask:0xf\n\t" "s_nop 1\n\t" "v_max_u32_dpp v112, v112, v112 quad_perm:[2,3,0,1] row_mask:0xf bank_mask:0xf\n\t" "s_nop 1\n\t" \
  "v_max_u32_dpp v112, v112, v112 row_half_mirror row_mask:0xf bank_mask:0xf\n\t" "s_nop 1\n\t" "v_max_u32_dpp v112, v112, v112 row_mirror row_mask:0xf bank_mask:0xf\n\t" "v_add_u32 v7, v10, v37\n\t" \
  "ds_write_b32 v7, v112 offset:80\n\t" ".Lnlg_%=:\n\t" "s_cmp_lg_u32 s33, 2\n\t" "s_cbranch_scc1 .Latt_%=\n\t" "v_dot2_f32_bf16 v1, v68, s77, 0\n\t" "v_dot2_f32_bf16 v1, v64, s77, v1\n\t" \
  "v_dot2_f32_bf16 v1, v65, s77, v1\n\t" "v_dot2_f32_bf16 v1, v66, s77, v1\n\t" "v_dot2_f32_bf16 v1, v67, s77, v1\n\t" "s_nop 2\n\t" \
  "v_add_f32_dpp v1, v1, v1 quad_perm:[1,0,3,2] row_mask:0xf bank_mask:0xf\n\t" "s_nop 1\n\t" "v_add_f32_dpp v1, v1, v1 quad_perm:[2,3,0,1] row_mask:0xf bank_mask:0xf\n\t" "s_nop 1\n\t" \
  "v_add_f32_dpp v1, v1, v1 row_half_mirror row_mask:0xf bank_mask:0xf\n\t" "s_nop 1\n\t" "v_add_f32_dpp v1, v1, v1 row_mirror row_mask:0xf bank_mask:0xf\n\t" "v_mov_b32 v2, v1\n\t" "s_nop 1\n\t" \
  "v_permlane32_swap_b32 v1, v2\n\t" "v_add_f32 v1, v1, v2\n\t" "v_mov_b32 v2, v1\n\t" "s_nop 1\n\t" "v_permlane16_swap_b32 v1, v2\n\t" "v_add_f32 v1, v1, v2\n\t" "ds_write_b32 v37, v1 offset:40\n\t" \
  "s_branch .Latt_%=\n\t" ".Ll_%=:\n\t" "s_movk_i32 s38, 1\n\t" "s_add_u32 s31, s31, 1\n\t" "s_cmp_eq_u32 s31, 10\n\t" "s_cselect_b32 s31, 0, s31\n\t" "s_mul_i32 s76, s31, 245760\n\t" \
  "s_lshl_b32 s77, s31, 7\n\t" "s_add_u32 s78, s10, s76\n\t" "s_addc_u32 s79, s11, 0\n\t" "s_add_u32 s80, s8, s77\n\t" "s_addc_u32 s81, s9, 0\n\t" "s_and_b32 s76, s38, 3\n\t" \
  "s_mul_i32 s76, s76, 36864\n\t" "s_add_u32 m0, s76, s87\n\t" "s_branch .Ll1_%=\n\t" ".Ll0_%=:\n\t" "s_add_u32 s31, s31, 1\n\t" "s_cmp_eq_u32 s31, 10\n\t" "s_cselect_b32 s31, 0, s31\n\t" \
  "s_mul_i32 s76, s31, 245760\n\t" "s_lshl_b32 s77, s31, 7\n\t" "s_add_u32 s78, s10, s76\n\t" "s_addc_u32 s79, s11, 0\n\t" "s_add_u32 s80, s8, s77\n\t" "s_addc_u32 s81, s9, 0\n\t" \
  "s_and_b32 s76, s38, 3\n\t" "s_mul_i32 s76, s76, 36864\n\t" "s_add_u32 m0, s76, s87\n\t" "s_waitcnt vmcnt(9)\n\t" "s_barrier\n\t" ".Ll1_%=:\n\t" \
  "global_load_lds_dwordx4 v46, s[80:81] offset:-2048\n\t" "global_load_lds_dwordx4 v47, s[80:81] offset:-1024\n\t" "global_load_lds_dwordx4 v48, s[80:81] offset:0\n\t" "s_add_u32 m0, s76, s86\n\t" \
  "s_add_u32 s38, s38, 1\n\t" "global_load_lds_dwordx4 v40, s[78:79] offset:-3072\n\t" "global_load_lds_dwordx4 v41, s[78:79] offset:-2048\n\t" "global_load_lds_dwordx4 v42, s[78:79] offset:-1024\n\t" \
  "global_load_lds_dwordx4 v43, s[78:79] offset:0\n\t" "global_load_lds_dwordx4 v44, s[78:79] offset:1024\n\t" "global_load_lds_dwordx4 v45, s[78:79] offset:2048\n\t" "s_cmp_lt_u32 s38, 9\n\t" \
  "s_cbranch_scc1 .Ll0_%=\n\t" "s_add_u32 s31, s31, 1\n\t" "s_cmp_eq_u32 s31, 10\n\t" "s_cselect_b32 s31, 0, s31\n\t" "s_mul_i32 s76, s31, 245760\n\t" "s_lshl_b32 s77, s31, 7\n\t" \
  "s_add_u32 s78, s10, s76\n\t" "s_addc_u32 s79, s11, 0\n\t" "s_add_u32 s80, s8, s77\n\t" "s_addc_u32 s81, s9, 0\n\t" "s_and_b32 s76, s38, 3\n\t" "s_mul_i32 s76, s76, 36864\n\t" \
  "s_add_u32 m0, s76, s87\n\t" "v_lshrrev_b32 v1, 3, v8\n\t" "v_lshrrev_b32 v4, 5, v8\n\t" "v_and_b32 v2, 6, v1\n\t" "v_and_b32 v1, 3, v1\n\t" "v_xor_b32 v2, v2, v8\n\t" \
  "v_mul_u32_u24 v1, 1280, v1\n\t" "v_and_b32 v2, 7, v2\n\t" "s_lshl_b32 s92, s28, 7\n\t" "v_lshl_add_u32 v1, v2, 4, v1\n\t" "s_and_b32 s93, s33, 1\n\t" "s_and_b32 s94, s33, 2\n\t" \
  "v_add_u32 v1, s92, v1\n\t" "s_mul_i32 s92, s93, 5\n\t" "s_add_u32 s92, s92, s94\n\t" "s_add_u32 s92, s92, 5\n\t" "v_add_u32 v2, s92, v4\n\t" "s_lshl2_add_u32 s92, s93, s94\n\t" \
  "s_movk_i32 s93, 5120\n\t" "v_add_lshl_u32 v50, v4, s92, 2\n\t" "v_mad_u32_u24 v49, v2, s93, v1\n\t" "s_lshl_b32 s92, s92, 9\n\t" "s_add_u32 s92, s92, 73728\n\t" "s_waitcnt vmcnt(9)\n\t" \
  "s_barrier\n\t" "global_load_lds_dwordx4 v46, s[80:81] offset:-2048\n\t" "global_load_lds_dwordx4 v47, s[80:81] offset:-1024\n\t" "global_load_lds_dwordx4 v48, s[80:81] offset:0\n\t" \
  "s_add_u32 m0, s76, s86\n\t" "s_nop 0\n\t" "global_load_lds_dwordx4 v40, s[78:79] offset:-3072\n\t" "global_load_lds_dwordx4 v41, s[78:79] offset:-2048\n\t" \
  "global_load_lds_dwordx4 v42, s[78:79] offset:-1024\n\t" "global_load_lds_dwordx4 v43, s[78:79] offset:0\n\t" "global_load_lds_dwordx4 v44, s[78:79] offset:1024\n\t" \
  "global_load_lds_dwordx4 v45, s[78:79] offset:2048\n\t" "s_waitcnt vmcnt(9)\n\t" "s_barrier\n\t" "s_sleep 2\n\t" "s_mov_b64 s[78:79], s[12:13]\n\t" "s_add_u32 m0, s92, 0\n\t" "s_nop 0\n\t" \
  "global_load_lds_dwordx4 v49, s[78:79]\n\t" "s_add_u32 s78, s78, 76800\n\t" "s_addc_u32 s79, s79, 0\n\t" "s_add_u32 m0, s92, 4096\n\t" "s_nop 0\n\t" "global_load_lds_dwordx4 v49, s[78:79]\n\t" \
  "s_add_u32 s78, s78, 76800\n\t" "s_addc_u32 s79, s79, 0\n\t" "s_add_u32 m0, s92, 8192\n\t" "s_nop 0\n\t" "global_load_lds_dwordx4 v49, s[78:79]\n\t" "s_add_u32 s78, s78, 76800\n\t" \
  "s_addc_u32 s79, s79, 0\n\t" "s_add_u32 m0, s92, 12288\n\t" "s_nop 0\n\t" "global_load_lds_dwordx4 v49, s[78:79]\n\t" "s_add_u32 s78, s78, 76800\n\t" "s_addc_u32 s79, s79, 0\n\t" \
  "s_add_u32 m0, s92, 16384\n\t" "s_waitcnt vmcnt(4)\n\t" "s_barrier\n\t" "global_load_lds_dwordx4 v49, s[78:79]\n\t" "s_add_u32 s78, s78, 102400\n\t" "s_addc_u32 s79, s79, 0\n\t" \
  "s_add_u32 m0, s92, 40960\n\t" "s_nop 0\n\t" "global_load_lds_dwordx4 v49, s[78:79]\n\t" "s_add_u32 s78, s78, 76800\n\t" "s_addc_u32 s79, s79, 0\n\t" "s_add_u32 m0, s92, 45056\n\t" "s_nop 0\n\t" \
  "global_load_lds_dwordx4 v49, s[78:79]\n\t" "s_add_u32 s78, s78, 76800\n\t" "s_addc_u32 s79, s79, 0\n\t" "s_add_u32 m0, s92, 49152\n\t" "s_nop 0\n\t" "global_load_lds_dwordx4 v49, s[78:79]\n\t" \
  "s_add_u32 s78, s78, 76800\n\t" "s_addc_u32 s79, s79, 0\n\t" "s_add_u32 m0, s92, 53248\n\t" "s_nop 0\n\t" "global_load_lds_dwordx4 v49, s[78:79]\n\t" "s_add_u32 s78, s78, 76800\n\t" \
  "s_addc_u32 s79, s79, 0\n\t" "s_add_u32 m0, s92, 57344\n\t" "s_nop 0\n\t" "global_load_lds_dwordx4 v49, s[78:79]\n\t" "s_setprio 0\n\t" "s_mov_b32 s94, 0xf41e3209\n\t" \
  "v_bfe_u32 v2, s94, v50, 4\n\t" "v_mad_u32_u24 v3, v2, s93, v1\n\t" "s_mov_b32 s94, 0xfe0d3218\n\t" "v_bfe_u32 v2, s94, v50, 4\n\t" "v_mad_u32_u24 v4, v2, s93, v1\n\t" \
  "s_mov_b32 s94, 0xfedc2107\n\t" "v_bfe_u32 v2, s94, v50, 4\n\t" "v_mad_u32_u24 v5, v2, s93, v1\n\t" "s_mov_b32 s94, 0xedcbf106\n\t" "v_bfe_u32 v2, s94, v50, 4\n\t" \
  "v_mad_u32_u24 v6, v2, s93, v1\n\t" "s_mov_b32 s94, 0xfdcabe05\n\t" "v_bfe_u32 v2, s94, v50, 4\n\t" "v_mad_u32_u24 v7, v2, s93, v1\n\t" "s_waitcnt vmcnt(0)\n\t" "s_mov_b64 exec, 1\n\t" \
  "ds_add_u32 v37, v38 offset:4\n\t" "s_mov_b64 exec, -1\n\t" "s_branch .Latt_%=\n\t" ".Lsg_%=:\n\t" "s_sub_u32 s39, s39, 1\n\t" "s_cmp_lt_i32 s39, 0\n\t" "s_cbranch_scc1 .Lrh_%=\n\t" \
  "global_load_dwordx4 v[72:75], v32, s[86:87] offset:0 sc1\n\t" "global_load_dwordx4 v[76:79], v32, s[86:87] offset:1024 sc1\n\t" "global_load_dwordx4 v[92:95], v32, s[90:91] offset:0 sc1\n\t" \
  "global_load_dwordx4 v[96:99], v32, s[90:91] offset:1024 sc1\n\t" "s_mov_b32 exec_hi, 0\n\t" "global_load_dwordx4 v[84:87], v32, s[86:87] offset:2048 sc1\n\t" \
  "global_load_dwordx4 v[104:107], v32, s[90:91] offset:2048 sc1\n\t" "s_mov_b32 exec_hi, -1\n\t" "s_branch .Lrg_%=\n\t" ".Lts_%=:\n\t" "ds_read_b32 v39, v37 offset:12\n\t" \
  "ds_read_b128 v[160:163], v12 offset:1024\n\t" "ds_read_b128 v[164:167], v12 offset:1088\n\t" "ds_read2_b32 v[136:137], v4 offset0:0 offset1:96\n\t" \
  "ds_read2_b32 v[138:139], v4 offset0:16 offset1:112\n\t" "ds_read2_b32 v[140:141], v4 offset0:32 offset1:128\n\t" "ds_read2_b32 v[142:143], v4 offset0:48 offset1:144\n\t" \
  "ds_read2_b32 v[144:145], v4 offset0:64 offset1:160\n\t" "ds_read2_b32 v[146:147], v4 offset0:80 offset1:176\n\t" "ds_read_b32 v14, v13 offset:1152\n\t" "s_sub_u32 s39, s39, 1\n\t" \
  "s_cmp_lt_i32 s39, 0\n\t" "s_waitcnt lgkmcnt(0)\n\t" "v_readfirstlane_b32 s38, v39\n\t" "s_cbranch_scc1 .Ltr_%=\n\t" "s_cmp_ge_u32 s38, 4\n\t" "s_cbranch_scc0 .Lts_%=\n\t" "s_branch .Ltr_%=\n\t" \
  ".Lsq_%=:\n\t" "ds_read_b32 v39, v37 offset:0\n\t" "s_sub_u32 s39, s39, 1\n\t" "s_cmp_lt_i32 s39, 0\n\t" "s_waitcnt lgkmcnt(0)\n\t" "v_readfirstlane_b32 s38, v39\n\t" "s_cbranch_scc1 .Lrq_%=\n\t" \
  "s_cmp_ge_u32 s38, 4\n\t" "s_cbranch_scc0 .Lsq_%=\n\t" "s_branch .Lq_%=\n\t" ".Lsqm_%=:\n\t" "ds_read_b32 v39, v37 offset:8\n\t" "s_sub_u32 s39, s39, 1\n\t" "s_cmp_lt_i32 s39, 0\n\t" \
  "s_waitcnt lgkmcnt(0)\n\t" "v_readfirstlane_b32 s38, v39\n\t" "s_cbranch_scc1 .Lrqm_%=\n\t" "s_cmp_ge_u32 s38, 4\n\t" "s_cbranch_scc0 .Lsqm_%=\n\t" "s_branch .Lrqm_%=\n\t" ".Lsq0_%=:\n\t" \
  "ds_read_b32 v39, v37 offset:0\n\t" "s_sub_u32 s39, s39, 1\n\t" "s_cmp_lt_i32 s39, 0\n\t" "s_waitcnt lgkmcnt(0)\n\t" "v_readfirstlane_b32 s38, v39\n\t" "s_cbranch_scc1 .Lrq0_%=\n\t" \
  "s_cmp_ge_u32 s38, 4\n\t" "s_cbranch_scc0 .Lsq0_%=\n\t" "s_branch .Lq0_%=\n\t" ".Lsv0_%=:\n\t" "ds_read_b32 v39, v37 offset:4\n\t" "s_sub_u32 s39, s39, 1\n\t" "s_cmp_lt_i32 s39, 0\n\t" \
  "s_waitcnt lgkmcnt(0)\n\t" "v_readfirstlane_b32 s38, v39\n\t" "s_cbranch_scc1 .Lrv0_%=\n\t" "s_cmp_ge_u32 s38, 9\n\t" "s_cbranch_scc0 .Lsv0_%=\n\t" "s_branch .Lv0_%=\n\t" ".Lsr_%=:\n\t" \
  "global_load_dwordx4 v[148:151], v32, s[58:59] sc1\n\t" "global_load_dwordx4 v[144:147], v32, s[56:57] sc1\n\t" "s_sub_u32 s39, s39, 1\n\t" "s_cmp_lt_i32 s39, 0\n\t" "s_waitcnt vmcnt(0)\n\t" \
  "s_cbranch_scc1 .Lrr_%=\n\t" "v_max_u32 v124, v144, v148\n\t" "v_cmp_eq_u32 vcc, -1, v124\n\t" "s_cbranch_vccnz .Lsr_%=\n\t" "s_branch .Lrr_%=\n\t" ".Lk0_%=:\n\t" "v_xor_b32 v0, 32, v35\n\t" \
  "v_xor_b32 v1, 64, v35\n\t" "v_xor_b32 v2, 96, v35\n\t" "ds_read_b32 v39, v37 offset:8\n\t" "s_waitcnt lgkmcnt(0)\n\t" "v_readfirstlane_b32 s38, v39\n\t" "s_cmp_ge_u32 s38, 4\n\t" \
  "s_cbranch_scc0 .Lsqm_%=\n\t" ".Lrqm_%=:\n\t" "ds_read_b32 v39, v37 offset:80\n\t" "s_memrealtime s[76:77]\n\t" "s_mul_i32 s80, s35, 5\n\t" "s_add_u32 s80, s80, 24\n\t" "s_waitcnt lgkmcnt(0)\n\t" \
  "v_readfirstlane_b32 s38, v39\n\t" "s_add_u32 s80, s80, s38\n\t" ".Lqd_%=:\n\t" "s_memrealtime s[78:79]\n\t" "s_waitcnt lgkmcnt(0)\n\t" "s_sub_u32 s78, s78, s76\n\t" "s_cmp_lt_u32 s78, s80\n\t" \
  "s_cbranch_scc1 .Lqd_%=\n\t" "global_load_dwordx4 v[148:151], v32, s[58:59] sc1\n\t" "global_load_dwordx4 v[144:147], v32, s[56:57] sc1\n\t" ".Lq0_%=:\n\t" "ds_read_b32 v39, v37\n\t" \
  "ds_read_b64 v[40:41], v30 offset:0\n\t" "ds_read_b64 v[42:43], v30 offset:3072\n\t" "ds_read_b64 v[44:45], v30 offset:11776\n\t" "ds_read_b64 v[46:47], v30 offset:14848\n\t" \
  "ds_read_b64 v[48:49], v30 offset:12288\n\t" "ds_read_b64 v[50:51], v30 offset:15360\n\t" "s_waitcnt lgkmcnt(6)\n\t" "v_readfirstlane_b32 s38, v39\n\t" "s_cmp_ge_u32 s38, 4\n\t" \
  "s_cbranch_scc0 .Lsq0_%=\n\t" ".Lrq0_%=:\n\t" "s_waitcnt lgkmcnt(0)\n\t" "v_cndmask_b32 v52, 0, v40, s[44:45]\n\t" "v_cndmask_b32 v53, 0, v41, s[44:45]\n\t" "v_cndmask_b32 v54, 0, v42, s[46:47]\n\t" \
  "v_cndmask_b32 v55, 0, v43, s[46:47]\n\t" "v_cndmask_b32 v40, 0, v40, s[40:41]\n\t" "v_cndmask_b32 v41, 0, v41, s[40:41]\n\t" "v_cndmask_b32 v42, 0, v42, s[42:43]\n\t" \
  "v_cndmask_b32 v43, 0, v43, s[42:43]\n\t" "s_nop 1\n\t" "v_mfma_f32_16x16x32_bf16 v[108:111], v[44:47], v[40:43], v[20:23]\n\t" \
  "v_mfma_f32_16x16x32_bf16 v[112:115], v[48:51], v[40:43], v[24:27]\n\t" "v_mfma_f32_16x16x32_bf16 v[116:119], v[44:47], v[52:55], v[20:23]\n\t" \
  "v_mfma_f32_16x16x32_bf16 v[120:123], v[48:51], v[52:55], v[24:27]\n\t" ".Lv0_%=:\n\t" "ds_read_b32 v39, v37 offset:4\n\t" "ds_read_b64 v[128:129], v30 offset:24064\n\t" \
  "ds_read_b64 v[130:131], v30 offset:24576\n\t" "ds_read_b64 v[132:133], v30 offset:27136\n\t" "ds_read_b64 v[134:135], v30 offset:27648\n\t" "s_waitcnt lgkmcnt(4)\n\t" \
  "v_readfirstlane_b32 s38, v39\n\t" "s_cmp_ge_u32 s38, 9\n\t" "s_cbranch_scc0 .Lsv0_%=\n\t" ".Lrv0_%=:\n\t" "s_waitcnt lgkmcnt(0)\n\t" "v_cndmask_b32 v136, v128, 0, s[50:51]\n\t" \
  "v_cndmask_b32 v128, 0, v128, s[50:51]\n\t" "v_cndmask_b32 v137, v129, 0, s[50:51]\n\t" "v_cndmask_b32 v129, 0, v129, s[50:51]\n\t" "v_cndmask_b32 v138, v130, 0, s[50:51]\n\t" \
  "v_cndmask_b32 v130, 0, v130, s[50:51]\n\t" "v_cndmask_b32 v139, v131, 0, s[50:51]\n\t" "v_cndmask_b32 v131, 0, v131, s[50:51]\n\t" "v_cndmask_b32 v140, v132, 0, s[52:53]\n\t" \
  "v_cndmask_b32 v132, 0, v132, s[52:53]\n\t" "v_cndmask_b32 v141, v133, 0, s[52:53]\n\t" "v_cndmask_b32 v133, 0, v133, s[52:53]\n\t" "v_cndmask_b32 v142, v134, 0, s[52:53]\n\t" \
  "v_cndmask_b32 v134, 0, v134, s[52:53]\n\t" "v_cndmask_b32 v143, v135, 0, s[52:53]\n\t" "v_cndmask_b32 v135, 0, v135, s[52:53]\n\t" "s_cmp_ge_u32 s26, 4\n\t" "s_cbranch_scc1 .Lnw2_%=\n\t" \
  "s_add_u32 m0, s92, 20480\n\t" "s_nop 0\n\t" "global_load_lds_dwordx4 v3, s[12:13]\n\t" "s_add_u32 s12, s12, 81920\n\t" "s_addc_u32 s13, s13, 0\n\t" "s_add_u32 m0, s92, 24576\n\t" "s_nop 0\n\t" \
  "global_load_lds_dwordx4 v4, s[12:13]\n\t" "s_add_u32 s12, s12, 81920\n\t" "s_addc_u32 s13, s13, 0\n\t" "s_add_u32 m0, s92, 28672\n\t" "s_nop 0\n\t" "global_load_lds_dwordx4 v5, s[12:13]\n\t" \
  "s_add_u32 s12, s12, 81920\n\t" "s_addc_u32 s13, s13, 0\n\t" "s_add_u32 m0, s92, 32768\n\t" "s_nop 0\n\t" "global_load_lds_dwordx4 v6, s[12:13]\n\t" "s_add_u32 s12, s12, 81920\n\t" \
  "s_addc_u32 s13, s13, 0\n\t" "s_add_u32 m0, s92, 36864\n\t" "s_nop 0\n\t" "global_load_lds_dwordx4 v7, s[12:13]\n\t" "s_add_u32 s12, s12, 81920\n\t" "s_addc_u32 s13, s13, 0\n\t" \
  "s_add_u32 m0, s92, 61440\n\t" "s_nop 0\n\t" "global_load_lds_dwordx4 v3, s[12:13]\n\t" "s_add_u32 s12, s12, 81920\n\t" "s_addc_u32 s13, s13, 0\n\t" "s_add_u32 m0, s92, 65536\n\t" "s_nop 0\n\t" \
  "global_load_lds_dwordx4 v4, s[12:13]\n\t" "s_add_u32 s12, s12, 81920\n\t" "s_addc_u32 s13, s13, 0\n\t" "s_add_u32 m0, s92, 69632\n\t" "s_nop 0\n\t" "global_load_lds_dwordx4 v5, s[12:13]\n\t" \
  "s_add_u32 s12, s12, 81920\n\t" "s_addc_u32 s13, s13, 0\n\t" "s_add_u32 m0, s92, 73728\n\t" "s_nop 0\n\t" "global_load_lds_dwordx4 v6, s[12:13]\n\t" "s_add_u32 s12, s12, 81920\n\t" \
  "s_addc_u32 s13, s13, 0\n\t" "s_add_u32 m0, s92, 77824\n\t" "s_nop 0\n\t" "global_load_lds_dwordx4 v7, s[12:13]\n\t" ".Lnw2_%=:\n\t" "s_cmp_ge_u32 s26, 4\n\t" "s_waitcnt vmcnt(10)\n\t" \
  "s_cbranch_scc0 .Lnw3_%=\n\t" "s_waitcnt vmcnt(0)\n\t" ".Lnw3_%=:\n\t" "s_setprio 2\n\t" "v_max_u32 v124, v144, v148\n\t" "v_cmp_eq_u32 vcc, -1, v124\n\t" "s_cbranch_vccnz .Lsr_%=\n\t" \
  ".Lrr_%=:\n\t" "v_cndmask_b32 v124, 0, v144, s[48:49]\n\t" "v_cndmask_b32 v125, 0, v145, s[48:49]\n\t" "v_cndmask_b32 v126, v144, 0, s[48:49]\n\t" "v_cndmask_b32 v127, v145, 0, s[48:49]\n\t" \
  "v_mfma_f32_16x16x16_bf16 v[108:111], v[150:151], v[124:125], v[108:111]\n\t" "v_mfma_f32_16x16x16_bf16 v[112:115], v[146:147], v[124:125], v[112:115]\n\t" \
  "v_mfma_f32_16x16x16_bf16 v[116:119], v[150:151], v[126:127], v[116:119]\n\t" "v_mfma_f32_16x16x16_bf16 v[120:123], v[146:147], v[126:127], v[120:123]\n\t" "s_branch .Lk1_%=\n\t" ".Lsv1_%=:\n\t" \
  "ds_read_b32 v39, v37 offset:4\n\t" "s_sub_u32 s39, s39, 1\n\t" "s_cmp_lt_i32 s39, 0\n\t" "s_waitcnt lgkmcnt(0)\n\t" "v_readfirstlane_b32 s38, v39\n\t" "s_cbranch_scc1 .Lrv1_%=\n\t" \
  "s_cmp_ge_u32 s38, 9\n\t" "s_cbranch_scc0 .Lsv1_%=\n\t" "ds_read_b64 v[128:129], v30 offset:24064\n\t" "ds_read_b64 v[130:131], v30 offset:24576\n\t" "ds_read_b64 v[132:133], v30 offset:27136\n\t" \
  "ds_read_b64 v[134:135], v30 offset:27648\n\t" "ds_read_b64 v[136:137], v31 offset:24064\n\t" "ds_read_b64 v[138:139], v31 offset:24576\n\t" "ds_read_b64 v[140:141], v31 offset:27136\n\t" \
  "ds_read_b64 v[142:143], v31 offset:27648\n\t" "s_branch .Lrv1_%=\n\t" ".Lk2_%=:\n\t" "s_waitcnt lgkmcnt(8)\n\t" "v_readfirstlane_b32 s38, v39\n\t" "s_cmp_ge_u32 s38, 9\n\t" \
  "s_cbranch_scc0 .Lsv1_%=\n\t" ".Lrv1_%=:\n\t" "s_waitcnt lgkmcnt(0)\n\t" "v_mfma_f32_16x16x32_bf16 v[152:155], v[128:131], v[112:115], 0\n\t" \
  "v_mfma_f32_16x16x32_bf16 v[164:167], v[140:143], v[120:123], 0\n\t" "v_mfma_f32_16x16x32_bf16 v[156:159], v[132:135], v[112:115], 0\n\t" \
  "v_mfma_f32_16x16x32_bf16 v[160:163], v[136:139], v[120:123], 0\n\t" "v_xor_b32 v0, 32, v35\n\t" "v_xor_b32 v1, 64, v35\n\t" "v_xor_b32 v2, 96, v35\n\t" "s_nop 1\n\t" \
  "v_cvt_pk_bf16_f32 v144, v152, v153\n\t" "v_cvt_pk_bf16_f32 v145, v154, v155\n\t" "v_cvt_pk_bf16_f32 v146, v164, v165\n\t" "v_cvt_pk_bf16_f32 v147, v166, v167\n\t" \
  "global_store_dwordx4 v32, v[144:147], s[60:61] offset:-2048 sc1\n\t" "v_cvt_pk_bf16_f32 v148, v156, v157\n\t" "v_cvt_pk_bf16_f32 v149, v158, v159\n\t" "v_cvt_pk_bf16_f32 v150, v160, v161\n\t" \
  "v_cvt_pk_bf16_f32 v151, v162, v163\n\t" "s_branch .Lk3_%=\n\t" ".Lso0_%=:\n\t" "global_load_dwordx4 v[152:155], v32, s[62:63] offset:-2048 sc1\n\t" "s_sub_u32 s39, s39, 1\n\t" \
  "s_cmp_lt_i32 s39, 0\n\t" "s_waitcnt vmcnt(0)\n\t" "s_cbranch_scc1 .Lro0_%=\n\t" "v_cmp_eq_u32 vcc, -1, v152\n\t" "s_cbranch_vccnz .Lso0_%=\n\t" "s_branch .Lro0_%=\n\t" ".Lso1_%=:\n\t" \
  "global_load_dwordx4 v[156:159], v32, s[62:63] offset:-1024 sc1\n\t" "s_sub_u32 s39, s39, 1\n\t" "s_cmp_lt_i32 s39, 0\n\t" "s_waitcnt vmcnt(0)\n\t" "s_cbranch_scc1 .Lro1_%=\n\t" \
  "v_cmp_eq_u32 vcc, -1, v156\n\t" "s_cbranch_vccnz .Lso1_%=\n\t" "s_branch .Lro1_%=\n\t" ".Lso2_%=:\n\t" "global_load_dwordx4 v[160:163], v32, s[62:63] offset:0 sc1\n\t" "s_sub_u32 s39, s39, 1\n\t" \
  "s_cmp_lt_i32 s39, 0\n\t" "s_waitcnt vmcnt(0)\n\t" "s_cbranch_scc1 .Lro2_%=\n\t" "v_cmp_eq_u32 vcc, -1, v160\n\t" "s_cbranch_vccnz .Lso2_%=\n\t" "s_branch .Lro2_%=\n\t" ".Lso3_%=:\n\t" \
  "global_load_dwordx4 v[164:167], v32, s[62:63] offset:1024 sc1\n\t" "s_sub_u32 s39, s39, 1\n\t" "s_cmp_lt_i32 s39, 0\n\t" "s_waitcnt vmcnt(0)\n\t" "s_cbranch_scc1 .Lro3_%=\n\t" \
  "v_cmp_eq_u32 vcc, -1, v164\n\t" "s_cbranch_vccnz .Lso3_%=\n\t" "s_branch .Lro3_%=\n\t" ".Lso4_%=:\n\t" "global_load_dwordx4 v[152:155], v32, s[62:63] offset:2048 sc1\n\t" \
  "s_sub_u32 s39, s39, 1\n\t" "s_cmp_lt_i32 s39, 0\n\t" "s_waitcnt vmcnt(0)\n\t" "s_cbranch_scc1 .Lro4_%=\n\t" "v_cmp_eq_u32 vcc, -1, v152\n\t" "s_cbranch_vccnz .Lso4_%=\n\t" "s_branch .Lro4_%=\n\t" \
  ".Lso5_%=:\n\t" "global_load_dwordx4 v[156:159], v32, s[64:65] offset:-2048 sc1\n\t" "s_sub_u32 s39, s39, 1\n\t" "s_cmp_lt_i32 s39, 0\n\t" "s_waitcnt vmcnt(0)\n\t" "s_cbranch_scc1 .Lro5_%=\n\t" \
  "v_cmp_eq_u32 vcc, -1, v156\n\t" "s_cbranch_vccnz .Lso5_%=\n\t" "s_branch .Lro5_%=\n\t" ".Lso6_%=:\n\t" "global_load_dwordx4 v[160:163], v32, s[64:65] offset:-1024 sc1\n\t" \
  "s_sub_u32 s39, s39, 1\n\t" "s_cmp_lt_i32 s39, 0\n\t" "s_waitcnt vmcnt(0)\n\t" "s_cbranch_scc1 .Lro6_%=\n\t" "v_cmp_eq_u32 vcc, -1, v160\n\t" "s_cbranch_vccnz .Lso6_%=\n\t" "s_branch .Lro6_%=\n\t" \
  ".Lso7_%=:\n\t" "global_load_dwordx4 v[164:167], v32, s[64:65] offset:0 sc1\n\t" "s_sub_u32 s39, s39, 1\n\t" "s_cmp_lt_i32 s39, 0\n\t" "s_waitcnt vmcnt(0)\n\t" "s_cbranch_scc1 .Lro7_%=\n\t" \
  "v_cmp_eq_u32 vcc, -1, v164\n\t" "s_cbranch_vccnz .Lso7_%=\n\t" "s_branch .Lro7_%=\n\t" ".Lso8_%=:\n\t" "global_load_dwordx4 v[152:155], v32, s[64:65] offset:1024 sc1\n\t" \
  "s_sub_u32 s39, s39, 1\n\t" "s_cmp_lt_i32 s39, 0\n\t" "s_waitcnt vmcnt(0)\n\t" "s_cbranch_scc1 .Lro8_%=\n\t" "v_cmp_eq_u32 vcc, -1, v152\n\t" "s_cbranch_vccnz .Lso8_%=\n\t" "s_branch .Lro8_%=\n\t" \
  ".Lso9_%=:\n\t" "global_load_dwordx4 v[156:159], v32, s[64:65] offset:2048 sc1\n\t" "s_sub_u32 s39, s39, 1\n\t" "s_cmp_lt_i32 s39, 0\n\t" "s_waitcnt vmcnt(0)\n\t" "s_cbranch_scc1 .Lro9_%=\n\t" \
  "v_cmp_eq_u32 vcc, -1, v156\n\t" "s_cbranch_vccnz .Lso9_%=\n\t" "s_branch .Lro9_%=\n\t" ".Lg_%=:\n\t" "v_bfe_u32 v13, s80, v10, 4\n\t" "v_lshlrev_b32 v12, 1, v9\n\t" "s_movk_i32 s76, 24576\n\t" \
  "v_and_b32 v12, 28, v12\n\t" "v_lshl_add_u32 v2, v9, 7, s76\n\t" "v_xor_b32 v12, v12, v10\n\t" "s_mov_b32 s76, 155648\n\t" "v_lshl_add_u32 v2, v12, 2, v2\n\t" "v_lshl_add_u32 v4, v9, 2, s76\n\t" \
  "v_xor_b32 v3, 64, v2\n\t" "v_lshl_add_u32 v5, v10, 2, s76\n\t" "s_mul_i32 s76, s33, 3072\n\t" "v_lshl_add_u32 v6, v8, 3, s76\n\t" "s_sub_u32 s76, 5, s27\n\t" "s_cmp_eq_u32 s27, 0\n\t" \
  "s_cselect_b32 s76, 0, s76\n\t" "s_sub_u32 s77, 15, s27\n\t" "s_cmp_eq_u32 s27, 4\n\t" "s_cselect_b32 s77, 16, s77\n\t" "v_cmp_gt_u32 s[68:69], s76, v13\n\t" "v_cmp_le_u32 vcc, s77, v13\n\t" \
  "v_add3_u32 v13, v13, s27, -1\n\t" "s_or_b64 s[68:69], s[68:69], vcc\n\t" "v_and_b32 v13, 3, v13\n\t" "v_cndmask_b32 v12, 0, 50, vcc\n\t" "v_xor_b32 v13, v13, v12\n\t" "v_lshlrev_b32 v7, 4, v9\n\t" \
  "v_lshl_add_u32 v7, v13, 8, v7\n\t" "v_lshl_add_u32 v12, s33, 8, v5\n\t" "v_lshl_add_u32 v13, s33, 8, v4\n\t" "s_setprio 1\n\t" "s_movk_i32 s38, 9\n\t" "s_mov_b32 s77, 36864\n\t" \
  "v_mov_b64 v[40:41], 0\n\t" "v_mov_b64 v[42:43], 0\n\t" "v_mov_b64 v[44:45], 0\n\t" "v_mov_b64 v[46:47], 0\n\t" "v_mov_b64 v[48:49], 0\n\t" "v_mov_b64 v[50:51], 0\n\t" "v_mov_b64 v[52:53], 0\n\t" \
  "v_mov_b64 v[54:55], 0\n\t" "v_mov_b64 v[56:57], 0\n\t" "v_mov_b64 v[58:59], 0\n\t" "v_mov_b64 v[60:61], 0\n\t" "v_mov_b64 v[62:63], 0\n\t" "v_mov_b64 v[64:65], 0\n\t" "v_mov_b64 v[66:67], 0\n\t" \
  "v_mov_b64 v[68:69], 0\n\t" "v_mov_b64 v[70:71], 0\n\t" "v_mov_b64 v[72:73], 0\n\t" "v_mov_b64 v[74:75], 0\n\t" "v_mov_b64 v[76:77], 0\n\t" "v_mov_b64 v[78:79], 0\n\t" "v_mov_b64 v[80:81], 0\n\t" \
  "v_mov_b64 v[82:83], 0\n\t" "v_mov_b64 v[84:85], 0\n\t" "v_mov_b64 v[86:87], 0\n\t" "v_mov_b64 v[88:89], 0\n\t" "v_mov_b64 v[90:91], 0\n\t" "v_mov_b64 v[92:93], 0\n\t" "v_mov_b64 v[94:95], 0\n\t" \
  "v_mov_b64 v[96:97], 0\n\t" "v_mov_b64 v[98:99], 0\n\t" "v_mov_b64 v[100:101], 0\n\t" "v_mov_b64 v[102:103], 0\n\t" "v_mov_b64 v[104:105], 0\n\t" "v_mov_b64 v[106:107], 0\n\t" \
  "v_mov_b64 v[108:109], 0\n\t" "v_mov_b64 v[110:111], 0\n\t" "s_barrier\n\t" "ds_read_b64_tr_b16 v[136:137], v1 offset:0\n\t" "ds_read_b64_tr_b16 v[138:139], v1 offset:512\n\t" \
  "ds_read_b128 v[112:115], v2 offset:0\n\t" "ds_read_b64_tr_b16 v[140:141], v1 offset:1024\n\t" "ds_read_b64_tr_b16 v[142:143], v1 offset:1536\n\t" "ds_read_b64_tr_b16 v[144:145], v1 offset:2048\n\t" \
  "ds_read_b64_tr_b16 v[146:147], v1 offset:2560\n\t" "ds_read_b128 v[116:119], v2 offset:2048\n\t" "ds_read_b128 v[120:123], v2 offset:4096\n\t" "ds_read_b128 v[124:127], v2 offset:6144\n\t" \
  "ds_read_b128 v[128:131], v2 offset:8192\n\t" "ds_read_b128 v[132:135], v2 offset:10240\n\t" "s_branch .Lb_%=\n\t" ".p2align 6\n\t" ".La_%=:\n\t" "s_waitcnt lgkmcnt(9)\n\t" \
  "v_mfma_f32_16x16x32_bf16 v[40:43], v[148:151], v[112:115], v[40:43]\n\t" "v_xor_b32 v3, 64, v2\n\t" "s_waitcnt lgkmcnt(7)\n\t" \
  "v_mfma_f32_16x16x32_bf16 v[64:67], v[152:155], v[112:115], v[64:67]\n\t" "s_waitcnt lgkmcnt(5)\n\t" "v_mfma_f32_16x16x32_bf16 v[88:91], v[112:115], v[156:159], v[88:91]\n\t" \
  "s_waitcnt lgkmcnt(4)\n\t" "v_mfma_f32_16x16x32_bf16 v[44:47], v[148:151], v[116:119], v[44:47]\n\t" "v_mfma_f32_16x16x32_bf16 v[68:71], v[152:155], v[116:119], v[68:71]\n\t" \
  "v_mfma_f32_16x16x32_bf16 v[92:95], v[116:119], v[156:159], v[92:95]\n\t" "s_waitcnt lgkmcnt(0)\n\t" "s_barrier\n\t" "v_mfma_f32_16x16x32_bf16 v[48:51], v[148:151], v[120:123], v[48:51]\n\t" \
  "ds_read_b64_tr_b16 v[136:137], v1 offset:0\n\t" "ds_read_b64_tr_b16 v[138:139], v1 offset:512\n\t" "v_mfma_f32_16x16x32_bf16 v[72:75], v[152:155], v[120:123], v[72:75]\n\t" \
  "ds_read_b128 v[112:115], v2 offset:0\n\t" "ds_read_b64_tr_b16 v[140:141], v1 offset:1024\n\t" "v_mfma_f32_16x16x32_bf16 v[96:99], v[120:123], v[156:159], v[96:99]\n\t" \
  "ds_read_b64_tr_b16 v[142:143], v1 offset:1536\n\t" "ds_read_b64_tr_b16 v[144:145], v1 offset:2048\n\t" "v_mfma_f32_16x16x32_bf16 v[52:55], v[148:151], v[124:127], v[52:55]\n\t" \
  "ds_read_b64_tr_b16 v[146:147], v1 offset:2560\n\t" "ds_read_b128 v[116:119], v2 offset:2048\n\t" "v_mfma_f32_16x16x32_bf16 v[76:79], v[152:155], v[124:127], v[76:79]\n\t" \
  "ds_read_b128 v[120:123], v2 offset:4096\n\t" "v_mfma_f32_16x16x32_bf16 v[100:103], v[124:127], v[156:159], v[100:103]\n\t" "ds_read_b128 v[124:127], v2 offset:6144\n\t" \
  "v_mfma_f32_16x16x32_bf16 v[56:59], v[148:151], v[128:131], v[56:59]\n\t" "v_mfma_f32_16x16x32_bf16 v[80:83], v[152:155], v[128:131], v[80:83]\n\t" \
  "v_mfma_f32_16x16x32_bf16 v[104:107], v[128:131], v[156:159], v[104:107]\n\t" "ds_read_b128 v[128:131], v2 offset:8192\n\t" "v_mfma_f32_16x16x32_bf16 v[60:63], v[148:151], v[132:135], v[60:63]\n\t" \
  "v_mfma_f32_16x16x32_bf16 v[84:87], v[152:155], v[132:135], v[84:87]\n\t" "v_mfma_f32_16x16x32_bf16 v[108:111], v[132:135], v[156:159], v[108:111]\n\t" "ds_read_b128 v[132:135], v2 offset:10240\n\t" \
  ".Lb_%=:\n\t" "s_waitcnt lgkmcnt(9)\n\t" "v_mfma_f32_16x16x32_bf16 v[40:43], v[136:139], v[112:115], v[40:43]\n\t" "ds_read_b64_tr_b16 v[148:149], v1 offset:12288\n\t" "s_waitcnt lgkmcnt(8)\n\t" \
  "v_mfma_f32_16x16x32_bf16 v[64:67], v[140:143], v[112:115], v[64:67]\n\t" "ds_read_b64_tr_b16 v[150:151], v1 offset:12800\n\t" "s_waitcnt lgkmcnt(7)\n\t" \
  "v_mfma_f32_16x16x32_bf16 v[88:91], v[112:115], v[144:147], v[88:91]\n\t" "ds_read_b128 v[112:115], v3 offset:0\n\t" "s_waitcnt lgkmcnt(7)\n\t" \
  "v_mfma_f32_16x16x32_bf16 v[44:47], v[136:139], v[116:119], v[44:47]\n\t" "ds_read_b64_tr_b16 v[152:153], v1 offset:13312\n\t" \
  "v_mfma_f32_16x16x32_bf16 v[68:71], v[140:143], v[116:119], v[68:71]\n\t" "ds_read_b64_tr_b16 v[154:155], v1 offset:13824\n\t" \
  "v_mfma_f32_16x16x32_bf16 v[92:95], v[116:119], v[144:147], v[92:95]\n\t" "ds_read_b64_tr_b16 v[156:157], v1 offset:14336\n\t" "s_waitcnt lgkmcnt(9)\n\t" \
  "v_mfma_f32_16x16x32_bf16 v[48:51], v[136:139], v[120:123], v[48:51]\n\t" "ds_read_b64_tr_b16 v[158:159], v1 offset:14848\n\t" \
  "v_mfma_f32_16x16x32_bf16 v[72:75], v[140:143], v[120:123], v[72:75]\n\t" "ds_read_b128 v[116:119], v3 offset:2048\n\t" "v_mfma_f32_16x16x32_bf16 v[96:99], v[120:123], v[144:147], v[96:99]\n\t" \
  "ds_read_b128 v[120:123], v3 offset:4096\n\t" "s_waitcnt lgkmcnt(11)\n\t" "v_mfma_f32_16x16x32_bf16 v[52:55], v[136:139], v[124:127], v[52:55]\n\t" "s_bitcmp1_b32 0x44, s38\n\t" \
  "v_mfma_f32_16x16x32_bf16 v[76:79], v[140:143], v[124:127], v[76:79]\n\t" "s_cselect_b32 s76, -110592, s77\n\t" "v_add_u32 v1, s76, v1\n\t" \
  "v_mfma_f32_16x16x32_bf16 v[100:103], v[124:127], v[144:147], v[100:103]\n\t" "ds_read_b128 v[124:127], v3 offset:6144\n\t" "s_waitcnt lgkmcnt(11)\n\t" \
  "v_mfma_f32_16x16x32_bf16 v[56:59], v[136:139], v[128:131], v[56:59]\n\t" "v_add_u32 v2, s76, v2\n\t" "v_mfma_f32_16x16x32_bf16 v[80:83], v[140:143], v[128:131], v[80:83]\n\t" \
  "s_sub_u32 s38, s38, 1\n\t" "v_mfma_f32_16x16x32_bf16 v[104:107], v[128:131], v[144:147], v[104:107]\n\t" "ds_read_b128 v[128:131], v3 offset:8192\n\t" "s_waitcnt lgkmcnt(11)\n\t" \
  "v_mfma_f32_16x16x32_bf16 v[60:63], v[136:139], v[132:135], v[60:63]\n\t" "v_mfma_f32_16x16x32_bf16 v[84:87], v[140:143], v[132:135], v[84:87]\n\t" \
  "v_mfma_f32_16x16x32_bf16 v[108:111], v[132:135], v[144:147], v[108:111]\n\t" "ds_read_b128 v[132:135], v3 offset:10240\n\t" "s_cbranch_scc0 .La_%=\n\t" "s_waitcnt lgkmcnt(9)\n\t" \
  "v_mfma_f32_16x16x32_bf16 v[40:43], v[148:151], v[112:115], v[40:43]\n\t" "s_waitcnt lgkmcnt(7)\n\t" "v_mfma_f32_16x16x32_bf16 v[64:67], v[152:155], v[112:115], v[64:67]\n\t" \
  "s_waitcnt lgkmcnt(4)\n\t" "v_mfma_f32_16x16x32_bf16 v[44:47], v[148:151], v[116:119], v[44:47]\n\t" "ds_read_b32 v39, v37 offset:12\n\t" "ds_read_b128 v[160:163], v12 offset:1024\n\t" \
  "v_mfma_f32_16x16x32_bf16 v[68:71], v[152:155], v[116:119], v[68:71]\n\t" "ds_read_b128 v[164:167], v12 offset:1088\n\t" "ds_read2_b32 v[136:137], v4 offset0:0 offset1:96\n\t" \
  "s_waitcnt lgkmcnt(7)\n\t" "v_mfma_f32_16x16x32_bf16 v[48:51], v[148:151], v[120:123], v[48:51]\n\t" "ds_read2_b32 v[138:139], v4 offset0:16 offset1:112\n\t" \
  "ds_read2_b32 v[140:141], v4 offset0:32 offset1:128\n\t" "v_mfma_f32_16x16x32_bf16 v[72:75], v[152:155], v[120:123], v[72:75]\n\t" "ds_read2_b32 v[142:143], v4 offset0:48 offset1:144\n\t" \
  "ds_read2_b32 v[144:145], v4 offset0:64 offset1:160\n\t" "s_waitcnt lgkmcnt(10)\n\t" "v_mfma_f32_16x16x32_bf16 v[52:55], v[148:151], v[124:127], v[52:55]\n\t" \
  "ds_read2_b32 v[146:147], v4 offset0:80 offset1:176\n\t" "ds_read_b32 v14, v13 offset:1152\n\t" "v_mfma_f32_16x16x32_bf16 v[76:79], v[152:155], v[124:127], v[76:79]\n\t" "s_waitcnt lgkmcnt(11)\n\t" \
  "v_mfma_f32_16x16x32_bf16 v[56:59], v[148:151], v[128:131], v[56:59]\n\t" "s_waitcnt lgkmcnt(0)\n\t" "v_readfirstlane_b32 s38, v39\n\t" "s_cmp_ge_u32 s38, 4\n\t" "s_cbranch_scc0 .Lts_%=\n\t" \
  ".Ltr_%=:\n\t" "v_fma_f32 v40, -v160, v136, v40\n\t" "v_fma_f32 v41, -v161, v136, v41\n\t" "v_mfma_f32_16x16x32_bf16 v[80:83], v[152:155], v[128:131], v[80:83]\n\t" \
  "v_fma_f32 v42, -v162, v136, v42\n\t" "v_fma_f32 v43, -v163, v136, v43\n\t" "v_mfma_f32_16x16x32_bf16 v[60:63], v[148:151], v[132:135], v[60:63]\n\t" "v_fma_f32 v64, -v164, v136, v64\n\t" \
  "v_fma_f32 v65, -v165, v136, v65\n\t" "v_mfma_f32_16x16x32_bf16 v[84:87], v[152:155], v[132:135], v[84:87]\n\t" "v_fma_f32 v66, -v166, v136, v66\n\t" "v_fma_f32 v67, -v167, v136, v67\n\t" \
  "v_mfma_f32_16x16x32_bf16 v[88:91], v[112:115], v[156:159], v[88:91]\n\t" "v_mul_f32 v40, v40, v137\n\t" "v_mul_f32 v41, v41, v137\n\t" \
  "v_mfma_f32_16x16x32_bf16 v[92:95], v[116:119], v[156:159], v[92:95]\n\t" "v_mul_f32 v42, v42, v137\n\t" "v_mul_f32 v43, v43, v137\n\t" \
  "v_mfma_f32_16x16x32_bf16 v[96:99], v[120:123], v[156:159], v[96:99]\n\t" "v_mul_f32 v64, v64, v137\n\t" "v_mul_f32 v65, v65, v137\n\t" \
  "v_mfma_f32_16x16x32_bf16 v[100:103], v[124:127], v[156:159], v[100:103]\n\t" "v_mul_f32 v66, v66, v137\n\t" "v_mul_f32 v67, v67, v137\n\t" \
  "v_mfma_f32_16x16x32_bf16 v[104:107], v[128:131], v[156:159], v[104:107]\n\t" "v_cvt_pk_bf16_f32 v40, v40, v41\n\t" "v_cvt_pk_bf16_f32 v41, v42, v43\n\t" \
  "v_mfma_f32_16x16x32_bf16 v[108:111], v[132:135], v[156:159], v[108:111]\n\t" "v_cvt_pk_bf16_f32 v42, v64, v65\n\t" "v_cvt_pk_bf16_f32 v43, v66, v67\n\t" "ds_read_b128 v[112:115], v5 offset:0\n\t" \
  "ds_read_b128 v[116:119], v5 offset:384\n\t" "ds_read_b128 v[120:123], v5 offset:64\n\t" "ds_read_b128 v[124:127], v5 offset:448\n\t" "ds_read_b128 v[128:131], v5 offset:128\n\t" \
  "ds_read_b128 v[132:135], v5 offset:512\n\t" "v_fma_f32 v44, -v160, v138, v44\n\t" "v_fma_f32 v45, -v161, v138, v45\n\t" "v_fma_f32 v46, -v162, v138, v46\n\t" "v_fma_f32 v47, -v163, v138, v47\n\t" \
  "s_mov_b64 exec, s[68:69]\n\t" "global_store_dwordx4 v7, v[40:43], s[66:67] offset:-2048 sc1\n\t" "s_mov_b64 exec, -1\n\t" "ds_write2st64_b64 v6, v[40:41], v[42:43] offset0:0 offset1:24\n\t" \
  "s_mov_b64 exec, 1\n\t" "ds_add_u32 v37, v38 offset:8\n\t" "s_mov_b64 exec, -1\n\t" "v_pk_fma_f32 v[68:69], v[164:165], v[138:139], v[68:69] op_sel_hi:[1,0,1] neg_lo:[1,0,0] neg_hi:[1,0,0]\n\t" \
  "v_pk_fma_f32 v[70:71], v[166:167], v[138:139], v[70:71] op_sel_hi:[1,0,1] neg_lo:[1,0,0] neg_hi:[1,0,0]\n\t" "v_pk_mul_f32 v[44:45], v[44:45], v[138:139] op_sel:[0,1] op_sel_hi:[1,1]\n\t" \
  "v_pk_mul_f32 v[46:47], v[46:47], v[138:139] op_sel:[0,1] op_sel_hi:[1,1]\n\t" "v_pk_mul_f32 v[68:69], v[68:69], v[138:139] op_sel:[0,1] op_sel_hi:[1,1]\n\t" \
  "v_pk_mul_f32 v[70:71], v[70:71], v[138:139] op_sel:[0,1] op_sel_hi:[1,1]\n\t" "v_cvt_pk_bf16_f32 v44, v44, v45\n\t" "v_cvt_pk_bf16_f32 v45, v46, v47\n\t" "v_cvt_pk_bf16_f32 v46, v68, v69\n\t" \
  "v_cvt_pk_bf16_f32 v47, v70, v71\n\t" "s_mov_b64 exec, s[68:69]\n\t" "global_store_dwordx4 v7, v[44:47], s[66:67] offset:-1024 sc1\n\t" "s_mov_b64 exec, -1\n\t" \
  "ds_write2st64_b64 v6, v[44:45], v[46:47] offset0:1 offset1:25\n\t" "ds_read_b128 v[64:67], v5 offset:192\n\t" "ds_read_b128 v[68:71], v5 offset:576\n\t" \
  "v_pk_fma_f32 v[48:49], v[160:161], v[140:141], v[48:49] op_sel_hi:[1,0,1] neg_lo:[1,0,0] neg_hi:[1,0,0]\n\t" \
  "v_pk_fma_f32 v[50:51], v[162:163], v[140:141], v[50:51] op_sel_hi:[1,0,1] neg_lo:[1,0,0] neg_hi:[1,0,0]\n\t" \
  "v_pk_fma_f32 v[72:73], v[164:165], v[140:141], v[72:73] op_sel_hi:[1,0,1] neg_lo:[1,0,0] neg_hi:[1,0,0]\n\t" \
  "v_pk_fma_f32 v[74:75], v[166:167], v[140:141], v[74:75] op_sel_hi:[1,0,1] neg_lo:[1,0,0] neg_hi:[1,0,0]\n\t" "v_pk_mul_f32 v[48:49], v[48:49], v[140:141] op_sel:[0,1] op_sel_hi:[1,1]\n\t" \
  "v_pk_mul_f32 v[50:51], v[50:51], v[140:141] op_sel:[0,1] op_sel_hi:[1,1]\n\t" "v_pk_mul_f32 v[72:73], v[72:73], v[140:141] op_sel:[0,1] op_sel_hi:[1,1]\n\t" \
  "v_pk_mul_f32 v[74:75], v[74:75], v[140:141] op_sel:[0,1] op_sel_hi:[1,1]\n\t" "v_cvt_pk_bf16_f32 v48, v48, v49\n\t" "v_cvt_pk_bf16_f32 v49, v50, v51\n\t" "v_cvt_pk_bf16_f32 v50, v72, v73\n\t" \
  "v_cvt_pk_bf16_f32 v51, v74, v75\n\t" "s_mov_b64 exec, s[68:69]\n\t" "global_store_dwordx4 v7, v[48:51], s[66:67] offset:0 sc1\n\t" "s_mov_b64 exec, -1\n\t" \
  "ds_write2st64_b64 v6, v[48:49], v[50:51] offset0:2 offset1:26\n\t" "v_pk_fma_f32 v[52:53], v[160:161], v[142:143], v[52:53] op_sel_hi:[1,0,1] neg_lo:[1,0,0] neg_hi:[1,0,0]\n\t" \
  "v_pk_fma_f32 v[54:55], v[162:163], v[142:143], v[54:55] op_sel_hi:[1,0,1] neg_lo:[1,0,0] neg_hi:[1,0,0]\n\t" \
  "v_pk_fma_f32 v[76:77], v[164:165], v[142:143], v[76:77] op_sel_hi:[1,0,1] neg_lo:[1,0,0] neg_hi:[1,0,0]\n\t" \
  "v_pk_fma_f32 v[78:79], v[166:167], v[142:143], v[78:79] op_sel_hi:[1,0,1] neg_lo:[1,0,0] neg_hi:[1,0,0]\n\t" "v_pk_mul_f32 v[52:53], v[52:53], v[142:143] op_sel:[0,1] op_sel_hi:[1,1]\n\t" \
  "v_pk_mul_f32 v[54:55], v[54:55], v[142:143] op_sel:[0,1] op_sel_hi:[1,1]\n\t" "v_pk_mul_f32 v[76:77], v[76:77], v[142:143] op_sel:[0,1] op_sel_hi:[1,1]\n\t" \
  "v_pk_mul_f32 v[78:79], v[78:79], v[142:143] op_sel:[0,1] op_sel_hi:[1,1]\n\t" "v_cvt_pk_bf16_f32 v52, v52, v53\n\t" "v_cvt_pk_bf16_f32 v53, v54, v55\n\t" "v_cvt_pk_bf16_f32 v54, v76, v77\n\t" \
  "v_cvt_pk_bf16_f32 v55, v78, v79\n\t" "s_mov_b64 exec, s[68:69]\n\t" "global_store_dwordx4 v7, v[52:55], s[66:67] offset:1024 sc1\n\t" "s_mov_b64 exec, -1\n\t" \
  "ds_write2st64_b64 v6, v[52:53], v[54:55] offset0:3 offset1:27\n\t" "ds_read_b128 v[72:75], v5 offset:256\n\t" "ds_read_b128 v[76:79], v5 offset:640\n\t" \
  "v_pk_fma_f32 v[56:57], v[160:161], v[144:145], v[56:57] op_sel_hi:[1,0,1] neg_lo:[1,0,0] neg_hi:[1,0,0]\n\t" \
  "v_pk_fma_f32 v[58:59], v[162:163], v[144:145], v[58:59] op_sel_hi:[1,0,1] neg_lo:[1,0,0] neg_hi:[1,0,0]\n\t" \
  "v_pk_fma_f32 v[80:81], v[164:165], v[144:145], v[80:81] op_sel_hi:[1,0,1] neg_lo:[1,0,0] neg_hi:[1,0,0]\n\t" \
  "v_pk_fma_f32 v[82:83], v[166:167], v[144:145], v[82:83] op_sel_hi:[1,0,1] neg_lo:[1,0,0] neg_hi:[1,0,0]\n\t" "v_pk_mul_f32 v[56:57], v[56:57], v[144:145] op_sel:[0,1] op_sel_hi:[1,1]\n\t" \
  "v_pk_mul_f32 v[58:59], v[58:59], v[144:145] op_sel:[0,1] op_sel_hi:[1,1]\n\t" "v_pk_mul_f32 v[80:81], v[80:81], v[144:145] op_sel:[0,1] op_sel_hi:[1,1]\n\t" \
  "v_pk_mul_f32 v[82:83], v[82:83], v[144:145] op_sel:[0,1] op_sel_hi:[1,1]\n\t" "v_cvt_pk_bf16_f32 v56, v56, v57\n\t" "v_cvt_pk_bf16_f32 v57, v58, v59\n\t" "v_cvt_pk_bf16_f32 v58, v80, v81\n\t" \
  "v_cvt_pk_bf16_f32 v59, v82, v83\n\t" "s_mov_b64 exec, s[68:69]\n\t" "global_store_dwordx4 v7, v[56:59], s[66:67] offset:2048 sc1\n\t" "s_mov_b64 exec, -1\n\t" \
  "ds_write2st64_b64 v6, v[56:57], v[58:59] offset0:4 offset1:28\n\t" "v_pk_fma_f32 v[60:61], v[160:161], v[146:147], v[60:61] op_sel_hi:[1,0,1] neg_lo:[1,0,0] neg_hi:[1,0,0]\n\t" \
  "v_pk_fma_f32 v[62:63], v[162:163], v[146:147], v[62:63] op_sel_hi:[1,0,1] neg_lo:[1,0,0] neg_hi:[1,0,0]\n\t" \
  "v_pk_fma_f32 v[84:85], v[164:165], v[146:147], v[84:85] op_sel_hi:[1,0,1] neg_lo:[1,0,0] neg_hi:[1,0,0]\n\t" \
  "v_pk_fma_f32 v[86:87], v[166:167], v[146:147], v[86:87] op_sel_hi:[1,0,1] neg_lo:[1,0,0] neg_hi:[1,0,0]\n\t" "v_pk_mul_f32 v[60:61], v[60:61], v[146:147] op_sel:[0,1] op_sel_hi:[1,1]\n\t" \
  "v_pk_mul_f32 v[62:63], v[62:63], v[146:147] op_sel:[0,1] op_sel_hi:[1,1]\n\t" "v_pk_mul_f32 v[84:85], v[84:85], v[146:147] op_sel:[0,1] op_sel_hi:[1,1]\n\t" \
  "v_pk_mul_f32 v[86:87], v[86:87], v[146:147] op_sel:[0,1] op_sel_hi:[1,1]\n\t" "v_cvt_pk_bf16_f32 v60, v60, v61\n\t" "v_cvt_pk_bf16_f32 v61, v62, v63\n\t" "v_cvt_pk_bf16_f32 v62, v84, v85\n\t" \
  "v_cvt_pk_bf16_f32 v63, v86, v87\n\t" "s_mov_b64 exec, s[68:69]\n\t" "global_store_dwordx4 v7, v[60:63], s[66:67] offset:3072 sc1\n\t" "s_mov_b64 exec, -1\n\t" \
  "ds_write2st64_b64 v6, v[60:61], v[62:63] offset0:5 offset1:29\n\t" "ds_read_b128 v[80:83], v5 offset:320\n\t" "ds_read_b128 v[84:87], v5 offset:704\n\t" "s_mov_b64 exec, 1\n\t" \
  "ds_add_u32 v37, v38 offset:0\n\t" "s_mov_b64 exec, -1\n\t" "s_waitcnt lgkmcnt(14)\n\t" "v_pk_fma_f32 v[88:89], v[112:113], v[14:15], v[88:89] op_sel_hi:[1,0,1] neg_lo:[1,0,0] neg_hi:[1,0,0]\n\t" \
  "v_pk_fma_f32 v[90:91], v[114:115], v[14:15], v[90:91] op_sel_hi:[1,0,1] neg_lo:[1,0,0] neg_hi:[1,0,0]\n\t" \
  "v_pk_fma_f32 v[92:93], v[120:121], v[14:15], v[92:93] op_sel_hi:[1,0,1] neg_lo:[1,0,0] neg_hi:[1,0,0]\n\t" \
  "v_pk_fma_f32 v[94:95], v[122:123], v[14:15], v[94:95] op_sel_hi:[1,0,1] neg_lo:[1,0,0] neg_hi:[1,0,0]\n\t" "v_pk_mul_f32 v[88:89], v[88:89], v[116:117]\n\t" \
  "v_pk_mul_f32 v[90:91], v[90:91], v[118:119]\n\t" "v_pk_mul_f32 v[92:93], v[92:93], v[124:125]\n\t" "v_pk_mul_f32 v[94:95], v[94:95], v[126:127]\n\t" "v_cvt_pk_bf16_f32 v88, v88, v89\n\t" \
  "v_cvt_pk_bf16_f32 v89, v90, v91\n\t" "v_cvt_pk_bf16_f32 v92, v92, v93\n\t" "v_cvt_pk_bf16_f32 v93, v94, v95\n\t" "ds_write2st64_b64 v6, v[88:89], v[92:93] offset0:48 offset1:49\n\t" \
  "s_waitcnt lgkmcnt(10)\n\t" "v_pk_fma_f32 v[96:97], v[128:129], v[14:15], v[96:97] op_sel_hi:[1,0,1] neg_lo:[1,0,0] neg_hi:[1,0,0]\n\t" \
  "v_pk_fma_f32 v[98:99], v[130:131], v[14:15], v[98:99] op_sel_hi:[1,0,1] neg_lo:[1,0,0] neg_hi:[1,0,0]\n\t" \
  "v_pk_fma_f32 v[100:101], v[64:65], v[14:15], v[100:101] op_sel_hi:[1,0,1] neg_lo:[1,0,0] neg_hi:[1,0,0]\n\t" \
  "v_pk_fma_f32 v[102:103], v[66:67], v[14:15], v[102:103] op_sel_hi:[1,0,1] neg_lo:[1,0,0] neg_hi:[1,0,0]\n\t" "v_pk_mul_f32 v[96:97], v[96:97], v[132:133]\n\t" \
  "v_pk_mul_f32 v[98:99], v[98:99], v[134:135]\n\t" "v_pk_mul_f32 v[100:101], v[100:101], v[68:69]\n\t" "v_pk_mul_f32 v[102:103], v[102:103], v[70:71]\n\t" "v_cvt_pk_bf16_f32 v96, v96, v97\n\t" \
  "v_cvt_pk_bf16_f32 v97, v98, v99\n\t" "v_cvt_pk_bf16_f32 v100, v100, v101\n\t" "v_cvt_pk_bf16_f32 v101, v102, v103\n\t" "ds_write2st64_b64 v6, v[96:97], v[100:101] offset0:50 offset1:51\n\t" \
  "s_waitcnt lgkmcnt(3)\n\t" "v_pk_fma_f32 v[104:105], v[72:73], v[14:15], v[104:105] op_sel_hi:[1,0,1] neg_lo:[1,0,0] neg_hi:[1,0,0]\n\t" \
  "v_pk_fma_f32 v[106:107], v[74:75], v[14:15], v[106:107] op_sel_hi:[1,0,1] neg_lo:[1,0,0] neg_hi:[1,0,0]\n\t" \
  "v_pk_fma_f32 v[108:109], v[80:81], v[14:15], v[108:109] op_sel_hi:[1,0,1] neg_lo:[1,0,0] neg_hi:[1,0,0]\n\t" \
  "v_pk_fma_f32 v[110:111], v[82:83], v[14:15], v[110:111] op_sel_hi:[1,0,1] neg_lo:[1,0,0] neg_hi:[1,0,0]\n\t" "v_pk_mul_f32 v[104:105], v[104:105], v[76:77]\n\t" \
  "v_pk_mul_f32 v[106:107], v[106:107], v[78:79]\n\t" "v_pk_mul_f32 v[108:109], v[108:109], v[84:85]\n\t" "v_pk_mul_f32 v[110:111], v[110:111], v[86:87]\n\t" "v_cvt_pk_bf16_f32 v104, v104, v105\n\t" \
  "v_cvt_pk_bf16_f32 v105, v106, v107\n\t" "v_cvt_pk_bf16_f32 v108, v108, v109\n\t" "v_cvt_pk_bf16_f32 v109, v110, v111\n\t" "ds_write2st64_b64 v6, v[104:105], v[108:109] offset0:52 offset1:53\n\t" \
  "s_mov_b64 exec, 1\n\t" "ds_add_u32 v37, v38 offset:4\n\t" "s_mov_b64 exec, -1\n\t" "s_setprio 1\n\t" ".Latt_%=:\n\t" "global_load_dwordx4 v[12:15], v33, s[8:9]\n\t" \
  "global_load_dwordx4 v[16:19], v34, s[14:15]\n\t" "s_cmp_eq_u32 s34, 0\n\t" "s_cbranch_scc1 .Lk0_%=\n\t" ".Lq_%=:\n\t" "ds_read_b32 v39, v37\n\t" "ds_read_b64 v[40:41], v30 offset:0\n\t" \
  "ds_read_b64 v[42:43], v30 offset:3072\n\t" "ds_read_b64 v[44:45], v30 offset:11776\n\t" "ds_read_b64 v[46:47], v30 offset:14848\n\t" "ds_read_b64 v[48:49], v30 offset:12288\n\t" \
  "ds_read_b64 v[50:51], v30 offset:15360\n\t" "ds_read_b64 v[52:53], v31 offset:0\n\t" "ds_read_b64 v[54:55], v31 offset:3072\n\t" "ds_read_b64 v[56:57], v31 offset:11776\n\t" \
  "ds_read_b64 v[58:59], v31 offset:14848\n\t" "ds_read_b64 v[60:61], v31 offset:12288\n\t" "ds_read_b64 v[62:63], v31 offset:15360\n\t" "s_waitcnt lgkmcnt(12)\n\t" "v_readfirstlane_b32 s38, v39\n\t" \
  "s_cmp_ge_u32 s38, 4\n\t" "s_cbranch_scc0 .Lsq_%=\n\t" ".Lrq_%=:\n\t" "s_waitcnt lgkmcnt(10)\n\t" "v_cndmask_b32 v42, 0, v42, s[54:55]\n\t" "v_cndmask_b32 v43, 0, v43, s[54:55]\n\t" \
  "s_waitcnt lgkmcnt(8)\n\t" "s_nop 0\n\t" "v_mfma_f32_16x16x32_bf16 v[108:111], v[44:47], v[40:43], v[20:23]\n\t" "s_waitcnt lgkmcnt(6)\n\t" \
  "v_mfma_f32_16x16x32_bf16 v[112:115], v[48:51], v[40:43], v[24:27]\n\t" "s_waitcnt lgkmcnt(4)\n\t" "v_cndmask_b32 v52, 0, v52, s[54:55]\n\t" "v_cndmask_b32 v53, 0, v53, s[54:55]\n\t" \
  "s_waitcnt lgkmcnt(2)\n\t" "s_nop 0\n\t" "v_mfma_f32_16x16x32_bf16 v[116:119], v[56:59], v[52:55], v[20:23]\n\t" "s_waitcnt lgkmcnt(0)\n\t" \
  "v_mfma_f32_16x16x32_bf16 v[120:123], v[60:63], v[52:55], v[24:27]\n\t" "ds_read_b32 v39, v37 offset:4\n\t" "ds_read_b64 v[128:129], v30 offset:24064\n\t" \
  "ds_read_b64 v[130:131], v30 offset:24576\n\t" "ds_read_b64 v[132:133], v30 offset:27136\n\t" "ds_read_b64 v[134:135], v30 offset:27648\n\t" "ds_read_b64 v[136:137], v31 offset:24064\n\t" \
  "ds_read_b64 v[138:139], v31 offset:24576\n\t" "ds_read_b64 v[140:141], v31 offset:27136\n\t" "ds_read_b64 v[142:143], v31 offset:27648\n\t" ".Lk1_%=:\n\t" "s_nop 4\n\t" \
  "v_max_f32 v108, v108, v112\n\t" "v_max_f32 v109, v109, v113\n\t" "v_max_f32 v110, v110, v114\n\t" "v_max_f32 v111, v111, v115\n\t" "v_max_f32 v116, v116, v120\n\t" "v_max_f32 v117, v117, v121\n\t" \
  "v_max_f32 v118, v118, v122\n\t" "v_max_f32 v119, v119, v123\n\t" "v_max3_f32 v124, v108, v109, v110\n\t" "v_max3_f32 v125, v116, v117, v118\n\t" "v_max_f32 v124, v124, v111\n\t" \
  "v_max_f32 v125, v125, v119\n\t" "s_mov_b32 s38, 0x3ea52b5c\n\t" "s_nop 0\n\t" "v_permlane32_swap_b32 v124, v125\n\t" "v_max_f32 v126, v124, v125\n\t" "v_max_f32 v127, v124, v125\n\t" "s_nop 1\n\t" \
  "v_permlane16_swap_b32 v126, v127\n\t" "v_max_f32 v124, v126, v127\n\t" "v_mul_f32 v125, s38, v124\n\t" "v_mul_f32 v124, s38, v124\n\t" "s_nop 1\n\t" "v_permlane32_swap_b32 v124, v125\n\t" \
  "v_fma_f32 v108, v108, s38, -v124\n\t" "v_fma_f32 v116, v116, s38, -v125\n\t" "v_fma_f32 v109, v109, s38, -v124\n\t" "v_fma_f32 v117, v117, s38, -v125\n\t" "v_fma_f32 v110, v110, s38, -v124\n\t" \
  "v_fma_f32 v118, v118, s38, -v125\n\t" "v_fma_f32 v111, v111, s38, -v124\n\t" "v_fma_f32 v119, v119, s38, -v125\n\t" "v_exp_f32 v108, v108\n\t" "v_exp_f32 v116, v116\n\t" "v_exp_f32 v109, v109\n\t" \
  "v_exp_f32 v117, v117\n\t" "v_exp_f32 v110, v110\n\t" "v_exp_f32 v118, v118\n\t" "v_add_f32 v124, v108, v109\n\t" "v_add_f32 v125, v116, v117\n\t" "v_exp_f32 v111, v111\n\t" \
  "v_exp_f32 v119, v119\n\t" "v_add_f32 v124, v124, v110\n\t" "v_add_f32 v125, v125, v118\n\t" "v_add_f32 v124, v124, v111\n\t" "v_add_f32 v125, v125, v119\n\t" "s_cmp_lg_u32 s34, 0\n\t" "s_nop 0\n\t" \
  "v_permlane32_swap_b32 v124, v125\n\t" "v_add_f32 v126, v124, v125\n\t" "v_add_f32 v127, v124, v125\n\t" "s_nop 1\n\t" "v_permlane16_swap_b32 v126, v127\n\t" "v_add_f32 v124, v126, v127\n\t" \
  "v_rcp_f32 v126, v124\n\t" "v_rcp_f32 v124, v124\n\t" "s_nop 1\n\t" "v_permlane32_swap_b32 v124, v126\n\t" "v_pk_mul_f32 v[108:109], v[108:109], v[124:125] op_sel_hi:[1,0]\n\t" \
  "v_pk_mul_f32 v[110:111], v[110:111], v[124:125] op_sel_hi:[1,0]\n\t" "v_pk_mul_f32 v[116:117], v[116:117], v[126:127] op_sel_hi:[1,0]\n\t" \
  "v_pk_mul_f32 v[118:119], v[118:119], v[126:127] op_sel_hi:[1,0]\n\t" "v_cvt_pk_bf16_f32 v112, v108, v109\n\t" "v_cvt_pk_bf16_f32 v113, v110, v111\n\t" "v_cvt_pk_bf16_f32 v120, v116, v117\n\t" \
  "v_cvt_pk_bf16_f32 v121, v118, v119\n\t" "v_and_b32 v114, v112, v28\n\t" "v_and_b32 v115, v113, v29\n\t" "v_and_b32 v122, v120, v28\n\t" "v_and_b32 v123, v121, v29\n\t" \
  "v_xor_b32 v112, v112, v114\n\t" "v_xor_b32 v113, v113, v115\n\t" "v_xor_b32 v120, v120, v122\n\t" "v_xor_b32 v121, v121, v123\n\t" "s_cbranch_scc1 .Lk2_%=\n\t" \
  "v_mfma_f32_16x16x32_bf16 v[152:155], v[128:131], v[112:115], 0\n\t" "v_mfma_f32_16x16x32_bf16 v[156:159], v[132:135], v[112:115], 0\n\t" \
  "v_mfma_f32_16x16x32_bf16 v[152:155], v[136:139], v[120:123], v[152:155]\n\t" "v_mfma_f32_16x16x32_bf16 v[156:159], v[140:143], v[120:123], v[156:159]\n\t" "s_nop 6\n\t" \
  "v_cvt_pk_bf16_f32 v148, v152, v153\n\t" "v_cvt_pk_bf16_f32 v149, v154, v155\n\t" "v_cvt_pk_bf16_f32 v150, v156, v157\n\t" "v_cvt_pk_bf16_f32 v151, v158, v159\n\t" ".Lk3_%=:\n\t" \
  "s_mov_b64 exec, s[54:55]\n\t" "global_store_dwordx4 v36, v[148:151], s[60:61] offset:-2048 sc1\n\t" "s_mov_b64 exec, -1\n\t" "s_cmp_ge_u32 s26, 6\n\t" "s_cbranch_scc1 .Lb3a_%=\n\t" \
  "s_cmp_ge_u32 s26, 4\n\t" "s_cbranch_scc1 .Lv21_%=\n\t" "s_waitcnt vmcnt(1)\n\t" ".Lv21_%=:\n\t" "s_setprio 1\n\t" "ds_read_b64 v[152:153], v37 offset:80\n\t" "s_waitcnt lgkmcnt(0)\n\t" \
  "v_readfirstlane_b32 s80, v152\n\t" "v_readfirstlane_b32 s81, v153\n\t" "s_sub_u32 s81, s81, s80\n\t" "s_sub_u32 s80, s81, 15\n\t" "s_barrier\n\t" "s_cbranch_scc1 .Llx1_%=\n\t" \
  "s_memrealtime s[76:77]\n\t" ".Llw1_%=:\n\t" "s_memrealtime s[78:79]\n\t" "s_waitcnt lgkmcnt(0)\n\t" "s_sub_u32 s78, s78, s76\n\t" "s_cmp_lt_u32 s78, s80\n\t" "s_cbranch_scc1 .Llw1_%=\n\t" \
  ".Llx1_%=:\n\t" "global_load_dwordx4 v[152:155], v32, s[62:63] offset:-2048 sc1\n\t" "global_load_dwordx4 v[156:159], v32, s[62:63] offset:-1024 sc1\n\t" \
  "global_load_dwordx4 v[160:163], v32, s[62:63] offset:0 sc1\n\t" ".Lb3a_%=:\n\t" "ds_read_b64_tr_b16 v[40:41], v35 offset:0\n\t" "ds_read_b64_tr_b16 v[42:43], v35 offset:2048\n\t" \
  "ds_read_b64_tr_b16 v[44:45], v0 offset:0\n\t" "ds_read_b64_tr_b16 v[46:47], v0 offset:2048\n\t" "ds_read_b64_tr_b16 v[48:49], v1 offset:0\n\t" "ds_read_b64_tr_b16 v[50:51], v1 offset:2048\n\t" \
  "ds_read_b64_tr_b16 v[52:53], v2 offset:0\n\t" "ds_read_b64_tr_b16 v[54:55], v2 offset:2048\n\t" "ds_read_b64_tr_b16 v[56:57], v35 offset:4096\n\t" "ds_read_b64_tr_b16 v[58:59], v35 offset:6144\n\t" \
  "ds_read_b64_tr_b16 v[60:61], v0 offset:4096\n\t" "ds_read_b64_tr_b16 v[62:63], v0 offset:6144\n\t" "ds_read_b64_tr_b16 v[64:65], v1 offset:4096\n\t" \
  "ds_read_b64_tr_b16 v[66:67], v1 offset:6144\n\t" "ds_read_b64_tr_b16 v[68:69], v2 offset:4096\n\t" "ds_read_b64_tr_b16 v[70:71], v2 offset:6144\n\t" \
  "ds_read_b64_tr_b16 v[72:73], v35 offset:8192\n\t" "ds_read_b64_tr_b16 v[74:75], v35 offset:10240\n\t" "ds_read_b64_tr_b16 v[76:77], v0 offset:8192\n\t" \
  "ds_read_b64_tr_b16 v[78:79], v0 offset:10240\n\t" "ds_read_b64_tr_b16 v[80:81], v1 offset:8192\n\t" "ds_read_b64_tr_b16 v[82:83], v1 offset:10240\n\t" \
  "ds_read_b64_tr_b16 v[84:85], v2 offset:8192\n\t" "ds_read_b64_tr_b16 v[86:87], v2 offset:10240\n\t" "ds_read_b64_tr_b16 v[88:89], v35 offset:12288\n\t" \
  "ds_read_b64_tr_b16 v[90:91], v35 offset:14336\n\t" "ds_read_b64_tr_b16 v[92:93], v0 offset:12288\n\t" "ds_read_b64_tr_b16 v[94:95], v0 offset:14336\n\t" \
  "ds_read_b64_tr_b16 v[96:97], v1 offset:12288\n\t" "ds_read_b64_tr_b16 v[98:99], v1 offset:14336\n\t" "ds_read_b64_tr_b16 v[100:101], v2 offset:12288\n\t" \
  "ds_read_b64_tr_b16 v[102:103], v2 offset:14336\n\t" "ds_read_b64_tr_b16 v[104:105], v35 offset:16384\n\t" "ds_read_b64_tr_b16 v[106:107], v35 offset:18432\n\t" \
  "ds_read_b64_tr_b16 v[108:109], v0 offset:16384\n\t" "ds_read_b64_tr_b16 v[110:111], v0 offset:18432\n\t" "ds_read_b64_tr_b16 v[112:113], v1 offset:16384\n\t" \
  "ds_read_b64_tr_b16 v[114:115], v1 offset:18432\n\t" "ds_read_b64_tr_b16 v[116:117], v2 offset:16384\n\t" "ds_read_b64_tr_b16 v[118:119], v2 offset:18432\n\t" "s_cmp_lt_u32 s26, 6\n\t" \
  "s_cbranch_scc1 .Lb3b_%=\n\t" "s_setprio 1\n\t" "ds_read_b64 v[152:153], v37 offset:80\n\t" "s_waitcnt lgkmcnt(0)\n\t" "v_readfirstlane_b32 s80, v152\n\t" "v_readfirstlane_b32 s81, v153\n\t" \
  "s_sub_u32 s81, s81, s80\n\t" "s_sub_u32 s80, s81, 15\n\t" "s_barrier\n\t" "s_cbranch_scc1 .Llx2_%=\n\t" "s_memrealtime s[76:77]\n\t" ".Llw2_%=:\n\t" "s_memrealtime s[78:79]\n\t" \
  "s_waitcnt lgkmcnt(0)\n\t" "s_sub_u32 s78, s78, s76\n\t" "s_cmp_lt_u32 s78, s80\n\t" "s_cbranch_scc1 .Llw2_%=\n\t" ".Llx2_%=:\n\t" \
  "global_load_dwordx4 v[152:155], v32, s[62:63] offset:-2048 sc1\n\t" "global_load_dwordx4 v[156:159], v32, s[62:63] offset:-1024 sc1\n\t" \
  "global_load_dwordx4 v[160:163], v32, s[62:63] offset:0 sc1\n\t" ".Lb3b_%=:\n\t" "ds_read_b64_tr_b16 v[120:121], v35 offset:20480\n\t" "ds_read_b64_tr_b16 v[122:123], v35 offset:22528\n\t" \
  "ds_read_b64_tr_b16 v[124:125], v0 offset:20480\n\t" "ds_read_b64_tr_b16 v[126:127], v0 offset:22528\n\t" "ds_read_b64_tr_b16 v[128:129], v1 offset:20480\n\t" \
  "ds_read_b64_tr_b16 v[130:131], v1 offset:22528\n\t" "ds_read_b64_tr_b16 v[132:133], v2 offset:20480\n\t" "ds_read_b64_tr_b16 v[134:135], v2 offset:22528\n\t" "s_waitcnt vmcnt(5)\n\t" \
  "v_permlane16_swap_b32 v12, v14\n\t" "v_permlane16_swap_b32 v13, v15\n\t" "v_mov_b64 v[144:145], 0\n\t" "v_mov_b64 v[146:147], 0\n\t" "v_mov_b64 v[148:149], 0\n\t" "v_mov_b64 v[150:151], 0\n\t" \
  "v_lshlrev_b32 v136, 16, v12\n\t" "v_and_b32 v137, 0xffff0000, v12\n\t" "v_lshlrev_b32 v138, 16, v13\n\t" "v_and_b32 v139, 0xffff0000, v13\n\t" "v_lshlrev_b32 v140, 16, v14\n\t" \
  "v_and_b32 v141, 0xffff0000, v14\n\t" "v_lshlrev_b32 v142, 16, v15\n\t" "v_and_b32 v143, 0xffff0000, v15\n\t" "s_lshl_b32 s38, s26, 11\n\t" "v_and_b32 v28, 0xf0, v32\n\t" \
  "v_add_u32 v24, s38, v32\n\t" "v_permlane16_swap_b32 v16, v18\n\t" "v_permlane16_swap_b32 v17, v19\n\t" "s_lshl_b32 s40, s36, 6\n\t" "s_add_u32 s40, s40, 0\n\t" "v_lshl_add_u32 v27, v10, 1, s40\n\t" \
  "s_mov_b32 s40, 0x3f803f80\n\t" "v_xor_b32 v25, 0x800, v24\n\t" "v_mad_u32_u24 v27, v28, 9, v27\n\t" "s_mul_i32 s38, s37, 2304\n\t" "v_add_u32 v28, v28, v10\n\t" "v_add_u32 v27, s38, v27\n\t" \
  "s_mul_i32 s38, s36, 6\n\t" "s_add_u32 s38, s38, s37\n\t" "s_lshl_b32 s38, s38, 8\n\t" "v_lshl_add_u32 v26, s37, 2, v37\n\t" "v_add3_u32 v28, v28, s38, v37\n\t" "s_mul_i32 s38, s34, 768\n\t" \
  "v_min_u32 v29, 0x2f0, v32\n\t" "v_lshl_add_u32 v30, s34, 2, v37\n\t" "v_add3_u32 v29, v29, s38, v37\n\t" "s_lshl1_add_u32 s41, s29, s34\n\t" "v_min_u32 v20, 47, v8\n\t" \
  "s_mul_i32 s38, s41, 6400\n\t" "s_add_u32 s38, s38, 4829184\n\t" "v_mul_u32_u24 v21, 12, v20\n\t" "s_add_u32 s44, s24, s38\n\t" "s_addc_u32 s45, s25, 0\n\t" "s_add_u32 s42, s44, 2560\n\t" \
  "s_addc_u32 s43, s45, 0\n\t" "s_mul_i32 s38, s28, 640\n\t" "s_add_u32 s44, s44, s38\n\t" "s_addc_u32 s45, s45, 0\n\t" "s_waitcnt vmcnt(2)\n\t" "v_cmp_eq_u32 vcc, -1, v152\n\t" \
  "s_cbranch_vccnz .Lso0_%=\n\t" ".Lro0_%=:\n\t" "s_waitcnt lgkmcnt(14)\n\t" "v_mfma_f32_16x16x32_bf16 v[136:139], v[40:43], v[152:155], v[136:139]\n\t" \
  "global_load_dwordx4 v[164:167], v32, s[62:63] offset:1024 sc1\n\t" "ds_read_b64_tr_b16 v[40:41], v35 offset:24576\n\t" "ds_read_b64_tr_b16 v[42:43], v35 offset:26624\n\t" \
  "v_mfma_f32_16x16x32_bf16 v[140:143], v[44:47], v[152:155], v[140:143]\n\t" "ds_read_b64_tr_b16 v[44:45], v0 offset:24576\n\t" "ds_read_b64_tr_b16 v[46:47], v0 offset:26624\n\t" \
  "v_mfma_f32_16x16x32_bf16 v[144:147], v[48:51], v[152:155], v[144:147]\n\t" "ds_read_b64_tr_b16 v[48:49], v1 offset:24576\n\t" "ds_read_b64_tr_b16 v[50:51], v1 offset:26624\n\t" \
  "v_mfma_f32_16x16x32_bf16 v[148:151], v[52:55], v[152:155], v[148:151]\n\t" "ds_read_b64_tr_b16 v[52:53], v2 offset:24576\n\t" "ds_read_b64_tr_b16 v[54:55], v2 offset:26624\n\t" \
  "s_waitcnt vmcnt(2)\n\t" "v_cmp_eq_u32 vcc, -1, v156\n\t" "s_cbranch_vccnz .Lso1_%=\n\t" ".Lro1_%=:\n\t" "v_mfma_f32_16x16x32_bf16 v[136:139], v[56:59], v[156:159], v[136:139]\n\t" \
  "global_load_dwordx4 v[152:155], v32, s[62:63] offset:2048 sc1\n\t" "ds_read_b64_tr_b16 v[56:57], v35 offset:28672\n\t" "ds_read_b64_tr_b16 v[58:59], v35 offset:30720\n\t" \
  "v_mfma_f32_16x16x32_bf16 v[140:143], v[60:63], v[156:159], v[140:143]\n\t" "ds_read_b64_tr_b16 v[60:61], v0 offset:28672\n\t" "ds_read_b64_tr_b16 v[62:63], v0 offset:30720\n\t" \
  "v_mfma_f32_16x16x32_bf16 v[144:147], v[64:67], v[156:159], v[144:147]\n\t" "ds_read_b64_tr_b16 v[64:65], v1 offset:28672\n\t" "ds_read_b64_tr_b16 v[66:67], v1 offset:30720\n\t" \
  "v_mfma_f32_16x16x32_bf16 v[148:151], v[68:71], v[156:159], v[148:151]\n\t" "ds_read_b64_tr_b16 v[68:69], v2 offset:28672\n\t" "ds_read_b64_tr_b16 v[70:71], v2 offset:30720\n\t" \
  "s_waitcnt vmcnt(2)\n\t" "v_cmp_eq_u32 vcc, -1, v160\n\t" "s_cbranch_vccnz .Lso2_%=\n\t" ".Lro2_%=:\n\t" "v_mfma_f32_16x16x32_bf16 v[136:139], v[72:75], v[160:163], v[136:139]\n\t" \
  "global_load_dwordx4 v[156:159], v32, s[64:65] offset:-2048 sc1\n\t" "ds_read_b64_tr_b16 v[72:73], v35 offset:32768\n\t" "ds_read_b64_tr_b16 v[74:75], v35 offset:34816\n\t" \
  "v_mfma_f32_16x16x32_bf16 v[140:143], v[76:79], v[160:163], v[140:143]\n\t" "ds_read_b64_tr_b16 v[76:77], v0 offset:32768\n\t" "ds_read_b64_tr_b16 v[78:79], v0 offset:34816\n\t" \
  "v_mfma_f32_16x16x32_bf16 v[144:147], v[80:83], v[160:163], v[144:147]\n\t" "ds_read_b64_tr_b16 v[80:81], v1 offset:32768\n\t" "ds_read_b64_tr_b16 v[82:83], v1 offset:34816\n\t" \
  "v_mfma_f32_16x16x32_bf16 v[148:151], v[84:87], v[160:163], v[148:151]\n\t" "ds_read_b64_tr_b16 v[84:85], v2 offset:32768\n\t" "ds_read_b64_tr_b16 v[86:87], v2 offset:34816\n\t" \
  "s_waitcnt vmcnt(2)\n\t" "v_cmp_eq_u32 vcc, -1, v164\n\t" "s_cbranch_vccnz .Lso3_%=\n\t" ".Lro3_%=:\n\t" "v_mfma_f32_16x16x32_bf16 v[136:139], v[88:91], v[164:167], v[136:139]\n\t" \
  "global_load_dwordx4 v[160:163], v32, s[64:65] offset:-1024 sc1\n\t" "ds_read_b64_tr_b16 v[88:89], v35 offset:36864\n\t" "ds_read_b64_tr_b16 v[90:91], v35 offset:38912\n\t" \
  "v_mfma_f32_16x16x32_bf16 v[140:143], v[92:95], v[164:167], v[140:143]\n\t" "ds_read_b64_tr_b16 v[92:93], v0 offset:36864\n\t" "ds_read_b64_tr_b16 v[94:95], v0 offset:38912\n\t" \
  "v_mfma_f32_16x16x32_bf16 v[144:147], v[96:99], v[164:167], v[144:147]\n\t" "ds_read_b64_tr_b16 v[96:97], v1 offset:36864\n\t" "ds_read_b64_tr_b16 v[98:99], v1 offset:38912\n\t" \
  "v_mfma_f32_16x16x32_bf16 v[148:151], v[100:103], v[164:167], v[148:151]\n\t" "ds_read_b64_tr_b16 v[100:101], v2 offset:36864\n\t" "ds_read_b64_tr_b16 v[102:103], v2 offset:38912\n\t" \
  "s_waitcnt vmcnt(2)\n\t" "v_cmp_eq_u32 vcc, -1, v152\n\t" "s_cbranch_vccnz .Lso4_%=\n\t" ".Lro4_%=:\n\t" "v_mfma_f32_16x16x32_bf16 v[136:139], v[104:107], v[152:155], v[136:139]\n\t" \
  "global_load_dwordx4 v[164:167], v32, s[64:65] offset:0 sc1\n\t" "s_waitcnt lgkmcnt(14)\n\t" "v_mfma_f32_16x16x32_bf16 v[140:143], v[108:111], v[152:155], v[140:143]\n\t" \
  "v_mfma_f32_16x16x32_bf16 v[144:147], v[112:115], v[152:155], v[144:147]\n\t" "v_mfma_f32_16x16x32_bf16 v[148:151], v[116:119], v[152:155], v[148:151]\n\t" "s_waitcnt vmcnt(2)\n\t" \
  "v_cmp_eq_u32 vcc, -1, v156\n\t" "s_cbranch_vccnz .Lso5_%=\n\t" ".Lro5_%=:\n\t" "v_mfma_f32_16x16x32_bf16 v[136:139], v[120:123], v[156:159], v[136:139]\n\t" \
  "global_load_dwordx4 v[152:155], v32, s[64:65] offset:1024 sc1\n\t" "v_mfma_f32_16x16x32_bf16 v[140:143], v[124:127], v[156:159], v[140:143]\n\t" \
  "v_mfma_f32_16x16x32_bf16 v[144:147], v[128:131], v[156:159], v[144:147]\n\t" "v_mfma_f32_16x16x32_bf16 v[148:151], v[132:135], v[156:159], v[148:151]\n\t" "s_waitcnt vmcnt(2)\n\t" \
  "v_cmp_eq_u32 vcc, -1, v160\n\t" "s_cbranch_vccnz .Lso6_%=\n\t" ".Lro6_%=:\n\t" "v_mfma_f32_16x16x32_bf16 v[136:139], v[40:43], v[160:163], v[136:139]\n\t" \
  "global_load_dwordx4 v[156:159], v32, s[64:65] offset:2048 sc1\n\t" "v_mfma_f32_16x16x32_bf16 v[140:143], v[44:47], v[160:163], v[140:143]\n\t" \
  "v_mfma_f32_16x16x32_bf16 v[144:147], v[48:51], v[160:163], v[144:147]\n\t" "v_mfma_f32_16x16x32_bf16 v[148:151], v[52:55], v[160:163], v[148:151]\n\t" "s_waitcnt vmcnt(2)\n\t" \
  "v_cmp_eq_u32 vcc, -1, v164\n\t" "s_cbranch_vccnz .Lso7_%=\n\t" ".Lro7_%=:\n\t" "v_mfma_f32_16x16x32_bf16 v[136:139], v[56:59], v[164:167], v[136:139]\n\t" \
  "v_mfma_f32_16x16x32_bf16 v[140:143], v[60:63], v[164:167], v[140:143]\n\t" "v_mfma_f32_16x16x32_bf16 v[144:147], v[64:67], v[164:167], v[144:147]\n\t" \
  "v_mfma_f32_16x16x32_bf16 v[148:151], v[68:71], v[164:167], v[148:151]\n\t" "s_waitcnt vmcnt(1)\n\t" "v_cmp_eq_u32 vcc, -1, v152\n\t" "s_cbranch_vccnz .Lso8_%=\n\t" ".Lro8_%=:\n\t" \
  "v_mfma_f32_16x16x32_bf16 v[136:139], v[72:75], v[152:155], v[136:139]\n\t" "s_waitcnt lgkmcnt(12)\n\t" "v_mfma_f32_16x16x32_bf16 v[140:143], v[76:79], v[152:155], v[140:143]\n\t" \
  "s_waitcnt lgkmcnt(10)\n\t" "v_mfma_f32_16x16x32_bf16 v[144:147], v[80:83], v[152:155], v[144:147]\n\t" "s_waitcnt lgkmcnt(8)\n\t" \
  "v_mfma_f32_16x16x32_bf16 v[148:151], v[84:87], v[152:155], v[148:151]\n\t" "s_waitcnt vmcnt(0)\n\t" "v_cmp_eq_u32 vcc, -1, v156\n\t" "s_cbranch_vccnz .Lso9_%=\n\t" ".Lro9_%=:\n\t" \
  "s_waitcnt lgkmcnt(2)\n\t" "v_mfma_f32_16x16x32_bf16 v[144:147], v[96:99], v[156:159], v[144:147]\n\t" "s_waitcnt lgkmcnt(0)\n\t" \
  "v_mfma_f32_16x16x32_bf16 v[148:151], v[100:103], v[156:159], v[148:151]\n\t" "v_mfma_f32_16x16x32_bf16 v[136:139], v[88:91], v[156:159], v[136:139]\n\t" \
  "v_mfma_f32_16x16x32_bf16 v[140:143], v[92:95], v[156:159], v[140:143]\n\t" "s_nop 3\n\t" "ds_write_b128 v24, v[144:147] offset:36864\n\t" "s_nop 0\n\t" \
  "ds_write_b128 v24, v[148:151] offset:37888\n\t" "s_mov_b64 exec, 1\n\t" "ds_add_u32 v26, v38 offset:48\n\t" "s_mov_b64 exec, -1\n\t" ".Lp_%=:\n\t" "ds_read_b32 v39, v26 offset:48\n\t" \
  "ds_read_b128 v[40:43], v25 offset:36864\n\t" "ds_read_b128 v[44:47], v25 offset:37888\n\t" "s_sub_u32 s39, s39, 1\n\t" "s_cmp_lt_i32 s39, 0\n\t" "s_waitcnt lgkmcnt(2)\n\t" \
  "v_readfirstlane_b32 s38, v39\n\t" "s_cselect_b32 s38, 2, s38\n\t" "s_cmp_ge_u32 s38, 2\n\t" "s_waitcnt lgkmcnt(0)\n\t" "s_cbranch_scc0 .Lp_%=\n\t" \
  "v_pk_add_f32 v[136:137], v[136:137], v[40:41]\n\t" "v_pk_add_f32 v[138:139], v[138:139], v[42:43]\n\t" "v_pk_add_f32 v[140:141], v[140:141], v[44:45]\n\t" \
  "v_pk_add_f32 v[142:143], v[142:143], v[46:47]\n\t" "v_cvt_pk_bf16_f32 v12, v136, v137\n\t" "v_cvt_pk_bf16_f32 v13, v138, v139\n\t" "v_cvt_pk_bf16_f32 v14, v140, v141\n\t" \
  "v_cvt_pk_bf16_f32 v15, v142, v143\n\t" "v_dot2_f32_bf16 v40, v12, s40, 0\n\t" "v_dot2_f32_bf16 v41, v12, v12, 0\n\t" "v_dot2_f32_bf16 v42, v12, v16, 0\n\t" "v_dot2_f32_bf16 v40, v13, s40, v40\n\t" \
  "v_dot2_f32_bf16 v41, v13, v13, v41\n\t" "v_dot2_f32_bf16 v42, v13, v17, v42\n\t" "v_dot2_f32_bf16 v40, v14, s40, v40\n\t" "v_dot2_f32_bf16 v41, v14, v14, v41\n\t" \
  "v_dot2_f32_bf16 v42, v14, v18, v42\n\t" "v_dot2_f32_bf16 v40, v15, s40, v40\n\t" "v_dot2_f32_bf16 v41, v15, v15, v41\n\t" "v_dot2_f32_bf16 v43, v15, v19, v42\n\t" \
  "v_dot2_f32_bf16 v42, v15, v19, v42\n\t" "ds_write2_b64 v27, v[12:13], v[14:15] offset1:4\n\t" "s_nop 0\n\t" "v_permlane32_swap_b32 v40, v41\n\t" "v_permlane32_swap_b32 v42, v43\n\t" \
  "v_add_f32 v40, v40, v41\n\t" "v_add_f32 v42, v42, v43\n\t" "s_nop 1\n\t" "v_permlane16_swap_b32 v40, v42\n\t" "v_add_f32 v40, v40, v42\n\t" "ds_write_b32 v28, v40 offset:256\n\t" \
  "s_mov_b64 exec, 1\n\t" "ds_add_rtn_u32 v39, v30, v38 offset:16\n\t" "s_mov_b64 exec, -1\n\t" "ds_read_b128 v[44:47], v29 offset:256\n\t" "ds_read_b128 v[48:51], v29 offset:1792\n\t" \
  "ds_read_b32 v52, v37 offset:40\n\t" "s_waitcnt lgkmcnt(3)\n\t" "v_readfirstlane_b32 s38, v39\n\t" "s_waitcnt lgkmcnt(0)\n\t" "s_cmp_lg_u32 s38, 5\n\t" "s_setprio 0\n\t" "s_cbranch_scc1 .Lnk_%=\n\t" \
  "s_setprio 3\n\t" "v_pk_add_f32 v[44:45], v[44:45], v[48:49]\n\t" "v_add_f32 v46, v46, v50\n\t" "s_mov_b32 exec_hi, 0xffff\n\t" "global_store_dwordx3 v21, v[44:46], s[44:45] sc1\n\t" \
  "s_mov_b32 exec_hi, -1\n\t" "s_memrealtime s[76:77]\n\t" "s_mul_i32 s50, s34, 48\n\t" "v_lshl_add_u32 v4, s41, 6, v20\n\t" "v_add_u32 v1, s50, v20\n\t" "s_cmp_eq_u32 s29, 0\n\t" \
  "s_cselect_b32 s51, 6, 7\n\t" "v_cmp_le_u32 vcc, s51, v1\n\t" "s_mov_b32 s51, 155648\n\t" "v_lshl_add_u32 v1, v1, 3, s51\n\t" "v_mov_b32 v3, 0xfffff000\n\t" "s_lshl_b32 s51, s41, 7\n\t" \
  "v_cndmask_b32 v3, 0, v3, vcc\n\t" "v_lshl_add_u32 v5, v9, 3, s51\n\t" "v_lshlrev_b32 v7, 2, v8\n\t" "s_mul_i32 s50, s50, 144\n\t" "v_and_b32 v6, 0x7c, v7\n\t" "v_add_u32 v6, s50, v6\n\t" \
  "s_mul_i32 s50, s28, 6144\n\t" "s_add_u32 s50, s50, 5302784\n\t" "s_add_u32 s46, s24, s50\n\t" "s_addc_u32 s47, s25, 0\n\t" "s_mul_i32 s50, s41, 10\n\t" "s_add_u32 s50, s50, s28\n\t" \
  "s_lshl_b32 s50, s50, 8\n\t" "s_add_u32 s50, s50, 5123584\n\t" "s_add_u32 s48, s24, s50\n\t" "s_addc_u32 s49, s25, 0\n\t" "s_mov_b32 s50, 0x3acccccd\n\t" ".Ltk_%=:\n\t" "s_memrealtime s[78:79]\n\t" \
  "s_waitcnt lgkmcnt(0)\n\t" "s_sub_u32 s78, s78, s76\n\t" "s_cmp_lt_u32 s78, 40\n\t" "s_cbranch_scc1 .Ltk_%=\n\t" ".Lkp_%=:\n\t" "global_load_dwordx3 v[60:62], v21, s[42:43] offset:-2560 sc1\n\t" \
  "global_load_dwordx3 v[64:66], v21, s[42:43] offset:-1920 sc1\n\t" "global_load_dwordx3 v[68:70], v21, s[42:43] offset:-1280 sc1\n\t" \
  "global_load_dwordx3 v[72:74], v21, s[42:43] offset:-640 sc1\n\t" "global_load_dwordx3 v[76:78], v21, s[42:43] offset:0 sc1\n\t" "global_load_dwordx3 v[80:82], v21, s[42:43] offset:640 sc1\n\t" \
  "global_load_dwordx3 v[84:86], v21, s[42:43] offset:1280 sc1\n\t" "global_load_dwordx3 v[88:90], v21, s[42:43] offset:1920 sc1\n\t" "global_load_dwordx3 v[92:94], v21, s[42:43] offset:2560 sc1\n\t" \
  "global_load_dwordx3 v[96:98], v21, s[42:43] offset:3200 sc1\n\t" "s_sub_u32 s39, s39, 1\n\t" "s_cmp_lt_i32 s39, 0\n\t" "s_waitcnt vmcnt(0)\n\t" "v_pk_add_f32 v[60:61], v[60:61], v[64:65]\n\t" \
  "v_add_f32 v62, v62, v66\n\t" "v_pk_add_f32 v[68:69], v[68:69], v[72:73]\n\t" "v_add_f32 v70, v70, v74\n\t" "v_pk_add_f32 v[76:77], v[76:77], v[80:81]\n\t" "v_add_f32 v78, v78, v82\n\t" \
  "v_pk_add_f32 v[84:85], v[84:85], v[88:89]\n\t" "v_add_f32 v86, v86, v90\n\t" "v_pk_add_f32 v[92:93], v[92:93], v[96:97]\n\t" "v_add_f32 v94, v94, v98\n\t" \
  "v_pk_add_f32 v[60:61], v[60:61], v[68:69]\n\t" "v_add_f32 v62, v62, v70\n\t" "v_pk_add_f32 v[76:77], v[76:77], v[84:85]\n\t" "v_add_f32 v78, v78, v86\n\t" \
  "v_pk_add_f32 v[60:61], v[60:61], v[92:93]\n\t" "v_add_f32 v62, v62, v94\n\t" "v_pk_add_f32 v[60:61], v[60:61], v[76:77]\n\t" "v_add_f32 v62, v62, v78\n\t" "v_mul_f32 v60, s50, v60\n\t" \
  "v_mul_f32 v63, v60, v60\n\t" "v_fma_f32 v61, -v60, v52, v61\n\t" "v_fma_f32 v62, v62, s50, -v63\n\t" "s_cbranch_scc1 .Lkq_%=\n\t" "v_cmp_u_f32 vcc, v61, v62\n\t" "s_cbranch_vccnz .Lkp_%=\n\t" \
  ".Lkq_%=:\n\t" "v_max_f32 v62, 0, v62\n\t" "v_add_f32 v63, 0x3727c5ac, v62\n\t" "v_rsq_f32 v63, v63\n\t" "v_cvt_pk_bf16_f32 v66, v62, v60\n\t" "v_mul_f32 v61, v61, v63\n\t" \
  "v_ashrrev_i32 v63, 31, v61\n\t" "v_or_b32 v63, 0x80000000, v63\n\t" "v_xor_b32 v61, v61, v63\n\t" "v_and_or_b32 v61, v61, v3, v4\n\t" "v_lshlrev_b32 v67, 16, v66\n\t" \
  "v_and_b32 v66, 0xffff0000, v66\n\t" "v_max_u32_dpp v64, v61, v61 quad_perm:[1,0,3,2] row_mask:0xf bank_mask:0xf\n\t" "v_add_f32 v67, 0x3727c5ac, v67\n\t" "v_cvt_pk_bf16_f32 v67, v67, v67\n\t" \
  "v_max_u32_dpp v64, v64, v64 quad_perm:[2,3,0,1] row_mask:0xf bank_mask:0xf\n\t" "v_lshlrev_b32 v67, 16, v67\n\t" "v_sqrt_f32 v67, v67\n\t" \
  "v_max_u32_dpp v64, v64, v64 row_half_mirror row_mask:0xf bank_mask:0xf\n\t" "s_nop 0\n\t" "v_cvt_pk_bf16_f32 v67, v67, v67\n\t" \
  "v_max_u32_dpp v64, v64, v64 row_mirror row_mask:0xf bank_mask:0xf\n\t" "v_lshlrev_b32 v67, 16, v67\n\t" "v_rcp_f32 v67, v67\n\t" \
  "v_max_u32_dpp v64, v64, v64 row_bcast:15 row_mask:0xa bank_mask:0xf\n\t" "s_nop 1\n\t" "v_max_u32_dpp v64, v64, v64 row_bcast:31 row_mask:0xc bank_mask:0xf\n\t" "v_mov_b32 v65, v64\n\t" \
  "s_mov_b32 exec_lo, 0\n\t" "s_mov_b32 exec_hi, 0xffff0000\n\t" "global_store_dwordx2 v5, v[64:65], s[46:47] sc1\n\t" "s_mov_b64 exec, -1\n\t" "v_readlane_b32 s50, v64, 63\n\t" \
  "s_and_b32 s51, s50, 63\n\t" "s_mul_i32 s52, s51, 144\n\t" "v_add_u32 v6, s52, v6\n\t" "s_or_b32 s50, s50, 0x1000\n\t" "ds_read_b32 v68, v6 offset:0\n\t" "v_mov_b32 v64, s50\n\t" \
  "ds_write_b64 v1, v[66:67]\n\t" "ds_write_b32 v30, v64 offset:24\n\t" "v_readlane_b32 s52, v66, s51\n\t" "v_readlane_b32 s53, v67, s51\n\t" "s_waitcnt lgkmcnt(2)\n\t" \
  "v_lshlrev_b32 v70, 16, v68\n\t" "v_and_b32 v71, 0xffff0000, v68\n\t" "v_subrev_f32 v70, s52, v70\n\t" "v_subrev_f32 v71, s52, v71\n\t" "v_cvt_pk_bf16_f32 v71, v70, v71\n\t" \
  "v_lshlrev_b32 v70, 16, v71\n\t" "v_and_b32 v71, 0xffff0000, v71\n\t" "v_mul_f32 v70, s53, v70\n\t" "v_mul_f32 v71, s53, v71\n\t" "s_mov_b32 exec_hi, 0\n\t" "v_cvt_pk_bf16_f32 v68, v70, v71\n\t" \
  "s_mov_b32 exec_hi, -1\n\t" "global_store_dword v7, v68, s[48:49] sc1\n\t" "s_setprio 0\n\t" ".Lnk_%=:\n\t" "v_lshrrev_b32 v113, 3, v8\n\t" "s_lshl_b32 s40, s26, 3\n\t" "v_add_u32 v2, s40, v113\n\t" \
  "s_mov_b32 s76, 155648\n\t" "v_lshl_add_u32 v1, v2, 3, s76\n\t" "s_cmp_eq_u32 s29, 0\n\t" "s_cselect_b32 s76, 6, 7\n\t" "v_cmp_le_u32 s[72:73], s76, v2\n\t" "v_add_u32 v2, s30, v2\n\t" \
  "v_sub_u32 v2, v2, 6 clamp\n\t" "v_lshrrev_b32 v3, 10, v2\n\t" "v_lshl_or_b32 v2, v2, 1, v3\n\t" "v_and_b32 v2, 0x7ff, v2\n\t" "v_and_b32 v3, 0x70, v32\n\t" "s_lshl_b32 s76, s28, 7\n\t" \
  "v_add_u32 v3, s76, v3\n\t" "s_movk_i32 s76, 1280\n\t" "v_mad_u32_u24 v33, v2, s76, v3\n\t" "s_mul_i32 s76, s26, 1152\n\t" "v_mad_u32_u24 v3, v113, 16, v32\n\t" "v_add_u32 v3, s76, v3\n\t" \
  "s_lshl_b32 s76, s35, 3\n\t" "v_add_u32 v113, s76, v113\n\t" "s_branch .Lw1_%=\n\t" ".Lw_%=:\n\t" "s_sleep 2\n\t" ".Lw1_%=:\n\t" "ds_read_b32 v112, v30 offset:24\n\t" \
  "ds_read_b64 v[110:111], v1\n\t" "ds_read_b128 v[12:15], v3 offset:0\n\t" "s_sub_u32 s39, s39, 1\n\t" "s_cmp_lt_i32 s39, 0\n\t" "s_waitcnt lgkmcnt(0)\n\t" "v_readfirstlane_b32 s66, v112\n\t" \
  "s_cselect_b32 s66, 1, s66\n\t" "s_cmp_eq_u32 s66, 0\n\t" "s_cbranch_scc1 .Lw_%=\n\t" "s_and_b32 s76, s66, 63\n\t" "v_cmp_eq_u32 s[68:69], s76, v113\n\t" "v_lshlrev_b32 v120, 16, v12\n\t" \
  "v_and_b32 v121, 0xffff0000, v12\n\t" "v_lshlrev_b32 v122, 16, v13\n\t" "v_and_b32 v123, 0xffff0000, v13\n\t" "v_lshlrev_b32 v124, 16, v14\n\t" "v_and_b32 v125, 0xffff0000, v14\n\t" \
  "v_lshlrev_b32 v126, 16, v15\n\t" "v_and_b32 v127, 0xffff0000, v15\n\t" "v_pk_add_f32 v[128:129], v[120:121], v[110:111] op_sel_hi:[1,0] neg_lo:[0,1] neg_hi:[0,1]\n\t" \
  "v_pk_add_f32 v[130:131], v[122:123], v[110:111] op_sel_hi:[1,0] neg_lo:[0,1] neg_hi:[0,1]\n\t" "v_pk_add_f32 v[132:133], v[124:125], v[110:111] op_sel_hi:[1,0] neg_lo:[0,1] neg_hi:[0,1]\n\t" \
  "v_pk_add_f32 v[134:135], v[126:127], v[110:111] op_sel_hi:[1,0] neg_lo:[0,1] neg_hi:[0,1]\n\t" "v_cvt_pk_bf16_f32 v129, v128, v129\n\t" "v_cvt_pk_bf16_f32 v131, v130, v131\n\t" \
  "v_cvt_pk_bf16_f32 v133, v132, v133\n\t" "v_cvt_pk_bf16_f32 v135, v134, v135\n\t" "v_lshlrev_b32 v128, 16, v129\n\t" "v_and_b32 v129, 0xffff0000, v129\n\t" "v_lshlrev_b32 v130, 16, v131\n\t" \
  "v_and_b32 v131, 0xffff0000, v131\n\t" "v_lshlrev_b32 v132, 16, v133\n\t" "v_and_b32 v133, 0xffff0000, v133\n\t" "v_lshlrev_b32 v134, 16, v135\n\t" "v_and_b32 v135, 0xffff0000, v135\n\t" \
  "v_pk_mul_f32 v[128:129], v[128:129], v[110:111] op_sel:[0,1] op_sel_hi:[1,1]\n\t" "v_pk_mul_f32 v[130:131], v[130:131], v[110:111] op_sel:[0,1] op_sel_hi:[1,1]\n\t" \
  "v_pk_mul_f32 v[132:133], v[132:133], v[110:111] op_sel:[0,1] op_sel_hi:[1,1]\n\t" "v_pk_mul_f32 v[134:135], v[134:135], v[110:111] op_sel:[0,1] op_sel_hi:[1,1]\n\t" \
  "v_cvt_pk_bf16_f32 v129, v128, v129\n\t" "v_cvt_pk_bf16_f32 v131, v130, v131\n\t" "v_cvt_pk_bf16_f32 v133, v132, v133\n\t" "v_cvt_pk_bf16_f32 v135, v134, v135\n\t" "v_lshlrev_b32 v128, 16, v129\n\t" \
  "v_and_b32 v129, 0xffff0000, v129\n\t" "v_lshlrev_b32 v130, 16, v131\n\t" "v_and_b32 v131, 0xffff0000, v131\n\t" "v_lshlrev_b32 v132, 16, v133\n\t" "v_and_b32 v133, 0xffff0000, v133\n\t" \
  "v_lshlrev_b32 v134, 16, v135\n\t" "v_and_b32 v135, 0xffff0000, v135\n\t" "v_pk_add_f32 v[128:129], v[128:129], v[120:121]\n\t" "v_pk_add_f32 v[130:131], v[130:131], v[122:123]\n\t" \
  "v_pk_add_f32 v[132:133], v[132:133], v[124:125]\n\t" "v_pk_add_f32 v[134:135], v[134:135], v[126:127]\n\t" "v_cvt_pk_bf16_f32 v116, v128, v129\n\t" "v_cvt_pk_bf16_f32 v117, v130, v131\n\t" \
  "v_cvt_pk_bf16_f32 v118, v132, v133\n\t" "v_cvt_pk_bf16_f32 v119, v134, v135\n\t" "s_sleep 16\n\t" "s_andn2_b64 exec, s[72:73], s[68:69]\n\t" "s_cbranch_scc0 .Ln1_%=\n\t" \
  "global_store_dwordx4 v33, v[116:119], s[22:23] sc0 sc1 nt\n\t" ".Ln1_%=:\n\t" "s_mov_b64 exec, -1\n\t" "s_cmp_eq_u32 s26, 11\n\t" "s_cbranch_scc1 .Lrz_%=\n\t" "s_cmp_eq_u64 s[68:69], 0\n\t" \
  "s_cbranch_scc1 .Lend_%=\n\t" ".Lb6_%=:\n\t" "s_waitcnt lgkmcnt(0)\n\t" "s_barrier\n\t" "ds_read_b32 v39, v37 offset:32\n\t" "s_waitcnt lgkmcnt(0)\n\t" "v_readfirstlane_b32 s76, v39\n\t" \
  "s_xor_b32 s76, s76, s66\n\t" "s_and_b32 s76, s76, 4095\n\t" "s_cbranch_scc0 .Lend_%=\n\t" "s_and_b64 exec, s[72:73], s[68:69]\n\t" "s_cbranch_scc0 .Ln2_%=\n\t" \
  "global_store_dwordx4 v33, v[116:119], s[22:23] sc0 sc1 nt\n\t" ".Ln2_%=:\n\t" "s_mov_b64 exec, -1\n\t" "s_branch .Lend_%=\n\t" ".Lrz_%=:\n\t" "s_sleep 35\n\t" ".Lrt_%=:\n\t" \
  "s_mul_i32 s40, s28, 6144\n\t" "s_add_u32 s40, s40, 5302784\n\t" "v_lshlrev_b32 v2, 7, v8\n\t" "s_add_u32 s40, s24, s40\n\t" "s_addc_u32 s41, s25, 0\n\t" "v_mov_b32 v40, -1\n\t" \
  "v_mov_b32 v41, -1\n\t" "v_mov_b32 v42, -1\n\t" "s_mov_b32 exec_hi, 0x3fff\n\t" "global_load_dword v40, v2, s[40:41] sc1\n\t" "s_sleep 2\n\t" "global_load_dword v41, v2, s[40:41] sc1\n\t" \
  "s_sleep 2\n\t" "global_load_dword v42, v2, s[40:41] sc1\n\t" "s_mov_b32 exec_hi, -1\n\t" "v_mov_b32 v43, 0xfc0\n\t" "s_mov_b32 s42, 0\n\t" "s_mov_b32 s43, 0xffff0000\n\t" \
  "v_add_u32 v3, 64, v37\n\t" "v_cndmask_b32 v3, v3, v37, s[42:43]\n\t" "s_mov_b32 s43, 0xffffc000\n\t" ".Lrl_%=:\n\t" "s_waitcnt vmcnt(2)\n\t" "v_cmp_ne_u32 vcc, -1, v40\n\t" \
  "s_or_b64 s[42:43], s[42:43], vcc\n\t" "s_cmp_eq_u64 s[42:43], -1\n\t" "v_cndmask_b32 v43, v43, v40, vcc\n\t" "s_cselect_b32 s44, 1, 0\n\t" "v_mov_b32 v45, s44\n\t" \
  "v_max_u32_dpp v44, v43, v43 quad_perm:[1,0,3,2] row_mask:0xf bank_mask:0xf\n\t" "s_nop 1\n\t" "v_max_u32_dpp v44, v44, v44 quad_perm:[2,3,0,1] row_mask:0xf bank_mask:0xf\n\t" "s_nop 1\n\t" \
  "v_max_u32_dpp v44, v44, v44 row_half_mirror row_mask:0xf bank_mask:0xf\n\t" "s_nop 1\n\t" "v_max_u32_dpp v44, v44, v44 row_mirror row_mask:0xf bank_mask:0xf\n\t" "s_nop 1\n\t" \
  "v_max_u32_dpp v44, v44, v44 row_bcast:15 row_mask:0xa bank_mask:0xf\n\t" "s_nop 1\n\t" "v_max_u32_dpp v44, v44, v44 row_bcast:31 row_mask:0xc bank_mask:0xf\n\t" \
  "ds_write_b64 v3, v[44:45] offset:32\n\t" "s_cbranch_scc1 .Lre_%=\n\t" "s_cmp_lg_u32 s75, 0\n\t" "s_cbranch_scc1 .Lrz0_%=\n\t" "s_sleep 5\n\t" ".Lrz0_%=:\n\t" "s_not_b64 exec, s[42:43]\n\t" \
  "global_load_dword v40, v2, s[40:41] sc1\n\t" "s_mov_b64 exec, -1\n\t" "s_waitcnt vmcnt(2)\n\t" "v_cmp_ne_u32 vcc, -1, v41\n\t" "s_or_b64 s[42:43], s[42:43], vcc\n\t" "s_cmp_eq_u64 s[42:43], -1\n\t" \
  "v_cndmask_b32 v43, v43, v41, vcc\n\t" "s_cselect_b32 s44, 1, 0\n\t" "v_mov_b32 v45, s44\n\t" "v_max_u32_dpp v44, v43, v43 quad_perm:[1,0,3,2] row_mask:0xf bank_mask:0xf\n\t" "s_nop 1\n\t" \
  "v_max_u32_dpp v44, v44, v44 quad_perm:[2,3,0,1] row_mask:0xf bank_mask:0xf\n\t" "s_nop 1\n\t" "v_max_u32_dpp v44, v44, v44 row_half_mirror row_mask:0xf bank_mask:0xf\n\t" "s_nop 1\n\t" \
  "v_max_u32_dpp v44, v44, v44 row_mirror row_mask:0xf bank_mask:0xf\n\t" "s_nop 1\n\t" "v_max_u32_dpp v44, v44, v44 row_bcast:15 row_mask:0xa bank_mask:0xf\n\t" "s_nop 1\n\t" \
  "v_max_u32_dpp v44, v44, v44 row_bcast:31 row_mask:0xc bank_mask:0xf\n\t" "ds_write_b64 v3, v[44:45] offset:32\n\t" "s_cbranch_scc1 .Lre_%=\n\t" "s_cmp_lg_u32 s75, 0\n\t" \
  "s_cbranch_scc1 .Lrz1_%=\n\t" "s_sleep 5\n\t" ".Lrz1_%=:\n\t" "s_not_b64 exec, s[42:43]\n\t" "global_load_dword v41, v2, s[40:41] sc1\n\t" "s_mov_b64 exec, -1\n\t" "s_waitcnt vmcnt(2)\n\t" \
  "v_cmp_ne_u32 vcc, -1, v42\n\t" "s_or_b64 s[42:43], s[42:43], vcc\n\t" "s_cmp_eq_u64 s[42:43], -1\n\t" "v_cndmask_b32 v43, v43, v42, vcc\n\t" "s_cselect_b32 s44, 1, 0\n\t" "v_mov_b32 v45, s44\n\t" \
  "v_max_u32_dpp v44, v43, v43 quad_perm:[1,0,3,2] row_mask:0xf bank_mask:0xf\n\t" "s_nop 1\n\t" "v_max_u32_dpp v44, v44, v44 quad_perm:[2,3,0,1] row_mask:0xf bank_mask:0xf\n\t" "s_nop 1\n\t" \
  "v_max_u32_dpp v44, v44, v44 row_half_mirror row_mask:0xf bank_mask:0xf\n\t" "s_nop 1\n\t" "v_max_u32_dpp v44, v44, v44 row_mirror row_mask:0xf bank_mask:0xf\n\t" "s_nop 1\n\t" \
  "v_max_u32_dpp v44, v44, v44 row_bcast:15 row_mask:0xa bank_mask:0xf\n\t" "s_nop 1\n\t" "v_max_u32_dpp v44, v44, v44 row_bcast:31 row_mask:0xc bank_mask:0xf\n\t" \
  "ds_write_b64 v3, v[44:45] offset:32\n\t" "s_cbranch_scc1 .Lre_%=\n\t" "s_cmp_lg_u32 s75, 0\n\t" "s_cbranch_scc1 .Lrz2_%=\n\t" "s_sleep 5\n\t" ".Lrz2_%=:\n\t" "s_not_b64 exec, s[42:43]\n\t" \
  "global_load_dword v42, v2, s[40:41] sc1\n\t" "s_mov_b64 exec, -1\n\t" "s_sub_u32 s39, s39, 1\n\t" "s_cmp_lt_i32 s39, 0\n\t" "s_cbranch_scc0 .Lrl_%=\n\t" ".Lre_%=:\n\t" "s_cmp_eq_u32 s75, 0\n\t" \
  "s_cbranch_scc1 .Lb6_%=\n\t" "s_waitcnt lgkmcnt(0)\n\t" "s_barrier\n\t" "s_barrier\n\t" "s_cmp_lg_u32 s29, 0\n\t" "s_cbranch_scc1 .Lend_%=\n\t" "v_mov_b32 v1, 0\n\t" "s_mov_b64 exec, 1\n\t" \
  "global_store_dwordx2 v1, v[16:17], s[24:25] sc1\n\t" "s_mov_b64 exec, -1\n\t" "s_branch .Lend_%=\n\t" ".Lsp_%=:\n\t" "v_mov_b32 v1, 0\n\t" "s_add_u32 s40, s28, 16\n\t" \
  "s_xor_b32 s41, %[bid], 2\n\t" "s_sub_u32 s41, s41, 240\n\t" "s_cselect_b32 s29, s40, s41\n\t" "v_lshlrev_b32 v32, 4, v8\n\t" "v_and_b32 v9, 15, v8\n\t" "v_mov_b32 v37, 156416\n\t" \
  "v_mov_b32 v38, 1\n\t" "v_mov_b32 v44, -1\n\t" "v_mov_b32 v45, -1\n\t" "v_mov_b32 v46, -1\n\t" "v_mov_b32 v47, -1\n\t" "s_mov_b32 s75, 1\n\t" "s_mov_b32 s39, 400000\n\t" "s_add_u32 s45, s29, 4\n\t" \
  "s_lshr_b32 s45, s45, 3\n\t" "s_sub_u32 s45, s29, s45\n\t" "s_and_b32 s48, s29, 7\n\t" "s_mul_i32 s46, s45, 232\n\t" "s_movk_i32 s47, 231\n\t" "s_mul_i32 s40, s45, 764\n\t" \
  "s_sub_u32 s40, s40, 11172\n\t" "s_cmp_ge_u32 s29, 24\n\t" "s_cselect_b32 s46, s40, s46\n\t" "s_cselect_b32 s47, 763, s47\n\t" "v_min_u32 v5, s47, v0\n\t" "v_add_u32 v5, s46, v5\n\t" \
  "v_min_u32 v5, 6399, v5\n\t" "v_lshlrev_b32 v5, 7, v5\n\t" "s_lshr_b32 s47, s47, 6\n\t" "s_mul_i32 s40, s45, 12\n\t" "s_add_u32 s40, s40, s26\n\t" "s_lshl_b32 s41, s40, 10\n\t" \
  "s_add_u32 s41, s41, 3416064\n\t" "s_lshl_b32 s44, s45, 7\n\t" "s_add_u32 s44, s44, 4096\n\t" "v_lshl_add_u32 v4, v9, 3, s44\n\t" "s_waitcnt lgkmcnt(0)\n\t" \
  "global_load_dwordx2 v[40:41], v1, s[24:25] sc1\n\t" "s_add_u32 s42, s24, s41\n\t" "s_addc_u32 s43, s25, 0\n\t" "v_lshl_add_u32 v3, v8, 2, v37\n\t" "s_movk_i32 s44, 0xfc0\n\t" "v_mov_b32 v2, 0\n\t" \
  "v_writelane_b32 v2, s44, 8\n\t" "ds_write_b32 v3, v2\n\t" "s_mul_i32 s50, s29, 81920\n\t" "s_mul_i32 s54, s33, 20480\n\t" "s_add_u32 s50, s50, s54\n\t" "s_add_u32 s54, s54, 65536\n\t" \
  "s_add_u32 s52, s20, s50\n\t" "s_addc_u32 s53, s21, 0\n\t" "s_memrealtime s[56:57]\n\t" "s_mov_b32 m0, 163072\n\t" "s_cmp_eq_u32 s48, 3\n\t" "s_cbranch_scc1 .Lnf_%=\n\t" ".Lts2_%=:\n\t" \
  "s_sleep 2\n\t" "s_memrealtime s[78:79]\n\t" "s_waitcnt lgkmcnt(0)\n\t" "s_sub_u32 s78, s78, s56\n\t" "s_cmp_lt_u32 s78, 255\n\t" "s_cbranch_scc1 .Lts2_%=\n\t" ".Lpf0_%=:\n\t" \
  "global_store_dwordx4 v32, v[44:47], s[42:43] sc1\n\t" "s_add_u32 s42, s42, 282624\n\t" "s_addc_u32 s43, s43, 0\n\t" "s_add_u32 s40, s40, 276\n\t" "s_cmp_lt_u32 s40, 1380\n\t" \
  "s_cbranch_scc1 .Lpf0_%=\n\t" ".Lpf1_%=:\n\t" "global_store_dwordx4 v32, v[44:47], s[42:43] sc1\n\t" "s_add_u32 s42, s42, 282624\n\t" "s_addc_u32 s43, s43, 0\n\t" "s_add_u32 s40, s40, 276\n\t" \
  "s_cmp_lt_u32 s40, 1903\n\t" "s_cbranch_scc1 .Lpf1_%=\n\t" "s_waitcnt vmcnt(0)\n\t" "v_add_co_u32 v16, vcc, 1, v40\n\t" "s_cmp_lg_u32 s26, 0\n\t" "s_nop 0\n\t" \
  "v_addc_co_u32 v17, vcc, 0, v41, vcc\n\t" "s_barrier\n\t" "s_cbranch_scc1 .Lnf1_%=\n\t" "global_store_dwordx2 v4, v[16:17], s[24:25] sc1\n\t" ".Lnf1_%=:\n\t" "s_cmp_gt_u32 s26, s47\n\t" \
  "s_cbranch_scc1 .Lnt_%=\n\t" "global_load_lds_dword v5, s[12:13]\n\t" ".Lnt_%=:\n\t" ".Lnf_%=:\n\t" "s_cmp_ge_u32 s29, 24\n\t" "s_cbranch_scc1 .Lend_%=\n\t" "s_cmp_ge_u32 s26, 8\n\t" \
  "s_cbranch_scc1 .Lsq_%=x\n\t" "v_lshrrev_b32 v1, 3, v8\n\t" "v_and_b32 v2, 6, v1\n\t" "v_xor_b32 v2, v2, v8\n\t" "v_and_b32 v2, 7, v2\n\t" "v_mul_u32_u24 v1, 3072, v1\n\t" \
  "v_lshl_add_u32 v1, v2, 4, v1\n\t" "v_add_u32 v17, 23552, v1\n\t" "v_add_u32 v18, 47104, v1\n\t" "v_add_u32 v19, 70656, v1\n\t" "s_cmp_ge_u32 s26, 4\n\t" \
  "s_cselect_b64 s[42:43], s[18:19], s[16:17]\n\t" "s_mul_i32 s40, s33, 491520\n\t" "s_lshl_b32 s41, s29, 7\n\t" "s_add_u32 s40, s40, s41\n\t" "s_add_u32 s42, s42, s40\n\t" \
  "s_addc_u32 s43, s43, 0\n\t" "s_lshl_b32 s44, s26, 13\n\t" "s_add_u32 m0, s44, 0\n\t" "s_nop 0\n\t" "global_load_lds_dwordx4 v1, s[42:43] offset:0 nt\n\t" \
  "global_load_lds_dwordx4 v17, s[42:43] offset:1024 nt\n\t" "global_load_lds_dwordx4 v18, s[42:43] offset:2048 nt\n\t" "global_load_lds_dwordx4 v19, s[42:43] offset:3072 nt\n\t" \
  "s_add_u32 s42, s42, 98304\n\t" "s_addc_u32 s43, s43, 0\n\t" "s_add_u32 m0, s44, 4096\n\t" "s_nop 0\n\t" "global_load_lds_dwordx4 v1, s[42:43] offset:0 nt\n\t" \
  "global_load_lds_dwordx4 v17, s[42:43] offset:1024 nt\n\t" "global_load_lds_dwordx4 v18, s[42:43] offset:2048 nt\n\t" "global_load_lds_dwordx4 v19, s[42:43] offset:3072 nt\n\t" \
  "s_add_u32 s42, s42, 98304\n\t" "s_addc_u32 s43, s43, 0\n\t" "v_lshrrev_b32 v10, 2, v8\n\t" "v_lshlrev_b32 v3, 2, v8\n\t" "v_and_b32 v4, 0x60, v3\n\t" "v_and_b32 v3, 12, v3\n\t" \
  "v_lshl_add_u32 v2, v10, 7, v4\n\t" "v_lshl_add_u32 v2, v3, 1, v2\n\t" "v_mul_u32_u24 v4, 1280, v10\n\t" "v_lshl_add_u32 v4, v3, 1, v4\n\t" "s_mul_i32 s40, s26, 160\n\t" \
  "s_add_u32 s40, s40, 65536\n\t" "v_add_u32 v4, s40, v4\n\t" "v_and_b32 v10, 12, v10\n\t" "v_xor_b32 v12, 0, v2\n\t" "v_add_u32 v12, s44, v12\n\t" "v_xor_b32 v13, 32, v2\n\t" \
  "v_add_u32 v13, s44, v13\n\t" "v_xor_b32 v14, 64, v2\n\t" "v_add_u32 v14, s44, v14\n\t" "v_xor_b32 v15, 96, v2\n\t" "v_add_u32 v15, s44, v15\n\t" "s_waitcnt vmcnt(4)\n\t" \
  "ds_read_b64_tr_b16 v[40:41], v12 offset:0\n\t" "ds_read_b64_tr_b16 v[42:43], v12 offset:2048\n\t" "ds_read_b64_tr_b16 v[44:45], v13 offset:0\n\t" "ds_read_b64_tr_b16 v[46:47], v13 offset:2048\n\t" \
  "ds_read_b64_tr_b16 v[48:49], v14 offset:0\n\t" "ds_read_b64_tr_b16 v[50:51], v14 offset:2048\n\t" "ds_read_b64_tr_b16 v[52:53], v15 offset:0\n\t" "ds_read_b64_tr_b16 v[54:55], v15 offset:2048\n\t" \
  "s_waitcnt lgkmcnt(0)\n\t" "s_add_u32 m0, s44, 0\n\t" "s_nop 0\n\t" "global_load_lds_dwordx4 v1, s[42:43] offset:0 nt\n\t" "global_load_lds_dwordx4 v17, s[42:43] offset:1024 nt\n\t" \
  "global_load_lds_dwordx4 v18, s[42:43] offset:2048 nt\n\t" "global_load_lds_dwordx4 v19, s[42:43] offset:3072 nt\n\t" "s_add_u32 s42, s42, 98304\n\t" "s_addc_u32 s43, s43, 0\n\t" \
  "s_waitcnt vmcnt(4)\n\t" "ds_read_b64_tr_b16 v[56:57], v12 offset:4096\n\t" "ds_read_b64_tr_b16 v[58:59], v12 offset:6144\n\t" "ds_read_b64_tr_b16 v[60:61], v13 offset:4096\n\t" \
  "ds_read_b64_tr_b16 v[62:63], v13 offset:6144\n\t" "ds_read_b64_tr_b16 v[64:65], v14 offset:4096\n\t" "ds_read_b64_tr_b16 v[66:67], v14 offset:6144\n\t" \
  "ds_read_b64_tr_b16 v[68:69], v15 offset:4096\n\t" "ds_read_b64_tr_b16 v[70:71], v15 offset:6144\n\t" "s_waitcnt lgkmcnt(0)\n\t" "s_add_u32 m0, s44, 4096\n\t" "s_nop 0\n\t" \
  "global_load_lds_dwordx4 v1, s[42:43] offset:0 nt\n\t" "global_load_lds_dwordx4 v17, s[42:43] offset:1024 nt\n\t" "global_load_lds_dwordx4 v18, s[42:43] offset:2048 nt\n\t" \
  "global_load_lds_dwordx4 v19, s[42:43] offset:3072 nt\n\t" "s_add_u32 s42, s42, 98304\n\t" "s_addc_u32 s43, s43, 0\n\t" "s_waitcnt vmcnt(4)\n\t" "ds_read_b64_tr_b16 v[72:73], v12 offset:0\n\t" \
  "ds_read_b64_tr_b16 v[74:75], v12 offset:2048\n\t" "ds_read_b64_tr_b16 v[76:77], v13 offset:0\n\t" "ds_read_b64_tr_b16 v[78:79], v13 offset:2048\n\t" "ds_read_b64_tr_b16 v[80:81], v14 offset:0\n\t" \
  "ds_read_b64_tr_b16 v[82:83], v14 offset:2048\n\t" "ds_read_b64_tr_b16 v[84:85], v15 offset:0\n\t" "ds_read_b64_tr_b16 v[86:87], v15 offset:2048\n\t" "s_waitcnt lgkmcnt(0)\n\t" \
  "s_add_u32 m0, s44, 0\n\t" "s_nop 0\n\t" "global_load_lds_dwordx4 v1, s[42:43] offset:0 nt\n\t" "global_load_lds_dwordx4 v17, s[42:43] offset:1024 nt\n\t" \
  "global_load_lds_dwordx4 v18, s[42:43] offset:2048 nt\n\t" "global_load_lds_dwordx4 v19, s[42:43] offset:3072 nt\n\t" "s_barrier\n\t" "v_add_u32 v2, 40960, v4\n\t" \
  "ds_read_b64_tr_b16 v[120:121], v4 offset:0\n\t" "ds_read_b64_tr_b16 v[122:123], v4 offset:20480\n\t" "ds_read_b64_tr_b16 v[124:125], v2 offset:0\n\t" \
  "ds_read_b64_tr_b16 v[126:127], v2 offset:20480\n\t" "ds_read_b64_tr_b16 v[128:129], v4 offset:32\n\t" "ds_read_b64_tr_b16 v[130:131], v4 offset:20512\n\t" \
  "ds_read_b64_tr_b16 v[132:133], v2 offset:32\n\t" "ds_read_b64_tr_b16 v[134:135], v2 offset:20512\n\t" "ds_read_b64_tr_b16 v[136:137], v4 offset:64\n\t" \
  "ds_read_b64_tr_b16 v[138:139], v4 offset:20544\n\t" "ds_read_b64_tr_b16 v[140:141], v2 offset:64\n\t" "ds_read_b64_tr_b16 v[142:143], v2 offset:20544\n\t" \
  "ds_read_b64_tr_b16 v[144:145], v4 offset:96\n\t" "ds_read_b64_tr_b16 v[146:147], v4 offset:20576\n\t" "ds_read_b64_tr_b16 v[148:149], v2 offset:96\n\t" \
  "ds_read_b64_tr_b16 v[150:151], v2 offset:20576\n\t" "ds_read_b64_tr_b16 v[152:153], v4 offset:128\n\t" "ds_read_b64_tr_b16 v[154:155], v4 offset:20608\n\t" \
  "ds_read_b64_tr_b16 v[156:157], v2 offset:128\n\t" "ds_read_b64_tr_b16 v[158:159], v2 offset:20608\n\t" "s_waitcnt vmcnt(4)\n\t" "ds_read_b64_tr_b16 v[88:89], v12 offset:4096\n\t" \
  "ds_read_b64_tr_b16 v[90:91], v12 offset:6144\n\t" "ds_read_b64_tr_b16 v[92:93], v13 offset:4096\n\t" "ds_read_b64_tr_b16 v[94:95], v13 offset:6144\n\t" \
  "ds_read_b64_tr_b16 v[96:97], v14 offset:4096\n\t" "ds_read_b64_tr_b16 v[98:99], v14 offset:6144\n\t" "ds_read_b64_tr_b16 v[100:101], v15 offset:4096\n\t" \
  "ds_read_b64_tr_b16 v[102:103], v15 offset:6144\n\t" "s_waitcnt vmcnt(0)\n\t" "ds_read_b64_tr_b16 v[104:105], v12 offset:0\n\t" "ds_read_b64_tr_b16 v[106:107], v12 offset:2048\n\t" \
  "ds_read_b64_tr_b16 v[108:109], v13 offset:0\n\t" "ds_read_b64_tr_b16 v[110:111], v13 offset:2048\n\t" "ds_read_b64_tr_b16 v[112:113], v14 offset:0\n\t" \
  "ds_read_b64_tr_b16 v[114:115], v14 offset:2048\n\t" "ds_read_b64_tr_b16 v[116:117], v15 offset:0\n\t" "ds_read_b64_tr_b16 v[118:119], v15 offset:2048\n\t" "s_mul_i32 s40, s33, 320\n\t" \
  "s_add_u32 s40, s40, 159744\n\t" "v_lshl_add_u32 v1, v10, 1, s40\n\t" "s_lshl_b32 s40, s26, 8\n\t" "s_add_u32 s40, s40, 161024\n\t" "v_lshl_add_u32 v2, v10, 2, s40\n\t" "v_and_b32 v4, 12, v8\n\t" \
  "v_lshl_add_u32 v4, v4, 2, v10\n\t" "s_mov_b32 s40, 161024\n\t" "v_lshl_add_u32 v4, v4, 2, s40\n\t" "v_add_u32 v3, v3, v4\n\t" "s_mul_i32 s40, s26, 320\n\t" "v_lshl_add_u32 v4, v9, 6, s40\n\t" \
  "v_lshl_add_u32 v4, v10, 2, v4\n\t" "s_mul_i32 s40, s29, 2560\n\t" "s_add_u32 s40, s40, 5241344\n\t" "s_add_u32 s62, s24, s40\n\t" "s_addc_u32 s63, s25, 0\n\t" "s_mov_b32 s40, 0x10001\n\t" \
  "s_mov_b32 s41, s40\n\t" "s_lshl_b64 s[42:43], s[40:41], 1\n\t" "s_lshl_b64 s[44:45], s[40:41], 2\n\t" "s_lshl_b64 s[46:47], s[40:41], 3\n\t" "s_lshl_b64 s[48:49], s[40:41], 4\n\t" \
  "s_mov_b32 s60, 0x1f001f\n\t" "s_mov_b32 s61, s60\n\t" "s_mov_b32 s76, 0xbfb8aa3b\n\t" "s_setprio 3\n\t" "s_waitcnt lgkmcnt(0)\n\t" "s_barrier\n\t" \
  "ds_read2_b64 v[28:31], v1 offset0:0 offset1:4\n\t" "ds_read2_b64 v[32:35], v1 offset0:8 offset1:12\n\t" "ds_read2_b64 v[36:39], v1 offset0:16 offset1:20\n\t" \
  "ds_read2_b64 v[160:163], v1 offset0:24 offset1:28\n\t" "ds_read2_b64 v[164:167], v1 offset0:32 offset1:36\n\t" "s_waitcnt lgkmcnt(4)\n\t" \
  "v_mfma_f32_16x16x32_bf16 v[12:15], v[40:43], v[28:31], 0\n\t" "v_mfma_f32_16x16x32_bf16 v[16:19], v[44:47], v[28:31], 0\n\t" "v_mfma_f32_16x16x32_bf16 v[20:23], v[48:51], v[28:31], 0\n\t" \
  "v_mfma_f32_16x16x32_bf16 v[24:27], v[52:55], v[28:31], 0\n\t" "s_waitcnt lgkmcnt(3)\n\t" "v_mfma_f32_16x16x32_bf16 v[12:15], v[56:59], v[32:35], v[12:15]\n\t" \
  "v_mfma_f32_16x16x32_bf16 v[16:19], v[60:63], v[32:35], v[16:19]\n\t" "v_mfma_f32_16x16x32_bf16 v[20:23], v[64:67], v[32:35], v[20:23]\n\t" \
  "v_mfma_f32_16x16x32_bf16 v[24:27], v[68:71], v[32:35], v[24:27]\n\t" "s_waitcnt lgkmcnt(2)\n\t" "v_mfma_f32_16x16x32_bf16 v[12:15], v[72:75], v[36:39], v[12:15]\n\t" \
  "v_mfma_f32_16x16x32_bf16 v[16:19], v[76:79], v[36:39], v[16:19]\n\t" "v_mfma_f32_16x16x32_bf16 v[20:23], v[80:83], v[36:39], v[20:23]\n\t" \
  "v_mfma_f32_16x16x32_bf16 v[24:27], v[84:87], v[36:39], v[24:27]\n\t" "s_waitcnt lgkmcnt(1)\n\t" "v_mfma_f32_16x16x32_bf16 v[12:15], v[88:91], v[160:163], v[12:15]\n\t" \
  "v_mfma_f32_16x16x32_bf16 v[16:19], v[92:95], v[160:163], v[16:19]\n\t" "v_mfma_f32_16x16x32_bf16 v[20:23], v[96:99], v[160:163], v[20:23]\n\t" \
  "v_mfma_f32_16x16x32_bf16 v[24:27], v[100:103], v[160:163], v[24:27]\n\t" "s_waitcnt lgkmcnt(0)\n\t" "v_mfma_f32_16x16x32_bf16 v[12:15], v[104:107], v[164:167], v[12:15]\n\t" \
  "v_mfma_f32_16x16x32_bf16 v[16:19], v[108:111], v[164:167], v[16:19]\n\t" "v_mfma_f32_16x16x32_bf16 v[20:23], v[112:115], v[164:167], v[20:23]\n\t" \
  "v_mfma_f32_16x16x32_bf16 v[24:27], v[116:119], v[164:167], v[24:27]\n\t" "s_mov_b64 exec, s[40:41]\n\t" "s_nop 3\n\t" "ds_write_b128 v2, v[12:15] offset:0\n\t" \
  "ds_write_b128 v2, v[16:19] offset:64\n\t" "ds_write_b128 v2, v[20:23] offset:128\n\t" "ds_write_b128 v2, v[24:27] offset:192\n\t" "s_mov_b64 exec, -1\n\t" "s_waitcnt lgkmcnt(0)\n\t" "s_barrier\n\t" \
  "ds_read2st64_b32 v[40:41], v3 offset0:0 offset1:4\n\t" "ds_read2st64_b32 v[42:43], v3 offset0:1 offset1:5\n\t" "ds_read2st64_b32 v[44:45], v3 offset0:2 offset1:6\n\t" \
  "ds_read2st64_b32 v[46:47], v3 offset0:3 offset1:7\n\t" "s_waitcnt lgkmcnt(0)\n\t" "v_pk_add_f32 v[40:41], v[40:41], v[42:43]\n\t" "v_pk_add_f32 v[44:45], v[44:45], v[46:47]\n\t" \
  "v_pk_add_f32 v[40:41], v[40:41], v[44:45]\n\t" "v_cvt_pk_bf16_f32 v41, v40, v41\n\t" "v_lshlrev_b32 v40, 16, v41\n\t" "v_mul_f32 v42, s76, v40\n\t" "v_exp_f32 v42, v42\n\t" \
  "v_and_b32 v41, 0xffff0000, v41\n\t" "v_add_f32 v42, 1.0, v42\n\t" "v_rcp_f32 v42, v42\n\t" "s_nop 0\n\t" "v_mul_f32 v40, v40, v42\n\t" "v_cvt_pk_bf16_f32 v40, v40, v40\n\t" \
  "v_lshlrev_b32 v40, 16, v40\n\t" "v_mul_f32 v40, v40, v41\n\t" "s_nop 1\n\t" "v_mov_b32_dpp v41, v40 quad_perm:[1,1,3,3] row_mask:0xf bank_mask:0xf\n\t" "v_cvt_pk_bf16_f32 v40, v40, v41\n\t" \
  "s_nop 1\n\t" "v_mov_b32_dpp v44, v40 row_newbcast:0 row_mask:0xf bank_mask:0xf\n\t" "v_mov_b32_dpp v45, v40 row_newbcast:2 row_mask:0xf bank_mask:0xf\n\t" \
  "v_mov_b32_dpp v46, v40 row_newbcast:4 row_mask:0xf bank_mask:0xf\n\t" "v_mov_b32_dpp v47, v40 row_newbcast:6 row_mask:0xf bank_mask:0xf\n\t" \
  "v_mov_b32_dpp v48, v40 row_newbcast:8 row_mask:0xf bank_mask:0xf\n\t" "v_mov_b32_dpp v49, v40 row_newbcast:10 row_mask:0xf bank_mask:0xf\n\t" \
  "v_mov_b32_dpp v50, v40 row_newbcast:12 row_mask:0xf bank_mask:0xf\n\t" "v_mov_b32_dpp v51, v40 row_newbcast:14 row_mask:0xf bank_mask:0xf\n\t" \
  "v_mfma_f32_16x16x32_bf16 v[56:59], v[120:123], v[44:47], 0\n\t" "s_nop 0\n\t" "v_mfma_f32_16x16x32_bf16 v[56:59], v[124:127], v[48:51], v[56:59]\n\t" \
  "v_mfma_f32_16x16x32_bf16 v[60:63], v[128:131], v[44:47], 0\n\t" "v_mfma_f32_16x16x32_bf16 v[60:63], v[132:135], v[48:51], v[60:63]\n\t" \
  "v_mfma_f32_16x16x32_bf16 v[64:67], v[136:139], v[44:47], 0\n\t" "v_mfma_f32_16x16x32_bf16 v[64:67], v[140:143], v[48:51], v[64:67]\n\t" \
  "v_mfma_f32_16x16x32_bf16 v[68:71], v[144:147], v[44:47], 0\n\t" "v_mfma_f32_16x16x32_bf16 v[68:71], v[148:151], v[48:51], v[68:71]\n\t" \
  "v_mfma_f32_16x16x32_bf16 v[72:75], v[152:155], v[44:47], 0\n\t" "v_mfma_f32_16x16x32_bf16 v[72:75], v[156:159], v[48:51], v[72:75]\n\t" "s_nop 1\n\t" "v_cndmask_b32 v56, v56, v60, s[42:43]\n\t" \
  "v_cndmask_b32 v57, v57, v61, s[42:43]\n\t" "v_cndmask_b32 v58, v58, v62, s[42:43]\n\t" "v_cndmask_b32 v59, v59, v63, s[42:43]\n\t" "v_cndmask_b32 v56, v56, v64, s[44:45]\n\t" \
  "v_cndmask_b32 v57, v57, v65, s[44:45]\n\t" "v_cndmask_b32 v58, v58, v66, s[44:45]\n\t" "v_cndmask_b32 v59, v59, v67, s[44:45]\n\t" "v_cndmask_b32 v56, v56, v68, s[46:47]\n\t" \
  "v_cndmask_b32 v57, v57, v69, s[46:47]\n\t" "v_cndmask_b32 v58, v58, v70, s[46:47]\n\t" "v_cndmask_b32 v59, v59, v71, s[46:47]\n\t" "v_cndmask_b32 v56, v56, v72, s[48:49]\n\t" \
  "v_cndmask_b32 v57, v57, v73, s[48:49]\n\t" "v_cndmask_b32 v58, v58, v74, s[48:49]\n\t" "v_cndmask_b32 v59, v59, v75, s[48:49]\n\t" "s_mov_b64 exec, s[60:61]\n\t" \
  "global_store_dwordx4 v4, v[56:59], s[62:63] sc1\n\t" "s_mov_b64 exec, -1\n\t" "s_branch .Lend_%=\n\t" ".Lsq_%=x:\n\t" "s_movk_i32 s50, 5\n\t" ".Ld3_%=:\n\t" "s_mov_b32 m0, s54\n\t" \
  "s_add_u32 s54, s54, 4096\n\t" "global_load_lds_dwordx4 v32, s[52:53] offset:0 nt\n\t" "global_load_lds_dwordx4 v32, s[52:53] offset:1024 nt\n\t" \
  "global_load_lds_dwordx4 v32, s[52:53] offset:2048 nt\n\t" "global_load_lds_dwordx4 v32, s[52:53] offset:3072 nt\n\t" "s_add_u32 s52, s52, 4096\n\t" "s_addc_u32 s53, s53, 0\n\t" \
  "s_sub_u32 s50, s50, 1\n\t" "s_cmp_lg_u32 s50, 0\n\t" "s_cbranch_scc1 .Ld3_%=\n\t" "s_waitcnt vmcnt(0) lgkmcnt(0)\n\t" "s_barrier\n\t" "v_add_co_u32 v16, vcc, 1, v40\n\t" "s_nop 1\n\t" \
  "v_addc_co_u32 v17, vcc, 0, v41, vcc\n\t" "s_mul_i32 s40, s29, 205\n\t" "s_lshr_b32 s40, s40, 11\n\t" "s_mul_i32 s40, s40, 10\n\t" "s_sub_u32 s28, s29, s40\n\t" "s_cmp_lt_u32 s26, 10\n\t" \
  "s_cbranch_scc1 .Lcb_%=\n\t" "s_setprio 3\n\t" "s_cmp_eq_u32 s26, 10\n\t" "s_cbranch_scc1 .Lfe_%=\n\t" "s_movk_i32 s40, 4096\n\t" "v_min_u32 v3, 22, v8\n\t" "v_lshl_add_u32 v3, v3, 7, s40\n\t" \
  ".Lsf_%=:\n\t" "global_load_dwordx2 v[40:41], v3, s[24:25] sc1\n\t" "s_sub_u32 s39, s39, 1\n\t" "s_cmp_lt_i32 s39, 0\n\t" "s_waitcnt vmcnt(0)\n\t" "s_cbranch_scc1 .Lrt_%=\n\t" \
  "v_cmp_ne_u64 vcc, v[40:41], v[16:17]\n\t" "s_cbranch_vccnz .Lsf_%=\n\t" "s_xor_b32 s40, s28, 1\n\t" "s_mul_i32 s40, s40, 6144\n\t" "s_add_u32 s40, s40, 5302784\n\t" "v_and_b32 v3, 7, v8\n\t" \
  "v_mul_u32_u24 v3, 640, v3\n\t" "v_add_u32 v3, s40, v3\n\t" ".Lsk_%=:\n\t" "s_sleep 6\n\t" "global_load_dword v40, v3, s[24:25] sc1\n\t" "s_sub_u32 s39, s39, 1\n\t" "s_cmp_lt_i32 s39, 0\n\t" \
  "s_waitcnt vmcnt(0)\n\t" "s_cbranch_scc1 .Lrt_%=\n\t" "v_cmp_ne_u32 vcc, -1, v40\n\t" "s_cbranch_vccz .Lsk_%=\n\t" "s_branch .Lrt_%=\n\t" ".Lfe_%=:\n\t" "v_lshrrev_b32 v2, 3, v8\n\t" \
  "v_lshl_add_u32 v2, v2, 7, v32\n\t" "v_lshlrev_b32 v4, 2, v8\n\t" "v_lshrrev_b32 v3, 5, v8\n\t" "v_lshl_add_u32 v3, v3, 7, v4\n\t" "v_add_u32 v4, 160768, v4\n\t" "v_add_u32 v5, 159744, v32\n\t" \
  "s_movk_i32 s44, 63\n\t" ".Lf_%=:\n\t" "ds_read_b64 v[40:41], v37 offset:32\n\t" "s_sub_u32 s39, s39, 1\n\t" "s_cmp_lt_i32 s39, 0\n\t" "s_cselect_b32 s46, 1, 0\n\t" "s_waitcnt lgkmcnt(0)\n\t" \
  "v_readfirstlane_b32 s45, v40\n\t" "v_readfirstlane_b32 s47, v41\n\t" "s_bfe_u32 s45, s45, 0x60006\n\t" "s_or_b32 s46, s46, s47\n\t" "s_xor_b32 s47, s45, s44\n\t" "s_or_b32 s48, s47, s46\n\t" \
  "s_cbranch_scc0 .Lf_%=\n\t" "s_cmp_eq_u32 s47, 0\n\t" "s_cbranch_scc1 .Lfd_%=\n\t" "s_mul_i32 s40, s45, 2560\n\t" "s_add_u32 s40, s40, 5123584\n\t" "s_add_u32 s40, s24, s40\n\t" \
  "s_addc_u32 s41, s25, 0\n\t" "global_load_dwordx4 v[44:47], v2, s[40:41] sc1\n\t" "global_load_dword v48, v3, s[40:41] offset:2048 sc1\n\t" "s_add_u32 s49, s45, 1\n\t" "v_mov_b32 v50, s49\n\t" \
  "v_mov_b32 v51, s46\n\t" "s_cmp_lt_i32 s39, 0\n\t" "s_waitcnt vmcnt(0)\n\t" "ds_write_b128 v5, v[44:47]\n\t" "ds_write_b32 v4, v48\n\t" "s_cbranch_scc1 .Lfd_%=\n\t" \
  "v_max3_u32 v49, v44, v45, v46\n\t" "v_max3_u32 v49, v49, v47, v48\n\t" "s_movk_i32 s44, 99\n\t" "v_cmp_eq_u32 vcc, -1, v49\n\t" "s_cbranch_vccnz .Lf_%=\n\t" "s_mov_b32 s44, s45\n\t" \
  "s_cmp_eq_u32 s46, 0\n\t" "s_cbranch_scc1 .Lf_%=\n\t" ".Lfd_%=:\n\t" "s_waitcnt lgkmcnt(0)\n\t" "s_barrier\n\t" "s_barrier\n\t" "s_branch .Lend_%=\n\t" ".Lcb_%=:\n\t" "s_cmp_ge_u32 s29, 20\n\t" \
  "s_cbranch_scc1 .Lend_%=\n\t" "global_load_dwordx4 v[20:23], v32, s[14:15]\n\t" "v_lshlrev_b32 v6, 2, v8\n\t" "global_load_dword v24, v6, s[14:15] offset:1024\n\t" "v_add_u32 v5, 159744, v32\n\t" \
  "v_add_u32 v4, 160768, v6\n\t" "s_lshl1_add_u32 s40, s29, s33\n\t" "v_and_b32 v1, 3, v8\n\t" "v_lshrrev_b32 v2, 2, v8\n\t" "v_and_b32 v3, 4, v2\n\t" "v_lshrrev_b32 v7, 4, v8\n\t" \
  "v_and_b32 v7, 2, v7\n\t" "v_lshl_add_u32 v3, v1, 3, v3\n\t" "v_add_u32 v3, v3, v7\n\t" "v_mul_u32_u24 v2, 2560, v2\n\t" "v_lshl_add_u32 v2, v1, 4, v2\n\t" "s_lshl_b32 s41, s40, 5\n\t" \
  "s_add_u32 s42, s41, 159744\n\t" "v_add_u32 v1, s42, v3\n\t" "s_lshl_b32 s42, s40, 6\n\t" "s_add_u32 s42, s42, 5241344\n\t" "s_add_u32 s44, s24, s42\n\t" "s_addc_u32 s45, s25, 0\n\t" \
  "s_add_u32 s46, s44, 40960\n\t" "s_addc_u32 s47, s45, 0\n\t" "v_mov_b64 v[64:65], 0\n\t" "v_mov_b64 v[66:67], 0\n\t" "s_waitcnt vmcnt(0)\n\t" "s_barrier\n\t" "s_barrier\n\t" \
  "s_memrealtime s[64:65]\n\t" "ds_read_b32 v39, v37 offset:32\n\t" "ds_read_b128 v[44:47], v5\n\t" "ds_read_b32 v48, v4\n\t" "ds_read_u16_d16_hi v49, v1\n\t" "s_waitcnt lgkmcnt(0)\n\t" \
  "v_readfirstlane_b32 s42, v39\n\t" "v_dot2_f32_bf16 v50, v48, v24, 0\n\t" "v_dot2_f32_bf16 v50, v44, v20, v50\n\t" "v_dot2_f32_bf16 v50, v45, v21, v50\n\t" "v_dot2_f32_bf16 v50, v46, v22, v50\n\t" \
  "v_dot2_f32_bf16 v50, v47, v23, v50\n\t" "s_bfe_u32 s43, s42, 0x60006\n\t" "s_lshr_b32 s56, s43, 1\n\t" "s_mul_i32 s56, s56, 89\n\t" "s_and_b32 s57, s43, 1\n\t" "s_mul_i32 s57, s57, 48\n\t" \
  "s_add_u32 s56, s56, s57\n\t" "s_and_b32 s42, s42, 63\n\t" "s_add_u32 s56, s56, s42\n\t" "s_sub_u32 s56, s56, 6\n\t" "v_add_f32_dpp v50, v50, v50 quad_perm:[1,0,3,2] row_mask:0xf bank_mask:0xf\n\t" \
  "s_nop 1\n\t" "v_add_f32_dpp v50, v50, v50 quad_perm:[2,3,0,1] row_mask:0xf bank_mask:0xf\n\t" "s_nop 1\n\t" "v_add_f32_dpp v50, v50, v50 row_half_mirror row_mask:0xf bank_mask:0xf\n\t" \
  "s_nop 1\n\t" "v_add_f32_dpp v50, v50, v50 row_mirror row_mask:0xf bank_mask:0xf\n\t" "s_bfe_u32 s57, s56, 0x1000a\n\t" "s_lshl1_add_u32 s56, s56, s57\n\t" "s_and_b32 s56, s56, 2047\n\t" \
  "v_mov_b32 v51, v50\n\t" "s_mul_i32 s56, s56, 1280\n\t" "s_add_u32 s56, s56, s41\n\t" "v_permlane32_swap_b32 v50, v51\n\t" "v_add_f32 v50, v50, v51\n\t" "v_mov_b32 v51, v50\n\t" \
  "s_add_u32 s56, s22, s56\n\t" "s_addc_u32 s57, s23, 0\n\t" "v_permlane16_swap_b32 v50, v51\n\t" "v_add_f32 v50, v50, v51\n\t" "v_cvt_pk_bf16_f32 v50, v50, v50\n\t" "v_lshlrev_b32 v50, 16, v50\n\t" \
  "v_mul_f32 v50, 0xbfb8aa3b, v50\n\t" "v_exp_f32 v50, v50\n\t" "s_mul_i32 s43, s43, 2560\n\t" "s_lshr_b32 s42, s40, 2\n\t" "s_lshl_b32 s42, s42, 7\n\t" "s_add_u32 s43, s43, s42\n\t" \
  "s_add_u32 s43, s43, s41\n\t" "v_add_f32 v50, 1.0, v50\n\t" "v_rcp_f32 v50, v50\n\t" "s_add_u32 s43, s43, 5123712\n\t" "s_add_u32 s58, s24, s43\n\t" "s_addc_u32 s59, s25, 0\n\t" \
  "v_cvt_pk_bf16_f32 v50, v50, v50\n\t" "v_lshlrev_b32 v50, 16, v50\n\t" "v_and_b32 v49, 0xffff0000, v49\n\t" ".Ltc_%=:\n\t" "s_memrealtime s[78:79]\n\t" "s_waitcnt lgkmcnt(0)\n\t" \
  "s_sub_u32 s78, s78, s64\n\t" "s_cmp_lt_u32 s78, 60\n\t" "s_cbranch_scc1 .Ltc_%=\n\t" ".Lcp_%=:\n\t" "global_load_short_d16_hi v51, v3, s[58:59] sc1\n\t" \
  "global_load_dwordx4 v[60:63], v2, s[44:45] sc1\n\t" "s_mov_b32 exec_hi, 0\n\t" "global_load_dwordx4 v[64:67], v2, s[46:47] sc1\n\t" "s_mov_b32 exec_hi, -1\n\t" "s_sub_u32 s39, s39, 1\n\t" \
  "s_cmp_lt_i32 s39, 0\n\t" "s_waitcnt vmcnt(0)\n\t" "v_pk_add_f32 v[60:61], v[60:61], v[64:65]\n\t" "v_pk_add_f32 v[62:63], v[62:63], v[66:67]\n\t" "v_and_b32 v51, 0xffff0000, v51\n\t" \
  "v_add_f32_dpp v60, v60, v60 row_ror:4 row_mask:0xf bank_mask:0xf\n\t" "v_add_f32_dpp v61, v61, v61 row_ror:4 row_mask:0xf bank_mask:0xf\n\t" \
  "v_add_f32_dpp v62, v62, v62 row_ror:4 row_mask:0xf bank_mask:0xf\n\t" "v_add_f32_dpp v63, v63, v63 row_ror:4 row_mask:0xf bank_mask:0xf\n\t" \
  "v_add_f32_dpp v60, v60, v60 row_ror:8 row_mask:0xf bank_mask:0xf\n\t" "v_add_f32_dpp v61, v61, v61 row_ror:8 row_mask:0xf bank_mask:0xf\n\t" \
  "v_add_f32_dpp v62, v62, v62 row_ror:8 row_mask:0xf bank_mask:0xf\n\t" "v_add_f32_dpp v63, v63, v63 row_ror:8 row_mask:0xf bank_mask:0xf\n\t" "v_permlane32_swap_b32 v60, v61\n\t" "s_nop 0\n\t" \
  "v_permlane32_swap_b32 v62, v63\n\t" "v_add_f32 v60, v60, v61\n\t" "v_add_f32 v62, v62, v63\n\t" "s_nop 1\n\t" "v_permlane16_swap_b32 v60, v62\n\t" "v_add_f32 v60, v60, v62\n\t" \
  "v_cvt_pk_bf16_f32 v60, v60, v60\n\t" "v_lshlrev_b32 v60, 16, v60\n\t" "v_mul_f32 v60, v60, v50\n\t" "v_cvt_pk_bf16_f32 v60, v60, v60\n\t" "v_lshlrev_b32 v60, 16, v60\n\t" \
  "v_add_f32 v60, v60, v49\n\t" "v_cvt_pk_bf16_f32 v60, v60, v60\n\t" "v_lshlrev_b32 v60, 16, v60\n\t" "v_add_f32 v60, v60, v51\n\t" "s_cbranch_scc1 .Lcq_%=\n\t" "v_cmp_u_f32 vcc, v60, v60\n\t" \
  "s_cbranch_vccnz .Lcp_%=\n\t" ".Lcq_%=:\n\t" "v_cvt_pk_bf16_f32 v60, v60, v60\n\t" "s_mov_b32 exec_lo, 0xf000f\n\t" "s_mov_b32 exec_hi, 0xf000f\n\t" "global_store_short v3, v60, s[56:57]\n\t" \
  "s_mov_b64 exec, -1\n\t" ".Lend_%=:\n\t"

extern "C" __global__ __launch_bounds__(768, 3) void fused_block(const u16* x, const u16* wqkv, const u16* wo, const u16* w_r,
                                                                 const u16* w_gate, const u16* w_up, const u16* w_down, u16* out,
                                                                 char* ws) {
  u32 tid = threadIdx.x;
  asm volatile(BODY
    : "+{v0}"(tid)
    : [ka] "s"(__builtin_amdgcn_kernarg_segment_ptr()), [bid] "s"(blockIdx.x)
    : "memory", "scc", "vcc", "v1", "v2", "v3", "v4", "v5", "v6", "v7", "v8", "v9", CL10(1), CL10(2), CL10(3), CL10(4), CL10(5), CL10(6),
      CL10(7), CL10(8), CL10(9), CL10(10), CL10(11), CL10(12), CL10(13), CL10(14), CL10(15), CL8(16), "s8", "s9", SL10(1), SL10(2), "s30",
      "s31", "s33", "s34", "s35", "s36", "s37", "s38", "s39", SL10(4), SL10(5), SL10(6), SL10(7), SL10(8), SL10(9));
}

extern "C" void kernel_launch(void* const* d_in, const int* in_sizes, int n_in,
                              void* d_out, int out_size, void* d_ws, size_t ws_size,
                              hipStream_t stream) {
  const u16* x = (const u16*)d_in[0];
  const u16* wqkv = (const u16*)d_in[1];
  const u16* wo = (const u16*)d_in[2];
  const u16* w_r = (const u16*)d_in[4];
  const u16* w_gate = (const u16*)d_in[5];
  const u16* w_up = (const u16*)d_in[6];
  const u16* w_down = (const u16*)d_in[7];
  (void)hipFuncSetAttribute((const void*)fused_block, hipFuncAttributeMaxDynamicSharedMemorySize, LDS_TOTAL);
  fused_block<<<256, 768, LDS_TOTAL, stream>>>(x, wqkv, wo, w_r, w_gate, w_up, w_down, (u16*)d_out, (char*)d_ws);
}
```

```cpp
#include <hip/hip_runtime.h>
#include <stdint.h>


typedef unsigned short u16;
typedef unsigned int u32;
#define LDS_TOTAL 163840
#define CL8(T) "v" #T "0", "v" #T "1", "v" #T "2", "v" #T "3", "v" #T "4", "v" #T "5", "v" #T "6", "v" #T "7"
#define CL10(T) CL8(T), "v" #T "8", "v" #T "9"
#define SL10(T) "s" #T "0", "s" #T "1", "s" #T "2", "s" #T "3", "s" #T "4", "s" #T "5", "s" #T "6", "s" #T "7", "s" #T "8", "s" #T "9"

#define BODY \
  ".Lst_%=:\n\t" "s_load_dwordx16 s[8:23], %[ka], 0x0\n\t" "s_load_dwordx2 s[24:25], %[ka], 0x40\n\t" "v_readfirstlane_b32 s26, v0\n\t" "s_lshr_b32 s76, %[bid], 3\n\t" "v_and_b32 v8, 63, v0\n\t" \
  "s_mul_i32 s77, s76, 205\n\t" "s_lshr_b32 s26, s26, 6\n\t" "s_lshr_b32 s77, s77, 10\n\t" "v_lshrrev_b32 v1, 3, v8\n\t" "s_mul_i32 s27, s77, 5\n\t" "s_and_b32 s33, s26, 3\n\t" \
  "s_sub_u32 s27, s76, s27\n\t" "s_and_b32 s78, %[bid], 1\n\t" "s_bfe_u32 s79, %[bid], 0x20001\n\t" "s_xor_b32 s79, s79, 3\n\t" "s_mul_i32 s28, s78, 5\n\t" "s_mul_i32 s29, s79, 6\n\t" \
  "s_add_u32 s28, s28, s27\n\t" "s_add_u32 s29, s29, s77\n\t" "s_cmp_ge_u32 s76, 30\n\t" "s_cselect_b32 s29, 24, s29\n\t" "s_cmp_ge_u32 s29, 23\n\t" "s_cbranch_scc1 .Lsp_%=\n\t" \
  "s_lshl_b32 s77, s77, 2\n\t" "s_lshl1_add_u32 s31, s27, s78\n\t" "s_lshr_b32 s77, 0x663300, s77\n\t" "s_add_u32 s31, s31, s79\n\t" "s_and_b32 s77, s77, 15\n\t" "s_mul_i32 s30, s29, 89\n\t" \
  "s_add_u32 s31, s31, s77\n\t" "s_mul_i32 s77, s31, 205\n\t" "s_lshr_b32 s77, s77, 11\n\t" "s_mul_i32 s77, s77, 10\n\t" "s_sub_u32 s31, s31, s77\n\t" "s_cmp_ge_u32 s26, 4\n\t" \
  "s_cbranch_scc1 .Lnl_%=\n\t" "s_setprio 2\n\t" "v_lshl_or_b32 v2, s33, 4, v1\n\t" "v_and_b32 v3, 2, v1\n\t" "v_mul_u32_u24 v2, 0xf00, v2\n\t" "v_xor_b32 v3, v3, v8\n\t" "s_lshl_b32 s76, s28, 7\n\t" \
  "v_and_b32 v3, 7, v3\n\t" "s_add_u32 s76, s76, 3072\n\t" "v_lshlrev_b32 v3, 4, v3\n\t" "s_mul_i32 s86, s33, 6144\n\t" "v_add3_u32 v40, v2, v3, s76\n\t" "s_add_u32 s86, s86, 3072\n\t" \
  "v_add_u32 v41, 256, v40\n\t" "v_add_u32 v42, 512, v40\n\t" "v_xor_b32 v43, 64, v40\n\t" "s_mov_b32 m0, s86\n\t" "v_add_u32 v43, 27648, v43\n\t" "s_waitcnt lgkmcnt(0)\n\t" \
  "s_mul_i32 s76, s31, 245760\n\t" "s_lshl_b32 s77, s31, 7\n\t" "s_add_u32 s78, s10, s76\n\t" "s_addc_u32 s79, s11, 0\n\t" "s_add_u32 s80, s8, s77\n\t" "s_addc_u32 s81, s9, 0\n\t" \
  "v_add_u32 v44, 256, v43\n\t" "v_add_u32 v45, 512, v43\n\t" "global_load_lds_dwordx4 v40, s[78:79] offset:-3072\n\t" "global_load_lds_dwordx4 v41, s[78:79] offset:-2048\n\t" \
  "global_load_lds_dwordx4 v42, s[78:79] offset:-1024\n\t" "global_load_lds_dwordx4 v43, s[78:79] offset:0\n\t" "global_load_lds_dwordx4 v44, s[78:79] offset:1024\n\t" \
  "global_load_lds_dwordx4 v45, s[78:79] offset:2048\n\t" "s_mul_i32 s76, s33, 24\n\t" "s_mul_i32 s87, s33, 3072\n\t" "v_add_u32 v2, s76, v1\n\t" "s_movk_i32 s77, 2048\n\t" \
  "v_lshrrev_b32 v3, 1, v2\n\t" "v_add_u32 v2, s30, v2\n\t" "v_xor_b32 v3, v3, v8\n\t" "s_add_u32 s87, s87, 26624\n\t" "v_and_b32 v3, 7, v3\n\t" "s_mov_b32 m0, s87\n\t" \
  "v_lshl_add_u32 v3, v3, 4, s77\n\t" "s_movk_i32 s77, 1280\n\t" "v_add_u32 v4, 0, v2\n\t" "v_sub_u32 v4, v4, 6 clamp\n\t" "v_lshrrev_b32 v5, 10, v4\n\t" "v_lshl_or_b32 v4, v4, 1, v5\n\t" \
  "v_and_b32 v4, 0x7ff, v4\n\t" "v_mad_u32_u24 v46, v4, s77, v3\n\t" "global_load_lds_dwordx4 v46, s[80:81] offset:-2048\n\t" "v_xor_b32 v3, 64, v3\n\t" "v_subrev_u32 v3, 1024, v3\n\t" \
  "v_add_u32 v4, 8, v2\n\t" "v_sub_u32 v4, v4, 6 clamp\n\t" "v_lshrrev_b32 v5, 10, v4\n\t" "v_lshl_or_b32 v4, v4, 1, v5\n\t" "v_and_b32 v4, 0x7ff, v4\n\t" "v_mad_u32_u24 v47, v4, s77, v3\n\t" \
  "global_load_lds_dwordx4 v47, s[80:81] offset:-1024\n\t" "v_xor_b32 v3, 64, v3\n\t" "v_subrev_u32 v3, 1024, v3\n\t" "v_add_u32 v4, 16, v2\n\t" "v_sub_u32 v4, v4, 6 clamp\n\t" \
  "v_lshrrev_b32 v5, 10, v4\n\t" "v_lshl_or_b32 v4, v4, 1, v5\n\t" "v_and_b32 v4, 0x7ff, v4\n\t" "v_mad_u32_u24 v48, v4, s77, v3\n\t" "global_load_lds_dwordx4 v48, s[80:81] offset:0\n\t" \
  ".Lnl_%=:\n\t" "s_cmp_lg_u32 s26, 11\n\t" "s_cbranch_scc1 .Lnc_%=\n\t" "s_getpc_b64 s[76:77]\n\t" ".Lpc_%=:\n\t" "s_and_b32 s78, %[bid], -8\n\t" "s_sub_u32 s76, s76, .Lpc_%=-.Lst_%=\n\t" \
  "s_subb_u32 s77, s77, 0\n\t" "v_add_u32 v1, s78, v8\n\t" "v_min_u32 v1, ((.Lend_%=-.Lst_%=)>>7)+0, v1\n\t" "s_mov_b32 m0, 163072\n\t" "v_lshlrev_b32 v1, 7, v1\n\t" "s_mov_b64 exec, 0xff\n\t" \
  "global_load_lds_dword v1, s[76:77]\n\t" "s_mov_b64 exec, -1\n\t" ".Lnc_%=:\n\t" "s_cmp_ge_u32 s26, 6\n\t" "s_cselect_b32 s34, 1, 0\n\t" "s_cselect_b32 s76, 6, 0\n\t" "v_and_b32 v9, 15, v8\n\t" \
  "s_sub_u32 s35, s26, s76\n\t" "v_lshrrev_b32 v10, 2, v8\n\t" "s_and_b32 s36, s26, 1\n\t" "s_lshr_b32 s37, s26, 1\n\t" "s_lshl4_add_u32 s76, s35, s30\n\t" "s_sub_u32 s76, s76, 6\n\t" \
  "v_lshlrev_b32 v32, 4, v8\n\t" "v_add_u32 v2, s76, v9\n\t" "s_mov_b32 s76, 0x1fe00\n\t" "v_and_b32 v2, 0x3ff, v2\n\t" "v_lshlrev_b32 v1, v9, s76\n\t" "v_min_u32 v2, 7, v2\n\t" \
  "v_lshlrev_b32 v35, 7, v10\n\t" "v_sub_u32 v2, v9, v2\n\t" "v_and_b32 v10, 12, v10\n\t" "v_add_u32 v2, 16, v2\n\t" "s_cmp_eq_u32 s35, 0\n\t" "s_cselect_b32 s76, 0xffff0000, -1\n\t" \
  "s_cselect_b32 s81, 0, 1024\n\t" "v_lshlrev_b32 v2, v2, -1\n\t" "v_and_b32 v1, s76, v1\n\t" "s_mov_b32 s77, 0xff80ff80\n\t" "s_cmp_eq_u32 s29, 0\n\t" "s_cselect_b32 s77, 0xffc0ffc0, s77\n\t" \
  "v_and_b32 v1, v1, v2\n\t" "s_cmp_eq_u32 s37, 0\n\t" "s_cselect_b32 s72, s77, -1\n\t" "v_not_b32 v1, v1\n\t" "s_mov_b32 s73, s72\n\t" "v_lshrrev_b32 v1, v10, v1\n\t" \
  "s_lshl4_add_u32 s76, s37, s30\n\t" "s_mov_b32 s77, 0xffff\n\t" "v_bfe_i32 v20, v1, 0, 1\n\t" "v_bfe_i32 v21, v1, 1, 1\n\t" "v_bfe_i32 v22, v1, 2, 1\n\t" "v_bfe_i32 v23, v1, 3, 1\n\t" \
  "v_add_u32 v2, s76, v9\n\t" "v_bfi_b32 v28, s77, v20, v21\n\t" "v_bfi_b32 v29, s77, v22, v23\n\t" "v_bfe_i32 v24, v1, 16, 1\n\t" "v_bfe_i32 v25, v1, 17, 1\n\t" "v_bfe_i32 v26, v1, 18, 1\n\t" \
  "v_bfe_i32 v27, v1, 19, 1\n\t" "v_sub_u32 v2, v2, 6 clamp\n\t" "v_lshrrev_b32 v3, 10, v2\n\t" "v_lshl_or_b32 v2, v2, 1, v3\n\t" "v_and_b32 v2, 0x7ff, v2\n\t" "s_lshl_b32 s76, s28, 7\n\t" \
  "s_lshl_b32 s77, s36, 6\n\t" "v_and_b32 v20, 0xff800000, v20\n\t" "v_and_b32 v21, 0xff800000, v21\n\t" "v_and_b32 v22, 0xff800000, v22\n\t" "v_and_b32 v23, 0xff800000, v23\n\t" \
  "v_and_b32 v24, 0xff800000, v24\n\t" "v_and_b32 v25, 0xff800000, v25\n\t" "v_and_b32 v26, 0xff800000, v26\n\t" "v_and_b32 v27, 0xff800000, v27\n\t" "s_add_u32 s76, s76, s77\n\t" \
  "v_lshlrev_b32 v1, 1, v8\n\t" "v_lshrrev_b32 v3, 1, v8\n\t" "v_and_b32 v1, 32, v1\n\t" "v_and_b32 v3, 16, v3\n\t" "s_movk_i32 s78, 1280\n\t" "v_or3_b32 v34, v1, v3, s76\n\t" \
  "v_lshlrev_b32 v5, 2, v8\n\t" "v_mad_u32_u24 v33, v2, s78, v34\n\t" "v_and_b32 v6, 0x60, v5\n\t" "s_mul_i32 s76, s36, 40960\n\t" "v_xor_b32 v1, s77, v6\n\t" "s_add_u32 s76, s76, 73728\n\t" \
  "v_and_b32 v5, 12, v5\n\t" "v_add3_u32 v35, v35, v1, s76\n\t" "v_mov_b32 v37, 156416\n\t" "v_lshl_add_u32 v35, v5, 1, v35\n\t" "v_mov_b32 v38, 1\n\t" "v_add_u32 v36, 1413120, v32\n\t" \
  "s_mul_i32 s78, s27, 12\n\t" "s_mov_b32 s76, 0x10321320\n\t" "s_mov_b32 s77, 0xbe0f102\n\t" "s_lshr_b64 s[76:77], s[76:77], s78\n\t" "s_and_b32 s74, s76, 0xfff\n\t" "s_mov_b32 s76, 0xedfe0f41\n\t" \
  "s_mov_b32 s77, 0xfdcedcf\n\t" "s_lshr_b64 s[76:77], s[76:77], s78\n\t" "s_pack_ll_b32_b16 s74, s74, s76\n\t" "s_mul_i32 s76, s27, 0x10001\n\t" "s_sub_u32 s76, 0xe0009, s76\n\t" \
  "s_lshl4_add_u32 s74, s74, s76\n\t" "s_mul_i32 s78, s34, 3072\n\t" "s_lshr_b32 s79, s74, 16\n\t" "v_bfe_u32 v1, s74, v10, 4\n\t" "s_lshl_b32 s76, s35, 9\n\t" "v_bfe_u32 v2, s79, v10, 4\n\t" \
  "v_and_b32 v4, 12, v8\n\t" "s_sub_u32 s76, s76, s78\n\t" "v_bfe_u32 v3, s74, v4, 4\n\t" "s_add_u32 s76, s76, 3072\n\t" "v_bfe_u32 v4, s79, v4, 4\n\t" "v_lshl_add_u32 v30, v8, 3, s76\n\t" \
  "s_sub_u32 s76, 5, s27\n\t" "v_lshl_add_u32 v31, s78, 1, v30\n\t" "s_sub_u32 s77, 15, s27\n\t" "v_cmp_gt_u32 s[40:41], s76, v1\n\t" "v_cmp_gt_u32 s[42:43], s76, v2\n\t" \
  "v_cmp_le_u32 s[44:45], s77, v1\n\t" "v_cmp_le_u32 s[46:47], s77, v2\n\t" "v_cmp_gt_u32 s[50:51], s76, v3\n\t" "v_cmp_gt_u32 s[52:53], s76, v4\n\t" "s_lshl_b32 s76, s27, 2\n\t" \
  "v_cmp_gt_u32 s[48:49], s76, v10\n\t" "s_mul_i32 s76, s29, 10\n\t" "s_add_u32 s76, s76, s28\n\t" "s_mul_i32 s76, s76, 6144\n\t" "s_add_u32 s76, s76, 585728\n\t" "s_waitcnt lgkmcnt(0)\n\t" \
  "s_add_u32 s66, s24, s76\n\t" "s_addc_u32 s67, s25, 0\n\t" "s_lshl_b32 s76, s35, 10\n\t" "s_add_u32 s76, s76, 4096\n\t" "s_add_u32 s56, s66, s76\n\t" "s_addc_u32 s57, s67, 0\n\t" \
  "s_sub_u32 s58, s56, s81\n\t" "s_subb_u32 s59, s57, 0\n\t" "s_mul_i32 s76, s29, 61440\n\t" "s_add_u32 s76, s76, 2004992\n\t" "s_mul_i32 s77, s26, 5120\n\t" "s_add_u32 s77, s77, s76\n\t" \
  "s_add_u32 s62, s24, s77\n\t" "s_addc_u32 s63, s25, 0\n\t" "s_add_u32 s64, s62, 1413120\n\t" "s_addc_u32 s65, s63, 0\n\t" "s_mul_i32 s77, s35, 10\n\t" "s_add_u32 s77, s77, s28\n\t" \
  "s_lshl_b32 s77, s77, 10\n\t" "s_add_u32 s77, s77, s76\n\t" "s_add_u32 s60, s24, s77\n\t" "s_addc_u32 s61, s25, 0\n\t" "s_mov_b64 s[76:77], 0xffff\n\t" "s_not_b64 s[54:55], s[76:77]\n\t" \
  "s_cmp_lg_u32 s34, 0\n\t" "s_cselect_b64 s[54:55], s[76:77], s[54:55]\n\t" "s_mov_b32 s39, 400000\n\t" "s_mov_b32 s75, 0\n\t" "s_cmp_lt_u32 s26, 4\n\t" "s_cbranch_scc1 .Ll_%=\n\t" \
  "s_mul_i32 s77, s27, 0x1111\n\t" "s_sub_u32 s80, 0x8765, s77\n\t" "s_sub_u32 s77, 0xdcba, s77\n\t" "s_cmp_eq_u32 s33, 1\n\t" "s_cselect_b32 s80, s74, s80\n\t" "s_cmp_eq_u32 s33, 2\n\t" \
  "s_cselect_b32 s80, s79, s80\n\t" "s_cmp_eq_u32 s33, 3\n\t" "s_cselect_b32 s80, s77, s80\n\t" "v_mul_u32_u24 v1, 768, v10\n\t" "v_bfe_u32 v5, s80, v5, 4\n\t" "v_lshlrev_b32 v2, 5, v9\n\t" \
  "v_lshlrev_b32 v5, 3, v5\n\t" "v_and_b32 v2, 0x180, v2\n\t" "v_xor_b32 v5, v5, v6\n\t" "s_cmp_ge_u32 s26, 8\n\t" "v_add3_u32 v1, v1, v5, v2\n\t" "s_cbranch_scc1 .Lg_%=\n\t" "v_mov_b32 v7, 0\n\t" \
  "s_setprio 3\n\t" "global_load_dwordx2 v[40:41], v7, s[24:25] sc1\n\t" "s_cmp_lg_u32 s33, 2\n\t" "s_cbranch_scc1 .Lre0_%=\n\t" "v_lshlrev_b32 v5, 2, v8\n\t" \
  "global_load_dwordx4 v[64:67], v32, s[14:15]\n\t" "global_load_dword v68, v5, s[14:15] offset:1024\n\t" ".Lre0_%=:\n\t" "v_lshl_add_u32 v3, v8, 2, v37\n\t" "s_mov_b32 exec_hi, 0\n\t" \
  "ds_write_b32 v3, v7\n\t" "s_mov_b32 exec_hi, -1\n\t" "s_lshl2_add_u32 s76, s29, s33\n\t" "s_mul_i32 s76, s76, 2560\n\t" "s_add_u32 s76, s76, 8192\n\t" "s_add_u32 s86, s24, s76\n\t" \
  "s_addc_u32 s87, s25, 0\n\t" "s_lshl_b32 s76, s28, 8\n\t" "s_add_u32 s88, s86, s76\n\t" "s_addc_u32 s89, s87, 0\n\t" "s_lshl2_add_u32 s76, s28, s33\n\t" "s_mul_i32 s76, s76, 5120\n\t" \
  "s_add_u32 s76, s76, 361472\n\t" "s_add_u32 s90, s24, s76\n\t" "s_addc_u32 s91, s25, 0\n\t" "s_lshl_b32 s76, s31, 8\n\t" "s_add_u32 s92, s90, s76\n\t" "s_addc_u32 s93, s91, 0\n\t" \
  "v_mov_b32 v116, -1\n\t" "v_mov_b32 v117, -1\n\t" "v_mov_b32 v118, -1\n\t" "v_mov_b32 v119, -1\n\t" "s_mul_i32 s76, s33, 1536\n\t" "s_add_u32 s76, s76, 4096\n\t" "s_add_u32 s94, s66, s76\n\t" \
  "s_addc_u32 s95, s67, 0\n\t" "s_lshr_b32 s76, s33, 1\n\t" "s_add_u32 s76, s76, 2\n\t" "s_sub_u32 s76, s76, s35\n\t" "s_mul_i32 s76, s76, 10240\n\t" "s_and_b32 s77, s33, 1\n\t" \
  "s_lshl_b32 s77, s77, 9\n\t" "s_add_u32 s76, s76, s77\n\t" "s_sub_u32 s76, s76, 2048\n\t" "s_ashr_i32 s77, s76, 31\n\t" "s_add_u32 s68, s60, s76\n\t" "s_addc_u32 s69, s61, s77\n\t" \
  "s_mul_i32 s76, s33, 3072\n\t" "v_add_u32 v2, s76, v32\n\t" "s_mov_b32 s77, 0x3f803f80\n\t" "v_mov_b32 v96, s77\n\t" "v_mov_b32 v97, s77\n\t" "v_mov_b32 v98, s77\n\t" "v_mov_b32 v99, s77\n\t" \
  "v_lshrrev_b32 v12, 2, v8\n\t" "v_and_or_b32 v12, v8, 1, v12\n\t" "v_lshlrev_b32 v12, 4, v12\n\t" "s_mov_b32 s98, 0x03030303\n\t" "s_mov_b32 s99, s98\n\t" "s_mov_b32 s70, 0xaaaaaaaa\n\t" \
  "s_mov_b32 s71, s70\n\t" "v_and_b32 v13, 16, v8\n\t" "v_lshrrev_b32 v7, 2, v8\n\t" "v_and_or_b32 v13, v7, 8, v13\n\t" "v_min_u32 v13, 16, v13\n\t" "v_lshrrev_b32 v7, 1, v9\n\t" \
  "v_add_u32 v13, v13, v7\n\t" "v_and_b32 v7, 1, v8\n\t" "v_mul_u32_u24 v7, 384, v7\n\t" "v_lshl_add_u32 v13, v13, 2, v7\n\t" "s_mul_i32 s76, s33, 96\n\t" "s_add_u32 s76, s76, 155648\n\t" \
  "v_add_u32 v13, s76, v13\n\t" "v_min_u32 v4, 22, v8\n\t" "s_movk_i32 s76, 4096\n\t" "v_lshl_add_u32 v4, v4, 7, s76\n\t" "v_and_b32 v5, 16, v8\n\t" "v_lshl_add_u32 v5, v5, 1, v9\n\t" \
  "v_lshrrev_b32 v7, 1, v8\n\t" "v_and_or_b32 v5, v7, 16, v5\n\t" "s_lshl_b32 s76, s33, 8\n\t" "s_add_u32 s76, s76, 156672\n\t" "v_lshl_add_u32 v5, v5, 2, s76\n\t" "s_mov_b32 s76, 0x3acccccd\n\t" \
  "s_bitcmp0_b32 0x555541, s29\n\t" "s_waitcnt lgkmcnt(0)\n\t" "s_barrier\n\t" "ds_read_b128 v[44:47], v2 offset:24576\n\t" "ds_read_b128 v[48:51], v2 offset:25600\n\t" \
  "ds_read_b128 v[52:55], v2 offset:26624\n\t" "s_cbranch_scc1 .Lc0_%=\n\t" "ds_read_b64_tr_b16 v[72:73], v1 offset:0\n\t" "ds_read_b64_tr_b16 v[74:75], v1 offset:512\n\t" \
  "ds_read_b64_tr_b16 v[76:77], v1 offset:1024\n\t" "ds_read_b64_tr_b16 v[78:79], v1 offset:1536\n\t" "ds_read_b64_tr_b16 v[80:81], v1 offset:2048\n\t" \
  "ds_read_b64_tr_b16 v[82:83], v1 offset:2560\n\t" "ds_read_b64_tr_b16 v[84:85], v1 offset:12288\n\t" "ds_read_b64_tr_b16 v[86:87], v1 offset:12800\n\t" \
  "ds_read_b64_tr_b16 v[88:89], v1 offset:13312\n\t" "ds_read_b64_tr_b16 v[90:91], v1 offset:13824\n\t" "ds_read_b64_tr_b16 v[92:93], v1 offset:14336\n\t" \
  "ds_read_b64_tr_b16 v[94:95], v1 offset:14848\n\t" ".Lc0_%=:\n\t" "s_waitcnt lgkmcnt(0)\n\t" "s_cbranch_scc1 .Lc1_%=\n\t" "v_mfma_f32_16x16x32_bf16 v[100:103], v[96:99], v[72:75], 0\n\t" \
  "v_mfma_f32_16x16x32_bf16 v[104:107], v[96:99], v[76:79], 0\n\t" "v_mfma_f32_16x16x32_bf16 v[108:111], v[96:99], v[80:83], 0\n\t" \
  "v_mfma_f32_16x16x32_bf16 v[100:103], v[96:99], v[84:87], v[100:103]\n\t" "v_mfma_f32_16x16x32_bf16 v[104:107], v[96:99], v[88:91], v[104:107]\n\t" \
  "v_mfma_f32_16x16x32_bf16 v[108:111], v[96:99], v[92:95], v[108:111]\n\t" ".Lc1_%=:\n\t" "s_barrier\n\t" "global_store_dwordx4 v32, v[116:119], s[94:95] sc1\n\t" "s_mov_b32 exec_hi, 0\n\t" \
  "global_store_dwordx4 v32, v[116:119], s[94:95] offset:1024 sc1\n\t" "s_mov_b32 exec_hi, -1\n\t" "global_store_dwordx4 v32, v[116:119], s[60:61] offset:-2048 sc1\n\t" "s_mov_b32 exec_hi, 0\n\t" \
  "global_store_dwordx4 v32, v[116:119], s[68:69] sc1\n\t" "s_mov_b32 exec_hi, -1\n\t" "v_dot2_f32_bf16 v60, v44, s77, 0\n\t" "v_dot2_f32_bf16 v120, v44, v44, 0\n\t" \
  "v_dot2_f32_bf16 v61, v48, s77, 0\n\t" "v_dot2_f32_bf16 v121, v48, v48, 0\n\t" "v_dot2_f32_bf16 v62, v52, s77, 0\n\t" "v_dot2_f32_bf16 v122, v52, v52, 0\n\t" "v_dot2_f32_bf16 v60, v45, s77, v60\n\t" \
  "v_dot2_f32_bf16 v120, v45, v45, v120\n\t" "v_dot2_f32_bf16 v61, v49, s77, v61\n\t" "v_dot2_f32_bf16 v121, v49, v49, v121\n\t" "v_dot2_f32_bf16 v62, v53, s77, v62\n\t" \
  "v_dot2_f32_bf16 v122, v53, v53, v122\n\t" "s_barrier\n\t" "v_dot2_f32_bf16 v60, v46, s77, v60\n\t" "v_dot2_f32_bf16 v120, v46, v46, v120\n\t" "v_dot2_f32_bf16 v61, v50, s77, v61\n\t" \
  "v_dot2_f32_bf16 v121, v50, v50, v121\n\t" "v_dot2_f32_bf16 v62, v54, s77, v62\n\t" "v_dot2_f32_bf16 v122, v54, v54, v122\n\t" "v_dot2_f32_bf16 v60, v47, s77, v60\n\t" \
  "v_dot2_f32_bf16 v120, v47, v47, v120\n\t" "v_dot2_f32_bf16 v61, v51, s77, v61\n\t" "v_dot2_f32_bf16 v121, v51, v51, v121\n\t" "v_dot2_f32_bf16 v62, v55, s77, v62\n\t" \
  "v_dot2_f32_bf16 v122, v55, v55, v122\n\t" "s_barrier\n\t" "s_nop 1\n\t" "v_add_f32_dpp v60, v60, v60 quad_perm:[1,0,3,2] row_mask:0xf bank_mask:0xf\n\t" \
  "v_add_f32_dpp v120, v120, v120 quad_perm:[1,0,3,2] row_mask:0xf bank_mask:0xf\n\t" "v_add_f32_dpp v61, v61, v61 quad_perm:[1,0,3,2] row_mask:0xf bank_mask:0xf\n\t" \
  "v_add_f32_dpp v121, v121, v121 quad_perm:[1,0,3,2] row_mask:0xf bank_mask:0xf\n\t" "v_add_f32_dpp v62, v62, v62 quad_perm:[1,0,3,2] row_mask:0xf bank_mask:0xf\n\t" \
  "v_add_f32_dpp v122, v122, v122 quad_perm:[1,0,3,2] row_mask:0xf bank_mask:0xf\n\t" "v_add_f32_dpp v60, v60, v60 quad_perm:[2,3,0,1] row_mask:0xf bank_mask:0xf\n\t" \
  "v_add_f32_dpp v120, v120, v120 quad_perm:[2,3,0,1] row_mask:0xf bank_mask:0xf\n\t" "v_add_f32_dpp v61, v61, v61 quad_perm:[2,3,0,1] row_mask:0xf bank_mask:0xf\n\t" \
  "v_add_f32_dpp v121, v121, v121 quad_perm:[2,3,0,1] row_mask:0xf bank_mask:0xf\n\t" "v_add_f32_dpp v62, v62, v62 quad_perm:[2,3,0,1] row_mask:0xf bank_mask:0xf\n\t" \
  "v_add_f32_dpp v122, v122, v122 quad_perm:[2,3,0,1] row_mask:0xf bank_mask:0xf\n\t" "v_add_f32_dpp v60, v60, v60 row_half_mirror row_mask:0xf bank_mask:0xf\n\t" \
  "v_add_f32_dpp v120, v120, v120 row_half_mirror row_mask:0xf bank_mask:0xf\n\t" "v_add_f32_dpp v61, v61, v61 row_half_mirror row_mask:0xf bank_mask:0xf\n\t" \
  "v_add_f32_dpp v121, v121, v121 row_half_mirror row_mask:0xf bank_mask:0xf\n\t" "v_add_f32_dpp v62, v62, v62 row_half_mirror row_mask:0xf bank_mask:0xf\n\t" \
  "v_add_f32_dpp v122, v122, v122 row_half_mirror row_mask:0xf bank_mask:0xf\n\t" "s_barrier\n\t" "v_cndmask_b32 v60, v60, v120, s[70:71]\n\t" "v_cndmask_b32 v61, v61, v121, s[70:71]\n\t" \
  "v_cndmask_b32 v62, v62, v122, s[70:71]\n\t" "v_mov_b32 v101, v104\n\t" "v_mov_b32 v102, v108\n\t" "s_waitcnt vmcnt(0)\n\t" "v_add_co_u32 v58, vcc, 1, v40\n\t" "v_add_u32 v63, 1, v40\n\t" \
  "v_add_u32 v103, 1, v40\n\t" "v_addc_co_u32 v59, vcc, 0, v41, vcc\n\t" "s_mov_b64 exec, s[98:99]\n\t" "global_store_dwordx4 v12, v[60:63], s[88:89] sc1\n\t" "s_mov_b64 exec, -1\n\t" \
  "s_cbranch_scc1 .Lc2_%=\n\t" "s_mov_b64 exec, 0xffff\n\t" "global_store_dwordx4 v32, v[100:103], s[92:93] sc1\n\t" "s_mov_b64 exec, -1\n\t" ".Lc2_%=:\n\t" "v_mov_b64 v[84:85], 0\n\t" \
  "v_mov_b32 v86, 0\n\t" "v_mov_b32 v87, v58\n\t" "v_mov_b64 v[104:105], 0\n\t" "v_mov_b32 v106, 0\n\t" "v_mov_b32 v107, v58\n\t" "s_barrier\n\t" "s_cmp_lg_u32 s33, 1\n\t" "s_cbranch_scc1 .Ltp_%=\n\t" \
  "s_memrealtime s[96:97]\n\t" "s_mul_i32 s98, s29, 1280\n\t" "s_add_u32 s98, s98, 5368320\n\t" "v_min_u32 v4, 9, v9\n\t" "v_lshl_add_u32 v4, v4, 7, s98\n\t" "s_lshl_b32 s99, s28, 7\n\t" \
  "s_add_u32 s98, s98, s99\n\t" "v_lshl_add_u32 v7, v9, 3, s98\n\t" "v_mov_b32 v57, v58\n\t" "s_waitcnt lgkmcnt(0)\n\t" "v_mov_b32 v56, s96\n\t" "s_mov_b64 exec, 0xffff\n\t" \
  "global_store_dwordx2 v7, v[56:57], s[24:25] sc1\n\t" "s_mov_b64 exec, -1\n\t" ".Ltp_%=:\n\t" "s_barrier\n\t" "s_barrier\n\t" "global_load_dwordx4 v[72:75], v32, s[86:87] offset:0 sc1\n\t" \
  "global_load_dwordx4 v[76:79], v32, s[86:87] offset:1024 sc1\n\t" "global_load_dwordx4 v[92:95], v32, s[90:91] offset:0 sc1\n\t" "global_load_dwordx4 v[96:99], v32, s[90:91] offset:1024 sc1\n\t" \
  "s_mov_b32 exec_hi, 0\n\t" "global_load_dwordx4 v[84:87], v32, s[86:87] offset:2048 sc1\n\t" "global_load_dwordx4 v[104:107], v32, s[90:91] offset:2048 sc1\n\t" "s_mov_b32 exec_hi, -1\n\t" \
  "s_cmp_lg_u32 s33, 1\n\t" "s_cbranch_scc1 .Lfl0_%=\n\t" "global_load_dwordx2 v[112:113], v4, s[24:25] sc1\n\t" ".Lfl0_%=:\n\t" "s_barrier\n\t" "s_mov_b32 s98, 0\n\t" ".Lrg_%=:\n\t" \
  "s_waitcnt vmcnt(0)\n\t" "v_pk_add_f32 v[120:121], v[72:73], v[76:77]\n\t" "v_pk_add_f32 v[126:127], v[92:93], v[96:97]\n\t" "v_add_f32 v122, v74, v78\n\t" "v_add_f32 v128, v94, v98\n\t" \
  "v_pk_add_f32 v[120:121], v[120:121], v[84:85]\n\t" "v_pk_add_f32 v[126:127], v[126:127], v[104:105]\n\t" "v_add_f32 v123, v122, v86\n\t" "v_add_f32 v129, v128, v106\n\t" \
  "v_add_f32 v122, v122, v86\n\t" "v_add_f32 v128, v128, v106\n\t" "v_permlane32_swap_b32 v120, v121\n\t" "v_permlane32_swap_b32 v126, v127\n\t" "v_permlane32_swap_b32 v122, v123\n\t" \
  "v_permlane32_swap_b32 v128, v129\n\t" "v_add_f32 v120, v120, v121\n\t" "v_add_f32 v126, v126, v127\n\t" "v_add_f32 v122, v122, v123\n\t" "v_add_f32 v128, v128, v129\n\t" "s_nop 0\n\t" \
  "v_permlane16_swap_b32 v120, v122\n\t" "v_permlane16_swap_b32 v126, v128\n\t" "v_add_f32 v120, v120, v122\n\t" "v_add_f32 v126, v126, v128\n\t" "v_mul_f32 v120, s76, v120\n\t" \
  "v_cmp_ne_u32 vcc, v75, v58\n\t" "s_cmp_lg_u32 s98, 0\n\t" "s_cbranch_scc1 .Lb8_%=\n\t" "s_barrier\n\t" ".Lb8_%=:\n\t" "s_mov_b32 s98, 1\n\t" "s_cmp_lg_u32 s33, 3\n\t" "s_cbranch_scc1 .Lfl8_%=\n\t" \
  "global_load_dwordx2 v[112:113], v4, s[24:25] sc1\n\t" ".Lfl8_%=:\n\t" "v_mov_b32_dpp v122, v120 quad_perm:[0,0,2,2] row_mask:0xf bank_mask:0xf\n\t" "v_cmp_ne_u32 s[80:81], v79, v58\n\t" \
  "s_or_b64 vcc, vcc, s[80:81]\n\t" "v_fma_f32 v121, -v122, v122, v120\n\t" "v_cmp_ne_u32 s[78:79], v87, v58\n\t" "s_or_b64 vcc, vcc, s[78:79]\n\t" "v_max_f32 v121, 0, v121\n\t" \
  "v_cmp_ne_u32 s[80:81], v95, v58\n\t" "s_or_b64 vcc, vcc, s[80:81]\n\t" "v_cvt_pk_bf16_f32 v120, v121, v120\n\t" "v_cmp_ne_u32 s[78:79], v99, v58\n\t" "s_or_b64 vcc, vcc, s[78:79]\n\t" \
  "v_lshlrev_b32 v121, 16, v120\n\t" "v_cmp_ne_u32 s[80:81], v107, v58\n\t" "s_or_b64 vcc, vcc, s[80:81]\n\t" "v_add_f32 v121, 0x3727c5ac, v121\n\t" "v_cvt_pk_bf16_f32 v121, v121, v121\n\t" \
  "v_lshlrev_b32 v121, 16, v121\n\t" "v_sqrt_f32 v121, v121\n\t" "v_and_b32 v120, 0xffff0000, v120\n\t" "v_cvt_pk_bf16_f32 v121, v121, v121\n\t" "v_lshlrev_b32 v121, 16, v121\n\t" \
  "s_cbranch_vccnz .Lsg_%=\n\t" ".Lrh_%=:\n\t" "s_mov_b64 exec, s[70:71]\n\t" "v_rcp_f32 v120, v121\n\t" "s_mov_b64 exec, -1\n\t" "ds_write_b32 v5, v126\n\t" "ds_write_b32 v13, v120\n\t" \
  "s_mov_b64 exec, 1\n\t" "ds_add_u32 v37, v38 offset:12\n\t" "s_mov_b64 exec, -1\n\t" "s_setprio 0\n\t" "s_cmp_lg_u32 s33, 3\n\t" "s_cbranch_scc1 .Lnfl_%=\n\t" "s_waitcnt vmcnt(0)\n\t" \
  "v_cmp_ne_u64 vcc, v[112:113], v[58:59]\n\t" "s_cbranch_vccz .Lsfm_%=\n\t" ".Lsfl_%=:\n\t" "global_load_dwordx2 v[112:113], v4, s[24:25] sc1\n\t" "s_sub_u32 s39, s39, 1\n\t" \
  "s_cmp_lt_i32 s39, 0\n\t" "s_waitcnt vmcnt(0)\n\t" "s_cbranch_scc1 .Lsfm_%=\n\t" "v_cmp_ne_u64 vcc, v[112:113], v[58:59]\n\t" "s_cbranch_vccnz .Lsfl_%=\n\t" ".Lsfm_%=:\n\t" "s_mov_b64 exec, 1\n\t" \
  "ds_add_u32 v37, v38 offset:4\n\t" "s_mov_b64 exec, -1\n\t" ".Lnfl_%=:\n\t" "s_cmp_lg_u32 s33, 1\n\t" "s_cbranch_scc1 .Lnlg_%=\n\t" "v_subrev_u32 v112, s96, v112\n\t" \
  "v_cmp_eq_u32 vcc, v113, v58\n\t" "s_lshl_b32 s98, 7, s27\n\t" "s_lshr_b32 s98, s98, 1\n\t" "s_and_b32 s98, s98, 31\n\t" "v_cndmask_b32 v112, 60, v112, vcc\n\t" "s_sub_u32 s99, s28, s27\n\t" \
  "s_lshl_b32 s98, s98, s99\n\t" "s_or_b32 s98, s98, 0xffff0000\n\t" "s_mov_b32 s99, 0\n\t" "v_med3_i32 v112, v112, 0, 64\n\t" "v_cndmask_b32 v112, 0, v112, s[98:99]\n\t" "s_nop 1\n\t" \
  "v_max_u32_dpp v112, v112, v112 quad_perm:[1,0,3,2] row_mask:0xf bank_mask:0xf\n\t" "s_nop 1\n\t" "v_max_u32_dpp v112, v112, v112 quad_perm:[2,3,0,1] row_mask:0xf bank_mask:0xf\n\t" "s_nop 1\n\t" \
  "v_max_u32_dpp v112, v112, v112 row_half_mirror row_mask:0xf bank_mask:0xf\n\t" "s_nop 1\n\t" "v_max_u32_dpp v112, v112, v112 row_mirror row_mask:0xf bank_mask:0xf\n\t" "v_add_u32 v7, v10, v37\n\t" \
  "ds_write_b32 v7, v112 offset:80\n\t" ".Lnlg_%=:\n\t" "s_cmp_lg_u32 s33, 2\n\t" "s_cbranch_scc1 .Latt_%=\n\t" "v_dot2_f32_bf16 v1, v68, s77, 0\n\t" "v_dot2_f32_bf16 v1, v64, s77, v1\n\t" \
  "v_dot2_f32_bf16 v1, v65, s77, v1\n\t" "v_dot2_f32_bf16 v1, v66, s77, v1\n\t" "v_dot2_f32_bf16 v1, v67, s77, v1\n\t" "s_nop 2\n\t" \
  "v_add_f32_dpp v1, v1, v1 quad_perm:[1,0,3,2] row_mask:0xf bank_mask:0xf\n\t" "s_nop 1\n\t" "v_add_f32_dpp v1, v1, v1 quad_perm:[2,3,0,1] row_mask:0xf bank_mask:0xf\n\t" "s_nop 1\n\t" \
  "v_add_f32_dpp v1, v1, v1 row_half_mirror row_mask:0xf bank_mask:0xf\n\t" "s_nop 1\n\t" "v_add_f32_dpp v1, v1, v1 row_mirror row_mask:0xf bank_mask:0xf\n\t" "v_mov_b32 v2, v1\n\t" "s_nop 1\n\t" \
  "v_permlane32_swap_b32 v1, v2\n\t" "v_add_f32 v1, v1, v2\n\t" "v_mov_b32 v2, v1\n\t" "s_nop 1\n\t" "v_permlane16_swap_b32 v1, v2\n\t" "v_add_f32 v1, v1, v2\n\t" "ds_write_b32 v37, v1 offset:40\n\t" \
  "s_branch .Latt_%=\n\t" ".Ll_%=:\n\t" "s_movk_i32 s38, 1\n\t" "s_add_u32 s31, s31, 1\n\t" "s_cmp_eq_u32 s31, 10\n\t" "s_cselect_b32 s31, 0, s31\n\t" "s_mul_i32 s76, s31, 245760\n\t" \
  "s_lshl_b32 s77, s31, 7\n\t" "s_add_u32 s78, s10, s76\n\t" "s_addc_u32 s79, s11, 0\n\t" "s_add_u32 s80, s8, s77\n\t" "s_addc_u32 s81, s9, 0\n\t" "s_and_b32 s76, s38, 3\n\t" \
  "s_mul_i32 s76, s76, 36864\n\t" "s_add_u32 m0, s76, s87\n\t" "s_branch .Ll1_%=\n\t" ".Ll0_%=:\n\t" "s_add_u32 s31, s31, 1\n\t" "s_cmp_eq_u32 s31, 10\n\t" "s_cselect_b32 s31, 0, s31\n\t" \
  "s_mul_i32 s76, s31, 245760\n\t" "s_lshl_b32 s77, s31, 7\n\t" "s_add_u32 s78, s10, s76\n\t" "s_addc_u32 s79, s11, 0\n\t" "s_add_u32 s80, s8, s77\n\t" "s_addc_u32 s81, s9, 0\n\t" \
  "s_and_b32 s76, s38, 3\n\t" "s_mul_i32 s76, s76, 36864\n\t" "s_add_u32 m0, s76, s87\n\t" "s_waitcnt vmcnt(9)\n\t" "s_barrier\n\t" ".Ll1_%=:\n\t" \
  "global_load_lds_dwordx4 v46, s[80:81] offset:-2048\n\t" "global_load_lds_dwordx4 v47, s[80:81] offset:-1024\n\t" "global_load_lds_dwordx4 v48, s[80:81] offset:0\n\t" "s_add_u32 m0, s76, s86\n\t" \
  "s_add_u32 s38, s38, 1\n\t" "global_load_lds_dwordx4 v40, s[78:79] offset:-3072\n\t" "global_load_lds_dwordx4 v41, s[78:79] offset:-2048\n\t" "global_load_lds_dwordx4 v42, s[78:79] offset:-1024\n\t" \
  "global_load_lds_dwordx4 v43, s[78:79] offset:0\n\t" "global_load_lds_dwordx4 v44, s[78:79] offset:1024\n\t" "global_load_lds_dwordx4 v45, s[78:79] offset:2048\n\t" "s_cmp_lt_u32 s38, 9\n\t" \
  "s_cbranch_scc1 .Ll0_%=\n\t" "s_add_u32 s31, s31, 1\n\t" "s_cmp_eq_u32 s31, 10\n\t" "s_cselect_b32 s31, 0, s31\n\t" "s_mul_i32 s76, s31, 245760\n\t" "s_lshl_b32 s77, s31, 7\n\t" \
  "s_add_u32 s78, s10, s76\n\t" "s_addc_u32 s79, s11, 0\n\t" "s_add_u32 s80, s8, s77\n\t" "s_addc_u32 s81, s9, 0\n\t" "s_and_b32 s76, s38, 3\n\t" "s_mul_i32 s76, s76, 36864\n\t" \
  "s_add_u32 m0, s76, s87\n\t" "v_lshrrev_b32 v1, 3, v8\n\t" "v_lshrrev_b32 v4, 5, v8\n\t" "v_and_b32 v2, 6, v1\n\t" "v_and_b32 v1, 3, v1\n\t" "v_xor_b32 v2, v2, v8\n\t" \
  "v_mul_u32_u24 v1, 1280, v1\n\t" "v_and_b32 v2, 7, v2\n\t" "s_lshl_b32 s92, s28, 7\n\t" "v_lshl_add_u32 v1, v2, 4, v1\n\t" "s_and_b32 s93, s33, 1\n\t" "s_and_b32 s94, s33, 2\n\t" \
  "v_add_u32 v1, s92, v1\n\t" "s_mul_i32 s92, s93, 5\n\t" "s_add_u32 s92, s92, s94\n\t" "s_add_u32 s92, s92, 5\n\t" "v_add_u32 v2, s92, v4\n\t" "s_lshl2_add_u32 s92, s93, s94\n\t" \
  "s_movk_i32 s93, 5120\n\t" "v_add_lshl_u32 v50, v4, s92, 2\n\t" "v_mad_u32_u24 v49, v2, s93, v1\n\t" "s_lshl_b32 s92, s92, 9\n\t" "s_add_u32 s92, s92, 73728\n\t" "s_waitcnt vmcnt(9)\n\t" \
  "s_barrier\n\t" "global_load_lds_dwordx4 v46, s[80:81] offset:-2048\n\t" "global_load_lds_dwordx4 v47, s[80:81] offset:-1024\n\t" "global_load_lds_dwordx4 v48, s[80:81] offset:0\n\t" \
  "s_add_u32 m0, s76, s86\n\t" "s_nop 0\n\t" "global_load_lds_dwordx4 v40, s[78:79] offset:-3072\n\t" "global_load_lds_dwordx4 v41, s[78:79] offset:-2048\n\t" \
  "global_load_lds_dwordx4 v42, s[78:79] offset:-1024\n\t" "global_load_lds_dwordx4 v43, s[78:79] offset:0\n\t" "global_load_lds_dwordx4 v44, s[78:79] offset:1024\n\t" \
  "global_load_lds_dwordx4 v45, s[78:79] offset:2048\n\t" "s_waitcnt vmcnt(9)\n\t" "s_barrier\n\t" "s_sleep 2\n\t" "s_mov_b64 s[78:79], s[12:13]\n\t" "s_add_u32 m0, s92, 0\n\t" "s_nop 0\n\t" \
  "global_load_lds_dwordx4 v49, s[78:79]\n\t" "s_add_u32 s78, s78, 76800\n\t" "s_addc_u32 s79, s79, 0\n\t" "s_add_u32 m0, s92, 4096\n\t" "s_nop 0\n\t" "global_load_lds_dwordx4 v49, s[78:79]\n\t" \
  "s_add_u32 s78, s78, 76800\n\t" "s_addc_u32 s79, s79, 0\n\t" "s_add_u32 m0, s92, 8192\n\t" "s_nop 0\n\t" "global_load_lds_dwordx4 v49, s[78:79]\n\t" "s_add_u32 s78, s78, 76800\n\t" \
  "s_addc_u32 s79, s79, 0\n\t" "s_add_u32 m0, s92, 12288\n\t" "s_nop 0\n\t" "global_load_lds_dwordx4 v49, s[78:79]\n\t" "s_add_u32 s78, s78, 76800\n\t" "s_addc_u32 s79, s79, 0\n\t" \
  "s_add_u32 m0, s92, 16384\n\t" "s_waitcnt vmcnt(4)\n\t" "s_barrier\n\t" "global_load_lds_dwordx4 v49, s[78:79]\n\t" "s_add_u32 s78, s78, 102400\n\t" "s_addc_u32 s79, s79, 0\n\t" \
  "s_add_u32 m0, s92, 40960\n\t" "s_nop 0\n\t" "global_load_lds_dwordx4 v49, s[78:79]\n\t" "s_add_u32 s78, s78, 76800\n\t" "s_addc_u32 s79, s79, 0\n\t" "s_add_u32 m0, s92, 45056\n\t" "s_nop 0\n\t" \
  "global_load_lds_dwordx4 v49, s[78:79]\n\t" "s_add_u32 s78, s78, 76800\n\t" "s_addc_u32 s79, s79, 0\n\t" "s_add_u32 m0, s92, 49152\n\t" "s_nop 0\n\t" "global_load_lds_dwordx4 v49, s[78:79]\n\t" \
  "s_add_u32 s78, s78, 76800\n\t" "s_addc_u32 s79, s79, 0\n\t" "s_add_u32 m0, s92, 53248\n\t" "s_nop 0\n\t" "global_load_lds_dwordx4 v49, s[78:79]\n\t" "s_add_u32 s78, s78, 76800\n\t" \
  "s_addc_u32 s79, s79, 0\n\t" "s_add_u32 m0, s92, 57344\n\t" "s_nop 0\n\t" "global_load_lds_dwordx4 v49, s[78:79]\n\t" "s_setprio 0\n\t" "s_mov_b32 s94, 0xf41e3209\n\t" \
  "v_bfe_u32 v2, s94, v50, 4\n\t" "v_mad_u32_u24 v3, v2, s93, v1\n\t" "s_mov_b32 s94, 0xfe0d3218\n\t" "v_bfe_u32 v2, s94, v50, 4\n\t" "v_mad_u32_u24 v4, v2, s93, v1\n\t" \
  "s_mov_b32 s94, 0xfedc2107\n\t" "v_bfe_u32 v2, s94, v50, 4\n\t" "v_mad_u32_u24 v5, v2, s93, v1\n\t" "s_mov_b32 s94, 0xedcbf106\n\t" "v_bfe_u32 v2, s94, v50, 4\n\t" \
  "v_mad_u32_u24 v6, v2, s93, v1\n\t" "s_mov_b32 s94, 0xfdcabe05\n\t" "v_bfe_u32 v2, s94, v50, 4\n\t" "v_mad_u32_u24 v7, v2, s93, v1\n\t" "s_waitcnt vmcnt(0)\n\t" "s_mov_b64 exec, 1\n\t" \
  "ds_add_u32 v37, v38 offset:4\n\t" "s_mov_b64 exec, -1\n\t" "s_branch .Latt_%=\n\t" ".Lsg_%=:\n\t" "s_sub_u32 s39, s39, 1\n\t" "s_cmp_lt_i32 s39, 0\n\t" "s_cbranch_scc1 .Lrh_%=\n\t" \
  "global_load_dwordx4 v[72:75], v32, s[86:87] offset:0 sc1\n\t" "global_load_dwordx4 v[76:79], v32, s[86:87] offset:1024 sc1\n\t" "global_load_dwordx4 v[92:95], v32, s[90:91] offset:0 sc1\n\t" \
  "global_load_dwordx4 v[96:99], v32, s[90:91] offset:1024 sc1\n\t" "s_mov_b32 exec_hi, 0\n\t" "global_load_dwordx4 v[84:87], v32, s[86:87] offset:2048 sc1\n\t" \
  "global_load_dwordx4 v[104:107], v32, s[90:91] offset:2048 sc1\n\t" "s_mov_b32 exec_hi, -1\n\t" "s_branch .Lrg_%=\n\t" ".Lts_%=:\n\t" "ds_read_b32 v39, v37 offset:12\n\t" \
  "ds_read_b128 v[160:163], v12 offset:1024\n\t" "ds_read_b128 v[164:167], v12 offset:1088\n\t" "ds_read2_b32 v[136:137], v4 offset0:0 offset1:96\n\t" \
  "ds_read2_b32 v[138:139], v4 offset0:16 offset1:112\n\t" "ds_read2_b32 v[140:141], v4 offset0:32 offset1:128\n\t" "ds_read2_b32 v[142:143], v4 offset0:48 offset1:144\n\t" \
  "ds_read2_b32 v[144:145], v4 offset0:64 offset1:160\n\t" "ds_read2_b32 v[146:147], v4 offset0:80 offset1:176\n\t" "ds_read_b32 v14, v13 offset:1152\n\t" "s_sub_u32 s39, s39, 1\n\t" \
  "s_cmp_lt_i32 s39, 0\n\t" "s_waitcnt lgkmcnt(0)\n\t" "v_readfirstlane_b32 s38, v39\n\t" "s_cbranch_scc1 .Ltr_%=\n\t" "s_cmp_ge_u32 s38, 4\n\t" "s_cbranch_scc0 .Lts_%=\n\t" "s_branch .Ltr_%=\n\t" \
  ".Lsq_%=:\n\t" "ds_read_b32 v39, v37 offset:0\n\t" "s_sub_u32 s39, s39, 1\n\t" "s_cmp_lt_i32 s39, 0\n\t" "s_waitcnt lgkmcnt(0)\n\t" "v_readfirstlane_b32 s38, v39\n\t" "s_cbranch_scc1 .Lrq_%=\n\t" \
  "s_cmp_ge_u32 s38, 4\n\t" "s_cbranch_scc0 .Lsq_%=\n\t" "s_branch .Lq_%=\n\t" ".Lsqm_%=:\n\t" "ds_read_b32 v39, v37 offset:8\n\t" "s_sub_u32 s39, s39, 1\n\t" "s_cmp_lt_i32 s39, 0\n\t" \
  "s_waitcnt lgkmcnt(0)\n\t" "v_readfirstlane_b32 s38, v39\n\t" "s_cbranch_scc1 .Lrqm_%=\n\t" "s_cmp_ge_u32 s38, 4\n\t" "s_cbranch_scc0 .Lsqm_%=\n\t" "s_branch .Lrqm_%=\n\t" ".Lsq0_%=:\n\t" \
  "ds_read_b32 v39, v37 offset:0\n\t" "s_sub_u32 s39, s39, 1\n\t" "s_cmp_lt_i32 s39, 0\n\t" "s_waitcnt lgkmcnt(0)\n\t" "v_readfirstlane_b32 s38, v39\n\t" "s_cbranch_scc1 .Lrq0_%=\n\t" \
  "s_cmp_ge_u32 s38, 4\n\t" "s_cbranch_scc0 .Lsq0_%=\n\t" "s_branch .Lq0_%=\n\t" ".Lsv0_%=:\n\t" "ds_read_b32 v39, v37 offset:4\n\t" "s_sub_u32 s39, s39, 1\n\t" "s_cmp_lt_i32 s39, 0\n\t" \
  "s_waitcnt lgkmcnt(0)\n\t" "v_readfirstlane_b32 s38, v39\n\t" "s_cbranch_scc1 .Lrv0_%=\n\t" "s_cmp_ge_u32 s38, 9\n\t" "s_cbranch_scc0 .Lsv0_%=\n\t" "s_branch .Lv0_%=\n\t" ".Lsr_%=:\n\t" \
  "global_load_dwordx4 v[148:151], v32, s[58:59] sc1\n\t" "global_load_dwordx4 v[144:147], v32, s[56:57] sc1\n\t" "s_sub_u32 s39, s39, 1\n\t" "s_cmp_lt_i32 s39, 0\n\t" "s_waitcnt vmcnt(0)\n\t" \
  "s_cbranch_scc1 .Lrr_%=\n\t" "v_max_u32 v124, v144, v148\n\t" "v_cmp_eq_u32 vcc, -1, v124\n\t" "s_cbranch_vccnz .Lsr_%=\n\t" "s_branch .Lrr_%=\n\t" ".Lk0_%=:\n\t" "v_xor_b32 v0, 32, v35\n\t" \
  "v_xor_b32 v1, 64, v35\n\t" "v_xor_b32 v2, 96, v35\n\t" "ds_read_b32 v39, v37 offset:8\n\t" "s_waitcnt lgkmcnt(0)\n\t" "v_readfirstlane_b32 s38, v39\n\t" "s_cmp_ge_u32 s38, 4\n\t" \
  "s_cbranch_scc0 .Lsqm_%=\n\t" ".Lrqm_%=:\n\t" "ds_read_b32 v39, v37 offset:80\n\t" "s_memrealtime s[76:77]\n\t" "s_mul_i32 s80, s35, 5\n\t" "s_add_u32 s80, s80, 24\n\t" "s_waitcnt lgkmcnt(0)\n\t" \
  "v_readfirstlane_b32 s38, v39\n\t" "s_add_u32 s80, s80, s38\n\t" ".Lqd_%=:\n\t" "s_memrealtime s[78:79]\n\t" "s_waitcnt lgkmcnt(0)\n\t" "s_sub_u32 s78, s78, s76\n\t" "s_cmp_lt_u32 s78, s80\n\t" \
  "s_cbranch_scc1 .Lqd_%=\n\t" "global_load_dwordx4 v[148:151], v32, s[58:59] sc1\n\t" "global_load_dwordx4 v[144:147], v32, s[56:57] sc1\n\t" ".Lq0_%=:\n\t" "ds_read_b32 v39, v37\n\t" \
  "ds_read_b64 v[40:41], v30 offset:0\n\t" "ds_read_b64 v[42:43], v30 offset:3072\n\t" "ds_read_b64 v[44:45], v30 offset:11776\n\t" "ds_read_b64 v[46:47], v30 offset:14848\n\t" \
  "ds_read_b64 v[48:49], v30 offset:12288\n\t" "ds_read_b64 v[50:51], v30 offset:15360\n\t" "s_waitcnt lgkmcnt(6)\n\t" "v_readfirstlane_b32 s38, v39\n\t" "s_cmp_ge_u32 s38, 4\n\t" \
  "s_cbranch_scc0 .Lsq0_%=\n\t" ".Lrq0_%=:\n\t" "s_waitcnt lgkmcnt(0)\n\t" "v_cndmask_b32 v52, 0, v40, s[44:45]\n\t" "v_cndmask_b32 v53, 0, v41, s[44:45]\n\t" "v_cndmask_b32 v54, 0, v42, s[46:47]\n\t" \
  "v_cndmask_b32 v55, 0, v43, s[46:47]\n\t" "v_cndmask_b32 v40, 0, v40, s[40:41]\n\t" "v_cndmask_b32 v41, 0, v41, s[40:41]\n\t" "v_cndmask_b32 v42, 0, v42, s[42:43]\n\t" \
  "v_cndmask_b32 v43, 0, v43, s[42:43]\n\t" "s_nop 1\n\t" "v_mfma_f32_16x16x32_bf16 v[108:111], v[44:47], v[40:43], v[20:23]\n\t" \
  "v_mfma_f32_16x16x32_bf16 v[112:115], v[48:51], v[40:43], v[24:27]\n\t" "v_mfma_f32_16x16x32_bf16 v[116:119], v[44:47], v[52:55], v[20:23]\n\t" \
  "v_mfma_f32_16x16x32_bf16 v[120:123], v[48:51], v[52:55], v[24:27]\n\t" ".Lv0_%=:\n\t" "ds_read_b32 v39, v37 offset:4\n\t" "ds_read_b64 v[128:129], v30 offset:24064\n\t" \
  "ds_read_b64 v[130:131], v30 offset:24576\n\t" "ds_read_b64 v[132:133], v30 offset:27136\n\t" "ds_read_b64 v[134:135], v30 offset:27648\n\t" "s_waitcnt lgkmcnt(4)\n\t" \
  "v_readfirstlane_b32 s38, v39\n\t" "s_cmp_ge_u32 s38, 9\n\t" "s_cbranch_scc0 .Lsv0_%=\n\t" ".Lrv0_%=:\n\t" "s_waitcnt lgkmcnt(0)\n\t" "v_cndmask_b32 v136, v128, 0, s[50:51]\n\t" \
  "v_cndmask_b32 v128, 0, v128, s[50:51]\n\t" "v_cndmask_b32 v137, v129, 0, s[50:51]\n\t" "v_cndmask_b32 v129, 0, v129, s[50:51]\n\t" "v_cndmask_b32 v138, v130, 0, s[50:51]\n\t" \
  "v_cndmask_b32 v130, 0, v130, s[50:51]\n\t" "v_cndmask_b32 v139, v131, 0, s[50:51]\n\t" "v_cndmask_b32 v131, 0, v131, s[50:51]\n\t" "v_cndmask_b32 v140, v132, 0, s[52:53]\n\t" \
  "v_cndmask_b32 v132, 0, v132, s[52:53]\n\t" "v_cndmask_b32 v141, v133, 0, s[52:53]\n\t" "v_cndmask_b32 v133, 0, v133, s[52:53]\n\t" "v_cndmask_b32 v142, v134, 0, s[52:53]\n\t" \
  "v_cndmask_b32 v134, 0, v134, s[52:53]\n\t" "v_cndmask_b32 v143, v135, 0, s[52:53]\n\t" "v_cndmask_b32 v135, 0, v135, s[52:53]\n\t" "s_cmp_ge_u32 s26, 4\n\t" "s_cbranch_scc1 .Lnw2_%=\n\t" \
  "s_add_u32 m0, s92, 20480\n\t" "s_nop 0\n\t" "global_load_lds_dwordx4 v3, s[12:13]\n\t" "s_add_u32 s12, s12, 81920\n\t" "s_addc_u32 s13, s13, 0\n\t" "s_add_u32 m0, s92, 24576\n\t" "s_nop 0\n\t" \
  "global_load_lds_dwordx4 v4, s[12:13]\n\t" "s_add_u32 s12, s12, 81920\n\t" "s_addc_u32 s13, s13, 0\n\t" "s_add_u32 m0, s92, 28672\n\t" "s_nop 0\n\t" "global_load_lds_dwordx4 v5, s[12:13]\n\t" \
  "s_add_u32 s12, s12, 81920\n\t" "s_addc_u32 s13, s13, 0\n\t" "s_add_u32 m0, s92, 32768\n\t" "s_nop 0\n\t" "global_load_lds_dwordx4 v6, s[12:13]\n\t" "s_add_u32 s12, s12, 81920\n\t" \
  "s_addc_u32 s13, s13, 0\n\t" "s_add_u32 m0, s92, 36864\n\t" "s_nop 0\n\t" "global_load_lds_dwordx4 v7, s[12:13]\n\t" "s_add_u32 s12, s12, 81920\n\t" "s_addc_u32 s13, s13, 0\n\t" \
  "s_add_u32 m0, s92, 61440\n\t" "s_nop 0\n\t" "global_load_lds_dwordx4 v3, s[12:13]\n\t" "s_add_u32 s12, s12, 81920\n\t" "s_addc_u32 s13, s13, 0\n\t" "s_add_u32 m0, s92, 65536\n\t" "s_nop 0\n\t" \
  "global_load_lds_dwordx4 v4, s[12:13]\n\t" "s_add_u32 s12, s12, 81920\n\t" "s_addc_u32 s13, s13, 0\n\t" "s_add_u32 m0, s92, 69632\n\t" "s_nop 0\n\t" "global_load_lds_dwordx4 v5, s[12:13]\n\t" \
  "s_add_u32 s12, s12, 81920\n\t" "s_addc_u32 s13, s13, 0\n\t" "s_add_u32 m0, s92, 73728\n\t" "s_nop 0\n\t" "global_load_lds_dwordx4 v6, s[12:13]\n\t" "s_add_u32 s12, s12, 81920\n\t" \
  "s_addc_u32 s13, s13, 0\n\t" "s_add_u32 m0, s92, 77824\n\t" "s_nop 0\n\t" "global_load_lds_dwordx4 v7, s[12:13]\n\t" ".Lnw2_%=:\n\t" "s_cmp_ge_u32 s26, 4\n\t" "s_waitcnt vmcnt(10)\n\t" \
  "s_cbranch_scc0 .Lnw3_%=\n\t" "s_waitcnt vmcnt(0)\n\t" ".Lnw3_%=:\n\t" "s_setprio 2\n\t" "v_max_u32 v124, v144, v148\n\t" "v_cmp_eq_u32 vcc, -1, v124\n\t" "s_cbranch_vccnz .Lsr_%=\n\t" \
  ".Lrr_%=:\n\t" "v_cndmask_b32 v124, 0, v144, s[48:49]\n\t" "v_cndmask_b32 v125, 0, v145, s[48:49]\n\t" "v_cndmask_b32 v126, v144, 0, s[48:49]\n\t" "v_cndmask_b32 v127, v145, 0, s[48:49]\n\t" \
  "v_mfma_f32_16x16x16_bf16 v[108:111], v[150:151], v[124:125], v[108:111]\n\t" "v_mfma_f32_16x16x16_bf16 v[112:115], v[146:147], v[124:125], v[112:115]\n\t" \
  "v_mfma_f32_16x16x16_bf16 v[116:119], v[150:151], v[126:127], v[116:119]\n\t" "v_mfma_f32_16x16x16_bf16 v[120:123], v[146:147], v[126:127], v[120:123]\n\t" "s_branch .Lk1_%=\n\t" ".Lsv1_%=:\n\t" \
  "ds_read_b32 v39, v37 offset:4\n\t" "s_sub_u32 s39, s39, 1\n\t" "s_cmp_lt_i32 s39, 0\n\t" "s_waitcnt lgkmcnt(0)\n\t" "v_readfirstlane_b32 s38, v39\n\t" "s_cbranch_scc1 .Lrv1_%=\n\t" \
  "s_cmp_ge_u32 s38, 9\n\t" "s_cbranch_scc0 .Lsv1_%=\n\t" "ds_read_b64 v[128:129], v30 offset:24064\n\t" "ds_read_b64 v[130:131], v30 offset:24576\n\t" "ds_read_b64 v[132:133], v30 offset:27136\n\t" \
  "ds_read_b64 v[134:135], v30 offset:27648\n\t" "ds_read_b64 v[136:137], v31 offset:24064\n\t" "ds_read_b64 v[138:139], v31 offset:24576\n\t" "ds_read_b64 v[140:141], v31 offset:27136\n\t" \
  "ds_read_b64 v[142:143], v31 offset:27648\n\t" "s_branch .Lrv1_%=\n\t" ".Lk2_%=:\n\t" "s_waitcnt lgkmcnt(8)\n\t" "v_readfirstlane_b32 s38, v39\n\t" "s_cmp_ge_u32 s38, 9\n\t" \
  "s_cbranch_scc0 .Lsv1_%=\n\t" ".Lrv1_%=:\n\t" "s_waitcnt lgkmcnt(0)\n\t" "v_mfma_f32_16x16x32_bf16 v[152:155], v[128:131], v[112:115], 0\n\t" \
  "v_mfma_f32_16x16x32_bf16 v[164:167], v[140:143], v[120:123], 0\n\t" "v_mfma_f32_16x16x32_bf16 v[156:159], v[132:135], v[112:115], 0\n\t" \
  "v_mfma_f32_16x16x32_bf16 v[160:163], v[136:139], v[120:123], 0\n\t" "v_xor_b32 v0, 32, v35\n\t" "v_xor_b32 v1, 64, v35\n\t" "v_xor_b32 v2, 96, v35\n\t" "s_nop 1\n\t" \
  "v_cvt_pk_bf16_f32 v144, v152, v153\n\t" "v_cvt_pk_bf16_f32 v145, v154, v155\n\t" "v_cvt_pk_bf16_f32 v146, v164, v165\n\t" "v_cvt_pk_bf16_f32 v147, v166, v167\n\t" \
  "global_store_dwordx4 v32, v[144:147], s[60:61] offset:-2048 sc1\n\t" "v_cvt_pk_bf16_f32 v148, v156, v157\n\t" "v_cvt_pk_bf16_f32 v149, v158, v159\n\t" "v_cvt_pk_bf16_f32 v150, v160, v161\n\t" \
  "v_cvt_pk_bf16_f32 v151, v162, v163\n\t" "s_branch .Lk3_%=\n\t" ".Lso0_%=:\n\t" "global_load_dwordx4 v[152:155], v32, s[62:63] offset:-2048 sc1\n\t" "s_sub_u32 s39, s39, 1\n\t" \
  "s_cmp_lt_i32 s39, 0\n\t" "s_waitcnt vmcnt(0)\n\t" "s_cbranch_scc1 .Lro0_%=\n\t" "v_cmp_eq_u32 vcc, -1, v152\n\t" "s_cbranch_vccnz .Lso0_%=\n\t" "s_branch .Lro0_%=\n\t" ".Lso1_%=:\n\t" \
  "global_load_dwordx4 v[156:159], v32, s[62:63] offset:-1024 sc1\n\t" "s_sub_u32 s39, s39, 1\n\t" "s_cmp_lt_i32 s39, 0\n\t" "s_waitcnt vmcnt(0)\n\t" "s_cbranch_scc1 .Lro1_%=\n\t" \
  "v_cmp_eq_u32 vcc, -1, v156\n\t" "s_cbranch_vccnz .Lso1_%=\n\t" "s_branch .Lro1_%=\n\t" ".Lso2_%=:\n\t" "global_load_dwordx4 v[160:163], v32, s[62:63] offset:0 sc1\n\t" "s_sub_u32 s39, s39, 1\n\t" \
  "s_cmp_lt_i32 s39, 0\n\t" "s_waitcnt vmcnt(0)\n\t" "s_cbranch_scc1 .Lro2_%=\n\t" "v_cmp_eq_u32 vcc, -1, v160\n\t" "s_cbranch_vccnz .Lso2_%=\n\t" "s_branch .Lro2_%=\n\t" ".Lso3_%=:\n\t" \
  "global_load_dwordx4 v[164:167], v32, s[62:63] offset:1024 sc1\n\t" "s_sub_u32 s39, s39, 1\n\t" "s_cmp_lt_i32 s39, 0\n\t" "s_waitcnt vmcnt(0)\n\t" "s_cbranch_scc1 .Lro3_%=\n\t" \
  "v_cmp_eq_u32 vcc, -1, v164\n\t" "s_cbranch_vccnz .Lso3_%=\n\t" "s_branch .Lro3_%=\n\t" ".Lso4_%=:\n\t" "global_load_dwordx4 v[152:155], v32, s[62:63] offset:2048 sc1\n\t" \
  "s_sub_u32 s39, s39, 1\n\t" "s_cmp_lt_i32 s39, 0\n\t" "s_waitcnt vmcnt(0)\n\t" "s_cbranch_scc1 .Lro4_%=\n\t" "v_cmp_eq_u32 vcc, -1, v152\n\t" "s_cbranch_vccnz .Lso4_%=\n\t" "s_branch .Lro4_%=\n\t" \
  ".Lso5_%=:\n\t" "global_load_dwordx4 v[156:159], v32, s[64:65] offset:-2048 sc1\n\t" "s_sub_u32 s39, s39, 1\n\t" "s_cmp_lt_i32 s39, 0\n\t" "s_waitcnt vmcnt(0)\n\t" "s_cbranch_scc1 .Lro5_%=\n\t" \
  "v_cmp_eq_u32 vcc, -1, v156\n\t" "s_cbranch_vccnz .Lso5_%=\n\t" "s_branch .Lro5_%=\n\t" ".Lso6_%=:\n\t" "global_load_dwordx4 v[160:163], v32, s[64:65] offset:-1024 sc1\n\t" \
  "s_sub_u32 s39, s39, 1\n\t" "s_cmp_lt_i32 s39, 0\n\t" "s_waitcnt vmcnt(0)\n\t" "s_cbranch_scc1 .Lro6_%=\n\t" "v_cmp_eq_u32 vcc, -1, v160\n\t" "s_cbranch_vccnz .Lso6_%=\n\t" "s_branch .Lro6_%=\n\t" \
  ".Lso7_%=:\n\t" "global_load_dwordx4 v[164:167], v32, s[64:65] offset:0 sc1\n\t" "s_sub_u32 s39, s39, 1\n\t" "s_cmp_lt_i32 s39, 0\n\t" "s_waitcnt vmcnt(0)\n\t" "s_cbranch_scc1 .Lro7_%=\n\t" \
  "v_cmp_eq_u32 vcc, -1, v164\n\t" "s_cbranch_vccnz .Lso7_%=\n\t" "s_branch .Lro7_%=\n\t" ".Lso8_%=:\n\t" "global_load_dwordx4 v[152:155], v32, s[64:65] offset:1024 sc1\n\t" \
  "s_sub_u32 s39, s39, 1\n\t" "s_cmp_lt_i32 s39, 0\n\t" "s_waitcnt vmcnt(0)\n\t" "s_cbranch_scc1 .Lro8_%=\n\t" "v_cmp_eq_u32 vcc, -1, v152\n\t" "s_cbranch_vccnz .Lso8_%=\n\t" "s_branch .Lro8_%=\n\t" \
  ".Lso9_%=:\n\t" "global_load_dwordx4 v[156:159], v32, s[64:65] offset:2048 sc1\n\t" "s_sub_u32 s39, s39, 1\n\t" "s_cmp_lt_i32 s39, 0\n\t" "s_waitcnt vmcnt(0)\n\t" "s_cbranch_scc1 .Lro9_%=\n\t" \
  "v_cmp_eq_u32 vcc, -1, v156\n\t" "s_cbranch_vccnz .Lso9_%=\n\t" "s_branch .Lro9_%=\n\t" ".Lg_%=:\n\t" "v_bfe_u32 v13, s80, v10, 4\n\t" "v_lshlrev_b32 v12, 1, v9\n\t" "s_movk_i32 s76, 24576\n\t" \
  "v_and_b32 v12, 28, v12\n\t" "v_lshl_add_u32 v2, v9, 7, s76\n\t" "v_xor_b32 v12, v12, v10\n\t" "s_mov_b32 s76, 155648\n\t" "v_lshl_add_u32 v2, v12, 2, v2\n\t" "v_lshl_add_u32 v4, v9, 2, s76\n\t" \
  "v_xor_b32 v3, 64, v2\n\t" "v_lshl_add_u32 v5, v10, 2, s76\n\t" "s_mul_i32 s76, s33, 3072\n\t" "v_lshl_add_u32 v6, v8, 3, s76\n\t" "s_sub_u32 s76, 5, s27\n\t" "s_cmp_eq_u32 s27, 0\n\t" \
  "s_cselect_b32 s76, 0, s76\n\t" "s_sub_u32 s77, 15, s27\n\t" "s_cmp_eq_u32 s27, 4\n\t" "s_cselect_b32 s77, 16, s77\n\t" "v_cmp_gt_u32 s[68:69], s76, v13\n\t" "v_cmp_le_u32 vcc, s77, v13\n\t" \
  "v_add3_u32 v13, v13, s27, -1\n\t" "s_or_b64 s[68:69], s[68:69], vcc\n\t" "v_and_b32 v13, 3, v13\n\t" "v_cndmask_b32 v12, 0, 50, vcc\n\t" "v_xor_b32 v13, v13, v12\n\t" "v_lshlrev_b32 v7, 4, v9\n\t" \
  "v_lshl_add_u32 v7, v13, 8, v7\n\t" "v_lshl_add_u32 v12, s33, 8, v5\n\t" "v_lshl_add_u32 v13, s33, 8, v4\n\t" "s_setprio 1\n\t" "s_movk_i32 s38, 9\n\t" "s_mov_b32 s77, 36864\n\t" \
  "v_mov_b64 v[40:41], 0\n\t" "v_mov_b64 v[42:43], 0\n\t" "v_mov_b64 v[44:45], 0\n\t" "v_mov_b64 v[46:47], 0\n\t" "v_mov_b64 v[48:49], 0\n\t" "v_mov_b64 v[50:51], 0\n\t" "v_mov_b64 v[52:53], 0\n\t" \
  "v_mov_b64 v[54:55], 0\n\t" "v_mov_b64 v[56:57], 0\n\t" "v_mov_b64 v[58:59], 0\n\t" "v_mov_b64 v[60:61], 0\n\t" "v_mov_b64 v[62:63], 0\n\t" "v_mov_b64 v[64:65], 0\n\t" "v_mov_b64 v[66:67], 0\n\t" \
  "v_mov_b64 v[68:69], 0\n\t" "v_mov_b64 v[70:71], 0\n\t" "v_mov_b64 v[72:73], 0\n\t" "v_mov_b64 v[74:75], 0\n\t" "v_mov_b64 v[76:77], 0\n\t" "v_mov_b64 v[78:79], 0\n\t" "v_mov_b64 v[80:81], 0\n\t" \
  "v_mov_b64 v[82:83], 0\n\t" "v_mov_b64 v[84:85], 0\n\t" "v_mov_b64 v[86:87], 0\n\t" "v_mov_b64 v[88:89], 0\n\t" "v_mov_b64 v[90:91], 0\n\t" "v_mov_b64 v[92:93], 0\n\t" "v_mov_b64 v[94:95], 0\n\t" \
  "v_mov_b64 v[96:97], 0\n\t" "v_mov_b64 v[98:99], 0\n\t" "v_mov_b64 v[100:101], 0\n\t" "v_mov_b64 v[102:103], 0\n\t" "v_mov_b64 v[104:105], 0\n\t" "v_mov_b64 v[106:107], 0\n\t" \
  "v_mov_b64 v[108:109], 0\n\t" "v_mov_b64 v[110:111], 0\n\t" "s_barrier\n\t" "ds_read_b64_tr_b16 v[136:137], v1 offset:0\n\t" "ds_read_b64_tr_b16 v[138:139], v1 offset:512\n\t" \
  "ds_read_b128 v[112:115], v2 offset:0\n\t" "ds_read_b64_tr_b16 v[140:141], v1 offset:1024\n\t" "ds_read_b64_tr_b16 v[142:143], v1 offset:1536\n\t" "ds_read_b64_tr_b16 v[144:145], v1 offset:2048\n\t" \
  "ds_read_b64_tr_b16 v[146:147], v1 offset:2560\n\t" "ds_read_b128 v[116:119], v2 offset:2048\n\t" "ds_read_b128 v[120:123], v2 offset:4096\n\t" "ds_read_b128 v[124:127], v2 offset:6144\n\t" \
  "ds_read_b128 v[128:131], v2 offset:8192\n\t" "ds_read_b128 v[132:135], v2 offset:10240\n\t" "s_branch .Lb_%=\n\t" ".p2align 6\n\t" ".La_%=:\n\t" "s_waitcnt lgkmcnt(9)\n\t" \
  "v_mfma_f32_16x16x32_bf16 v[40:43], v[148:151], v[112:115], v[40:43]\n\t" "v_xor_b32 v3, 64, v2\n\t" "s_waitcnt lgkmcnt(7)\n\t" \
  "v_mfma_f32_16x16x32_bf16 v[64:67], v[152:155], v[112:115], v[64:67]\n\t" "s_waitcnt lgkmcnt(5)\n\t" "v_mfma_f32_16x16x32_bf16 v[88:91], v[112:115], v[156:159], v[88:91]\n\t" \
  "s_waitcnt lgkmcnt(4)\n\t" "v_mfma_f32_16x16x32_bf16 v[44:47], v[148:151], v[116:119], v[44:47]\n\t" "v_mfma_f32_16x16x32_bf16 v[68:71], v[152:155], v[116:119], v[68:71]\n\t" \
  "v_mfma_f32_16x16x32_bf16 v[92:95], v[116:119], v[156:159], v[92:95]\n\t" "s_waitcnt lgkmcnt(0)\n\t" "s_barrier\n\t" "v_mfma_f32_16x16x32_bf16 v[48:51], v[148:151], v[120:123], v[48:51]\n\t" \
  "ds_read_b64_tr_b16 v[136:137], v1 offset:0\n\t" "ds_read_b64_tr_b16 v[138:139], v1 offset:512\n\t" "v_mfma_f32_16x16x32_bf16 v[72:75], v[152:155], v[120:123], v[72:75]\n\t" \
  "ds_read_b128 v[112:115], v2 offset:0\n\t" "ds_read_b64_tr_b16 v[140:141], v1 offset:1024\n\t" "v_mfma_f32_16x16x32_bf16 v[96:99], v[120:123], v[156:159], v[96:99]\n\t" \
  "ds_read_b64_tr_b16 v[142:143], v1 offset:1536\n\t" "ds_read_b64_tr_b16 v[144:145], v1 offset:2048\n\t" "v_mfma_f32_16x16x32_bf16 v[52:55], v[148:151], v[124:127], v[52:55]\n\t" \
  "ds_read_b64_tr_b16 v[146:147], v1 offset:2560\n\t" "ds_read_b128 v[116:119], v2 offset:2048\n\t" "v_mfma_f32_16x16x32_bf16 v[76:79], v[152:155], v[124:127], v[76:79]\n\t" \
  "ds_read_b128 v[120:123], v2 offset:4096\n\t" "v_mfma_f32_16x16x32_bf16 v[100:103], v[124:127], v[156:159], v[100:103]\n\t" "ds_read_b128 v[124:127], v2 offset:6144\n\t" \
  "v_mfma_f32_16x16x32_bf16 v[56:59], v[148:151], v[128:131], v[56:59]\n\t" "v_mfma_f32_16x16x32_bf16 v[80:83], v[152:155], v[128:131], v[80:83]\n\t" \
  "v_mfma_f32_16x16x32_bf16 v[104:107], v[128:131], v[156:159], v[104:107]\n\t" "ds_read_b128 v[128:131], v2 offset:8192\n\t" "v_mfma_f32_16x16x32_bf16 v[60:63], v[148:151], v[132:135], v[60:63]\n\t" \
  "v_mfma_f32_16x16x32_bf16 v[84:87], v[152:155], v[132:135], v[84:87]\n\t" "v_mfma_f32_16x16x32_bf16 v[108:111], v[132:135], v[156:159], v[108:111]\n\t" "ds_read_b128 v[132:135], v2 offset:10240\n\t" \
  ".Lb_%=:\n\t" "s_waitcnt lgkmcnt(9)\n\t" "v_mfma_f32_16x16x32_bf16 v[40:43], v[136:139], v[112:115], v[40:43]\n\t" "ds_read_b64_tr_b16 v[148:149], v1 offset:12288\n\t" "s_waitcnt lgkmcnt(8)\n\t" \
  "v_mfma_f32_16x16x32_bf16 v[64:67], v[140:143], v[112:115], v[64:67]\n\t" "ds_read_b64_tr_b16 v[150:151], v1 offset:12800\n\t" "s_waitcnt lgkmcnt(7)\n\t" \
  "v_mfma_f32_16x16x32_bf16 v[88:91], v[112:115], v[144:147], v[88:91]\n\t" "ds_read_b128 v[112:115], v3 offset:0\n\t" "s_waitcnt lgkmcnt(7)\n\t" \
  "v_mfma_f32_16x16x32_bf16 v[44:47], v[136:139], v[116:119], v[44:47]\n\t" "ds_read_b64_tr_b16 v[152:153], v1 offset:13312\n\t" \
  "v_mfma_f32_16x16x32_bf16 v[68:71], v[140:143], v[116:119], v[68:71]\n\t" "ds_read_b64_tr_b16 v[154:155], v1 offset:13824\n\t" \
  "v_mfma_f32_16x16x32_bf16 v[92:95], v[116:119], v[144:147], v[92:95]\n\t" "ds_read_b64_tr_b16 v[156:157], v1 offset:14336\n\t" "s_waitcnt lgkmcnt(9)\n\t" \
  "v_mfma_f32_16x16x32_bf16 v[48:51], v[136:139], v[120:123], v[48:51]\n\t" "ds_read_b64_tr_b16 v[158:159], v1 offset:14848\n\t" \
  "v_mfma_f32_16x16x32_bf16 v[72:75], v[140:143], v[120:123], v[72:75]\n\t" "ds_read_b128 v[116:119], v3 offset:2048\n\t" "v_mfma_f32_16x16x32_bf16 v[96:99], v[120:123], v[144:147], v[96:99]\n\t" \
  "ds_read_b128 v[120:123], v3 offset:4096\n\t" "s_waitcnt lgkmcnt(11)\n\t" "v_mfma_f32_16x16x32_bf16 v[52:55], v[136:139], v[124:127], v[52:55]\n\t" "s_bitcmp1_b32 0x44, s38\n\t" \
  "v_mfma_f32_16x16x32_bf16 v[76:79], v[140:143], v[124:127], v[76:79]\n\t" "s_cselect_b32 s76, -110592, s77\n\t" "v_add_u32 v1, s76, v1\n\t" \
  "v_mfma_f32_16x16x32_bf16 v[100:103], v[124:127], v[144:147], v[100:103]\n\t" "ds_read_b128 v[124:127], v3 offset:6144\n\t" "s_waitcnt lgkmcnt(11)\n\t" \
  "v_mfma_f32_16x16x32_bf16 v[56:59], v[136:139], v[128:131], v[56:59]\n\t" "v_add_u32 v2, s76, v2\n\t" "v_mfma_f32_16x16x32_bf16 v[80:83], v[140:143], v[128:131], v[80:83]\n\t" \
  "s_sub_u32 s38, s38, 1\n\t" "v_mfma_f32_16x16x32_bf16 v[104:107], v[128:131], v[144:147], v[104:107]\n\t" "ds_read_b128 v[128:131], v3 offset:8192\n\t" "s_waitcnt lgkmcnt(11)\n\t" \
  "v_mfma_f32_16x16x32_bf16 v[60:63], v[136:139], v[132:135], v[60:63]\n\t" "v_mfma_f32_16x16x32_bf16 v[84:87], v[140:143], v[132:135], v[84:87]\n\t" \
  "v_mfma_f32_16x16x32_bf16 v[108:111], v[132:135], v[144:147], v[108:111]\n\t" "ds_read_b128 v[132:135], v3 offset:10240\n\t" "s_cbranch_scc0 .La_%=\n\t" "s_waitcnt lgkmcnt(9)\n\t" \
  "v_mfma_f32_16x16x32_bf16 v[40:43], v[148:151], v[112:115], v[40:43]\n\t" "s_waitcnt lgkmcnt(7)\n\t" "v_mfma_f32_16x16x32_bf16 v[64:67], v[152:155], v[112:115], v[64:67]\n\t" \
  "s_waitcnt lgkmcnt(4)\n\t" "v_mfma_f32_16x16x32_bf16 v[44:47], v[148:151], v[116:119], v[44:47]\n\t" "ds_read_b32 v39, v37 offset:12\n\t" "ds_read_b128 v[160:163], v12 offset:1024\n\t" \
  "v_mfma_f32_16x16x32_bf16 v[68:71], v[152:155], v[116:119], v[68:71]\n\t" "ds_read_b128 v[164:167], v12 offset:1088\n\t" "ds_read2_b32 v[136:137], v4 offset0:0 offset1:96\n\t" \
  "s_waitcnt lgkmcnt(7)\n\t" "v_mfma_f32_16x16x32_bf16 v[48:51], v[148:151], v[120:123], v[48:51]\n\t" "ds_read2_b32 v[138:139], v4 offset0:16 offset1:112\n\t" \
  "ds_read2_b32 v[140:141], v4 offset0:32 offset1:128\n\t" "v_mfma_f32_16x16x32_bf16 v[72:75], v[152:155], v[120:123], v[72:75]\n\t" "ds_read2_b32 v[142:143], v4 offset0:48 offset1:144\n\t" \
  "ds_read2_b32 v[144:145], v4 offset0:64 offset1:160\n\t" "s_waitcnt lgkmcnt(10)\n\t" "v_mfma_f32_16x16x32_bf16 v[52:55], v[148:151], v[124:127], v[52:55]\n\t" \
  "ds_read2_b32 v[146:147], v4 offset0:80 offset1:176\n\t" "ds_read_b32 v14, v13 offset:1152\n\t" "v_mfma_f32_16x16x32_bf16 v[76:79], v[152:155], v[124:127], v[76:79]\n\t" "s_waitcnt lgkmcnt(11)\n\t" \
  "v_mfma_f32_16x16x32_bf16 v[56:59], v[148:151], v[128:131], v[56:59]\n\t" "s_waitcnt lgkmcnt(0)\n\t" "v_readfirstlane_b32 s38, v39\n\t" "s_cmp_ge_u32 s38, 4\n\t" "s_cbranch_scc0 .Lts_%=\n\t" \
  ".Ltr_%=:\n\t" "v_fma_f32 v40, -v160, v136, v40\n\t" "v_fma_f32 v41, -v161, v136, v41\n\t" "v_mfma_f32_16x16x32_bf16 v[80:83], v[152:155], v[128:131], v[80:83]\n\t" \
  "v_fma_f32 v42, -v162, v136, v42\n\t" "v_fma_f32 v43, -v163, v136, v43\n\t" "v_mfma_f32_16x16x32_bf16 v[60:63], v[148:151], v[132:135], v[60:63]\n\t" "v_fma_f32 v64, -v164, v136, v64\n\t" \
  "v_fma_f32 v65, -v165, v136, v65\n\t" "v_mfma_f32_16x16x32_bf16 v[84:87], v[152:155], v[132:135], v[84:87]\n\t" "v_fma_f32 v66, -v166, v136, v66\n\t" "v_fma_f32 v67, -v167, v136, v67\n\t" \
  "v_mfma_f32_16x16x32_bf16 v[88:91], v[112:115], v[156:159], v[88:91]\n\t" "v_mul_f32 v40, v40, v137\n\t" "v_mul_f32 v41, v41, v137\n\t" \
  "v_mfma_f32_16x16x32_bf16 v[92:95], v[116:119], v[156:159], v[92:95]\n\t" "v_mul_f32 v42, v42, v137\n\t" "v_mul_f32 v43, v43, v137\n\t" \
  "v_mfma_f32_16x16x32_bf16 v[96:99], v[120:123], v[156:159], v[96:99]\n\t" "v_mul_f32 v64, v64, v137\n\t" "v_mul_f32 v65, v65, v137\n\t" \
  "v_mfma_f32_16x16x32_bf16 v[100:103], v[124:127], v[156:159], v[100:103]\n\t" "v_mul_f32 v66, v66, v137\n\t" "v_mul_f32 v67, v67, v137\n\t" \
  "v_mfma_f32_16x16x32_bf16 v[104:107], v[128:131], v[156:159], v[104:107]\n\t" "v_cvt_pk_bf16_f32 v40, v40, v41\n\t" "v_cvt_pk_bf16_f32 v41, v42, v43\n\t" \
  "v_mfma_f32_16x16x32_bf16 v[108:111], v[132:135], v[156:159], v[108:111]\n\t" "v_cvt_pk_bf16_f32 v42, v64, v65\n\t" "v_cvt_pk_bf16_f32 v43, v66, v67\n\t" "ds_read_b128 v[112:115], v5 offset:0\n\t" \
  "ds_read_b128 v[116:119], v5 offset:384\n\t" "ds_read_b128 v[120:123], v5 offset:64\n\t" "ds_read_b128 v[124:127], v5 offset:448\n\t" "ds_read_b128 v[128:131], v5 offset:128\n\t" \
  "ds_read_b128 v[132:135], v5 offset:512\n\t" "v_fma_f32 v44, -v160, v138, v44\n\t" "v_fma_f32 v45, -v161, v138, v45\n\t" "v_fma_f32 v46, -v162, v138, v46\n\t" "v_fma_f32 v47, -v163, v138, v47\n\t" \
  "s_mov_b64 exec, s[68:69]\n\t" "global_store_dwordx4 v7, v[40:43], s[66:67] offset:-2048 sc1\n\t" "s_mov_b64 exec, -1\n\t" "ds_write2st64_b64 v6, v[40:41], v[42:43] offset0:0 offset1:24\n\t" \
  "s_mov_b64 exec, 1\n\t" "ds_add_u32 v37, v38 offset:8\n\t" "s_mov_b64 exec, -1\n\t" "v_pk_fma_f32 v[68:69], v[164:165], v[138:139], v[68:69] op_sel_hi:[1,0,1] neg_lo:[1,0,0] neg_hi:[1,0,0]\n\t" \
  "v_pk_fma_f32 v[70:71], v[166:167], v[138:139], v[70:71] op_sel_hi:[1,0,1] neg_lo:[1,0,0] neg_hi:[1,0,0]\n\t" "v_pk_mul_f32 v[44:45], v[44:45], v[138:139] op_sel:[0,1] op_sel_hi:[1,1]\n\t" \
  "v_pk_mul_f32 v[46:47], v[46:47], v[138:139] op_sel:[0,1] op_sel_hi:[1,1]\n\t" "v_pk_mul_f32 v[68:69], v[68:69], v[138:139] op_sel:[0,1] op_sel_hi:[1,1]\n\t" \
  "v_pk_mul_f32 v[70:71], v[70:71], v[138:139] op_sel:[0,1] op_sel_hi:[1,1]\n\t" "v_cvt_pk_bf16_f32 v44, v44, v45\n\t" "v_cvt_pk_bf16_f32 v45, v46, v47\n\t" "v_cvt_pk_bf16_f32 v46, v68, v69\n\t" \
  "v_cvt_pk_bf16_f32 v47, v70, v71\n\t" "s_mov_b64 exec, s[68:69]\n\t" "global_store_dwordx4 v7, v[44:47], s[66:67] offset:-1024 sc1\n\t" "s_mov_b64 exec, -1\n\t" \
  "ds_write2st64_b64 v6, v[44:45], v[46:47] offset0:1 offset1:25\n\t" "ds_read_b128 v[64:67], v5 offset:192\n\t" "ds_read_b128 v[68:71], v5 offset:576\n\t" \
  "v_pk_fma_f32 v[48:49], v[160:161], v[140:141], v[48:49] op_sel_hi:[1,0,1] neg_lo:[1,0,0] neg_hi:[1,0,0]\n\t" \
  "v_pk_fma_f32 v[50:51], v[162:163], v[140:141], v[50:51] op_sel_hi:[1,0,1] neg_lo:[1,0,0] neg_hi:[1,0,0]\n\t" \
  "v_pk_fma_f32 v[72:73], v[164:165], v[140:141], v[72:73] op_sel_hi:[1,0,1] neg_lo:[1,0,0] neg_hi:[1,0,0]\n\t" \
  "v_pk_fma_f32 v[74:75], v[166:167], v[140:141], v[74:75] op_sel_hi:[1,0,1] neg_lo:[1,0,0] neg_hi:[1,0,0]\n\t" "v_pk_mul_f32 v[48:49], v[48:49], v[140:141] op_sel:[0,1] op_sel_hi:[1,1]\n\t" \
  "v_pk_mul_f32 v[50:51], v[50:51], v[140:141] op_sel:[0,1] op_sel_hi:[1,1]\n\t" "v_pk_mul_f32 v[72:73], v[72:73], v[140:141] op_sel:[0,1] op_sel_hi:[1,1]\n\t" \
  "v_pk_mul_f32 v[74:75], v[74:75], v[140:141] op_sel:[0,1] op_sel_hi:[1,1]\n\t" "v_cvt_pk_bf16_f32 v48, v48, v49\n\t" "v_cvt_pk_bf16_f32 v49, v50, v51\n\t" "v_cvt_pk_bf16_f32 v50, v72, v73\n\t" \
  "v_cvt_pk_bf16_f32 v51, v74, v75\n\t" "s_mov_b64 exec, s[68:69]\n\t" "global_store_dwordx4 v7, v[48:51], s[66:67] offset:0 sc1\n\t" "s_mov_b64 exec, -1\n\t" \
  "ds_write2st64_b64 v6, v[48:49], v[50:51] offset0:2 offset1:26\n\t" "v_pk_fma_f32 v[52:53], v[160:161], v[142:143], v[52:53] op_sel_hi:[1,0,1] neg_lo:[1,0,0] neg_hi:[1,0,0]\n\t" \
  "v_pk_fma_f32 v[54:55], v[162:163], v[142:143], v[54:55] op_sel_hi:[1,0,1] neg_lo:[1,0,0] neg_hi:[1,0,0]\n\t" \
  "v_pk_fma_f32 v[76:77], v[164:165], v[142:143], v[76:77] op_sel_hi:[1,0,1] neg_lo:[1,0,0] neg_hi:[1,0,0]\n\t" \
  "v_pk_fma_f32 v[78:79], v[166:167], v[142:143], v[78:79] op_sel_hi:[1,0,1] neg_lo:[1,0,0] neg_hi:[1,0,0]\n\t" "v_pk_mul_f32 v[52:53], v[52:53], v[142:143] op_sel:[0,1] op_sel_hi:[1,1]\n\t" \
  "v_pk_mul_f32 v[54:55], v[54:55], v[142:143] op_sel:[0,1] op_sel_hi:[1,1]\n\t" "v_pk_mul_f32 v[76:77], v[76:77], v[142:143] op_sel:[0,1] op_sel_hi:[1,1]\n\t" \
  "v_pk_mul_f32 v[78:79], v[78:79], v[142:143] op_sel:[0,1] op_sel_hi:[1,1]\n\t" "v_cvt_pk_bf16_f32 v52, v52, v53\n\t" "v_cvt_pk_bf16_f32 v53, v54, v55\n\t" "v_cvt_pk_bf16_f32 v54, v76, v77\n\t" \
  "v_cvt_pk_bf16_f32 v55, v78, v79\n\t" "s_mov_b64 exec, s[68:69]\n\t" "global_store_dwordx4 v7, v[52:55], s[66:67] offset:1024 sc1\n\t" "s_mov_b64 exec, -1\n\t" \
  "ds_write2st64_b64 v6, v[52:53], v[54:55] offset0:3 offset1:27\n\t" "ds_read_b128 v[72:75], v5 offset:256\n\t" "ds_read_b128 v[76:79], v5 offset:640\n\t" \
  "v_pk_fma_f32 v[56:57], v[160:161], v[144:145], v[56:57] op_sel_hi:[1,0,1] neg_lo:[1,0,0] neg_hi:[1,0,0]\n\t" \
  "v_pk_fma_f32 v[58:59], v[162:163], v[144:145], v[58:59] op_sel_hi:[1,0,1] neg_lo:[1,0,0] neg_hi:[1,0,0]\n\t" \
  "v_pk_fma_f32 v[80:81], v[164:165], v[144:145], v[80:81] op_sel_hi:[1,0,1] neg_lo:[1,0,0] neg_hi:[1,0,0]\n\t" \
  "v_pk_fma_f32 v[82:83], v[166:167], v[144:145], v[82:83] op_sel_hi:[1,0,1] neg_lo:[1,0,0] neg_hi:[1,0,0]\n\t" "v_pk_mul_f32 v[56:57], v[56:57], v[144:145] op_sel:[0,1] op_sel_hi:[1,1]\n\t" \
  "v_pk_mul_f32 v[58:59], v[58:59], v[144:145] op_sel:[0,1] op_sel_hi:[1,1]\n\t" "v_pk_mul_f32 v[80:81], v[80:81], v[144:145] op_sel:[0,1] op_sel_hi:[1,1]\n\t" \
  "v_pk_mul_f32 v[82:83], v[82:83], v[144:145] op_sel:[0,1] op_sel_hi:[1,1]\n\t" "v_cvt_pk_bf16_f32 v56, v56, v57\n\t" "v_cvt_pk_bf16_f32 v57, v58, v59\n\t" "v_cvt_pk_bf16_f32 v58, v80, v81\n\t" \
  "v_cvt_pk_bf16_f32 v59, v82, v83\n\t" "s_mov_b64 exec, s[68:69]\n\t" "global_store_dwordx4 v7, v[56:59], s[66:67] offset:2048 sc1\n\t" "s_mov_b64 exec, -1\n\t" \
  "ds_write2st64_b64 v6, v[56:57], v[58:59] offset0:4 offset1:28\n\t" "v_pk_fma_f32 v[60:61], v[160:161], v[146:147], v[60:61] op_sel_hi:[1,0,1] neg_lo:[1,0,0] neg_hi:[1,0,0]\n\t" \
  "v_pk_fma_f32 v[62:63], v[162:163], v[146:147], v[62:63] op_sel_hi:[1,0,1] neg_lo:[1,0,0] neg_hi:[1,0,0]\n\t" \
  "v_pk_fma_f32 v[84:85], v[164:165], v[146:147], v[84:85] op_sel_hi:[1,0,1] neg_lo:[1,0,0] neg_hi:[1,0,0]\n\t" \
  "v_pk_fma_f32 v[86:87], v[166:167], v[146:147], v[86:87] op_sel_hi:[1,0,1] neg_lo:[1,0,0] neg_hi:[1,0,0]\n\t" "v_pk_mul_f32 v[60:61], v[60:61], v[146:147] op_sel:[0,1] op_sel_hi:[1,1]\n\t" \
  "v_pk_mul_f32 v[62:63], v[62:63], v[146:147] op_sel:[0,1] op_sel_hi:[1,1]\n\t" "v_pk_mul_f32 v[84:85], v[84:85], v[146:147] op_sel:[0,1] op_sel_hi:[1,1]\n\t" \
  "v_pk_mul_f32 v[86:87], v[86:87], v[146:147] op_sel:[0,1] op_sel_hi:[1,1]\n\t" "v_cvt_pk_bf16_f32 v60, v60, v61\n\t" "v_cvt_pk_bf16_f32 v61, v62, v63\n\t" "v_cvt_pk_bf16_f32 v62, v84, v85\n\t" \
  "v_cvt_pk_bf16_f32 v63, v86, v87\n\t" "s_mov_b64 exec, s[68:69]\n\t" "global_store_dwordx4 v7, v[60:63], s[66:67] offset:3072 sc1\n\t" "s_mov_b64 exec, -1\n\t" \
  "ds_write2st64_b64 v6, v[60:61], v[62:63] offset0:5 offset1:29\n\t" "ds_read_b128 v[80:83], v5 offset:320\n\t" "ds_read_b128 v[84:87], v5 offset:704\n\t" "s_mov_b64 exec, 1\n\t" \
  "ds_add_u32 v37, v38 offset:0\n\t" "s_mov_b64 exec, -1\n\t" "s_waitcnt lgkmcnt(14)\n\t" "v_pk_fma_f32 v[88:89], v[112:113], v[14:15], v[88:89] op_sel_hi:[1,0,1] neg_lo:[1,0,0] neg_hi:[1,0,0]\n\t" \
  "v_pk_fma_f32 v[90:91], v[114:115], v[14:15], v[90:91] op_sel_hi:[1,0,1] neg_lo:[1,0,0] neg_hi:[1,0,0]\n\t" \
  "v_pk_fma_f32 v[92:93], v[120:121], v[14:15], v[92:93] op_sel_hi:[1,0,1] neg_lo:[1,0,0] neg_hi:[1,0,0]\n\t" \
  "v_pk_fma_f32 v[94:95], v[122:123], v[14:15], v[94:95] op_sel_hi:[1,0,1] neg_lo:[1,0,0] neg_hi:[1,0,0]\n\t" "v_pk_mul_f32 v[88:89], v[88:89], v[116:117]\n\t" \
  "v_pk_mul_f32 v[90:91], v[90:91], v[118:119]\n\t" "v_pk_mul_f32 v[92:93], v[92:93], v[124:125]\n\t" "v_pk_mul_f32 v[94:95], v[94:95], v[126:127]\n\t" "v_cvt_pk_bf16_f32 v88, v88, v89\n\t" \
  "v_cvt_pk_bf16_f32 v89, v90, v91\n\t" "v_cvt_pk_bf16_f32 v92, v92, v93\n\t" "v_cvt_pk_bf16_f32 v93, v94, v95\n\t" "ds_write2st64_b64 v6, v[88:89], v[92:93] offset0:48 offset1:49\n\t" \
  "s_waitcnt lgkmcnt(10)\n\t" "v_pk_fma_f32 v[96:97], v[128:129], v[14:15], v[96:97] op_sel_hi:[1,0,1] neg_lo:[1,0,0] neg_hi:[1,0,0]\n\t" \
  "v_pk_fma_f32 v[98:99], v[130:131], v[14:15], v[98:99] op_sel_hi:[1,0,1] neg_lo:[1,0,0] neg_hi:[1,0,0]\n\t" \
  "v_pk_fma_f32 v[100:101], v[64:65], v[14:15], v[100:101] op_sel_hi:[1,0,1] neg_lo:[1,0,0] neg_hi:[1,0,0]\n\t" \
  "v_pk_fma_f32 v[102:103], v[66:67], v[14:15], v[102:103] op_sel_hi:[1,0,1] neg_lo:[1,0,0] neg_hi:[1,0,0]\n\t" "v_pk_mul_f32 v[96:97], v[96:97], v[132:133]\n\t" \
  "v_pk_mul_f32 v[98:99], v[98:99], v[134:135]\n\t" "v_pk_mul_f32 v[100:101], v[100:101], v[68:69]\n\t" "v_pk_mul_f32 v[102:103], v[102:103], v[70:71]\n\t" "v_cvt_pk_bf16_f32 v96, v96, v97\n\t" \
  "v_cvt_pk_bf16_f32 v97, v98, v99\n\t" "v_cvt_pk_bf16_f32 v100, v100, v101\n\t" "v_cvt_pk_bf16_f32 v101, v102, v103\n\t" "ds_write2st64_b64 v6, v[96:97], v[100:101] offset0:50 offset1:51\n\t" \
  "s_waitcnt lgkmcnt(3)\n\t" "v_pk_fma_f32 v[104:105], v[72:73], v[14:15], v[104:105] op_sel_hi:[1,0,1] neg_lo:[1,0,0] neg_hi:[1,0,0]\n\t" \
  "v_pk_fma_f32 v[106:107], v[74:75], v[14:15], v[106:107] op_sel_hi:[1,0,1] neg_lo:[1,0,0] neg_hi:[1,0,0]\n\t" \
  "v_pk_fma_f32 v[108:109], v[80:81], v[14:15], v[108:109] op_sel_hi:[1,0,1] neg_lo:[1,0,0] neg_hi:[1,0,0]\n\t" \
  "v_pk_fma_f32 v[110:111], v[82:83], v[14:15], v[110:111] op_sel_hi:[1,0,1] neg_lo:[1,0,0] neg_hi:[1,0,0]\n\t" "v_pk_mul_f32 v[104:105], v[104:105], v[76:77]\n\t" \
  "v_pk_mul_f32 v[106:107], v[106:107], v[78:79]\n\t" "v_pk_mul_f32 v[108:109], v[108:109], v[84:85]\n\t" "v_pk_mul_f32 v[110:111], v[110:111], v[86:87]\n\t" "v_cvt_pk_bf16_f32 v104, v104, v105\n\t" \
  "v_cvt_pk_bf16_f32 v105, v106, v107\n\t" "v_cvt_pk_bf16_f32 v108, v108, v109\n\t" "v_cvt_pk_bf16_f32 v109, v110, v111\n\t" "ds_write2st64_b64 v6, v[104:105], v[108:109] offset0:52 offset1:53\n\t" \
  "s_mov_b64 exec, 1\n\t" "ds_add_u32 v37, v38 offset:4\n\t" "s_mov_b64 exec, -1\n\t" "s_setprio 1\n\t" ".Latt_%=:\n\t" "global_load_dwordx4 v[12:15], v33, s[8:9]\n\t" \
  "global_load_dwordx4 v[16:19], v34, s[14:15]\n\t" "s_cmp_eq_u32 s34, 0\n\t" "s_cbranch_scc1 .Lk0_%=\n\t" ".Lq_%=:\n\t" "ds_read_b32 v39, v37\n\t" "ds_read_b64 v[40:41], v30 offset:0\n\t" \
  "ds_read_b64 v[42:43], v30 offset:3072\n\t" "ds_read_b64 v[44:45], v30 offset:11776\n\t" "ds_read_b64 v[46:47], v30 offset:14848\n\t" "ds_read_b64 v[48:49], v30 offset:12288\n\t" \
  "ds_read_b64 v[50:51], v30 offset:15360\n\t" "ds_read_b64 v[52:53], v31 offset:0\n\t" "ds_read_b64 v[54:55], v31 offset:3072\n\t" "ds_read_b64 v[56:57], v31 offset:11776\n\t" \
  "ds_read_b64 v[58:59], v31 offset:14848\n\t" "ds_read_b64 v[60:61], v31 offset:12288\n\t" "ds_read_b64 v[62:63], v31 offset:15360\n\t" "s_waitcnt lgkmcnt(12)\n\t" "v_readfirstlane_b32 s38, v39\n\t" \
  "s_cmp_ge_u32 s38, 4\n\t" "s_cbranch_scc0 .Lsq_%=\n\t" ".Lrq_%=:\n\t" "s_waitcnt lgkmcnt(10)\n\t" "v_cndmask_b32 v42, 0, v42, s[54:55]\n\t" "v_cndmask_b32 v43, 0, v43, s[54:55]\n\t" \
  "s_waitcnt lgkmcnt(8)\n\t" "s_nop 0\n\t" "v_mfma_f32_16x16x32_bf16 v[108:111], v[44:47], v[40:43], v[20:23]\n\t" "s_waitcnt lgkmcnt(6)\n\t" \
  "v_mfma_f32_16x16x32_bf16 v[112:115], v[48:51], v[40:43], v[24:27]\n\t" "s_waitcnt lgkmcnt(4)\n\t" "v_cndmask_b32 v52, 0, v52, s[54:55]\n\t" "v_cndmask_b32 v53, 0, v53, s[54:55]\n\t" \
  "s_waitcnt lgkmcnt(2)\n\t" "s_nop 0\n\t" "v_mfma_f32_16x16x32_bf16 v[116:119], v[56:59], v[52:55], v[20:23]\n\t" "s_waitcnt lgkmcnt(0)\n\t" \
  "v_mfma_f32_16x16x32_bf16 v[120:123], v[60:63], v[52:55], v[24:27]\n\t" "ds_read_b32 v39, v37 offset:4\n\t" "ds_read_b64 v[128:129], v30 offset:24064\n\t" \
  "ds_read_b64 v[130:131], v30 offset:24576\n\t" "ds_read_b64 v[132:133], v30 offset:27136\n\t" "ds_read_b64 v[134:135], v30 offset:27648\n\t" "ds_read_b64 v[136:137], v31 offset:24064\n\t" \
  "ds_read_b64 v[138:139], v31 offset:24576\n\t" "ds_read_b64 v[140:141], v31 offset:27136\n\t" "ds_read_b64 v[142:143], v31 offset:27648\n\t" ".Lk1_%=:\n\t" "s_nop 4\n\t" \
  "v_max_f32 v108, v108, v112\n\t" "v_max_f32 v109, v109, v113\n\t" "v_max_f32 v110, v110, v114\n\t" "v_max_f32 v111, v111, v115\n\t" "v_max_f32 v116, v116, v120\n\t" "v_max_f32 v117, v117, v121\n\t" \
  "v_max_f32 v118, v118, v122\n\t" "v_max_f32 v119, v119, v123\n\t" "v_max3_f32 v124, v108, v109, v110\n\t" "v_max3_f32 v125, v116, v117, v118\n\t" "v_max_f32 v124, v124, v111\n\t" \
  "v_max_f32 v125, v125, v119\n\t" "s_mov_b32 s38, 0x3ea52b5c\n\t" "s_nop 0\n\t" "v_permlane32_swap_b32 v124, v125\n\t" "v_max_f32 v126, v124, v125\n\t" "v_max_f32 v127, v124, v125\n\t" "s_nop 1\n\t" \
  "v_permlane16_swap_b32 v126, v127\n\t" "v_max_f32 v124, v126, v127\n\t" "v_mul_f32 v125, s38, v124\n\t" "v_mul_f32 v124, s38, v124\n\t" "s_nop 1\n\t" "v_permlane32_swap_b32 v124, v125\n\t" \
  "v_fma_f32 v108, v108, s38, -v124\n\t" "v_fma_f32 v116, v116, s38, -v125\n\t" "v_fma_f32 v109, v109, s38, -v124\n\t" "v_fma_f32 v117, v117, s38, -v125\n\t" "v_fma_f32 v110, v110, s38, -v124\n\t" \
  "v_fma_f32 v118, v118, s38, -v125\n\t" "v_fma_f32 v111, v111, s38, -v124\n\t" "v_fma_f32 v119, v119, s38, -v125\n\t" "v_exp_f32 v108, v108\n\t" "v_exp_f32 v116, v116\n\t" "v_exp_f32 v109, v109\n\t" \
  "v_exp_f32 v117, v117\n\t" "v_exp_f32 v110, v110\n\t" "v_exp_f32 v118, v118\n\t" "v_add_f32 v124, v108, v109\n\t" "v_add_f32 v125, v116, v117\n\t" "v_exp_f32 v111, v111\n\t" \
  "v_exp_f32 v119, v119\n\t" "v_add_f32 v124, v124, v110\n\t" "v_add_f32 v125, v125, v118\n\t" "v_add_f32 v124, v124, v111\n\t" "v_add_f32 v125, v125, v119\n\t" "s_cmp_lg_u32 s34, 0\n\t" "s_nop 0\n\t" \
  "v_permlane32_swap_b32 v124, v125\n\t" "v_add_f32 v126, v124, v125\n\t" "v_add_f32 v127, v124, v125\n\t" "s_nop 1\n\t" "v_permlane16_swap_b32 v126, v127\n\t" "v_add_f32 v124, v126, v127\n\t" \
  "v_rcp_f32 v126, v124\n\t" "v_rcp_f32 v124, v124\n\t" "s_nop 1\n\t" "v_permlane32_swap_b32 v124, v126\n\t" "v_pk_mul_f32 v[108:109], v[108:109], v[124:125] op_sel_hi:[1,0]\n\t" \
  "v_pk_mul_f32 v[110:111], v[110:111], v[124:125] op_sel_hi:[1,0]\n\t" "v_pk_mul_f32 v[116:117], v[116:117], v[126:127] op_sel_hi:[1,0]\n\t" \
  "v_pk_mul_f32 v[118:119], v[118:119], v[126:127] op_sel_hi:[1,0]\n\t" "v_cvt_pk_bf16_f32 v112, v108, v109\n\t" "v_cvt_pk_bf16_f32 v113, v110, v111\n\t" "v_cvt_pk_bf16_f32 v120, v116, v117\n\t" \
  "v_cvt_pk_bf16_f32 v121, v118, v119\n\t" "v_and_b32 v114, v112, v28\n\t" "v_and_b32 v115, v113, v29\n\t" "v_and_b32 v122, v120, v28\n\t" "v_and_b32 v123, v121, v29\n\t" \
  "v_xor_b32 v112, v112, v114\n\t" "v_xor_b32 v113, v113, v115\n\t" "v_xor_b32 v120, v120, v122\n\t" "v_xor_b32 v121, v121, v123\n\t" "s_cbranch_scc1 .Lk2_%=\n\t" \
  "v_mfma_f32_16x16x32_bf16 v[152:155], v[128:131], v[112:115], 0\n\t" "v_mfma_f32_16x16x32_bf16 v[156:159], v[132:135], v[112:115], 0\n\t" \
  "v_mfma_f32_16x16x32_bf16 v[152:155], v[136:139], v[120:123], v[152:155]\n\t" "v_mfma_f32_16x16x32_bf16 v[156:159], v[140:143], v[120:123], v[156:159]\n\t" "s_nop 6\n\t" \
  "v_cvt_pk_bf16_f32 v148, v152, v153\n\t" "v_cvt_pk_bf16_f32 v149, v154, v155\n\t" "v_cvt_pk_bf16_f32 v150, v156, v157\n\t" "v_cvt_pk_bf16_f32 v151, v158, v159\n\t" ".Lk3_%=:\n\t" \
  "s_mov_b64 exec, s[54:55]\n\t" "global_store_dwordx4 v36, v[148:151], s[60:61] offset:-2048 sc1\n\t" "s_mov_b64 exec, -1\n\t" "s_cmp_ge_u32 s26, 6\n\t" "s_cbranch_scc1 .Lb3a_%=\n\t" \
  "s_cmp_ge_u32 s26, 4\n\t" "s_cbranch_scc1 .Lv21_%=\n\t" "s_waitcnt vmcnt(1)\n\t" ".Lv21_%=:\n\t" "s_setprio 1\n\t" "ds_read_b64 v[152:153], v37 offset:80\n\t" "s_waitcnt lgkmcnt(0)\n\t" \
  "v_readfirstlane_b32 s80, v152\n\t" "v_readfirstlane_b32 s81, v153\n\t" "s_sub_u32 s81, s81, s80\n\t" "s_sub_u32 s80, s81, 15\n\t" "s_barrier\n\t" "s_cbranch_scc1 .Llx1_%=\n\t" \
  "s_memrealtime s[76:77]\n\t" ".Llw1_%=:\n\t" "s_memrealtime s[78:79]\n\t" "s_waitcnt lgkmcnt(0)\n\t" "s_sub_u32 s78, s78, s76\n\t" "s_cmp_lt_u32 s78, s80\n\t" "s_cbranch_scc1 .Llw1_%=\n\t" \
  ".Llx1_%=:\n\t" "global_load_dwordx4 v[152:155], v32, s[62:63] offset:-2048 sc1\n\t" "global_load_dwordx4 v[156:159], v32, s[62:63] offset:-1024 sc1\n\t" \
  "global_load_dwordx4 v[160:163], v32, s[62:63] offset:0 sc1\n\t" ".Lb3a_%=:\n\t" "ds_read_b64_tr_b16 v[40:41], v35 offset:0\n\t" "ds_read_b64_tr_b16 v[42:43], v35 offset:2048\n\t" \
  "ds_read_b64_tr_b16 v[44:45], v0 offset:0\n\t" "ds_read_b64_tr_b16 v[46:47], v0 offset:2048\n\t" "ds_read_b64_tr_b16 v[48:49], v1 offset:0\n\t" "ds_read_b64_tr_b16 v[50:51], v1 offset:2048\n\t" \
  "ds_read_b64_tr_b16 v[52:53], v2 offset:0\n\t" "ds_read_b64_tr_b16 v[54:55], v2 offset:2048\n\t" "ds_read_b64_tr_b16 v[56:57], v35 offset:4096\n\t" "ds_read_b64_tr_b16 v[58:59], v35 offset:6144\n\t" \
  "ds_read_b64_tr_b16 v[60:61], v0 offset:4096\n\t" "ds_read_b64_tr_b16 v[62:63], v0 offset:6144\n\t" "ds_read_b64_tr_b16 v[64:65], v1 offset:4096\n\t" \
  "ds_read_b64_tr_b16 v[66:67], v1 offset:6144\n\t" "ds_read_b64_tr_b16 v[68:69], v2 offset:4096\n\t" "ds_read_b64_tr_b16 v[70:71], v2 offset:6144\n\t" \
  "ds_read_b64_tr_b16 v[72:73], v35 offset:8192\n\t" "ds_read_b64_tr_b16 v[74:75], v35 offset:10240\n\t" "ds_read_b64_tr_b16 v[76:77], v0 offset:8192\n\t" \
  "ds_read_b64_tr_b16 v[78:79], v0 offset:10240\n\t" "ds_read_b64_tr_b16 v[80:81], v1 offset:8192\n\t" "ds_read_b64_tr_b16 v[82:83], v1 offset:10240\n\t" \
  "ds_read_b64_tr_b16 v[84:85], v2 offset:8192\n\t" "ds_read_b64_tr_b16 v[86:87], v2 offset:10240\n\t" "ds_read_b64_tr_b16 v[88:89], v35 offset:12288\n\t" \
  "ds_read_b64_tr_b16 v[90:91], v35 offset:14336\n\t" "ds_read_b64_tr_b16 v[92:93], v0 offset:12288\n\t" "ds_read_b64_tr_b16 v[94:95], v0 offset:14336\n\t" \
  "ds_read_b64_tr_b16 v[96:97], v1 offset:12288\n\t" "ds_read_b64_tr_b16 v[98:99], v1 offset:14336\n\t" "ds_read_b64_tr_b16 v[100:101], v2 offset:12288\n\t" \
  "ds_read_b64_tr_b16 v[102:103], v2 offset:14336\n\t" "ds_read_b64_tr_b16 v[104:105], v35 offset:16384\n\t" "ds_read_b64_tr_b16 v[106:107], v35 offset:18432\n\t" \
  "ds_read_b64_tr_b16 v[108:109], v0 offset:16384\n\t" "ds_read_b64_tr_b16 v[110:111], v0 offset:18432\n\t" "ds_read_b64_tr_b16 v[112:113], v1 offset:16384\n\t" \
  "ds_read_b64_tr_b16 v[114:115], v1 offset:18432\n\t" "ds_read_b64_tr_b16 v[116:117], v2 offset:16384\n\t" "ds_read_b64_tr_b16 v[118:119], v2 offset:18432\n\t" "s_cmp_lt_u32 s26, 6\n\t" \
  "s_cbranch_scc1 .Lb3b_%=\n\t" "s_setprio 1\n\t" "ds_read_b64 v[152:153], v37 offset:80\n\t" "s_waitcnt lgkmcnt(0)\n\t" "v_readfirstlane_b32 s80, v152\n\t" "v_readfirstlane_b32 s81, v153\n\t" \
  "s_sub_u32 s81, s81, s80\n\t" "s_sub_u32 s80, s81, 15\n\t" "s_barrier\n\t" "s_cbranch_scc1 .Llx2_%=\n\t" "s_memrealtime s[76:77]\n\t" ".Llw2_%=:\n\t" "s_memrealtime s[78:79]\n\t" \
  "s_waitcnt lgkmcnt(0)\n\t" "s_sub_u32 s78, s78, s76\n\t" "s_cmp_lt_u32 s78, s80\n\t" "s_cbranch_scc1 .Llw2_%=\n\t" ".Llx2_%=:\n\t" \
  "global_load_dwordx4 v[152:155], v32, s[62:63] offset:-2048 sc1\n\t" "global_load_dwordx4 v[156:159], v32, s[62:63] offset:-1024 sc1\n\t" \
  "global_load_dwordx4 v[160:163], v32, s[62:63] offset:0 sc1\n\t" ".Lb3b_%=:\n\t" "ds_read_b64_tr_b16 v[120:121], v35 offset:20480\n\t" "ds_read_b64_tr_b16 v[122:123], v35 offset:22528\n\t" \
  "ds_read_b64_tr_b16 v[124:125], v0 offset:20480\n\t" "ds_read_b64_tr_b16 v[126:127], v0 offset:22528\n\t" "ds_read_b64_tr_b16 v[128:129], v1 offset:20480\n\t" \
  "ds_read_b64_tr_b16 v[130:131], v1 offset:22528\n\t" "ds_read_b64_tr_b16 v[132:133], v2 offset:20480\n\t" "ds_read_b64_tr_b16 v[134:135], v2 offset:22528\n\t" "s_waitcnt vmcnt(5)\n\t" \
  "v_permlane16_swap_b32 v12, v14\n\t" "v_permlane16_swap_b32 v13, v15\n\t" "v_mov_b64 v[144:145], 0\n\t" "v_mov_b64 v[146:147], 0\n\t" "v_mov_b64 v[148:149], 0\n\t" "v_mov_b64 v[150:151], 0\n\t" \
  "v_lshlrev_b32 v136, 16, v12\n\t" "v_and_b32 v137, 0xffff0000, v12\n\t" "v_lshlrev_b32 v138, 16, v13\n\t" "v_and_b32 v139, 0xffff0000, v13\n\t" "v_lshlrev_b32 v140, 16, v14\n\t" \
  "v_and_b32 v141, 0xffff0000, v14\n\t" "v_lshlrev_b32 v142, 16, v15\n\t" "v_and_b32 v143, 0xffff0000, v15\n\t" "s_lshl_b32 s38, s26, 11\n\t" "v_and_b32 v28, 0xf0, v32\n\t" \
  "v_add_u32 v24, s38, v32\n\t" "v_permlane16_swap_b32 v16, v18\n\t" "v_permlane16_swap_b32 v17, v19\n\t" "s_lshl_b32 s40, s36, 6\n\t" "s_add_u32 s40, s40, 0\n\t" "v_lshl_add_u32 v27, v10, 1, s40\n\t" \
  "s_mov_b32 s40, 0x3f803f80\n\t" "v_xor_b32 v25, 0x800, v24\n\t" "v_mad_u32_u24 v27, v28, 9, v27\n\t" "s_mul_i32 s38, s37, 2304\n\t" "v_add_u32 v28, v28, v10\n\t" "v_add_u32 v27, s38, v27\n\t" \
  "s_mul_i32 s38, s36, 6\n\t" "s_add_u32 s38, s38, s37\n\t" "s_lshl_b32 s38, s38, 8\n\t" "v_lshl_add_u32 v26, s37, 2, v37\n\t" "v_add3_u32 v28, v28, s38, v37\n\t" "s_mul_i32 s38, s34, 768\n\t" \
  "v_min_u32 v29, 0x2f0, v32\n\t" "v_lshl_add_u32 v30, s34, 2, v37\n\t" "v_add3_u32 v29, v29, s38, v37\n\t" "s_lshl1_add_u32 s41, s29, s34\n\t" "v_min_u32 v20, 47, v8\n\t" \
  "s_mul_i32 s38, s41, 6400\n\t" "s_add_u32 s38, s38, 4829184\n\t" "v_mul_u32_u24 v21, 12, v20\n\t" "s_add_u32 s44, s24, s38\n\t" "s_addc_u32 s45, s25, 0\n\t" "s_add_u32 s42, s44, 2560\n\t" \
  "s_addc_u32 s43, s45, 0\n\t" "s_mul_i32 s38, s28, 640\n\t" "s_add_u32 s44, s44, s38\n\t" "s_addc_u32 s45, s45, 0\n\t" "s_waitcnt vmcnt(2)\n\t" "v_cmp_eq_u32 vcc, -1, v152\n\t" \
  "s_cbranch_vccnz .Lso0_%=\n\t" ".Lro0_%=:\n\t" "s_waitcnt lgkmcnt(14)\n\t" "v_mfma_f32_16x16x32_bf16 v[136:139], v[40:43], v[152:155], v[136:139]\n\t" \
  "global_load_dwordx4 v[164:167], v32, s[62:63] offset:1024 sc1\n\t" "ds_read_b64_tr_b16 v[40:41], v35 offset:24576\n\t" "ds_read_b64_tr_b16 v[42:43], v35 offset:26624\n\t" \
  "v_mfma_f32_16x16x32_bf16 v[140:143], v[44:47], v[152:155], v[140:143]\n\t" "ds_read_b64_tr_b16 v[44:45], v0 offset:24576\n\t" "ds_read_b64_tr_b16 v[46:47], v0 offset:26624\n\t" \
  "v_mfma_f32_16x16x32_bf16 v[144:147], v[48:51], v[152:155], v[144:147]\n\t" "ds_read_b64_tr_b16 v[48:49], v1 offset:24576\n\t" "ds_read_b64_tr_b16 v[50:51], v1 offset:26624\n\t" \
  "v_mfma_f32_16x16x32_bf16 v[148:151], v[52:55], v[152:155], v[148:151]\n\t" "ds_read_b64_tr_b16 v[52:53], v2 offset:24576\n\t" "ds_read_b64_tr_b16 v[54:55], v2 offset:26624\n\t" \
  "s_waitcnt vmcnt(2)\n\t" "v_cmp_eq_u32 vcc, -1, v156\n\t" "s_cbranch_vccnz .Lso1_%=\n\t" ".Lro1_%=:\n\t" "v_mfma_f32_16x16x32_bf16 v[136:139], v[56:59], v[156:159], v[136:139]\n\t" \
  "global_load_dwordx4 v[152:155], v32, s[62:63] offset:2048 sc1\n\t" "ds_read_b64_tr_b16 v[56:57], v35 offset:28672\n\t" "ds_read_b64_tr_b16 v[58:59], v35 offset:30720\n\t" \
  "v_mfma_f32_16x16x32_bf16 v[140:143], v[60:63], v[156:159], v[140:143]\n\t" "ds_read_b64_tr_b16 v[60:61], v0 offset:28672\n\t" "ds_read_b64_tr_b16 v[62:63], v0 offset:30720\n\t" \
  "v_mfma_f32_16x16x32_bf16 v[144:147], v[64:67], v[156:159], v[144:147]\n\t" "ds_read_b64_tr_b16 v[64:65], v1 offset:28672\n\t" "ds_read_b64_tr_b16 v[66:67], v1 offset:30720\n\t" \
  "v_mfma_f32_16x16x32_bf16 v[148:151], v[68:71], v[156:159], v[148:151]\n\t" "ds_read_b64_tr_b16 v[68:69], v2 offset:28672\n\t" "ds_read_b64_tr_b16 v[70:71], v2 offset:30720\n\t" \
  "s_waitcnt vmcnt(2)\n\t" "v_cmp_eq_u32 vcc, -1, v160\n\t" "s_cbranch_vccnz .Lso2_%=\n\t" ".Lro2_%=:\n\t" "v_mfma_f32_16x16x32_bf16 v[136:139], v[72:75], v[160:163], v[136:139]\n\t" \
  "global_load_dwordx4 v[156:159], v32, s[64:65] offset:-2048 sc1\n\t" "ds_read_b64_tr_b16 v[72:73], v35 offset:32768\n\t" "ds_read_b64_tr_b16 v[74:75], v35 offset:34816\n\t" \
  "v_mfma_f32_16x16x32_bf16 v[140:143], v[76:79], v[160:163], v[140:143]\n\t" "ds_read_b64_tr_b16 v[76:77], v0 offset:32768\n\t" "ds_read_b64_tr_b16 v[78:79], v0 offset:34816\n\t" \
  "v_mfma_f32_16x16x32_bf16 v[144:147], v[80:83], v[160:163], v[144:147]\n\t" "ds_read_b64_tr_b16 v[80:81], v1 offset:32768\n\t" "ds_read_b64_tr_b16 v[82:83], v1 offset:34816\n\t" \
  "v_mfma_f32_16x16x32_bf16 v[148:151], v[84:87], v[160:163], v[148:151]\n\t" "ds_read_b64_tr_b16 v[84:85], v2 offset:32768\n\t" "ds_read_b64_tr_b16 v[86:87], v2 offset:34816\n\t" \
  "s_waitcnt vmcnt(2)\n\t" "v_cmp_eq_u32 vcc, -1, v164\n\t" "s_cbranch_vccnz .Lso3_%=\n\t" ".Lro3_%=:\n\t" "v_mfma_f32_16x16x32_bf16 v[136:139], v[88:91], v[164:167], v[136:139]\n\t" \
  "global_load_dwordx4 v[160:163], v32, s[64:65] offset:-1024 sc1\n\t" "ds_read_b64_tr_b16 v[88:89], v35 offset:36864\n\t" "ds_read_b64_tr_b16 v[90:91], v35 offset:38912\n\t" \
  "v_mfma_f32_16x16x32_bf16 v[140:143], v[92:95], v[164:167], v[140:143]\n\t" "ds_read_b64_tr_b16 v[92:93], v0 offset:36864\n\t" "ds_read_b64_tr_b16 v[94:95], v0 offset:38912\n\t" \
  "v_mfma_f32_16x16x32_bf16 v[144:147], v[96:99], v[164:167], v[144:147]\n\t" "ds_read_b64_tr_b16 v[96:97], v1 offset:36864\n\t" "ds_read_b64_tr_b16 v[98:99], v1 offset:38912\n\t" \
  "v_mfma_f32_16x16x32_bf16 v[148:151], v[100:103], v[164:167], v[148:151]\n\t" "ds_read_b64_tr_b16 v[100:101], v2 offset:36864\n\t" "ds_read_b64_tr_b16 v[102:103], v2 offset:38912\n\t" \
  "s_waitcnt vmcnt(2)\n\t" "v_cmp_eq_u32 vcc, -1, v152\n\t" "s_cbranch_vccnz .Lso4_%=\n\t" ".Lro4_%=:\n\t" "v_mfma_f32_16x16x32_bf16 v[136:139], v[104:107], v[152:155], v[136:139]\n\t" \
  "global_load_dwordx4 v[164:167], v32, s[64:65] offset:0 sc1\n\t" "s_waitcnt lgkmcnt(14)\n\t" "v_mfma_f32_16x16x32_bf16 v[140:143], v[108:111], v[152:155], v[140:143]\n\t" \
  "v_mfma_f32_16x16x32_bf16 v[144:147], v[112:115], v[152:155], v[144:147]\n\t" "v_mfma_f32_16x16x32_bf16 v[148:151], v[116:119], v[152:155], v[148:151]\n\t" "s_waitcnt vmcnt(2)\n\t" \
  "v_cmp_eq_u32 vcc, -1, v156\n\t" "s_cbranch_vccnz .Lso5_%=\n\t" ".Lro5_%=:\n\t" "v_mfma_f32_16x16x32_bf16 v[136:139], v[120:123], v[156:159], v[136:139]\n\t" \
  "global_load_dwordx4 v[152:155], v32, s[64:65] offset:1024 sc1\n\t" "v_mfma_f32_16x16x32_bf16 v[140:143], v[124:127], v[156:159], v[140:143]\n\t" \
  "v_mfma_f32_16x16x32_bf16 v[144:147], v[128:131], v[156:159], v[144:147]\n\t" "v_mfma_f32_16x16x32_bf16 v[148:151], v[132:135], v[156:159], v[148:151]\n\t" "s_waitcnt vmcnt(2)\n\t" \
  "v_cmp_eq_u32 vcc, -1, v160\n\t" "s_cbranch_vccnz .Lso6_%=\n\t" ".Lro6_%=:\n\t" "v_mfma_f32_16x16x32_bf16 v[136:139], v[40:43], v[160:163], v[136:139]\n\t" \
  "global_load_dwordx4 v[156:159], v32, s[64:65] offset:2048 sc1\n\t" "v_mfma_f32_16x16x32_bf16 v[140:143], v[44:47], v[160:163], v[140:143]\n\t" \
  "v_mfma_f32_16x16x32_bf16 v[144:147], v[48:51], v[160:163], v[144:147]\n\t" "v_mfma_f32_16x16x32_bf16 v[148:151], v[52:55], v[160:163], v[148:151]\n\t" "s_waitcnt vmcnt(2)\n\t" \
  "v_cmp_eq_u32 vcc, -1, v164\n\t" "s_cbranch_vccnz .Lso7_%=\n\t" ".Lro7_%=:\n\t" "v_mfma_f32_16x16x32_bf16 v[136:139], v[56:59], v[164:167], v[136:139]\n\t" \
  "v_mfma_f32_16x16x32_bf16 v[140:143], v[60:63], v[164:167], v[140:143]\n\t" "v_mfma_f32_16x16x32_bf16 v[144:147], v[64:67], v[164:167], v[144:147]\n\t" \
  "v_mfma_f32_16x16x32_bf16 v[148:151], v[68:71], v[164:167], v[148:151]\n\t" "s_waitcnt vmcnt(1)\n\t" "v_cmp_eq_u32 vcc, -1, v152\n\t" "s_cbranch_vccnz .Lso8_%=\n\t" ".Lro8_%=:\n\t" \
  "v_mfma_f32_16x16x32_bf16 v[136:139], v[72:75], v[152:155], v[136:139]\n\t" "s_waitcnt lgkmcnt(12)\n\t" "v_mfma_f32_16x16x32_bf16 v[140:143], v[76:79], v[152:155], v[140:143]\n\t" \
  "s_waitcnt lgkmcnt(10)\n\t" "v_mfma_f32_16x16x32_bf16 v[144:147], v[80:83], v[152:155], v[144:147]\n\t" "s_waitcnt lgkmcnt(8)\n\t" \
  "v_mfma_f32_16x16x32_bf16 v[148:151], v[84:87], v[152:155], v[148:151]\n\t" "s_waitcnt vmcnt(0)\n\t" "v_cmp_eq_u32 vcc, -1, v156\n\t" "s_cbranch_vccnz .Lso9_%=\n\t" ".Lro9_%=:\n\t" \
  "s_waitcnt lgkmcnt(2)\n\t" "v_mfma_f32_16x16x32_bf16 v[144:147], v[96:99], v[156:159], v[144:147]\n\t" "s_waitcnt lgkmcnt(0)\n\t" \
  "v_mfma_f32_16x16x32_bf16 v[148:151], v[100:103], v[156:159], v[148:151]\n\t" "v_mfma_f32_16x16x32_bf16 v[136:139], v[88:91], v[156:159], v[136:139]\n\t" \
  "v_mfma_f32_16x16x32_bf16 v[140:143], v[92:95], v[156:159], v[140:143]\n\t" "s_nop 3\n\t" "ds_write_b128 v24, v[144:147] offset:36864\n\t" "s_nop 0\n\t" \
  "ds_write_b128 v24, v[148:151] offset:37888\n\t" "s_mov_b64 exec, 1\n\t" "ds_add_u32 v26, v38 offset:48\n\t" "s_mov_b64 exec, -1\n\t" ".Lp_%=:\n\t" "ds_read_b32 v39, v26 offset:48\n\t" \
  "ds_read_b128 v[40:43], v25 offset:36864\n\t" "ds_read_b128 v[44:47], v25 offset:37888\n\t" "s_sub_u32 s39, s39, 1\n\t" "s_cmp_lt_i32 s39, 0\n\t" "s_waitcnt lgkmcnt(2)\n\t" \
  "v_readfirstlane_b32 s38, v39\n\t" "s_cselect_b32 s38, 2, s38\n\t" "s_cmp_ge_u32 s38, 2\n\t" "s_waitcnt lgkmcnt(0)\n\t" "s_cbranch_scc0 .Lp_%=\n\t" \
  "v_pk_add_f32 v[136:137], v[136:137], v[40:41]\n\t" "v_pk_add_f32 v[138:139], v[138:139], v[42:43]\n\t" "v_pk_add_f32 v[140:141], v[140:141], v[44:45]\n\t" \
  "v_pk_add_f32 v[142:143], v[142:143], v[46:47]\n\t" "v_cvt_pk_bf16_f32 v12, v136, v137\n\t" "v_cvt_pk_bf16_f32 v13, v138, v139\n\t" "v_cvt_pk_bf16_f32 v14, v140, v141\n\t" \
  "v_cvt_pk_bf16_f32 v15, v142, v143\n\t" "v_dot2_f32_bf16 v40, v12, s40, 0\n\t" "v_dot2_f32_bf16 v41, v12, v12, 0\n\t" "v_dot2_f32_bf16 v42, v12, v16, 0\n\t" "v_dot2_f32_bf16 v40, v13, s40, v40\n\t" \
  "v_dot2_f32_bf16 v41, v13, v13, v41\n\t" "v_dot2_f32_bf16 v42, v13, v17, v42\n\t" "v_dot2_f32_bf16 v40, v14, s40, v40\n\t" "v_dot2_f32_bf16 v41, v14, v14, v41\n\t" \
  "v_dot2_f32_bf16 v42, v14, v18, v42\n\t" "v_dot2_f32_bf16 v40, v15, s40, v40\n\t" "v_dot2_f32_bf16 v41, v15, v15, v41\n\t" "v_dot2_f32_bf16 v43, v15, v19, v42\n\t" \
  "v_dot2_f32_bf16 v42, v15, v19, v42\n\t" "ds_write2_b64 v27, v[12:13], v[14:15] offset1:4\n\t" "s_nop 0\n\t" "v_permlane32_swap_b32 v40, v41\n\t" "v_permlane32_swap_b32 v42, v43\n\t" \
  "v_add_f32 v40, v40, v41\n\t" "v_add_f32 v42, v42, v43\n\t" "s_nop 1\n\t" "v_permlane16_swap_b32 v40, v42\n\t" "v_add_f32 v40, v40, v42\n\t" "ds_write_b32 v28, v40 offset:256\n\t" \
  "s_mov_b64 exec, 1\n\t" "ds_add_rtn_u32 v39, v30, v38 offset:16\n\t" "s_mov_b64 exec, -1\n\t" "ds_read_b128 v[44:47], v29 offset:256\n\t" "ds_read_b128 v[48:51], v29 offset:1792\n\t" \
  "ds_read_b32 v52, v37 offset:40\n\t" "s_waitcnt lgkmcnt(3)\n\t" "v_readfirstlane_b32 s38, v39\n\t" "s_waitcnt lgkmcnt(0)\n\t" "s_cmp_lg_u32 s38, 5\n\t" "s_setprio 0\n\t" "s_cbranch_scc1 .Lnk_%=\n\t" \
  "s_setprio 3\n\t" "v_pk_add_f32 v[44:45], v[44:45], v[48:49]\n\t" "v_add_f32 v46, v46, v50\n\t" "s_mov_b32 exec_hi, 0xffff\n\t" "global_store_dwordx3 v21, v[44:46], s[44:45] sc1\n\t" \
  "s_mov_b32 exec_hi, -1\n\t" "s_memrealtime s[76:77]\n\t" "s_mul_i32 s50, s34, 48\n\t" "v_lshl_add_u32 v4, s41, 6, v20\n\t" "v_add_u32 v1, s50, v20\n\t" "s_cmp_eq_u32 s29, 0\n\t" \
  "s_cselect_b32 s51, 6, 7\n\t" "v_cmp_le_u32 vcc, s51, v1\n\t" "s_mov_b32 s51, 155648\n\t" "v_lshl_add_u32 v1, v1, 3, s51\n\t" "v_mov_b32 v3, 0xfffff000\n\t" "s_lshl_b32 s51, s41, 7\n\t" \
  "v_cndmask_b32 v3, 0, v3, vcc\n\t" "v_lshl_add_u32 v5, v9, 3, s51\n\t" "v_lshlrev_b32 v7, 2, v8\n\t" "s_mul_i32 s50, s50, 144\n\t" "v_and_b32 v6, 0x7c, v7\n\t" "v_add_u32 v6, s50, v6\n\t" \
  "s_mul_i32 s50, s28, 6144\n\t" "s_add_u32 s50, s50, 5302784\n\t" "s_add_u32 s46, s24, s50\n\t" "s_addc_u32 s47, s25, 0\n\t" "s_mul_i32 s50, s41, 10\n\t" "s_add_u32 s50, s50, s28\n\t" \
  "s_lshl_b32 s50, s50, 8\n\t" "s_add_u32 s50, s50, 5123584\n\t" "s_add_u32 s48, s24, s50\n\t" "s_addc_u32 s49, s25, 0\n\t" "s_mov_b32 s50, 0x3acccccd\n\t" ".Ltk_%=:\n\t" "s_memrealtime s[78:79]\n\t" \
  "s_waitcnt lgkmcnt(0)\n\t" "s_sub_u32 s78, s78, s76\n\t" "s_cmp_lt_u32 s78, 40\n\t" "s_cbranch_scc1 .Ltk_%=\n\t" ".Lkp_%=:\n\t" "global_load_dwordx3 v[60:62], v21, s[42:43] offset:-2560 sc1\n\t" \
  "global_load_dwordx3 v[64:66], v21, s[42:43] offset:-1920 sc1\n\t" "global_load_dwordx3 v[68:70], v21, s[42:43] offset:-1280 sc1\n\t" \
  "global_load_dwordx3 v[72:74], v21, s[42:43] offset:-640 sc1\n\t" "global_load_dwordx3 v[76:78], v21, s[42:43] offset:0 sc1\n\t" "global_load_dwordx3 v[80:82], v21, s[42:43] offset:640 sc1\n\t" \
  "global_load_dwordx3 v[84:86], v21, s[42:43] offset:1280 sc1\n\t" "global_load_dwordx3 v[88:90], v21, s[42:43] offset:1920 sc1\n\t" "global_load_dwordx3 v[92:94], v21, s[42:43] offset:2560 sc1\n\t" \
  "global_load_dwordx3 v[96:98], v21, s[42:43] offset:3200 sc1\n\t" "s_sub_u32 s39, s39, 1\n\t" "s_cmp_lt_i32 s39, 0\n\t" "s_waitcnt vmcnt(0)\n\t" "v_pk_add_f32 v[60:61], v[60:61], v[64:65]\n\t" \
  "v_add_f32 v62, v62, v66\n\t" "v_pk_add_f32 v[68:69], v[68:69], v[72:73]\n\t" "v_add_f32 v70, v70, v74\n\t" "v_pk_add_f32 v[76:77], v[76:77], v[80:81]\n\t" "v_add_f32 v78, v78, v82\n\t" \
  "v_pk_add_f32 v[84:85], v[84:85], v[88:89]\n\t" "v_add_f32 v86, v86, v90\n\t" "v_pk_add_f32 v[92:93], v[92:93], v[96:97]\n\t" "v_add_f32 v94, v94, v98\n\t" \
  "v_pk_add_f32 v[60:61], v[60:61], v[68:69]\n\t" "v_add_f32 v62, v62, v70\n\t" "v_pk_add_f32 v[76:77], v[76:77], v[84:85]\n\t" "v_add_f32 v78, v78, v86\n\t" \
  "v_pk_add_f32 v[60:61], v[60:61], v[92:93]\n\t" "v_add_f32 v62, v62, v94\n\t" "v_pk_add_f32 v[60:61], v[60:61], v[76:77]\n\t" "v_add_f32 v62, v62, v78\n\t" "v_mul_f32 v60, s50, v60\n\t" \
  "v_mul_f32 v63, v60, v60\n\t" "v_fma_f32 v61, -v60, v52, v61\n\t" "v_fma_f32 v62, v62, s50, -v63\n\t" "s_cbranch_scc1 .Lkq_%=\n\t" "v_cmp_u_f32 vcc, v61, v62\n\t" "s_cbranch_vccnz .Lkp_%=\n\t" \
  ".Lkq_%=:\n\t" "v_max_f32 v62, 0, v62\n\t" "v_add_f32 v63, 0x3727c5ac, v62\n\t" "v_rsq_f32 v63, v63\n\t" "v_cvt_pk_bf16_f32 v66, v62, v60\n\t" "v_mul_f32 v61, v61, v63\n\t" \
  "v_ashrrev_i32 v63, 31, v61\n\t" "v_or_b32 v63, 0x80000000, v63\n\t" "v_xor_b32 v61, v61, v63\n\t" "v_and_or_b32 v61, v61, v3, v4\n\t" "v_lshlrev_b32 v67, 16, v66\n\t" \
  "v_and_b32 v66, 0xffff0000, v66\n\t" "v_max_u32_dpp v64, v61, v61 quad_perm:[1,0,3,2] row_mask:0xf bank_mask:0xf\n\t" "v_add_f32 v67, 0x3727c5ac, v67\n\t" "v_cvt_pk_bf16_f32 v67, v67, v67\n\t" \
  "v_max_u32_dpp v64, v64, v64 quad_perm:[2,3,0,1] row_mask:0xf bank_mask:0xf\n\t" "v_lshlrev_b32 v67, 16, v67\n\t" "v_sqrt_f32 v67, v67\n\t" \
  "v_max_u32_dpp v64, v64, v64 row_half_mirror row_mask:0xf bank_mask:0xf\n\t" "s_nop 0\n\t" "v_cvt_pk_bf16_f32 v67, v67, v67\n\t" \
  "v_max_u32_dpp v64, v64, v64 row_mirror row_mask:0xf bank_mask:0xf\n\t" "v_lshlrev_b32 v67, 16, v67\n\t" "v_rcp_f32 v67, v67\n\t" \
  "v_max_u32_dpp v64, v64, v64 row_bcast:15 row_mask:0xa bank_mask:0xf\n\t" "s_nop 1\n\t" "v_max_u32_dpp v64, v64, v64 row_bcast:31 row_mask:0xc bank_mask:0xf\n\t" "v_mov_b32 v65, v64\n\t" \
  "s_mov_b32 exec_lo, 0\n\t" "s_mov_b32 exec_hi, 0xffff0000\n\t" "global_store_dwordx2 v5, v[64:65], s[46:47] sc1\n\t" "s_mov_b64 exec, -1\n\t" "v_readlane_b32 s50, v64, 63\n\t" \
  "s_and_b32 s51, s50, 63\n\t" "s_mul_i32 s52, s51, 144\n\t" "v_add_u32 v6, s52, v6\n\t" "s_or_b32 s50, s50, 0x1000\n\t" "ds_read_b32 v68, v6 offset:0\n\t" "v_mov_b32 v64, s50\n\t" \
  "ds_write_b64 v1, v[66:67]\n\t" "ds_write_b32 v30, v64 offset:24\n\t" "v_readlane_b32 s52, v66, s51\n\t" "v_readlane_b32 s53, v67, s51\n\t" "s_waitcnt lgkmcnt(2)\n\t" \
  "v_lshlrev_b32 v70, 16, v68\n\t" "v_and_b32 v71, 0xffff0000, v68\n\t" "v_subrev_f32 v70, s52, v70\n\t" "v_subrev_f32 v71, s52, v71\n\t" "v_cvt_pk_bf16_f32 v71, v70, v71\n\t" \
  "v_lshlrev_b32 v70, 16, v71\n\t" "v_and_b32 v71, 0xffff0000, v71\n\t" "v_mul_f32 v70, s53, v70\n\t" "v_mul_f32 v71, s53, v71\n\t" "s_mov_b32 exec_hi, 0\n\t" "v_cvt_pk_bf16_f32 v68, v70, v71\n\t" \
  "s_mov_b32 exec_hi, -1\n\t" "global_store_dword v7, v68, s[48:49] sc1\n\t" "s_setprio 0\n\t" ".Lnk_%=:\n\t" "v_lshrrev_b32 v113, 3, v8\n\t" "s_lshl_b32 s40, s26, 3\n\t" "v_add_u32 v2, s40, v113\n\t" \
  "s_mov_b32 s76, 155648\n\t" "v_lshl_add_u32 v1, v2, 3, s76\n\t" "s_cmp_eq_u32 s29, 0\n\t" "s_cselect_b32 s76, 6, 7\n\t" "v_cmp_le_u32 s[72:73], s76, v2\n\t" "v_add_u32 v2, s30, v2\n\t" \
  "v_sub_u32 v2, v2, 6 clamp\n\t" "v_lshrrev_b32 v3, 10, v2\n\t" "v_lshl_or_b32 v2, v2, 1, v3\n\t" "v_and_b32 v2, 0x7ff, v2\n\t" "v_and_b32 v3, 0x70, v32\n\t" "s_lshl_b32 s76, s28, 7\n\t" \
  "v_add_u32 v3, s76, v3\n\t" "s_movk_i32 s76, 1280\n\t" "v_mad_u32_u24 v33, v2, s76, v3\n\t" "s_mul_i32 s76, s26, 1152\n\t" "v_mad_u32_u24 v3, v113, 16, v32\n\t" "v_add_u32 v3, s76, v3\n\t" \
  "s_lshl_b32 s76, s35, 3\n\t" "v_add_u32 v113, s76, v113\n\t" "s_branch .Lw1_%=\n\t" ".Lw_%=:\n\t" "s_sleep 2\n\t" ".Lw1_%=:\n\t" "ds_read_b32 v112, v30 offset:24\n\t" \
  "ds_read_b64 v[110:111], v1\n\t" "ds_read_b128 v[12:15], v3 offset:0\n\t" "s_sub_u32 s39, s39, 1\n\t" "s_cmp_lt_i32 s39, 0\n\t" "s_waitcnt lgkmcnt(0)\n\t" "v_readfirstlane_b32 s66, v112\n\t" \
  "s_cselect_b32 s66, 1, s66\n\t" "s_cmp_eq_u32 s66, 0\n\t" "s_cbranch_scc1 .Lw_%=\n\t" "s_and_b32 s76, s66, 63\n\t" "v_cmp_eq_u32 s[68:69], s76, v113\n\t" "v_lshlrev_b32 v120, 16, v12\n\t" \
  "v_and_b32 v121, 0xffff0000, v12\n\t" "v_lshlrev_b32 v122, 16, v13\n\t" "v_and_b32 v123, 0xffff0000, v13\n\t" "v_lshlrev_b32 v124, 16, v14\n\t" "v_and_b32 v125, 0xffff0000, v14\n\t" \
  "v_lshlrev_b32 v126, 16, v15\n\t" "v_and_b32 v127, 0xffff0000, v15\n\t" "v_pk_add_f32 v[128:129], v[120:121], v[110:111] op_sel_hi:[1,0] neg_lo:[0,1] neg_hi:[0,1]\n\t" \
  "v_pk_add_f32 v[130:131], v[122:123], v[110:111] op_sel_hi:[1,0] neg_lo:[0,1] neg_hi:[0,1]\n\t" "v_pk_add_f32 v[132:133], v[124:125], v[110:111] op_sel_hi:[1,0] neg_lo:[0,1] neg_hi:[0,1]\n\t" \
  "v_pk_add_f32 v[134:135], v[126:127], v[110:111] op_sel_hi:[1,0] neg_lo:[0,1] neg_hi:[0,1]\n\t" "v_cvt_pk_bf16_f32 v129, v128, v129\n\t" "v_cvt_pk_bf16_f32 v131, v130, v131\n\t" \
  "v_cvt_pk_bf16_f32 v133, v132, v133\n\t" "v_cvt_pk_bf16_f32 v135, v134, v135\n\t" "v_lshlrev_b32 v128, 16, v129\n\t" "v_and_b32 v129, 0xffff0000, v129\n\t" "v_lshlrev_b32 v130, 16, v131\n\t" \
  "v_and_b32 v131, 0xffff0000, v131\n\t" "v_lshlrev_b32 v132, 16, v133\n\t" "v_and_b32 v133, 0xffff0000, v133\n\t" "v_lshlrev_b32 v134, 16, v135\n\t" "v_and_b32 v135, 0xffff0000, v135\n\t" \
  "v_pk_mul_f32 v[128:129], v[128:129], v[110:111] op_sel:[0,1] op_sel_hi:[1,1]\n\t" "v_pk_mul_f32 v[130:131], v[130:131], v[110:111] op_sel:[0,1] op_sel_hi:[1,1]\n\t" \
  "v_pk_mul_f32 v[132:133], v[132:133], v[110:111] op_sel:[0,1] op_sel_hi:[1,1]\n\t" "v_pk_mul_f32 v[134:135], v[134:135], v[110:111] op_sel:[0,1] op_sel_hi:[1,1]\n\t" \
  "v_cvt_pk_bf16_f32 v129, v128, v129\n\t" "v_cvt_pk_bf16_f32 v131, v130, v131\n\t" "v_cvt_pk_bf16_f32 v133, v132, v133\n\t" "v_cvt_pk_bf16_f32 v135, v134, v135\n\t" "v_lshlrev_b32 v128, 16, v129\n\t" \
  "v_and_b32 v129, 0xffff0000, v129\n\t" "v_lshlrev_b32 v130, 16, v131\n\t" "v_and_b32 v131, 0xffff0000, v131\n\t" "v_lshlrev_b32 v132, 16, v133\n\t" "v_and_b32 v133, 0xffff0000, v133\n\t" \
  "v_lshlrev_b32 v134, 16, v135\n\t" "v_and_b32 v135, 0xffff0000, v135\n\t" "v_pk_add_f32 v[128:129], v[128:129], v[120:121]\n\t" "v_pk_add_f32 v[130:131], v[130:131], v[122:123]\n\t" \
  "v_pk_add_f32 v[132:133], v[132:133], v[124:125]\n\t" "v_pk_add_f32 v[134:135], v[134:135], v[126:127]\n\t" "v_cvt_pk_bf16_f32 v116, v128, v129\n\t" "v_cvt_pk_bf16_f32 v117, v130, v131\n\t" \
  "v_cvt_pk_bf16_f32 v118, v132, v133\n\t" "v_cvt_pk_bf16_f32 v119, v134, v135\n\t" "s_sleep 16\n\t" "s_andn2_b64 exec, s[72:73], s[68:69]\n\t" "s_cbranch_scc0 .Ln1_%=\n\t" \
  "global_store_dwordx4 v33, v[116:119], s[22:23] sc0 sc1 nt\n\t" ".Ln1_%=:\n\t" "s_mov_b64 exec, -1\n\t" "s_cmp_eq_u32 s26, 11\n\t" "s_cbranch_scc1 .Lrz_%=\n\t" "s_cmp_eq_u64 s[68:69], 0\n\t" \
  "s_cbranch_scc1 .Lend_%=\n\t" ".Lb6_%=:\n\t" "s_waitcnt lgkmcnt(0)\n\t" "s_barrier\n\t" "ds_read_b32 v39, v37 offset:32\n\t" "s_waitcnt lgkmcnt(0)\n\t" "v_readfirstlane_b32 s76, v39\n\t" \
  "s_xor_b32 s76, s76, s66\n\t" "s_and_b32 s76, s76, 4095\n\t" "s_cbranch_scc0 .Lend_%=\n\t" "s_and_b64 exec, s[72:73], s[68:69]\n\t" "s_cbranch_scc0 .Ln2_%=\n\t" \
  "global_store_dwordx4 v33, v[116:119], s[22:23] sc0 sc1 nt\n\t" ".Ln2_%=:\n\t" "s_mov_b64 exec, -1\n\t" "s_branch .Lend_%=\n\t" ".Lrz_%=:\n\t" "s_sleep 35\n\t" ".Lrt_%=:\n\t" \
  "s_mul_i32 s40, s28, 6144\n\t" "s_add_u32 s40, s40, 5302784\n\t" "v_lshlrev_b32 v2, 7, v8\n\t" "s_add_u32 s40, s24, s40\n\t" "s_addc_u32 s41, s25, 0\n\t" "v_mov_b32 v40, -1\n\t" \
  "v_mov_b32 v41, -1\n\t" "v_mov_b32 v42, -1\n\t" "s_mov_b32 exec_hi, 0x3fff\n\t" "global_load_dword v40, v2, s[40:41] sc1\n\t" "s_sleep 2\n\t" "global_load_dword v41, v2, s[40:41] sc1\n\t" \
  "s_sleep 2\n\t" "global_load_dword v42, v2, s[40:41] sc1\n\t" "s_mov_b32 exec_hi, -1\n\t" "v_mov_b32 v43, 0xfc0\n\t" "s_mov_b32 s42, 0\n\t" "s_mov_b32 s43, 0xffff0000\n\t" \
  "v_add_u32 v3, 64, v37\n\t" "v_cndmask_b32 v3, v3, v37, s[42:43]\n\t" "s_mov_b32 s43, 0xffffc000\n\t" ".Lrl_%=:\n\t" "s_waitcnt vmcnt(2)\n\t" "v_cmp_ne_u32 vcc, -1, v40\n\t" \
  "s_or_b64 s[42:43], s[42:43], vcc\n\t" "s_cmp_eq_u64 s[42:43], -1\n\t" "v_cndmask_b32 v43, v43, v40, vcc\n\t" "s_cselect_b32 s44, 1, 0\n\t" "v_mov_b32 v45, s44\n\t" \
  "v_max_u32_dpp v44, v43, v43 quad_perm:[1,0,3,2] row_mask:0xf bank_mask:0xf\n\t" "s_nop 1\n\t" "v_max_u32_dpp v44, v44, v44 quad_perm:[2,3,0,1] row_mask:0xf bank_mask:0xf\n\t" "s_nop 1\n\t" \
  "v_max_u32_dpp v44, v44, v44 row_half_mirror row_mask:0xf bank_mask:0xf\n\t" "s_nop 1\n\t" "v_max_u32_dpp v44, v44, v44 row_mirror row_mask:0xf bank_mask:0xf\n\t" "s_nop 1\n\t" \
  "v_max_u32_dpp v44, v44, v44 row_bcast:15 row_mask:0xa bank_mask:0xf\n\t" "s_nop 1\n\t" "v_max_u32_dpp v44, v44, v44 row_bcast:31 row_mask:0xc bank_mask:0xf\n\t" \
  "ds_write_b64 v3, v[44:45] offset:32\n\t" "s_cbranch_scc1 .Lre_%=\n\t" "s_bcnt1_i32_b64 s45, s[42:43]\n\t" "s_mul_i32 s45, s45, s75\n\t" "s_cmp_ge_u32 s45, 19\n\t" "s_cbranch_scc1 .Lrz0_%=\n\t" \
  "s_sleep 5\n\t" ".Lrz0_%=:\n\t" "s_not_b64 exec, s[42:43]\n\t" "global_load_dword v40, v2, s[40:41] sc1\n\t" "s_mov_b64 exec, -1\n\t" "s_waitcnt vmcnt(2)\n\t" "v_cmp_ne_u32 vcc, -1, v41\n\t" \
  "s_or_b64 s[42:43], s[42:43], vcc\n\t" "s_cmp_eq_u64 s[42:43], -1\n\t" "v_cndmask_b32 v43, v43, v41, vcc\n\t" "s_cselect_b32 s44, 1, 0\n\t" "v_mov_b32 v45, s44\n\t" \
  "v_max_u32_dpp v44, v43, v43 quad_perm:[1,0,3,2] row_mask:0xf bank_mask:0xf\n\t" "s_nop 1\n\t" "v_max_u32_dpp v44, v44, v44 quad_perm:[2,3,0,1] row_mask:0xf bank_mask:0xf\n\t" "s_nop 1\n\t" \
  "v_max_u32_dpp v44, v44, v44 row_half_mirror row_mask:0xf bank_mask:0xf\n\t" "s_nop 1\n\t" "v_max_u32_dpp v44, v44, v44 row_mirror row_mask:0xf bank_mask:0xf\n\t" "s_nop 1\n\t" \
  "v_max_u32_dpp v44, v44, v44 row_bcast:15 row_mask:0xa bank_mask:0xf\n\t" "s_nop 1\n\t" "v_max_u32_dpp v44, v44, v44 row_bcast:31 row_mask:0xc bank_mask:0xf\n\t" \
  "ds_write_b64 v3, v[44:45] offset:32\n\t" "s_cbranch_scc1 .Lre_%=\n\t" "s_bcnt1_i32_b64 s45, s[42:43]\n\t" "s_mul_i32 s45, s45, s75\n\t" "s_cmp_ge_u32 s45, 19\n\t" "s_cbranch_scc1 .Lrz1_%=\n\t" \
  "s_sleep 5\n\t" ".Lrz1_%=:\n\t" "s_not_b64 exec, s[42:43]\n\t" "global_load_dword v41, v2, s[40:41] sc1\n\t" "s_mov_b64 exec, -1\n\t" "s_waitcnt vmcnt(2)\n\t" "v_cmp_ne_u32 vcc, -1, v42\n\t" \
  "s_or_b64 s[42:43], s[42:43], vcc\n\t" "s_cmp_eq_u64 s[42:43], -1\n\t" "v_cndmask_b32 v43, v43, v42, vcc\n\t" "s_cselect_b32 s44, 1, 0\n\t" "v_mov_b32 v45, s44\n\t" \
  "v_max_u32_dpp v44, v43, v43 quad_perm:[1,0,3,2] row_mask:0xf bank_mask:0xf\n\t" "s_nop 1\n\t" "v_max_u32_dpp v44, v44, v44 quad_perm:[2,3,0,1] row_mask:0xf bank_mask:0xf\n\t" "s_nop 1\n\t" \
  "v_max_u32_dpp v44, v44, v44 row_half_mirror row_mask:0xf bank_mask:0xf\n\t" "s_nop 1\n\t" "v_max_u32_dpp v44, v44, v44 row_mirror row_mask:0xf bank_mask:0xf\n\t" "s_nop 1\n\t" \
  "v_max_u32_dpp v44, v44, v44 row_bcast:15 row_mask:0xa bank_mask:0xf\n\t" "s_nop 1\n\t" "v_max_u32_dpp v44, v44, v44 row_bcast:31 row_mask:0xc bank_mask:0xf\n\t" \
  "ds_write_b64 v3, v[44:45] offset:32\n\t" "s_cbranch_scc1 .Lre_%=\n\t" "s_bcnt1_i32_b64 s45, s[42:43]\n\t" "s_mul_i32 s45, s45, s75\n\t" "s_cmp_ge_u32 s45, 19\n\t" "s_cbranch_scc1 .Lrz2_%=\n\t" \
  "s_sleep 5\n\t" ".Lrz2_%=:\n\t" "s_not_b64 exec, s[42:43]\n\t" "global_load_dword v42, v2, s[40:41] sc1\n\t" "s_mov_b64 exec, -1\n\t" "s_sub_u32 s39, s39, 1\n\t" "s_cmp_lt_i32 s39, 0\n\t" \
  "s_cbranch_scc0 .Lrl_%=\n\t" ".Lre_%=:\n\t" "s_cmp_eq_u32 s75, 0\n\t" "s_cbranch_scc1 .Lb6_%=\n\t" "s_waitcnt lgkmcnt(0)\n\t" "s_barrier\n\t" "s_barrier\n\t" "s_cmp_lg_u32 s29, 0\n\t" \
  "s_cbranch_scc1 .Lend_%=\n\t" "v_mov_b32 v1, 0\n\t" "s_mov_b64 exec, 1\n\t" "global_store_dwordx2 v1, v[16:17], s[24:25] sc1\n\t" "s_mov_b64 exec, -1\n\t" "s_branch .Lend_%=\n\t" ".Lsp_%=:\n\t" \
  "v_mov_b32 v1, 0\n\t" "s_add_u32 s40, s28, 16\n\t" "s_xor_b32 s41, %[bid], 2\n\t" "s_sub_u32 s41, s41, 240\n\t" "s_cselect_b32 s29, s40, s41\n\t" "v_lshlrev_b32 v32, 4, v8\n\t" \
  "v_and_b32 v9, 15, v8\n\t" "v_mov_b32 v37, 156416\n\t" "v_mov_b32 v38, 1\n\t" "v_mov_b32 v44, -1\n\t" "v_mov_b32 v45, -1\n\t" "v_mov_b32 v46, -1\n\t" "v_mov_b32 v47, -1\n\t" "s_mov_b32 s75, 1\n\t" \
  "s_mov_b32 s39, 400000\n\t" "s_add_u32 s45, s29, 4\n\t" "s_lshr_b32 s45, s45, 3\n\t" "s_sub_u32 s45, s29, s45\n\t" "s_and_b32 s48, s29, 7\n\t" "s_mul_i32 s46, s45, 232\n\t" "s_movk_i32 s47, 231\n\t" \
  "s_mul_i32 s40, s45, 764\n\t" "s_sub_u32 s40, s40, 11172\n\t" "s_cmp_ge_u32 s29, 24\n\t" "s_cselect_b32 s46, s40, s46\n\t" "s_cselect_b32 s47, 763, s47\n\t" "v_min_u32 v5, s47, v0\n\t" \
  "v_add_u32 v5, s46, v5\n\t" "v_min_u32 v5, 6399, v5\n\t" "v_lshlrev_b32 v5, 7, v5\n\t" "s_lshr_b32 s47, s47, 6\n\t" "s_mul_i32 s40, s45, 12\n\t" "s_add_u32 s40, s40, s26\n\t" \
  "s_lshl_b32 s41, s40, 10\n\t" "s_add_u32 s41, s41, 3416064\n\t" "s_lshl_b32 s44, s45, 7\n\t" "s_add_u32 s44, s44, 4096\n\t" "v_lshl_add_u32 v4, v9, 3, s44\n\t" "s_waitcnt lgkmcnt(0)\n\t" \
  "global_load_dwordx2 v[40:41], v1, s[24:25] sc1\n\t" "s_add_u32 s42, s24, s41\n\t" "s_addc_u32 s43, s25, 0\n\t" "v_lshl_add_u32 v3, v8, 2, v37\n\t" "s_movk_i32 s44, 0xfc0\n\t" "v_mov_b32 v2, 0\n\t" \
  "v_writelane_b32 v2, s44, 8\n\t" "ds_write_b32 v3, v2\n\t" "s_mul_i32 s50, s29, 81920\n\t" "s_mul_i32 s54, s33, 20480\n\t" "s_add_u32 s50, s50, s54\n\t" "s_lshl_b32 s54, s33, 11\n\t" \
  "s_add_u32 s54, s54, 65536\n\t" "s_add_u32 s52, s20, s50\n\t" "s_addc_u32 s53, s21, 0\n\t" "v_lshrrev_b32 v20, 3, v8\n\t" "v_and_b32 v21, 6, v20\n\t" "v_xor_b32 v21, v21, v8\n\t" \
  "v_and_b32 v21, 7, v21\n\t" "v_mul_u32_u24 v20, 1280, v20\n\t" "v_lshl_add_u32 v20, v21, 4, v20\n\t" "v_add_u32 v21, 9216, v20\n\t" "s_memrealtime s[56:57]\n\t" "s_mov_b32 m0, 163072\n\t" \
  "s_cmp_eq_u32 s48, 3\n\t" "s_cbranch_scc1 .Lnf_%=\n\t" ".Lts2_%=:\n\t" "s_sleep 2\n\t" "s_memrealtime s[78:79]\n\t" "s_waitcnt lgkmcnt(0)\n\t" "s_sub_u32 s78, s78, s56\n\t" \
  "s_cmp_lt_u32 s78, 255\n\t" "s_cbranch_scc1 .Lts2_%=\n\t" ".Lpf0_%=:\n\t" "global_store_dwordx4 v32, v[44:47], s[42:43] sc1\n\t" "s_add_u32 s42, s42, 282624\n\t" "s_addc_u32 s43, s43, 0\n\t" \
  "s_add_u32 s40, s40, 276\n\t" "s_cmp_lt_u32 s40, 1380\n\t" "s_cbranch_scc1 .Lpf0_%=\n\t" ".Lpf1_%=:\n\t" "global_store_dwordx4 v32, v[44:47], s[42:43] sc1\n\t" "s_add_u32 s42, s42, 282624\n\t" \
  "s_addc_u32 s43, s43, 0\n\t" "s_add_u32 s40, s40, 276\n\t" "s_cmp_lt_u32 s40, 1903\n\t" "s_cbranch_scc1 .Lpf1_%=\n\t" "s_waitcnt vmcnt(0)\n\t" "v_add_co_u32 v16, vcc, 1, v40\n\t" \
  "s_cmp_lg_u32 s26, 0\n\t" "s_nop 0\n\t" "v_addc_co_u32 v17, vcc, 0, v41, vcc\n\t" "s_barrier\n\t" "s_cbranch_scc1 .Lnf1_%=\n\t" "global_store_dwordx2 v4, v[16:17], s[24:25] sc1\n\t" ".Lnf1_%=:\n\t" \
  "s_cmp_gt_u32 s26, s47\n\t" "s_cbranch_scc1 .Lnt_%=\n\t" "global_load_lds_dword v5, s[12:13]\n\t" ".Lnt_%=:\n\t" ".Lnf_%=:\n\t" "s_cmp_ge_u32 s29, 24\n\t" "s_cbranch_scc1 .Lend_%=\n\t" \
  "s_cmp_ge_u32 s26, 8\n\t" "s_cbranch_scc1 .Lsq_%=x\n\t" "v_lshrrev_b32 v1, 3, v8\n\t" "v_and_b32 v2, 6, v1\n\t" "v_xor_b32 v2, v2, v8\n\t" "v_and_b32 v2, 7, v2\n\t" "v_mul_u32_u24 v1, 3072, v1\n\t" \
  "v_lshl_add_u32 v1, v2, 4, v1\n\t" "v_add_u32 v17, 23552, v1\n\t" "v_add_u32 v18, 47104, v1\n\t" "v_add_u32 v19, 70656, v1\n\t" "s_cmp_ge_u32 s26, 4\n\t" \
  "s_cselect_b64 s[42:43], s[18:19], s[16:17]\n\t" "s_mul_i32 s40, s33, 491520\n\t" "s_lshl_b32 s41, s29, 7\n\t" "s_add_u32 s40, s40, s41\n\t" "s_add_u32 s42, s42, s40\n\t" \
  "s_addc_u32 s43, s43, 0\n\t" "s_lshl_b32 s44, s26, 13\n\t" "s_add_u32 m0, s44, 0\n\t" "s_nop 0\n\t" "global_load_lds_dwordx4 v1, s[42:43] offset:0 nt\n\t" \
  "global_load_lds_dwordx4 v17, s[42:43] offset:1024 nt\n\t" "global_load_lds_dwordx4 v18, s[42:43] offset:2048 nt\n\t" "global_load_lds_dwordx4 v19, s[42:43] offset:3072 nt\n\t" \
  "s_add_u32 s42, s42, 98304\n\t" "s_addc_u32 s43, s43, 0\n\t" "s_add_u32 m0, s44, 4096\n\t" "s_nop 0\n\t" "global_load_lds_dwordx4 v1, s[42:43] offset:0 nt\n\t" \
  "global_load_lds_dwordx4 v17, s[42:43] offset:1024 nt\n\t" "global_load_lds_dwordx4 v18, s[42:43] offset:2048 nt\n\t" "global_load_lds_dwordx4 v19, s[42:43] offset:3072 nt\n\t" \
  "s_add_u32 s42, s42, 98304\n\t" "s_addc_u32 s43, s43, 0\n\t" "v_lshrrev_b32 v10, 2, v8\n\t" "v_lshlrev_b32 v3, 2, v8\n\t" "v_and_b32 v4, 0x60, v3\n\t" "v_and_b32 v3, 12, v3\n\t" \
  "v_lshl_add_u32 v2, v10, 7, v4\n\t" "v_lshl_add_u32 v2, v3, 1, v2\n\t" "s_mul_i32 s40, s26, 5\n\t" "s_add_u32 s40, s40, 0\n\t" "s_and_b32 s41, s40, 3\n\t" "s_lshl_b32 s41, s41, 5\n\t" \
  "s_lshr_b32 s40, s40, 2\n\t" "s_lshl_b32 s40, s40, 13\n\t" "s_add_u32 s40, s40, 65536\n\t" "v_xor_b32 v20, s41, v2\n\t" "v_add_u32 v20, s40, v20\n\t" "s_mul_i32 s40, s26, 5\n\t" \
  "s_add_u32 s40, s40, 1\n\t" "s_and_b32 s41, s40, 3\n\t" "s_lshl_b32 s41, s41, 5\n\t" "s_lshr_b32 s40, s40, 2\n\t" "s_lshl_b32 s40, s40, 13\n\t" "s_add_u32 s40, s40, 65536\n\t" \
  "v_xor_b32 v21, s41, v2\n\t" "v_add_u32 v21, s40, v21\n\t" "s_mul_i32 s40, s26, 5\n\t" "s_add_u32 s40, s40, 2\n\t" "s_and_b32 s41, s40, 3\n\t" "s_lshl_b32 s41, s41, 5\n\t" \
  "s_lshr_b32 s40, s40, 2\n\t" "s_lshl_b32 s40, s40, 13\n\t" "s_add_u32 s40, s40, 65536\n\t" "v_xor_b32 v22, s41, v2\n\t" "v_add_u32 v22, s40, v22\n\t" "s_mul_i32 s40, s26, 5\n\t" \
  "s_add_u32 s40, s40, 3\n\t" "s_and_b32 s41, s40, 3\n\t" "s_lshl_b32 s41, s41, 5\n\t" "s_lshr_b32 s40, s40, 2\n\t" "s_lshl_b32 s40, s40, 13\n\t" "s_add_u32 s40, s40, 65536\n\t" \
  "v_xor_b32 v23, s41, v2\n\t" "v_add_u32 v23, s40, v23\n\t" "s_mul_i32 s40, s26, 5\n\t" "s_add_u32 s40, s40, 4\n\t" "s_and_b32 s41, s40, 3\n\t" "s_lshl_b32 s41, s41, 5\n\t" \
  "s_lshr_b32 s40, s40, 2\n\t" "s_lshl_b32 s40, s40, 13\n\t" "s_add_u32 s40, s40, 65536\n\t" "v_xor_b32 v24, s41, v2\n\t" "v_add_u32 v24, s40, v24\n\t" "v_and_b32 v10, 12, v10\n\t" \
  "v_xor_b32 v12, 0, v2\n\t" "v_add_u32 v12, s44, v12\n\t" "v_xor_b32 v13, 32, v2\n\t" "v_add_u32 v13, s44, v13\n\t" "v_xor_b32 v14, 64, v2\n\t" "v_add_u32 v14, s44, v14\n\t" \
  "v_xor_b32 v15, 96, v2\n\t" "v_add_u32 v15, s44, v15\n\t" "s_waitcnt vmcnt(4)\n\t" "ds_read_b64_tr_b16 v[40:41], v12 offset:0\n\t" "ds_read_b64_tr_b16 v[42:43], v12 offset:2048\n\t" \
  "ds_read_b64_tr_b16 v[44:45], v13 offset:0\n\t" "ds_read_b64_tr_b16 v[46:47], v13 offset:2048\n\t" "ds_read_b64_tr_b16 v[48:49], v14 offset:0\n\t" "ds_read_b64_tr_b16 v[50:51], v14 offset:2048\n\t" \
  "ds_read_b64_tr_b16 v[52:53], v15 offset:0\n\t" "ds_read_b64_tr_b16 v[54:55], v15 offset:2048\n\t" "s_waitcnt lgkmcnt(0)\n\t" "s_add_u32 m0, s44, 0\n\t" "s_nop 0\n\t" \
  "global_load_lds_dwordx4 v1, s[42:43] offset:0 nt\n\t" "global_load_lds_dwordx4 v17, s[42:43] offset:1024 nt\n\t" "global_load_lds_dwordx4 v18, s[42:43] offset:2048 nt\n\t" \
  "global_load_lds_dwordx4 v19, s[42:43] offset:3072 nt\n\t" "s_add_u32 s42, s42, 98304\n\t" "s_addc_u32 s43, s43, 0\n\t" "s_waitcnt vmcnt(4)\n\t" "ds_read_b64_tr_b16 v[56:57], v12 offset:4096\n\t" \
  "ds_read_b64_tr_b16 v[58:59], v12 offset:6144\n\t" "ds_read_b64_tr_b16 v[60:61], v13 offset:4096\n\t" "ds_read_b64_tr_b16 v[62:63], v13 offset:6144\n\t" \
  "ds_read_b64_tr_b16 v[64:65], v14 offset:4096\n\t" "ds_read_b64_tr_b16 v[66:67], v14 offset:6144\n\t" "ds_read_b64_tr_b16 v[68:69], v15 offset:4096\n\t" \
  "ds_read_b64_tr_b16 v[70:71], v15 offset:6144\n\t" "s_waitcnt lgkmcnt(0)\n\t" "s_add_u32 m0, s44, 4096\n\t" "s_nop 0\n\t" "global_load_lds_dwordx4 v1, s[42:43] offset:0 nt\n\t" \
  "global_load_lds_dwordx4 v17, s[42:43] offset:1024 nt\n\t" "global_load_lds_dwordx4 v18, s[42:43] offset:2048 nt\n\t" "global_load_lds_dwordx4 v19, s[42:43] offset:3072 nt\n\t" \
  "s_add_u32 s42, s42, 98304\n\t" "s_addc_u32 s43, s43, 0\n\t" "s_waitcnt vmcnt(4)\n\t" "ds_read_b64_tr_b16 v[72:73], v12 offset:0\n\t" "ds_read_b64_tr_b16 v[74:75], v12 offset:2048\n\t" \
  "ds_read_b64_tr_b16 v[76:77], v13 offset:0\n\t" "ds_read_b64_tr_b16 v[78:79], v13 offset:2048\n\t" "ds_read_b64_tr_b16 v[80:81], v14 offset:0\n\t" "ds_read_b64_tr_b16 v[82:83], v14 offset:2048\n\t" \
  "ds_read_b64_tr_b16 v[84:85], v15 offset:0\n\t" "ds_read_b64_tr_b16 v[86:87], v15 offset:2048\n\t" "s_waitcnt lgkmcnt(0)\n\t" "s_add_u32 m0, s44, 0\n\t" "s_nop 0\n\t" \
  "global_load_lds_dwordx4 v1, s[42:43] offset:0 nt\n\t" "global_load_lds_dwordx4 v17, s[42:43] offset:1024 nt\n\t" "global_load_lds_dwordx4 v18, s[42:43] offset:2048 nt\n\t" \
  "global_load_lds_dwordx4 v19, s[42:43] offset:3072 nt\n\t" "s_barrier\n\t" "ds_read_b64_tr_b16 v[120:121], v20 offset:0\n\t" "ds_read_b64_tr_b16 v[122:123], v20 offset:2048\n\t" \
  "ds_read_b64_tr_b16 v[124:125], v20 offset:4096\n\t" "ds_read_b64_tr_b16 v[126:127], v20 offset:6144\n\t" "ds_read_b64_tr_b16 v[128:129], v21 offset:0\n\t" \
  "ds_read_b64_tr_b16 v[130:131], v21 offset:2048\n\t" "ds_read_b64_tr_b16 v[132:133], v21 offset:4096\n\t" "ds_read_b64_tr_b16 v[134:135], v21 offset:6144\n\t" \
  "ds_read_b64_tr_b16 v[136:137], v22 offset:0\n\t" "ds_read_b64_tr_b16 v[138:139], v22 offset:2048\n\t" "ds_read_b64_tr_b16 v[140:141], v22 offset:4096\n\t" \
  "ds_read_b64_tr_b16 v[142:143], v22 offset:6144\n\t" "ds_read_b64_tr_b16 v[144:145], v23 offset:0\n\t" "ds_read_b64_tr_b16 v[146:147], v23 offset:2048\n\t" \
  "ds_read_b64_tr_b16 v[148:149], v23 offset:4096\n\t" "ds_read_b64_tr_b16 v[150:151], v23 offset:6144\n\t" "ds_read_b64_tr_b16 v[152:153], v24 offset:0\n\t" \
  "ds_read_b64_tr_b16 v[154:155], v24 offset:2048\n\t" "ds_read_b64_tr_b16 v[156:157], v24 offset:4096\n\t" "ds_read_b64_tr_b16 v[158:159], v24 offset:6144\n\t" "s_waitcnt vmcnt(4)\n\t" \
  "ds_read_b64_tr_b16 v[88:89], v12 offset:4096\n\t" "ds_read_b64_tr_b16 v[90:91], v12 offset:6144\n\t" "ds_read_b64_tr_b16 v[92:93], v13 offset:4096\n\t" \
  "ds_read_b64_tr_b16 v[94:95], v13 offset:6144\n\t" "ds_read_b64_tr_b16 v[96:97], v14 offset:4096\n\t" "ds_read_b64_tr_b16 v[98:99], v14 offset:6144\n\t" \
  "ds_read_b64_tr_b16 v[100:101], v15 offset:4096\n\t" "ds_read_b64_tr_b16 v[102:103], v15 offset:6144\n\t" "s_waitcnt vmcnt(0)\n\t" "ds_read_b64_tr_b16 v[104:105], v12 offset:0\n\t" \
  "ds_read_b64_tr_b16 v[106:107], v12 offset:2048\n\t" "ds_read_b64_tr_b16 v[108:109], v13 offset:0\n\t" "ds_read_b64_tr_b16 v[110:111], v13 offset:2048\n\t" \
  "ds_read_b64_tr_b16 v[112:113], v14 offset:0\n\t" "ds_read_b64_tr_b16 v[114:115], v14 offset:2048\n\t" "ds_read_b64_tr_b16 v[116:117], v15 offset:0\n\t" \
  "ds_read_b64_tr_b16 v[118:119], v15 offset:2048\n\t" "s_mul_i32 s40, s33, 320\n\t" "s_add_u32 s40, s40, 159744\n\t" "v_lshl_add_u32 v1, v10, 1, s40\n\t" "s_lshl_b32 s40, s26, 8\n\t" \
  "s_add_u32 s40, s40, 161024\n\t" "v_lshl_add_u32 v2, v10, 2, s40\n\t" "v_and_b32 v4, 12, v8\n\t" "v_lshl_add_u32 v4, v4, 2, v10\n\t" "s_mov_b32 s40, 161024\n\t" "v_lshl_add_u32 v4, v4, 2, s40\n\t" \
  "v_add_u32 v3, v3, v4\n\t" "s_mul_i32 s40, s26, 320\n\t" "v_lshl_add_u32 v4, v9, 6, s40\n\t" "v_lshl_add_u32 v4, v10, 2, v4\n\t" "s_mul_i32 s40, s29, 2560\n\t" "s_add_u32 s40, s40, 5241344\n\t" \
  "s_add_u32 s62, s24, s40\n\t" "s_addc_u32 s63, s25, 0\n\t" "s_mov_b32 s40, 0x10001\n\t" "s_mov_b32 s41, s40\n\t" "s_lshl_b64 s[42:43], s[40:41], 1\n\t" "s_lshl_b64 s[44:45], s[40:41], 2\n\t" \
  "s_lshl_b64 s[46:47], s[40:41], 3\n\t" "s_lshl_b64 s[48:49], s[40:41], 4\n\t" "s_mov_b32 s60, 0x1f001f\n\t" "s_mov_b32 s61, s60\n\t" "s_mov_b32 s76, 0xbfb8aa3b\n\t" "s_setprio 3\n\t" \
  "s_waitcnt lgkmcnt(0)\n\t" "s_barrier\n\t" "ds_read2_b64 v[28:31], v1 offset0:0 offset1:4\n\t" "ds_read2_b64 v[32:35], v1 offset0:8 offset1:12\n\t" \
  "ds_read2_b64 v[36:39], v1 offset0:16 offset1:20\n\t" "ds_read2_b64 v[160:163], v1 offset0:24 offset1:28\n\t" "ds_read2_b64 v[164:167], v1 offset0:32 offset1:36\n\t" "s_waitcnt lgkmcnt(4)\n\t" \
  "v_mfma_f32_16x16x32_bf16 v[12:15], v[40:43], v[28:31], 0\n\t" "v_mfma_f32_16x16x32_bf16 v[16:19], v[44:47], v[28:31], 0\n\t" "v_mfma_f32_16x16x32_bf16 v[20:23], v[48:51], v[28:31], 0\n\t" \
  "v_mfma_f32_16x16x32_bf16 v[24:27], v[52:55], v[28:31], 0\n\t" "s_waitcnt lgkmcnt(3)\n\t" "v_mfma_f32_16x16x32_bf16 v[12:15], v[56:59], v[32:35], v[12:15]\n\t" \
  "v_mfma_f32_16x16x32_bf16 v[16:19], v[60:63], v[32:35], v[16:19]\n\t" "v_mfma_f32_16x16x32_bf16 v[20:23], v[64:67], v[32:35], v[20:23]\n\t" \
  "v_mfma_f32_16x16x32_bf16 v[24:27], v[68:71], v[32:35], v[24:27]\n\t" "s_waitcnt lgkmcnt(2)\n\t" "v_mfma_f32_16x16x32_bf16 v[12:15], v[72:75], v[36:39], v[12:15]\n\t" \
  "v_mfma_f32_16x16x32_bf16 v[16:19], v[76:79], v[36:39], v[16:19]\n\t" "v_mfma_f32_16x16x32_bf16 v[20:23], v[80:83], v[36:39], v[20:23]\n\t" \
  "v_mfma_f32_16x16x32_bf16 v[24:27], v[84:87], v[36:39], v[24:27]\n\t" "s_waitcnt lgkmcnt(1)\n\t" "v_mfma_f32_16x16x32_bf16 v[12:15], v[88:91], v[160:163], v[12:15]\n\t" \
  "v_mfma_f32_16x16x32_bf16 v[16:19], v[92:95], v[160:163], v[16:19]\n\t" "v_mfma_f32_16x16x32_bf16 v[20:23], v[96:99], v[160:163], v[20:23]\n\t" \
  "v_mfma_f32_16x16x32_bf16 v[24:27], v[100:103], v[160:163], v[24:27]\n\t" "s_waitcnt lgkmcnt(0)\n\t" "v_mfma_f32_16x16x32_bf16 v[12:15], v[104:107], v[164:167], v[12:15]\n\t" \
  "v_mfma_f32_16x16x32_bf16 v[16:19], v[108:111], v[164:167], v[16:19]\n\t" "v_mfma_f32_16x16x32_bf16 v[20:23], v[112:115], v[164:167], v[20:23]\n\t" \
  "v_mfma_f32_16x16x32_bf16 v[24:27], v[116:119], v[164:167], v[24:27]\n\t" "s_mov_b64 exec, s[40:41]\n\t" "s_nop 3\n\t" "ds_write_b128 v2, v[12:15] offset:0\n\t" \
  "ds_write_b128 v2, v[16:19] offset:64\n\t" "ds_write_b128 v2, v[20:23] offset:128\n\t" "ds_write_b128 v2, v[24:27] offset:192\n\t" "s_mov_b64 exec, -1\n\t" "s_waitcnt lgkmcnt(0)\n\t" "s_barrier\n\t" \
  "ds_read2st64_b32 v[40:41], v3 offset0:0 offset1:4\n\t" "ds_read2st64_b32 v[42:43], v3 offset0:1 offset1:5\n\t" "ds_read2st64_b32 v[44:45], v3 offset0:2 offset1:6\n\t" \
  "ds_read2st64_b32 v[46:47], v3 offset0:3 offset1:7\n\t" "s_waitcnt lgkmcnt(0)\n\t" "v_pk_add_f32 v[40:41], v[40:41], v[42:43]\n\t" "v_pk_add_f32 v[44:45], v[44:45], v[46:47]\n\t" \
  "v_pk_add_f32 v[40:41], v[40:41], v[44:45]\n\t" "v_cvt_pk_bf16_f32 v41, v40, v41\n\t" "v_lshlrev_b32 v40, 16, v41\n\t" "v_mul_f32 v42, s76, v40\n\t" "v_exp_f32 v42, v42\n\t" \
  "v_and_b32 v41, 0xffff0000, v41\n\t" "v_add_f32 v42, 1.0, v42\n\t" "v_rcp_f32 v42, v42\n\t" "s_nop 0\n\t" "v_mul_f32 v40, v40, v42\n\t" "v_cvt_pk_bf16_f32 v40, v40, v40\n\t" \
  "v_lshlrev_b32 v40, 16, v40\n\t" "v_mul_f32 v40, v40, v41\n\t" "s_nop 1\n\t" "v_mov_b32_dpp v41, v40 quad_perm:[1,1,3,3] row_mask:0xf bank_mask:0xf\n\t" "v_cvt_pk_bf16_f32 v40, v40, v41\n\t" \
  "s_nop 1\n\t" "v_mov_b32_dpp v44, v40 row_newbcast:0 row_mask:0xf bank_mask:0xf\n\t" "v_mov_b32_dpp v45, v40 row_newbcast:2 row_mask:0xf bank_mask:0xf\n\t" \
  "v_mov_b32_dpp v46, v40 row_newbcast:4 row_mask:0xf bank_mask:0xf\n\t" "v_mov_b32_dpp v47, v40 row_newbcast:6 row_mask:0xf bank_mask:0xf\n\t" \
  "v_mov_b32_dpp v48, v40 row_newbcast:8 row_mask:0xf bank_mask:0xf\n\t" "v_mov_b32_dpp v49, v40 row_newbcast:10 row_mask:0xf bank_mask:0xf\n\t" \
  "v_mov_b32_dpp v50, v40 row_newbcast:12 row_mask:0xf bank_mask:0xf\n\t" "v_mov_b32_dpp v51, v40 row_newbcast:14 row_mask:0xf bank_mask:0xf\n\t" \
  "v_mfma_f32_16x16x32_bf16 v[56:59], v[120:123], v[44:47], 0\n\t" "s_nop 0\n\t" "v_mfma_f32_16x16x32_bf16 v[56:59], v[124:127], v[48:51], v[56:59]\n\t" \
  "v_mfma_f32_16x16x32_bf16 v[60:63], v[128:131], v[44:47], 0\n\t" "v_mfma_f32_16x16x32_bf16 v[60:63], v[132:135], v[48:51], v[60:63]\n\t" \
  "v_mfma_f32_16x16x32_bf16 v[64:67], v[136:139], v[44:47], 0\n\t" "v_mfma_f32_16x16x32_bf16 v[64:67], v[140:143], v[48:51], v[64:67]\n\t" \
  "v_mfma_f32_16x16x32_bf16 v[68:71], v[144:147], v[44:47], 0\n\t" "v_mfma_f32_16x16x32_bf16 v[68:71], v[148:151], v[48:51], v[68:71]\n\t" \
  "v_mfma_f32_16x16x32_bf16 v[72:75], v[152:155], v[44:47], 0\n\t" "v_mfma_f32_16x16x32_bf16 v[72:75], v[156:159], v[48:51], v[72:75]\n\t" "s_nop 1\n\t" "v_cndmask_b32 v56, v56, v60, s[42:43]\n\t" \
  "v_cndmask_b32 v57, v57, v61, s[42:43]\n\t" "v_cndmask_b32 v58, v58, v62, s[42:43]\n\t" "v_cndmask_b32 v59, v59, v63, s[42:43]\n\t" "v_cndmask_b32 v56, v56, v64, s[44:45]\n\t" \
  "v_cndmask_b32 v57, v57, v65, s[44:45]\n\t" "v_cndmask_b32 v58, v58, v66, s[44:45]\n\t" "v_cndmask_b32 v59, v59, v67, s[44:45]\n\t" "v_cndmask_b32 v56, v56, v68, s[46:47]\n\t" \
  "v_cndmask_b32 v57, v57, v69, s[46:47]\n\t" "v_cndmask_b32 v58, v58, v70, s[46:47]\n\t" "v_cndmask_b32 v59, v59, v71, s[46:47]\n\t" "v_cndmask_b32 v56, v56, v72, s[48:49]\n\t" \
  "v_cndmask_b32 v57, v57, v73, s[48:49]\n\t" "v_cndmask_b32 v58, v58, v74, s[48:49]\n\t" "v_cndmask_b32 v59, v59, v75, s[48:49]\n\t" "s_mov_b64 exec, s[60:61]\n\t" \
  "global_store_dwordx4 v4, v[56:59], s[62:63] sc1\n\t" "s_mov_b64 exec, -1\n\t" "s_branch .Lend_%=\n\t" ".Lsq_%=x:\n\t" "s_setprio 1\n\t" "s_movk_i32 s50, 10\n\t" ".Ld3_%=:\n\t" \
  "s_mov_b32 m0, s54\n\t" "s_add_u32 s54, s54, 8192\n\t" "global_load_lds_dwordx4 v20, s[52:53] nt\n\t" "global_load_lds_dwordx4 v21, s[52:53] offset:1024 nt\n\t" "s_add_u32 s52, s52, 128\n\t" \
  "s_addc_u32 s53, s53, 0\n\t" "s_sub_u32 s50, s50, 1\n\t" "s_cmp_lg_u32 s50, 0\n\t" "s_cbranch_scc1 .Ld3_%=\n\t" "s_movk_i32 s40, 4096\n\t" "v_min_u32 v3, 22, v8\n\t" \
  "v_lshl_add_u32 v3, v3, 7, s40\n\t" "s_cmp_lg_u32 s26, 11\n\t" "s_cbranch_scc1 .Lf11_%=\n\t" "global_load_dwordx2 v[42:43], v3, s[24:25] sc1\n\t" ".Lf11_%=:\n\t" "s_waitcnt vmcnt(0) lgkmcnt(0)\n\t" \
  "s_barrier\n\t" "v_add_co_u32 v16, vcc, 1, v40\n\t" "s_nop 1\n\t" "v_addc_co_u32 v17, vcc, 0, v41, vcc\n\t" "s_mul_i32 s40, s29, 205\n\t" "s_lshr_b32 s40, s40, 11\n\t" "s_mul_i32 s40, s40, 10\n\t" \
  "s_sub_u32 s28, s29, s40\n\t" "s_cmp_lt_u32 s26, 10\n\t" "s_cbranch_scc1 .Lcb_%=\n\t" "s_setprio 3\n\t" "s_cmp_eq_u32 s26, 10\n\t" "s_cbranch_scc1 .Lfe_%=\n\t" "s_branch .Lsf1_%=\n\t" ".Lsf_%=:\n\t" \
  "global_load_dwordx2 v[42:43], v3, s[24:25] sc1\n\t" "s_sub_u32 s39, s39, 1\n\t" "s_cmp_lt_i32 s39, 0\n\t" "s_waitcnt vmcnt(0)\n\t" "s_cbranch_scc1 .Lrt_%=\n\t" ".Lsf1_%=:\n\t" \
  "v_cmp_ne_u64 vcc, v[42:43], v[16:17]\n\t" "s_cbranch_vccnz .Lsf_%=\n\t" "s_branch .Lrt_%=\n\t" "s_xor_b32 s40, s28, 1\n\t" "s_mul_i32 s40, s40, 6144\n\t" "s_add_u32 s40, s40, 5302784\n\t" \
  "v_and_b32 v3, 7, v8\n\t" "v_mul_u32_u24 v3, 640, v3\n\t" "v_add_u32 v3, s40, v3\n\t" "s_branch .Lsk1_%=\n\t" ".Lsk_%=:\n\t" "s_sleep 6\n\t" ".Lsk1_%=:\n\t" \
  "global_load_dword v40, v3, s[24:25] sc1\n\t" "s_sub_u32 s39, s39, 1\n\t" "s_cmp_lt_i32 s39, 0\n\t" "s_waitcnt vmcnt(0)\n\t" "s_cbranch_scc1 .Lrt_%=\n\t" "v_cmp_ne_u32 vcc, -1, v40\n\t" \
  "s_cbranch_vccz .Lsk_%=\n\t" "s_branch .Lrt_%=\n\t" ".Lfe_%=:\n\t" "v_lshrrev_b32 v2, 3, v8\n\t" "v_lshl_add_u32 v2, v2, 7, v32\n\t" "v_lshlrev_b32 v4, 2, v8\n\t" "v_lshrrev_b32 v3, 5, v8\n\t" \
  "v_lshl_add_u32 v3, v3, 7, v4\n\t" "v_add_u32 v4, 160768, v4\n\t" "v_add_u32 v5, 159744, v32\n\t" "s_movk_i32 s44, 63\n\t" ".Lf_%=:\n\t" "ds_read_b64 v[40:41], v37 offset:32\n\t" \
  "s_sub_u32 s39, s39, 1\n\t" "s_cmp_lt_i32 s39, 0\n\t" "s_cselect_b32 s46, 1, 0\n\t" "s_waitcnt lgkmcnt(0)\n\t" "v_readfirstlane_b32 s45, v40\n\t" "v_readfirstlane_b32 s47, v41\n\t" \
  "s_bfe_u32 s45, s45, 0x60006\n\t" "s_or_b32 s46, s46, s47\n\t" "s_xor_b32 s47, s45, s44\n\t" "s_or_b32 s48, s47, s46\n\t" "s_cbranch_scc0 .Lf_%=\n\t" "s_cmp_eq_u32 s47, 0\n\t" \
  "s_cbranch_scc1 .Lfd_%=\n\t" "s_mul_i32 s40, s45, 2560\n\t" "s_add_u32 s40, s40, 5123584\n\t" "s_add_u32 s40, s24, s40\n\t" "s_addc_u32 s41, s25, 0\n\t" \
  "global_load_dwordx4 v[44:47], v2, s[40:41] sc1\n\t" "global_load_dword v48, v3, s[40:41] offset:2048 sc1\n\t" "s_add_u32 s49, s45, 1\n\t" "v_mov_b32 v50, s49\n\t" "v_mov_b32 v51, s46\n\t" \
  "s_cmp_lt_i32 s39, 0\n\t" "s_waitcnt vmcnt(0)\n\t" "ds_write_b128 v5, v[44:47]\n\t" "ds_write_b32 v4, v48\n\t" "s_cbranch_scc1 .Lfd_%=\n\t" "v_max3_u32 v49, v44, v45, v46\n\t" \
  "v_max3_u32 v49, v49, v47, v48\n\t" "s_movk_i32 s44, 99\n\t" "v_cmp_eq_u32 vcc, -1, v49\n\t" "s_cbranch_vccnz .Lf_%=\n\t" "s_mov_b32 s44, s45\n\t" "s_cmp_eq_u32 s46, 0\n\t" \
  "s_cbranch_scc1 .Lf_%=\n\t" ".Lfd_%=:\n\t" "s_waitcnt lgkmcnt(0)\n\t" "s_barrier\n\t" "s_barrier\n\t" "s_branch .Lend_%=\n\t" ".Lcb_%=:\n\t" "s_cmp_ge_u32 s29, 20\n\t" "s_cbranch_scc1 .Lend_%=\n\t" \
  "global_load_dwordx4 v[20:23], v32, s[14:15]\n\t" "v_lshlrev_b32 v6, 2, v8\n\t" "global_load_dword v24, v6, s[14:15] offset:1024\n\t" "v_add_u32 v5, 159744, v32\n\t" "v_add_u32 v4, 160768, v6\n\t" \
  "s_lshl1_add_u32 s40, s29, s33\n\t" "v_and_b32 v1, 3, v8\n\t" "v_lshrrev_b32 v2, 2, v8\n\t" "v_and_b32 v3, 4, v2\n\t" "v_lshrrev_b32 v7, 4, v8\n\t" "v_and_b32 v7, 2, v7\n\t" \
  "v_lshl_add_u32 v3, v1, 3, v3\n\t" "v_add_u32 v3, v3, v7\n\t" "v_mul_u32_u24 v2, 2560, v2\n\t" "v_lshl_add_u32 v2, v1, 4, v2\n\t" "s_lshl_b32 s41, s40, 5\n\t" "s_add_u32 s42, s41, 159744\n\t" \
  "v_add_u32 v1, s42, v3\n\t" "s_lshl_b32 s42, s40, 6\n\t" "s_add_u32 s42, s42, 5241344\n\t" "s_add_u32 s44, s24, s42\n\t" "s_addc_u32 s45, s25, 0\n\t" "s_add_u32 s46, s44, 40960\n\t" \
  "s_addc_u32 s47, s45, 0\n\t" "v_mov_b64 v[64:65], 0\n\t" "v_mov_b64 v[66:67], 0\n\t" "s_waitcnt vmcnt(0)\n\t" "s_barrier\n\t" "s_barrier\n\t" "s_memrealtime s[64:65]\n\t" \
  "ds_read_b32 v39, v37 offset:32\n\t" "ds_read_b128 v[44:47], v5\n\t" "ds_read_b32 v48, v4\n\t" "ds_read_u16_d16_hi v49, v1\n\t" "s_waitcnt lgkmcnt(0)\n\t" "v_readfirstlane_b32 s42, v39\n\t" \
  "v_dot2_f32_bf16 v50, v48, v24, 0\n\t" "v_dot2_f32_bf16 v50, v44, v20, v50\n\t" "v_dot2_f32_bf16 v50, v45, v21, v50\n\t" "v_dot2_f32_bf16 v50, v46, v22, v50\n\t" \
  "v_dot2_f32_bf16 v50, v47, v23, v50\n\t" "s_bfe_u32 s43, s42, 0x60006\n\t" "s_lshr_b32 s56, s43, 1\n\t" "s_mul_i32 s56, s56, 89\n\t" "s_and_b32 s57, s43, 1\n\t" "s_mul_i32 s57, s57, 48\n\t" \
  "s_add_u32 s56, s56, s57\n\t" "s_and_b32 s42, s42, 63\n\t" "s_add_u32 s56, s56, s42\n\t" "s_sub_u32 s56, s56, 6\n\t" "v_add_f32_dpp v50, v50, v50 quad_perm:[1,0,3,2] row_mask:0xf bank_mask:0xf\n\t" \
  "s_nop 1\n\t" "v_add_f32_dpp v50, v50, v50 quad_perm:[2,3,0,1] row_mask:0xf bank_mask:0xf\n\t" "s_nop 1\n\t" "v_add_f32_dpp v50, v50, v50 row_half_mirror row_mask:0xf bank_mask:0xf\n\t" \
  "s_nop 1\n\t" "v_add_f32_dpp v50, v50, v50 row_mirror row_mask:0xf bank_mask:0xf\n\t" "s_bfe_u32 s57, s56, 0x1000a\n\t" "s_lshl1_add_u32 s56, s56, s57\n\t" "s_and_b32 s56, s56, 2047\n\t" \
  "v_mov_b32 v51, v50\n\t" "s_mul_i32 s56, s56, 1280\n\t" "s_add_u32 s56, s56, s41\n\t" "v_permlane32_swap_b32 v50, v51\n\t" "v_add_f32 v50, v50, v51\n\t" "v_mov_b32 v51, v50\n\t" \
  "s_add_u32 s56, s22, s56\n\t" "s_addc_u32 s57, s23, 0\n\t" "v_permlane16_swap_b32 v50, v51\n\t" "v_add_f32 v50, v50, v51\n\t" "v_cvt_pk_bf16_f32 v50, v50, v50\n\t" "v_lshlrev_b32 v50, 16, v50\n\t" \
  "v_mul_f32 v50, 0xbfb8aa3b, v50\n\t" "v_exp_f32 v50, v50\n\t" "s_mul_i32 s43, s43, 2560\n\t" "s_lshr_b32 s42, s40, 2\n\t" "s_lshl_b32 s42, s42, 7\n\t" "s_add_u32 s43, s43, s42\n\t" \
  "s_add_u32 s43, s43, s41\n\t" "v_add_f32 v50, 1.0, v50\n\t" "v_rcp_f32 v50, v50\n\t" "s_add_u32 s43, s43, 5123712\n\t" "s_add_u32 s58, s24, s43\n\t" "s_addc_u32 s59, s25, 0\n\t" \
  "v_cvt_pk_bf16_f32 v50, v50, v50\n\t" "v_lshlrev_b32 v50, 16, v50\n\t" "v_and_b32 v49, 0xffff0000, v49\n\t" ".Ltc_%=:\n\t" "s_memrealtime s[78:79]\n\t" "s_waitcnt lgkmcnt(0)\n\t" \
  "s_sub_u32 s78, s78, s64\n\t" "s_cmp_lt_u32 s78, 60\n\t" "s_cbranch_scc1 .Ltc_%=\n\t" ".Lcp_%=:\n\t" "global_load_short_d16_hi v51, v3, s[58:59] sc1\n\t" \
  "global_load_dwordx4 v[60:63], v2, s[44:45] sc1\n\t" "s_mov_b32 exec_hi, 0\n\t" "global_load_dwordx4 v[64:67], v2, s[46:47] sc1\n\t" "s_mov_b32 exec_hi, -1\n\t" "s_sub_u32 s39, s39, 1\n\t" \
  "s_cmp_lt_i32 s39, 0\n\t" "s_waitcnt vmcnt(0)\n\t" "v_pk_add_f32 v[60:61], v[60:61], v[64:65]\n\t" "v_pk_add_f32 v[62:63], v[62:63], v[66:67]\n\t" "v_and_b32 v51, 0xffff0000, v51\n\t" \
  "v_add_f32_dpp v60, v60, v60 row_ror:4 row_mask:0xf bank_mask:0xf\n\t" "v_add_f32_dpp v61, v61, v61 row_ror:4 row_mask:0xf bank_mask:0xf\n\t" \
  "v_add_f32_dpp v62, v62, v62 row_ror:4 row_mask:0xf bank_mask:0xf\n\t" "v_add_f32_dpp v63, v63, v63 row_ror:4 row_mask:0xf bank_mask:0xf\n\t" \
  "v_add_f32_dpp v60, v60, v60 row_ror:8 row_mask:0xf bank_mask:0xf\n\t" "v_add_f32_dpp v61, v61, v61 row_ror:8 row_mask:0xf bank_mask:0xf\n\t" \
  "v_add_f32_dpp v62, v62, v62 row_ror:8 row_mask:0xf bank_mask:0xf\n\t" "v_add_f32_dpp v63, v63, v63 row_ror:8 row_mask:0xf bank_mask:0xf\n\t" "v_permlane32_swap_b32 v60, v61\n\t" "s_nop 0\n\t" \
  "v_permlane32_swap_b32 v62, v63\n\t" "v_add_f32 v60, v60, v61\n\t" "v_add_f32 v62, v62, v63\n\t" "s_nop 1\n\t" "v_permlane16_swap_b32 v60, v62\n\t" "v_add_f32 v60, v60, v62\n\t" \
  "v_cvt_pk_bf16_f32 v60, v60, v60\n\t" "v_lshlrev_b32 v60, 16, v60\n\t" "v_mul_f32 v60, v60, v50\n\t" "v_cvt_pk_bf16_f32 v60, v60, v60\n\t" "v_lshlrev_b32 v60, 16, v60\n\t" \
  "v_add_f32 v60, v60, v49\n\t" "v_cvt_pk_bf16_f32 v60, v60, v60\n\t" "v_lshlrev_b32 v60, 16, v60\n\t" "v_add_f32 v60, v60, v51\n\t" "s_cbranch_scc1 .Lcq_%=\n\t" "v_cmp_u_f32 vcc, v60, v60\n\t" \
  "s_cbranch_vccnz .Lcp_%=\n\t" ".Lcq_%=:\n\t" "v_cvt_pk_bf16_f32 v60, v60, v60\n\t" "s_mov_b32 exec_lo, 0xf000f\n\t" "s_mov_b32 exec_hi, 0xf000f\n\t" "global_store_short v3, v60, s[56:57]\n\t" \
  "s_mov_b64 exec, -1\n\t" ".Lend_%=:\n\t"

extern "C" __global__ __launch_bounds__(768, 3) void fused_block(const u16* x, const u16* wqkv, const u16* wo, const u16* w_r,
                                                                 const u16* w_gate, const u16* w_up, const u16* w_down, u16* out,
                                                                 char* ws) {
  u32 tid = threadIdx.x;
  asm volatile(BODY
    : "+{v0}"(tid)
    : [ka] "s"(__builtin_amdgcn_kernarg_segment_ptr()), [bid] "s"(blockIdx.x)
    : "memory", "scc", "vcc", "v1", "v2", "v3", "v4", "v5", "v6", "v7", "v8", "v9", CL10(1), CL10(2), CL10(3), CL10(4), CL10(5), CL10(6),
      CL10(7), CL10(8), CL10(9), CL10(10), CL10(11), CL10(12), CL10(13), CL10(14), CL10(15), CL8(16), "s8", "s9", SL10(1), SL10(2), "s30",
      "s31", "s33", "s34", "s35", "s36", "s37", "s38", "s39", SL10(4), SL10(5), SL10(6), SL10(7), SL10(8), SL10(9));
}

extern "C" void kernel_launch(void* const* d_in, const int* in_sizes, int n_in,
                              void* d_out, int out_size, void* d_ws, size_t ws_size,
                              hipStream_t stream) {
  const u16* x = (const u16*)d_in[0];
  const u16* wqkv = (const u16*)d_in[1];
  const u16* wo = (const u16*)d_in[2];
  const u16* w_r = (const u16*)d_in[4];
  const u16* w_gate = (const u16*)d_in[5];
  const u16* w_up = (const u16*)d_in[6];
  const u16* w_down = (const u16*)d_in[7];
  (void)hipFuncSetAttribute((const void*)fused_block, hipFuncAttributeMaxDynamicSharedMemorySize, LDS_TOTAL);
  fused_block<<<256, 768, LDS_TOTAL, stream>>>(x, wqkv, wo, w_r, w_gate, w_up, w_down, (u16*)d_out, (char*)d_ws);
}
```

```cpp
#include <hip/hip_runtime.h>
#include <stdint.h>


typedef unsigned short u16;
typedef unsigned int u32;
#define LDS_TOTAL 163840
#define CL8(T) "v" #T "0", "v" #T "1", "v" #T "2", "v" #T "3", "v" #T "4", "v" #T "5", "v" #T "6", "v" #T "7"
#define CL10(T) CL8(T), "v" #T "8", "v" #T "9"
#define SL10(T) "s" #T "0", "s" #T "1", "s" #T "2", "s" #T "3", "s" #T "4", "s" #T "5", "s" #T "6", "s" #T "7", "s" #T "8", "s" #T "9"

#define BODY \
  ".Lst_%=:\n\t" "s_load_dwordx16 s[8:23], %[ka], 0x0\n\t" "s_load_dwordx2 s[24:25], %[ka], 0x40\n\t" "v_readfirstlane_b32 s26, v0\n\t" "s_lshr_b32 s76, %[bid], 3\n\t" "v_and_b32 v8, 63, v0\n\t" \
  "s_mul_i32 s77, s76, 205\n\t" "s_lshr_b32 s26, s26, 6\n\t" "s_lshr_b32 s77, s77, 10\n\t" "v_lshrrev_b32 v1, 3, v8\n\t" "s_mul_i32 s27, s77, 5\n\t" "s_and_b32 s33, s26, 3\n\t" \
  "s_sub_u32 s27, s76, s27\n\t" "s_and_b32 s78, %[bid], 1\n\t" "s_bfe_u32 s79, %[bid], 0x20001\n\t" "s_xor_b32 s79, s79, 3\n\t" "s_mul_i32 s28, s78, 5\n\t" "s_mul_i32 s29, s79, 6\n\t" \
  "s_add_u32 s28, s28, s27\n\t" "s_add_u32 s29, s29, s77\n\t" "s_cmp_ge_u32 s76, 30\n\t" "s_cselect_b32 s29, 24, s29\n\t" "s_cmp_ge_u32 s29, 23\n\t" "s_cbranch_scc1 .Lsp_%=\n\t" \
  "s_lshl_b32 s77, s77, 2\n\t" "s_lshl1_add_u32 s31, s27, s78\n\t" "s_lshr_b32 s77, 0x663300, s77\n\t" "s_add_u32 s31, s31, s79\n\t" "s_and_b32 s77, s77, 15\n\t" "s_mul_i32 s30, s29, 89\n\t" \
  "s_add_u32 s31, s31, s77\n\t" "s_mul_i32 s77, s31, 205\n\t" "s_lshr_b32 s77, s77, 11\n\t" "s_mul_i32 s77, s77, 10\n\t" "s_sub_u32 s31, s31, s77\n\t" "s_cmp_ge_u32 s26, 4\n\t" \
  "s_cbranch_scc1 .Lnl_%=\n\t" "s_setprio 3\n\t" "v_lshl_or_b32 v2, s33, 4, v1\n\t" "v_and_b32 v3, 2, v1\n\t" "v_mul_u32_u24 v2, 0xf00, v2\n\t" "v_xor_b32 v3, v3, v8\n\t" "s_lshl_b32 s76, s28, 7\n\t" \
  "v_and_b32 v3, 7, v3\n\t" "s_add_u32 s76, s76, 3072\n\t" "v_lshlrev_b32 v3, 4, v3\n\t" "s_mul_i32 s86, s33, 6144\n\t" "v_add3_u32 v40, v2, v3, s76\n\t" "s_add_u32 s86, s86, 3072\n\t" \
  "v_add_u32 v41, 256, v40\n\t" "v_add_u32 v42, 512, v40\n\t" "v_xor_b32 v43, 64, v40\n\t" "s_mov_b32 m0, s86\n\t" "v_add_u32 v43, 27648, v43\n\t" "s_waitcnt lgkmcnt(0)\n\t" \
  "s_mul_i32 s76, s31, 245760\n\t" "s_lshl_b32 s77, s31, 7\n\t" "s_add_u32 s78, s10, s76\n\t" "s_addc_u32 s79, s11, 0\n\t" "s_add_u32 s80, s8, s77\n\t" "s_addc_u32 s81, s9, 0\n\t" \
  "v_add_u32 v44, 256, v43\n\t" "v_add_u32 v45, 512, v43\n\t" "global_load_lds_dwordx4 v40, s[78:79] offset:-3072\n\t" "global_load_lds_dwordx4 v41, s[78:79] offset:-2048\n\t" \
  "global_load_lds_dwordx4 v42, s[78:79] offset:-1024\n\t" "global_load_lds_dwordx4 v43, s[78:79] offset:0\n\t" "global_load_lds_dwordx4 v44, s[78:79] offset:1024\n\t" \
  "global_load_lds_dwordx4 v45, s[78:79] offset:2048\n\t" "s_mul_i32 s76, s33, 24\n\t" "s_mul_i32 s87, s33, 3072\n\t" "v_add_u32 v2, s76, v1\n\t" "s_movk_i32 s77, 2048\n\t" \
  "v_lshrrev_b32 v3, 1, v2\n\t" "v_add_u32 v2, s30, v2\n\t" "v_xor_b32 v3, v3, v8\n\t" "s_add_u32 s87, s87, 26624\n\t" "v_and_b32 v3, 7, v3\n\t" "s_mov_b32 m0, s87\n\t" \
  "v_lshl_add_u32 v3, v3, 4, s77\n\t" "s_movk_i32 s77, 1280\n\t" "v_add_u32 v4, 0, v2\n\t" "v_sub_u32 v4, v4, 6 clamp\n\t" "v_lshrrev_b32 v5, 10, v4\n\t" "v_lshl_or_b32 v4, v4, 1, v5\n\t" \
  "v_and_b32 v4, 0x7ff, v4\n\t" "v_mad_u32_u24 v46, v4, s77, v3\n\t" "global_load_lds_dwordx4 v46, s[80:81] offset:-2048\n\t" "v_xor_b32 v3, 64, v3\n\t" "v_subrev_u32 v3, 1024, v3\n\t" \
  "v_add_u32 v4, 8, v2\n\t" "v_sub_u32 v4, v4, 6 clamp\n\t" "v_lshrrev_b32 v5, 10, v4\n\t" "v_lshl_or_b32 v4, v4, 1, v5\n\t" "v_and_b32 v4, 0x7ff, v4\n\t" "v_mad_u32_u24 v47, v4, s77, v3\n\t" \
  "global_load_lds_dwordx4 v47, s[80:81] offset:-1024\n\t" "v_xor_b32 v3, 64, v3\n\t" "v_subrev_u32 v3, 1024, v3\n\t" "v_add_u32 v4, 16, v2\n\t" "v_sub_u32 v4, v4, 6 clamp\n\t" \
  "v_lshrrev_b32 v5, 10, v4\n\t" "v_lshl_or_b32 v4, v4, 1, v5\n\t" "v_and_b32 v4, 0x7ff, v4\n\t" "v_mad_u32_u24 v48, v4, s77, v3\n\t" "global_load_lds_dwordx4 v48, s[80:81] offset:0\n\t" \
  ".Lnl_%=:\n\t" "s_cmp_lg_u32 s26, 11\n\t" "s_cbranch_scc1 .Lnc_%=\n\t" "s_getpc_b64 s[76:77]\n\t" ".Lpc_%=:\n\t" "s_and_b32 s78, %[bid], -8\n\t" "s_sub_u32 s76, s76, .Lpc_%=-.Lst_%=\n\t" \
  "s_subb_u32 s77, s77, 0\n\t" "v_add_u32 v1, s78, v8\n\t" "v_min_u32 v1, ((.Lend_%=-.Lst_%=)>>7)+0, v1\n\t" "s_mov_b32 m0, 163072\n\t" "v_lshlrev_b32 v1, 7, v1\n\t" "s_mov_b64 exec, 0xff\n\t" \
  "global_load_lds_dword v1, s[76:77]\n\t" "s_mov_b64 exec, -1\n\t" ".Lnc_%=:\n\t" "s_cmp_ge_u32 s26, 6\n\t" "s_cselect_b32 s34, 1, 0\n\t" "s_cselect_b32 s76, 6, 0\n\t" "v_and_b32 v9, 15, v8\n\t" \
  "s_sub_u32 s35, s26, s76\n\t" "v_lshrrev_b32 v10, 2, v8\n\t" "s_and_b32 s36, s26, 1\n\t" "s_lshr_b32 s37, s26, 1\n\t" "s_lshl4_add_u32 s76, s35, s30\n\t" "s_sub_u32 s76, s76, 6\n\t" \
  "v_lshlrev_b32 v32, 4, v8\n\t" "v_add_u32 v2, s76, v9\n\t" "s_mov_b32 s76, 0x1fe00\n\t" "v_and_b32 v2, 0x3ff, v2\n\t" "v_lshlrev_b32 v1, v9, s76\n\t" "v_min_u32 v2, 7, v2\n\t" \
  "v_lshlrev_b32 v35, 7, v10\n\t" "v_sub_u32 v2, v9, v2\n\t" "v_and_b32 v10, 12, v10\n\t" "v_add_u32 v2, 16, v2\n\t" "s_cmp_eq_u32 s35, 0\n\t" "s_cselect_b32 s76, 0xffff0000, -1\n\t" \
  "s_cselect_b32 s81, 0, 1024\n\t" "v_lshlrev_b32 v2, v2, -1\n\t" "v_and_b32 v1, s76, v1\n\t" "s_mov_b32 s77, 0xff80ff80\n\t" "s_cmp_eq_u32 s29, 0\n\t" "s_cselect_b32 s77, 0xffc0ffc0, s77\n\t" \
  "v_and_b32 v1, v1, v2\n\t" "s_cmp_eq_u32 s37, 0\n\t" "s_cselect_b32 s72, s77, -1\n\t" "v_not_b32 v1, v1\n\t" "s_mov_b32 s73, s72\n\t" "v_lshrrev_b32 v1, v10, v1\n\t" \
  "s_lshl4_add_u32 s76, s37, s30\n\t" "s_mov_b32 s77, 0xffff\n\t" "v_bfe_i32 v20, v1, 0, 1\n\t" "v_bfe_i32 v21, v1, 1, 1\n\t" "v_bfe_i32 v22, v1, 2, 1\n\t" "v_bfe_i32 v23, v1, 3, 1\n\t" \
  "v_add_u32 v2, s76, v9\n\t" "v_bfi_b32 v28, s77, v20, v21\n\t" "v_bfi_b32 v29, s77, v22, v23\n\t" "v_bfe_i32 v24, v1, 16, 1\n\t" "v_bfe_i32 v25, v1, 17, 1\n\t" "v_bfe_i32 v26, v1, 18, 1\n\t" \
  "v_bfe_i32 v27, v1, 19, 1\n\t" "v_sub_u32 v2, v2, 6 clamp\n\t" "v_lshrrev_b32 v3, 10, v2\n\t" "v_lshl_or_b32 v2, v2, 1, v3\n\t" "v_and_b32 v2, 0x7ff, v2\n\t" "s_lshl_b32 s76, s28, 7\n\t" \
  "s_lshl_b32 s77, s36, 6\n\t" "v_and_b32 v20, 0xff800000, v20\n\t" "v_and_b32 v21, 0xff800000, v21\n\t" "v_and_b32 v22, 0xff800000, v22\n\t" "v_and_b32 v23, 0xff800000, v23\n\t" \
  "v_and_b32 v24, 0xff800000, v24\n\t" "v_and_b32 v25, 0xff800000, v25\n\t" "v_and_b32 v26, 0xff800000, v26\n\t" "v_and_b32 v27, 0xff800000, v27\n\t" "s_add_u32 s76, s76, s77\n\t" \
  "v_lshlrev_b32 v1, 1, v8\n\t" "v_lshrrev_b32 v3, 1, v8\n\t" "v_and_b32 v1, 32, v1\n\t" "v_and_b32 v3, 16, v3\n\t" "s_movk_i32 s78, 1280\n\t" "v_or3_b32 v34, v1, v3, s76\n\t" \
  "v_lshlrev_b32 v5, 2, v8\n\t" "v_mad_u32_u24 v33, v2, s78, v34\n\t" "v_and_b32 v6, 0x60, v5\n\t" "s_mul_i32 s76, s36, 40960\n\t" "v_xor_b32 v1, s77, v6\n\t" "s_add_u32 s76, s76, 73728\n\t" \
  "v_and_b32 v5, 12, v5\n\t" "v_add3_u32 v35, v35, v1, s76\n\t" "v_mov_b32 v37, 156416\n\t" "v_lshl_add_u32 v35, v5, 1, v35\n\t" "v_mov_b32 v38, 1\n\t" "v_add_u32 v36, 1413120, v32\n\t" \
  "s_mul_i32 s78, s27, 12\n\t" "s_mov_b32 s76, 0x10321320\n\t" "s_mov_b32 s77, 0xbe0f102\n\t" "s_lshr_b64 s[76:77], s[76:77], s78\n\t" "s_and_b32 s74, s76, 0xfff\n\t" "s_mov_b32 s76, 0xedfe0f41\n\t" \
  "s_mov_b32 s77, 0xfdcedcf\n\t" "s_lshr_b64 s[76:77], s[76:77], s78\n\t" "s_pack_ll_b32_b16 s74, s74, s76\n\t" "s_mul_i32 s76, s27, 0x10001\n\t" "s_sub_u32 s76, 0xe0009, s76\n\t" \
  "s_lshl4_add_u32 s74, s74, s76\n\t" "s_mul_i32 s78, s34, 3072\n\t" "s_lshr_b32 s79, s74, 16\n\t" "v_bfe_u32 v1, s74, v10, 4\n\t" "s_lshl_b32 s76, s35, 9\n\t" "v_bfe_u32 v2, s79, v10, 4\n\t" \
  "v_and_b32 v4, 12, v8\n\t" "s_sub_u32 s76, s76, s78\n\t" "v_bfe_u32 v3, s74, v4, 4\n\t" "s_add_u32 s76, s76, 3072\n\t" "v_bfe_u32 v4, s79, v4, 4\n\t" "v_lshl_add_u32 v30, v8, 3, s76\n\t" \
  "s_sub_u32 s76, 5, s27\n\t" "v_lshl_add_u32 v31, s78, 1, v30\n\t" "s_sub_u32 s77, 15, s27\n\t" "v_cmp_gt_u32 s[40:41], s76, v1\n\t" "v_cmp_gt_u32 s[42:43], s76, v2\n\t" \
  "v_cmp_le_u32 s[44:45], s77, v1\n\t" "v_cmp_le_u32 s[46:47], s77, v2\n\t" "v_cmp_gt_u32 s[50:51], s76, v3\n\t" "v_cmp_gt_u32 s[52:53], s76, v4\n\t" "s_lshl_b32 s76, s27, 2\n\t" \
  "v_cmp_gt_u32 s[48:49], s76, v10\n\t" "s_mul_i32 s76, s29, 10\n\t" "s_add_u32 s76, s76, s28\n\t" "s_mul_i32 s76, s76, 6144\n\t" "s_add_u32 s76, s76, 585728\n\t" "s_waitcnt lgkmcnt(0)\n\t" \
  "s_add_u32 s66, s24, s76\n\t" "s_addc_u32 s67, s25, 0\n\t" "s_lshl_b32 s76, s35, 10\n\t" "s_add_u32 s76, s76, 4096\n\t" "s_add_u32 s56, s66, s76\n\t" "s_addc_u32 s57, s67, 0\n\t" \
  "s_sub_u32 s58, s56, s81\n\t" "s_subb_u32 s59, s57, 0\n\t" "s_mul_i32 s76, s29, 61440\n\t" "s_add_u32 s76, s76, 2004992\n\t" "s_mul_i32 s77, s26, 5120\n\t" "s_add_u32 s77, s77, s76\n\t" \
  "s_add_u32 s62, s24, s77\n\t" "s_addc_u32 s63, s25, 0\n\t" "s_add_u32 s64, s62, 1413120\n\t" "s_addc_u32 s65, s63, 0\n\t" "s_mul_i32 s77, s35, 10\n\t" "s_add_u32 s77, s77, s28\n\t" \
  "s_lshl_b32 s77, s77, 10\n\t" "s_add_u32 s77, s77, s76\n\t" "s_add_u32 s60, s24, s77\n\t" "s_addc_u32 s61, s25, 0\n\t" "s_mov_b64 s[76:77], 0xffff\n\t" "s_not_b64 s[54:55], s[76:77]\n\t" \
  "s_cmp_lg_u32 s34, 0\n\t" "s_cselect_b64 s[54:55], s[76:77], s[54:55]\n\t" "s_mov_b32 s39, 400000\n\t" "s_mov_b32 s75, 0\n\t" "s_cmp_lt_u32 s26, 4\n\t" "s_cbranch_scc1 .Ll_%=\n\t" \
  "s_mul_i32 s77, s27, 0x1111\n\t" "s_sub_u32 s80, 0x8765, s77\n\t" "s_sub_u32 s77, 0xdcba, s77\n\t" "s_cmp_eq_u32 s33, 1\n\t" "s_cselect_b32 s80, s74, s80\n\t" "s_cmp_eq_u32 s33, 2\n\t" \
  "s_cselect_b32 s80, s79, s80\n\t" "s_cmp_eq_u32 s33, 3\n\t" "s_cselect_b32 s80, s77, s80\n\t" "v_mul_u32_u24 v1, 768, v10\n\t" "v_bfe_u32 v5, s80, v5, 4\n\t" "v_lshlrev_b32 v2, 5, v9\n\t" \
  "v_lshlrev_b32 v5, 3, v5\n\t" "v_and_b32 v2, 0x180, v2\n\t" "v_xor_b32 v5, v5, v6\n\t" "s_cmp_ge_u32 s26, 8\n\t" "v_add3_u32 v1, v1, v5, v2\n\t" "s_cbranch_scc1 .Lg_%=\n\t" "v_mov_b32 v7, 0\n\t" \
  "s_setprio 1\n\t" "global_load_dwordx2 v[40:41], v7, s[24:25] sc1\n\t" "s_cmp_lg_u32 s33, 2\n\t" "s_cbranch_scc1 .Lre0_%=\n\t" "v_lshlrev_b32 v5, 2, v8\n\t" \
  "global_load_dwordx4 v[64:67], v32, s[14:15]\n\t" "global_load_dword v68, v5, s[14:15] offset:1024\n\t" ".Lre0_%=:\n\t" "v_lshl_add_u32 v3, v8, 2, v37\n\t" "s_mov_b32 exec_hi, 0\n\t" \
  "ds_write_b32 v3, v7\n\t" "s_mov_b32 exec_hi, -1\n\t" "s_lshl2_add_u32 s76, s29, s33\n\t" "s_mul_i32 s76, s76, 2560\n\t" "s_add_u32 s76, s76, 8192\n\t" "s_add_u32 s86, s24, s76\n\t" \
  "s_addc_u32 s87, s25, 0\n\t" "s_lshl_b32 s76, s28, 8\n\t" "s_add_u32 s88, s86, s76\n\t" "s_addc_u32 s89, s87, 0\n\t" "s_lshl2_add_u32 s76, s28, s33\n\t" "s_mul_i32 s76, s76, 5120\n\t" \
  "s_add_u32 s76, s76, 361472\n\t" "s_add_u32 s90, s24, s76\n\t" "s_addc_u32 s91, s25, 0\n\t" "s_lshl_b32 s76, s31, 8\n\t" "s_add_u32 s92, s90, s76\n\t" "s_addc_u32 s93, s91, 0\n\t" \
  "v_mov_b32 v116, -1\n\t" "v_mov_b32 v117, -1\n\t" "v_mov_b32 v118, -1\n\t" "v_mov_b32 v119, -1\n\t" "s_mul_i32 s76, s33, 1536\n\t" "s_add_u32 s76, s76, 4096\n\t" "s_add_u32 s94, s66, s76\n\t" \
  "s_addc_u32 s95, s67, 0\n\t" "s_lshr_b32 s76, s33, 1\n\t" "s_add_u32 s76, s76, 2\n\t" "s_sub_u32 s76, s76, s35\n\t" "s_mul_i32 s76, s76, 10240\n\t" "s_and_b32 s77, s33, 1\n\t" \
  "s_lshl_b32 s77, s77, 9\n\t" "s_add_u32 s76, s76, s77\n\t" "s_sub_u32 s76, s76, 2048\n\t" "s_ashr_i32 s77, s76, 31\n\t" "s_add_u32 s68, s60, s76\n\t" "s_addc_u32 s69, s61, s77\n\t" \
  "s_mul_i32 s76, s33, 3072\n\t" "v_add_u32 v2, s76, v32\n\t" "s_mov_b32 s77, 0x3f803f80\n\t" "v_mov_b32 v96, s77\n\t" "v_mov_b32 v97, s77\n\t" "v_mov_b32 v98, s77\n\t" "v_mov_b32 v99, s77\n\t" \
  "v_lshrrev_b32 v12, 2, v8\n\t" "v_and_or_b32 v12, v8, 1, v12\n\t" "v_lshlrev_b32 v12, 4, v12\n\t" "s_mov_b32 s98, 0x03030303\n\t" "s_mov_b32 s99, s98\n\t" "s_mov_b32 s70, 0xaaaaaaaa\n\t" \
  "s_mov_b32 s71, s70\n\t" "v_and_b32 v13, 16, v8\n\t" "v_lshrrev_b32 v7, 2, v8\n\t" "v_and_or_b32 v13, v7, 8, v13\n\t" "v_min_u32 v13, 16, v13\n\t" "v_lshrrev_b32 v7, 1, v9\n\t" \
  "v_add_u32 v13, v13, v7\n\t" "v_and_b32 v7, 1, v8\n\t" "v_mul_u32_u24 v7, 384, v7\n\t" "v_lshl_add_u32 v13, v13, 2, v7\n\t" "s_mul_i32 s76, s33, 96\n\t" "s_add_u32 s76, s76, 155648\n\t" \
  "v_add_u32 v13, s76, v13\n\t" "v_min_u32 v4, 22, v8\n\t" "s_movk_i32 s76, 4096\n\t" "v_lshl_add_u32 v4, v4, 7, s76\n\t" "v_and_b32 v5, 16, v8\n\t" "v_lshl_add_u32 v5, v5, 1, v9\n\t" \
  "v_lshrrev_b32 v7, 1, v8\n\t" "v_and_or_b32 v5, v7, 16, v5\n\t" "s_lshl_b32 s76, s33, 8\n\t" "s_add_u32 s76, s76, 156672\n\t" "v_lshl_add_u32 v5, v5, 2, s76\n\t" "s_mov_b32 s76, 0x3acccccd\n\t" \
  "s_bitcmp0_b32 0x555541, s29\n\t" "s_waitcnt lgkmcnt(0)\n\t" "s_barrier\n\t" "ds_read_b128 v[44:47], v2 offset:24576\n\t" "ds_read_b128 v[48:51], v2 offset:25600\n\t" \
  "ds_read_b128 v[52:55], v2 offset:26624\n\t" "s_cbranch_scc1 .Lc0_%=\n\t" "ds_read_b64_tr_b16 v[72:73], v1 offset:0\n\t" "ds_read_b64_tr_b16 v[74:75], v1 offset:512\n\t" \
  "ds_read_b64_tr_b16 v[76:77], v1 offset:1024\n\t" "ds_read_b64_tr_b16 v[78:79], v1 offset:1536\n\t" "ds_read_b64_tr_b16 v[80:81], v1 offset:2048\n\t" \
  "ds_read_b64_tr_b16 v[82:83], v1 offset:2560\n\t" "ds_read_b64_tr_b16 v[84:85], v1 offset:12288\n\t" "ds_read_b64_tr_b16 v[86:87], v1 offset:12800\n\t" \
  "ds_read_b64_tr_b16 v[88:89], v1 offset:13312\n\t" "ds_read_b64_tr_b16 v[90:91], v1 offset:13824\n\t" "ds_read_b64_tr_b16 v[92:93], v1 offset:14336\n\t" \
  "ds_read_b64_tr_b16 v[94:95], v1 offset:14848\n\t" ".Lc0_%=:\n\t" "s_waitcnt lgkmcnt(0)\n\t" "s_cbranch_scc1 .Lc1_%=\n\t" "v_mfma_f32_16x16x32_bf16 v[100:103], v[96:99], v[72:75], 0\n\t" \
  "v_mfma_f32_16x16x32_bf16 v[104:107], v[96:99], v[76:79], 0\n\t" "v_mfma_f32_16x16x32_bf16 v[108:111], v[96:99], v[80:83], 0\n\t" \
  "v_mfma_f32_16x16x32_bf16 v[100:103], v[96:99], v[84:87], v[100:103]\n\t" "v_mfma_f32_16x16x32_bf16 v[104:107], v[96:99], v[88:91], v[104:107]\n\t" \
  "v_mfma_f32_16x16x32_bf16 v[108:111], v[96:99], v[92:95], v[108:111]\n\t" ".Lc1_%=:\n\t" "s_barrier\n\t" "global_store_dwordx4 v32, v[116:119], s[94:95] sc1\n\t" "s_mov_b32 exec_hi, 0\n\t" \
  "global_store_dwordx4 v32, v[116:119], s[94:95] offset:1024 sc1\n\t" "s_mov_b32 exec_hi, -1\n\t" "global_store_dwordx4 v32, v[116:119], s[60:61] offset:-2048 sc1\n\t" "s_mov_b32 exec_hi, 0\n\t" \
  "global_store_dwordx4 v32, v[116:119], s[68:69] sc1\n\t" "s_mov_b32 exec_hi, -1\n\t" "v_dot2_f32_bf16 v60, v44, s77, 0\n\t" "v_dot2_f32_bf16 v120, v44, v44, 0\n\t" \
  "v_dot2_f32_bf16 v61, v48, s77, 0\n\t" "v_dot2_f32_bf16 v121, v48, v48, 0\n\t" "v_dot2_f32_bf16 v62, v52, s77, 0\n\t" "v_dot2_f32_bf16 v122, v52, v52, 0\n\t" "v_dot2_f32_bf16 v60, v45, s77, v60\n\t" \
  "v_dot2_f32_bf16 v120, v45, v45, v120\n\t" "v_dot2_f32_bf16 v61, v49, s77, v61\n\t" "v_dot2_f32_bf16 v121, v49, v49, v121\n\t" "v_dot2_f32_bf16 v62, v53, s77, v62\n\t" \
  "v_dot2_f32_bf16 v122, v53, v53, v122\n\t" "s_barrier\n\t" "v_dot2_f32_bf16 v60, v46, s77, v60\n\t" "v_dot2_f32_bf16 v120, v46, v46, v120\n\t" "v_dot2_f32_bf16 v61, v50, s77, v61\n\t" \
  "v_dot2_f32_bf16 v121, v50, v50, v121\n\t" "v_dot2_f32_bf16 v62, v54, s77, v62\n\t" "v_dot2_f32_bf16 v122, v54, v54, v122\n\t" "v_dot2_f32_bf16 v60, v47, s77, v60\n\t" \
  "v_dot2_f32_bf16 v120, v47, v47, v120\n\t" "v_dot2_f32_bf16 v61, v51, s77, v61\n\t" "v_dot2_f32_bf16 v121, v51, v51, v121\n\t" "v_dot2_f32_bf16 v62, v55, s77, v62\n\t" \
  "v_dot2_f32_bf16 v122, v55, v55, v122\n\t" "s_barrier\n\t" "s_nop 1\n\t" "v_add_f32_dpp v60, v60, v60 quad_perm:[1,0,3,2] row_mask:0xf bank_mask:0xf\n\t" \
  "v_add_f32_dpp v120, v120, v120 quad_perm:[1,0,3,2] row_mask:0xf bank_mask:0xf\n\t" "v_add_f32_dpp v61, v61, v61 quad_perm:[1,0,3,2] row_mask:0xf bank_mask:0xf\n\t" \
  "v_add_f32_dpp v121, v121, v121 quad_perm:[1,0,3,2] row_mask:0xf bank_mask:0xf\n\t" "v_add_f32_dpp v62, v62, v62 quad_perm:[1,0,3,2] row_mask:0xf bank_mask:0xf\n\t" \
  "v_add_f32_dpp v122, v122, v122 quad_perm:[1,0,3,2] row_mask:0xf bank_mask:0xf\n\t" "v_add_f32_dpp v60, v60, v60 quad_perm:[2,3,0,1] row_mask:0xf bank_mask:0xf\n\t" \
  "v_add_f32_dpp v120, v120, v120 quad_perm:[2,3,0,1] row_mask:0xf bank_mask:0xf\n\t" "v_add_f32_dpp v61, v61, v61 quad_perm:[2,3,0,1] row_mask:0xf bank_mask:0xf\n\t" \
  "v_add_f32_dpp v121, v121, v121 quad_perm:[2,3,0,1] row_mask:0xf bank_mask:0xf\n\t" "v_add_f32_dpp v62, v62, v62 quad_perm:[2,3,0,1] row_mask:0xf bank_mask:0xf\n\t" \
  "v_add_f32_dpp v122, v122, v122 quad_perm:[2,3,0,1] row_mask:0xf bank_mask:0xf\n\t" "v_add_f32_dpp v60, v60, v60 row_half_mirror row_mask:0xf bank_mask:0xf\n\t" \
  "v_add_f32_dpp v120, v120, v120 row_half_mirror row_mask:0xf bank_mask:0xf\n\t" "v_add_f32_dpp v61, v61, v61 row_half_mirror row_mask:0xf bank_mask:0xf\n\t" \
  "v_add_f32_dpp v121, v121, v121 row_half_mirror row_mask:0xf bank_mask:0xf\n\t" "v_add_f32_dpp v62, v62, v62 row_half_mirror row_mask:0xf bank_mask:0xf\n\t" \
  "v_add_f32_dpp v122, v122, v122 row_half_mirror row_mask:0xf bank_mask:0xf\n\t" "s_barrier\n\t" "v_cndmask_b32 v60, v60, v120, s[70:71]\n\t" "v_cndmask_b32 v61, v61, v121, s[70:71]\n\t" \
  "v_cndmask_b32 v62, v62, v122, s[70:71]\n\t" "v_mov_b32 v101, v104\n\t" "v_mov_b32 v102, v108\n\t" "s_waitcnt vmcnt(0)\n\t" "v_add_co_u32 v58, vcc, 1, v40\n\t" "v_add_u32 v63, 1, v40\n\t" \
  "v_add_u32 v103, 1, v40\n\t" "v_addc_co_u32 v59, vcc, 0, v41, vcc\n\t" "s_mov_b64 exec, s[98:99]\n\t" "global_store_dwordx4 v12, v[60:63], s[88:89] sc1\n\t" "s_mov_b64 exec, -1\n\t" \
  "s_cbranch_scc1 .Lc2_%=\n\t" "s_mov_b64 exec, 0xffff\n\t" "global_store_dwordx4 v32, v[100:103], s[92:93] sc1\n\t" "s_mov_b64 exec, -1\n\t" ".Lc2_%=:\n\t" "v_mov_b64 v[84:85], 0\n\t" \
  "v_mov_b32 v86, 0\n\t" "v_mov_b32 v87, v58\n\t" "v_mov_b64 v[104:105], 0\n\t" "v_mov_b32 v106, 0\n\t" "v_mov_b32 v107, v58\n\t" "s_barrier\n\t" "s_cmp_lg_u32 s33, 1\n\t" "s_cbranch_scc1 .Ltp_%=\n\t" \
  "s_memrealtime s[96:97]\n\t" "s_mul_i32 s98, s29, 1280\n\t" "s_add_u32 s98, s98, 5368320\n\t" "v_min_u32 v4, 9, v9\n\t" "v_lshl_add_u32 v4, v4, 7, s98\n\t" "s_lshl_b32 s99, s28, 7\n\t" \
  "s_add_u32 s98, s98, s99\n\t" "v_lshl_add_u32 v7, v9, 3, s98\n\t" "v_mov_b32 v57, v58\n\t" "s_waitcnt lgkmcnt(0)\n\t" "v_mov_b32 v56, s96\n\t" "s_mov_b64 exec, 0xffff\n\t" \
  "global_store_dwordx2 v7, v[56:57], s[24:25] sc1\n\t" "s_mov_b64 exec, -1\n\t" ".Ltp_%=:\n\t" "s_barrier\n\t" "s_barrier\n\t" "global_load_dwordx4 v[72:75], v32, s[86:87] offset:0 sc1\n\t" \
  "global_load_dwordx4 v[76:79], v32, s[86:87] offset:1024 sc1\n\t" "global_load_dwordx4 v[92:95], v32, s[90:91] offset:0 sc1\n\t" "global_load_dwordx4 v[96:99], v32, s[90:91] offset:1024 sc1\n\t" \
  "s_mov_b32 exec_hi, 0\n\t" "global_load_dwordx4 v[84:87], v32, s[86:87] offset:2048 sc1\n\t" "global_load_dwordx4 v[104:107], v32, s[90:91] offset:2048 sc1\n\t" "s_mov_b32 exec_hi, -1\n\t" \
  "s_cmp_lg_u32 s33, 1\n\t" "s_cbranch_scc1 .Lfl0_%=\n\t" "global_load_dwordx2 v[112:113], v4, s[24:25] sc1\n\t" ".Lfl0_%=:\n\t" "s_barrier\n\t" "s_mov_b32 s98, 0\n\t" ".Lrg_%=:\n\t" \
  "s_waitcnt vmcnt(0)\n\t" "v_pk_add_f32 v[120:121], v[72:73], v[76:77]\n\t" "v_pk_add_f32 v[126:127], v[92:93], v[96:97]\n\t" "v_add_f32 v122, v74, v78\n\t" "v_add_f32 v128, v94, v98\n\t" \
  "v_pk_add_f32 v[120:121], v[120:121], v[84:85]\n\t" "v_pk_add_f32 v[126:127], v[126:127], v[104:105]\n\t" "v_add_f32 v123, v122, v86\n\t" "v_add_f32 v129, v128, v106\n\t" \
  "v_add_f32 v122, v122, v86\n\t" "v_add_f32 v128, v128, v106\n\t" "v_permlane32_swap_b32 v120, v121\n\t" "v_permlane32_swap_b32 v126, v127\n\t" "v_permlane32_swap_b32 v122, v123\n\t" \
  "v_permlane32_swap_b32 v128, v129\n\t" "v_add_f32 v120, v120, v121\n\t" "v_add_f32 v126, v126, v127\n\t" "v_add_f32 v122, v122, v123\n\t" "v_add_f32 v128, v128, v129\n\t" "s_cmp_lg_u32 s98, 0\n\t" \
  "s_cbranch_scc1 .Lb8_%=\n\t" "s_barrier\n\t" ".Lb8_%=:\n\t" "s_mov_b32 s98, 1\n\t" "s_cmp_lg_u32 s33, 3\n\t" "s_cbranch_scc1 .Lfl8_%=\n\t" "global_load_dwordx2 v[112:113], v4, s[24:25] sc1\n\t" \
  ".Lfl8_%=:\n\t" "v_permlane16_swap_b32 v120, v122\n\t" "v_permlane16_swap_b32 v126, v128\n\t" "v_add_f32 v120, v120, v122\n\t" "v_add_f32 v126, v126, v128\n\t" "v_mul_f32 v120, s76, v120\n\t" \
  "v_cmp_ne_u32 vcc, v75, v58\n\t" "s_nop 0\n\t" "v_mov_b32_dpp v122, v120 quad_perm:[0,0,2,2] row_mask:0xf bank_mask:0xf\n\t" "v_cmp_ne_u32 s[80:81], v79, v58\n\t" "s_or_b64 vcc, vcc, s[80:81]\n\t" \
  "v_fma_f32 v121, -v122, v122, v120\n\t" "v_cmp_ne_u32 s[78:79], v87, v58\n\t" "s_or_b64 vcc, vcc, s[78:79]\n\t" "v_max_f32 v121, 0, v121\n\t" "v_cmp_ne_u32 s[80:81], v95, v58\n\t" \
  "s_or_b64 vcc, vcc, s[80:81]\n\t" "v_cvt_pk_bf16_f32 v120, v121, v120\n\t" "v_cmp_ne_u32 s[78:79], v99, v58\n\t" "s_or_b64 vcc, vcc, s[78:79]\n\t" "v_lshlrev_b32 v121, 16, v120\n\t" \
  "v_cmp_ne_u32 s[80:81], v107, v58\n\t" "s_or_b64 vcc, vcc, s[80:81]\n\t" "v_add_f32 v121, 0x3727c5ac, v121\n\t" "v_cvt_pk_bf16_f32 v121, v121, v121\n\t" "v_lshlrev_b32 v121, 16, v121\n\t" \
  "v_sqrt_f32 v121, v121\n\t" "v_and_b32 v120, 0xffff0000, v120\n\t" "v_cvt_pk_bf16_f32 v121, v121, v121\n\t" "v_lshlrev_b32 v121, 16, v121\n\t" "s_cbranch_vccnz .Lsg_%=\n\t" ".Lrh_%=:\n\t" \
  "s_mov_b64 exec, s[70:71]\n\t" "v_rcp_f32 v120, v121\n\t" "s_mov_b64 exec, -1\n\t" "ds_write_b32 v5, v126\n\t" "ds_write_b32 v13, v120\n\t" "s_mov_b64 exec, 1\n\t" \
  "ds_add_u32 v37, v38 offset:12\n\t" "s_mov_b64 exec, -1\n\t" "s_setprio 0\n\t" "s_cmp_lg_u32 s33, 3\n\t" "s_cbranch_scc1 .Lnfl_%=\n\t" "s_waitcnt vmcnt(0)\n\t" \
  "v_cmp_ne_u64 vcc, v[112:113], v[58:59]\n\t" "s_cbranch_vccz .Lsfm_%=\n\t" ".Lsfl_%=:\n\t" "global_load_dwordx2 v[112:113], v4, s[24:25] sc1\n\t" "s_sub_u32 s39, s39, 1\n\t" \
  "s_cmp_lt_i32 s39, 0\n\t" "s_waitcnt vmcnt(0)\n\t" "s_cbranch_scc1 .Lsfm_%=\n\t" "v_cmp_ne_u64 vcc, v[112:113], v[58:59]\n\t" "s_cbranch_vccnz .Lsfl_%=\n\t" ".Lsfm_%=:\n\t" "s_mov_b64 exec, 1\n\t" \
  "ds_add_u32 v37, v38 offset:4\n\t" "s_mov_b64 exec, -1\n\t" ".Lnfl_%=:\n\t" "s_cmp_lg_u32 s33, 1\n\t" "s_cbranch_scc1 .Lnlg_%=\n\t" "v_subrev_u32 v112, s96, v112\n\t" \
  "v_cmp_eq_u32 vcc, v113, v58\n\t" "s_lshl_b32 s98, 7, s27\n\t" "s_lshr_b32 s98, s98, 1\n\t" "s_and_b32 s98, s98, 31\n\t" "v_cndmask_b32 v112, 60, v112, vcc\n\t" "s_sub_u32 s99, s28, s27\n\t" \
  "s_lshl_b32 s98, s98, s99\n\t" "s_or_b32 s98, s98, 0xffff0000\n\t" "s_mov_b32 s99, 0\n\t" "v_med3_i32 v112, v112, 0, 64\n\t" "v_cndmask_b32 v112, 0, v112, s[98:99]\n\t" "s_nop 1\n\t" \
  "v_max_u32_dpp v112, v112, v112 quad_perm:[1,0,3,2] row_mask:0xf bank_mask:0xf\n\t" "s_nop 1\n\t" "v_max_u32_dpp v112, v112, v112 quad_perm:[2,3,0,1] row_mask:0xf bank_mask:0xf\n\t" "s_nop 1\n\t" \
  "v_max_u32_dpp v112, v112, v112 row_half_mirror row_mask:0xf bank_mask:0xf\n\t" "s_nop 1\n\t" "v_max_u32_dpp v112, v112, v112 row_mirror row_mask:0xf bank_mask:0xf\n\t" "v_add_u32 v7, v10, v37\n\t" \
  "ds_write_b32 v7, v112 offset:80\n\t" ".Lnlg_%=:\n\t" "s_cmp_lg_u32 s33, 2\n\t" "s_cbranch_scc1 .Latt_%=\n\t" "v_dot2_f32_bf16 v1, v68, s77, 0\n\t" "v_dot2_f32_bf16 v1, v64, s77, v1\n\t" \
  "v_dot2_f32_bf16 v1, v65, s77, v1\n\t" "v_dot2_f32_bf16 v1, v66, s77, v1\n\t" "v_dot2_f32_bf16 v1, v67, s77, v1\n\t" "s_nop 2\n\t" \
  "v_add_f32_dpp v1, v1, v1 quad_perm:[1,0,3,2] row_mask:0xf bank_mask:0xf\n\t" "s_nop 1\n\t" "v_add_f32_dpp v1, v1, v1 quad_perm:[2,3,0,1] row_mask:0xf bank_mask:0xf\n\t" "s_nop 1\n\t" \
  "v_add_f32_dpp v1, v1, v1 row_half_mirror row_mask:0xf bank_mask:0xf\n\t" "s_nop 1\n\t" "v_add_f32_dpp v1, v1, v1 row_mirror row_mask:0xf bank_mask:0xf\n\t" "v_mov_b32 v2, v1\n\t" "s_nop 1\n\t" \
  "v_permlane32_swap_b32 v1, v2\n\t" "v_add_f32 v1, v1, v2\n\t" "v_mov_b32 v2, v1\n\t" "s_nop 1\n\t" "v_permlane16_swap_b32 v1, v2\n\t" "v_add_f32 v1, v1, v2\n\t" "ds_write_b32 v37, v1 offset:40\n\t" \
  "s_branch .Latt_%=\n\t" ".Ll_%=:\n\t" "s_movk_i32 s38, 1\n\t" "s_add_u32 s31, s31, 1\n\t" "s_cmp_eq_u32 s31, 10\n\t" "s_cselect_b32 s31, 0, s31\n\t" "s_mul_i32 s76, s31, 245760\n\t" \
  "s_lshl_b32 s77, s31, 7\n\t" "s_add_u32 s78, s10, s76\n\t" "s_addc_u32 s79, s11, 0\n\t" "s_add_u32 s80, s8, s77\n\t" "s_addc_u32 s81, s9, 0\n\t" "s_and_b32 s76, s38, 3\n\t" \
  "s_mul_i32 s76, s76, 36864\n\t" "s_add_u32 m0, s76, s87\n\t" "s_nop 0\n\t" "global_load_lds_dwordx4 v46, s[80:81] offset:-2048\n\t" "global_load_lds_dwordx4 v47, s[80:81] offset:-1024\n\t" \
  "global_load_lds_dwordx4 v48, s[80:81] offset:0\n\t" "s_add_u32 m0, s76, s86\n\t" "s_add_u32 s38, s38, 1\n\t" "global_load_lds_dwordx4 v40, s[78:79] offset:-3072\n\t" "v_lshrrev_b32 v1, 3, v8\n\t" \
  "v_lshrrev_b32 v4, 5, v8\n\t" "v_and_b32 v2, 6, v1\n\t" "v_and_b32 v1, 3, v1\n\t" "v_xor_b32 v2, v2, v8\n\t" "v_mul_u32_u24 v1, 1280, v1\n\t" "v_and_b32 v2, 7, v2\n\t" "s_lshl_b32 s92, s28, 7\n\t" \
  "v_lshl_add_u32 v1, v2, 4, v1\n\t" "s_and_b32 s93, s33, 1\n\t" "s_and_b32 s94, s33, 2\n\t" "v_add_u32 v1, s92, v1\n\t" "s_mul_i32 s92, s93, 5\n\t" "s_add_u32 s92, s92, s94\n\t" \
  "s_add_u32 s92, s92, 5\n\t" "v_add_u32 v2, s92, v4\n\t" "s_lshl2_add_u32 s92, s93, s94\n\t" "s_movk_i32 s93, 5120\n\t" "v_add_lshl_u32 v50, v4, s92, 2\n\t" "v_mad_u32_u24 v49, v2, s93, v1\n\t" \
  "s_lshl_b32 s92, s92, 9\n\t" "s_add_u32 s92, s92, 73728\n\t" "s_mov_b32 s94, 0xf41e3209\n\t" "v_bfe_u32 v2, s94, v50, 4\n\t" "v_mad_u32_u24 v3, v2, s93, v1\n\t" "s_mov_b32 s94, 0xfe0d3218\n\t" \
  "v_bfe_u32 v2, s94, v50, 4\n\t" "v_mad_u32_u24 v4, v2, s93, v1\n\t" "s_mov_b32 s94, 0xfedc2107\n\t" "v_bfe_u32 v2, s94, v50, 4\n\t" "v_mad_u32_u24 v5, v2, s93, v1\n\t" \
  "s_mov_b32 s94, 0xedcbf106\n\t" "v_bfe_u32 v2, s94, v50, 4\n\t" "v_mad_u32_u24 v6, v2, s93, v1\n\t" "s_mov_b32 s94, 0xfdcabe05\n\t" "v_bfe_u32 v2, s94, v50, 4\n\t" \
  "v_mad_u32_u24 v7, v2, s93, v1\n\t" "global_load_lds_dwordx4 v41, s[78:79] offset:-2048\n\t" "global_load_lds_dwordx4 v42, s[78:79] offset:-1024\n\t" \
  "global_load_lds_dwordx4 v43, s[78:79] offset:0\n\t" "global_load_lds_dwordx4 v44, s[78:79] offset:1024\n\t" "global_load_lds_dwordx4 v45, s[78:79] offset:2048\n\t" "s_add_u32 s31, s31, 1\n\t" \
  "s_cmp_eq_u32 s31, 10\n\t" "s_cselect_b32 s31, 0, s31\n\t" "s_mul_i32 s76, s31, 245760\n\t" "s_lshl_b32 s77, s31, 7\n\t" "s_add_u32 s78, s10, s76\n\t" "s_addc_u32 s79, s11, 0\n\t" \
  "s_add_u32 s80, s8, s77\n\t" "s_addc_u32 s81, s9, 0\n\t" "s_and_b32 s76, s38, 3\n\t" "s_mul_i32 s76, s76, 36864\n\t" "s_add_u32 m0, s76, s87\n\t" ".Ll0_%=:\n\t" "s_waitcnt vmcnt(9)\n\t" \
  "s_barrier\n\t" "global_load_lds_dwordx4 v46, s[80:81] offset:-2048\n\t" "global_load_lds_dwordx4 v47, s[80:81] offset:-1024\n\t" "global_load_lds_dwordx4 v48, s[80:81] offset:0\n\t" \
  "s_add_u32 m0, s76, s86\n\t" "s_add_u32 s38, s38, 1\n\t" "global_load_lds_dwordx4 v40, s[78:79] offset:-3072\n\t" "s_add_u32 s31, s31, 1\n\t" "s_cmp_eq_u32 s31, 10\n\t" \
  "s_cselect_b32 s31, 0, s31\n\t" "global_load_lds_dwordx4 v41, s[78:79] offset:-2048\n\t" "s_lshl_b32 s77, s31, 7\n\t" "s_add_u32 s80, s8, s77\n\t" "s_addc_u32 s81, s9, 0\n\t" \
  "global_load_lds_dwordx4 v42, s[78:79] offset:-1024\n\t" "s_mul_i32 s77, s31, 245760\n\t" "s_add_u32 s70, s10, s77\n\t" "s_addc_u32 s71, s11, 0\n\t" \
  "global_load_lds_dwordx4 v43, s[78:79] offset:0\n\t" "s_and_b32 s76, s38, 3\n\t" "s_mul_i32 s76, s76, 36864\n\t" "s_add_u32 s77, s76, s87\n\t" "global_load_lds_dwordx4 v44, s[78:79] offset:1024\n\t" \
  "s_cmp_lt_u32 s38, 9\n\t" "global_load_lds_dwordx4 v45, s[78:79] offset:2048\n\t" "s_mov_b64 s[78:79], s[70:71]\n\t" "s_mov_b32 m0, s77\n\t" "s_cbranch_scc1 .Ll0_%=\n\t" "s_waitcnt vmcnt(9)\n\t" \
  "s_barrier\n\t" "global_load_lds_dwordx4 v46, s[80:81] offset:-2048\n\t" "global_load_lds_dwordx4 v47, s[80:81] offset:-1024\n\t" "global_load_lds_dwordx4 v48, s[80:81] offset:0\n\t" \
  "s_add_u32 m0, s76, s86\n\t" "s_nop 0\n\t" "global_load_lds_dwordx4 v40, s[78:79] offset:-3072\n\t" "global_load_lds_dwordx4 v41, s[78:79] offset:-2048\n\t" \
  "global_load_lds_dwordx4 v42, s[78:79] offset:-1024\n\t" "global_load_lds_dwordx4 v43, s[78:79] offset:0\n\t" "global_load_lds_dwordx4 v44, s[78:79] offset:1024\n\t" "s_waitcnt vmcnt(8)\n\t" \
  "s_barrier\n\t" "global_load_lds_dwordx4 v45, s[78:79] offset:2048\n\t" "s_mov_b64 s[78:79], s[12:13]\n\t" "s_add_u32 m0, s92, 0\n\t" "s_nop 0\n\t" "global_load_lds_dwordx4 v49, s[78:79]\n\t" \
  "s_add_u32 s78, s78, 76800\n\t" "s_addc_u32 s79, s79, 0\n\t" "s_add_u32 m0, s92, 4096\n\t" "s_nop 0\n\t" "global_load_lds_dwordx4 v49, s[78:79]\n\t" "s_add_u32 s78, s78, 76800\n\t" \
  "s_addc_u32 s79, s79, 0\n\t" "s_add_u32 m0, s92, 8192\n\t" "s_nop 0\n\t" "global_load_lds_dwordx4 v49, s[78:79]\n\t" "s_add_u32 s78, s78, 76800\n\t" "s_addc_u32 s79, s79, 0\n\t" \
  "s_add_u32 m0, s92, 12288\n\t" "s_nop 0\n\t" "global_load_lds_dwordx4 v49, s[78:79]\n\t" "s_add_u32 s78, s78, 76800\n\t" "s_addc_u32 s79, s79, 0\n\t" "s_add_u32 m0, s92, 16384\n\t" "s_nop 0\n\t" \
  "global_load_lds_dwordx4 v49, s[78:79]\n\t" "s_add_u32 s78, s78, 102400\n\t" "s_addc_u32 s79, s79, 0\n\t" "s_add_u32 m0, s92, 40960\n\t" "s_nop 0\n\t" "global_load_lds_dwordx4 v49, s[78:79]\n\t" \
  "s_add_u32 s78, s78, 76800\n\t" "s_addc_u32 s79, s79, 0\n\t" "s_add_u32 m0, s92, 45056\n\t" "s_nop 0\n\t" "global_load_lds_dwordx4 v49, s[78:79]\n\t" "s_add_u32 s78, s78, 76800\n\t" \
  "s_addc_u32 s79, s79, 0\n\t" "s_add_u32 m0, s92, 49152\n\t" "s_nop 0\n\t" "global_load_lds_dwordx4 v49, s[78:79]\n\t" "s_add_u32 s78, s78, 76800\n\t" "s_addc_u32 s79, s79, 0\n\t" \
  "s_add_u32 m0, s92, 53248\n\t" "s_waitcnt vmcnt(8)\n\t" "s_barrier\n\t" "s_sleep 4\n\t" "global_load_lds_dwordx4 v49, s[78:79]\n\t" "s_add_u32 s78, s78, 76800\n\t" "s_addc_u32 s79, s79, 0\n\t" \
  "s_add_u32 m0, s92, 57344\n\t" "s_nop 0\n\t" "global_load_lds_dwordx4 v49, s[78:79]\n\t" "s_setprio 0\n\t" "s_waitcnt vmcnt(0)\n\t" "s_mov_b64 exec, 1\n\t" "ds_add_u32 v37, v38 offset:4\n\t" \
  "s_mov_b64 exec, -1\n\t" "s_branch .Latt_%=\n\t" ".Lsg_%=:\n\t" "s_sub_u32 s39, s39, 1\n\t" "s_cmp_lt_i32 s39, 0\n\t" "s_cbranch_scc1 .Lrh_%=\n\t" \
  "global_load_dwordx4 v[72:75], v32, s[86:87] offset:0 sc1\n\t" "global_load_dwordx4 v[76:79], v32, s[86:87] offset:1024 sc1\n\t" "global_load_dwordx4 v[92:95], v32, s[90:91] offset:0 sc1\n\t" \
  "global_load_dwordx4 v[96:99], v32, s[90:91] offset:1024 sc1\n\t" "s_mov_b32 exec_hi, 0\n\t" "global_load_dwordx4 v[84:87], v32, s[86:87] offset:2048 sc1\n\t" \
  "global_load_dwordx4 v[104:107], v32, s[90:91] offset:2048 sc1\n\t" "s_mov_b32 exec_hi, -1\n\t" "s_branch .Lrg_%=\n\t" ".Lts_%=:\n\t" "ds_read_b32 v39, v37 offset:12\n\t" \
  "ds_read_b128 v[160:163], v12 offset:1024\n\t" "ds_read_b128 v[164:167], v12 offset:1088\n\t" "ds_read2_b32 v[136:137], v4 offset0:0 offset1:96\n\t" \
  "ds_read2_b32 v[138:139], v4 offset0:16 offset1:112\n\t" "ds_read2_b32 v[140:141], v4 offset0:32 offset1:128\n\t" "ds_read2_b32 v[142:143], v4 offset0:48 offset1:144\n\t" \
  "ds_read2_b32 v[144:145], v4 offset0:64 offset1:160\n\t" "ds_read2_b32 v[146:147], v4 offset0:80 offset1:176\n\t" "ds_read_b32 v14, v13 offset:1152\n\t" "s_sub_u32 s39, s39, 1\n\t" \
  "s_cmp_lt_i32 s39, 0\n\t" "s_waitcnt lgkmcnt(0)\n\t" "v_readfirstlane_b32 s38, v39\n\t" "s_cbranch_scc1 .Ltr_%=\n\t" "s_cmp_ge_u32 s38, 4\n\t" "s_cbranch_scc0 .Lts_%=\n\t" "s_branch .Ltr_%=\n\t" \
  ".Lsq_%=:\n\t" "ds_read_b32 v39, v37 offset:0\n\t" "s_sub_u32 s39, s39, 1\n\t" "s_cmp_lt_i32 s39, 0\n\t" "s_waitcnt lgkmcnt(0)\n\t" "v_readfirstlane_b32 s38, v39\n\t" "s_cbranch_scc1 .Lrq_%=\n\t" \
  "s_cmp_ge_u32 s38, 4\n\t" "s_cbranch_scc0 .Lsq_%=\n\t" "s_branch .Lq_%=\n\t" ".Lsqm_%=:\n\t" "ds_read_b32 v39, v37 offset:8\n\t" "s_sub_u32 s39, s39, 1\n\t" "s_cmp_lt_i32 s39, 0\n\t" \
  "s_waitcnt lgkmcnt(0)\n\t" "v_readfirstlane_b32 s38, v39\n\t" "s_cbranch_scc1 .Lrqm_%=\n\t" "s_cmp_ge_u32 s38, 4\n\t" "s_cbranch_scc0 .Lsqm_%=\n\t" "s_branch .Lrqm_%=\n\t" ".Lsq0_%=:\n\t" \
  "ds_read_b32 v39, v37 offset:0\n\t" "s_sub_u32 s39, s39, 1\n\t" "s_cmp_lt_i32 s39, 0\n\t" "s_waitcnt lgkmcnt(0)\n\t" "v_readfirstlane_b32 s38, v39\n\t" "s_cbranch_scc1 .Lrq0_%=\n\t" \
  "s_cmp_ge_u32 s38, 4\n\t" "s_cbranch_scc0 .Lsq0_%=\n\t" "s_branch .Lq0_%=\n\t" ".Lsv0_%=:\n\t" "ds_read_b32 v39, v37 offset:4\n\t" "s_sub_u32 s39, s39, 1\n\t" "s_cmp_lt_i32 s39, 0\n\t" \
  "s_waitcnt lgkmcnt(0)\n\t" "v_readfirstlane_b32 s38, v39\n\t" "s_cbranch_scc1 .Lrv0_%=\n\t" "s_cmp_ge_u32 s38, 9\n\t" "s_cbranch_scc0 .Lsv0_%=\n\t" "s_branch .Lv0_%=\n\t" ".Lsr_%=:\n\t" \
  "global_load_dwordx4 v[148:151], v32, s[58:59] sc1\n\t" "global_load_dwordx4 v[144:147], v32, s[56:57] sc1\n\t" "s_sub_u32 s39, s39, 1\n\t" "s_cmp_lt_i32 s39, 0\n\t" "s_waitcnt vmcnt(0)\n\t" \
  "s_cbranch_scc1 .Lrr_%=\n\t" "v_max_u32 v124, v144, v148\n\t" "v_cmp_eq_u32 vcc, -1, v124\n\t" "s_cbranch_vccnz .Lsr_%=\n\t" "s_branch .Lrr_%=\n\t" ".Lk0_%=:\n\t" "v_xor_b32 v0, 32, v35\n\t" \
  "v_xor_b32 v1, 64, v35\n\t" "v_xor_b32 v2, 96, v35\n\t" "ds_read_b32 v39, v37 offset:8\n\t" "s_waitcnt lgkmcnt(0)\n\t" "v_readfirstlane_b32 s38, v39\n\t" "s_cmp_ge_u32 s38, 4\n\t" \
  "s_cbranch_scc0 .Lsqm_%=\n\t" ".Lrqm_%=:\n\t" "ds_read_b32 v39, v37 offset:80\n\t" "s_memrealtime s[76:77]\n\t" "s_mul_i32 s80, s35, 5\n\t" "s_add_u32 s80, s80, 24\n\t" "s_waitcnt lgkmcnt(0)\n\t" \
  "v_readfirstlane_b32 s38, v39\n\t" "s_add_u32 s80, s80, s38\n\t" ".Lqd_%=:\n\t" "s_memrealtime s[78:79]\n\t" "s_waitcnt lgkmcnt(0)\n\t" "s_sub_u32 s78, s78, s76\n\t" "s_cmp_lt_u32 s78, s80\n\t" \
  "s_cbranch_scc1 .Lqd_%=\n\t" "global_load_dwordx4 v[148:151], v32, s[58:59] sc1\n\t" "global_load_dwordx4 v[144:147], v32, s[56:57] sc1\n\t" ".Lq0_%=:\n\t" "ds_read_b32 v39, v37\n\t" \
  "ds_read_b64 v[40:41], v30 offset:0\n\t" "ds_read_b64 v[42:43], v30 offset:3072\n\t" "ds_read_b64 v[44:45], v30 offset:11776\n\t" "ds_read_b64 v[46:47], v30 offset:14848\n\t" \
  "ds_read_b64 v[48:49], v30 offset:12288\n\t" "ds_read_b64 v[50:51], v30 offset:15360\n\t" "s_waitcnt lgkmcnt(6)\n\t" "v_readfirstlane_b32 s38, v39\n\t" "s_cmp_ge_u32 s38, 4\n\t" \
  "s_cbranch_scc0 .Lsq0_%=\n\t" ".Lrq0_%=:\n\t" "s_waitcnt lgkmcnt(0)\n\t" "v_cndmask_b32 v52, 0, v40, s[44:45]\n\t" "v_cndmask_b32 v53, 0, v41, s[44:45]\n\t" "v_cndmask_b32 v54, 0, v42, s[46:47]\n\t" \
  "v_cndmask_b32 v55, 0, v43, s[46:47]\n\t" "v_cndmask_b32 v40, 0, v40, s[40:41]\n\t" "v_cndmask_b32 v41, 0, v41, s[40:41]\n\t" "v_cndmask_b32 v42, 0, v42, s[42:43]\n\t" \
  "v_cndmask_b32 v43, 0, v43, s[42:43]\n\t" "s_nop 1\n\t" "v_mfma_f32_16x16x32_bf16 v[108:111], v[44:47], v[40:43], v[20:23]\n\t" \
  "v_mfma_f32_16x16x32_bf16 v[112:115], v[48:51], v[40:43], v[24:27]\n\t" "v_mfma_f32_16x16x32_bf16 v[116:119], v[44:47], v[52:55], v[20:23]\n\t" \
  "v_mfma_f32_16x16x32_bf16 v[120:123], v[48:51], v[52:55], v[24:27]\n\t" ".Lv0_%=:\n\t" "ds_read_b32 v39, v37 offset:4\n\t" "ds_read_b64 v[128:129], v30 offset:24064\n\t" \
  "ds_read_b64 v[130:131], v30 offset:24576\n\t" "ds_read_b64 v[132:133], v30 offset:27136\n\t" "ds_read_b64 v[134:135], v30 offset:27648\n\t" "s_waitcnt lgkmcnt(4)\n\t" \
  "v_readfirstlane_b32 s38, v39\n\t" "s_cmp_ge_u32 s38, 9\n\t" "s_cbranch_scc0 .Lsv0_%=\n\t" ".Lrv0_%=:\n\t" "s_waitcnt lgkmcnt(0)\n\t" "v_cndmask_b32 v136, v128, 0, s[50:51]\n\t" \
  "v_cndmask_b32 v128, 0, v128, s[50:51]\n\t" "v_cndmask_b32 v137, v129, 0, s[50:51]\n\t" "v_cndmask_b32 v129, 0, v129, s[50:51]\n\t" "v_cndmask_b32 v138, v130, 0, s[50:51]\n\t" \
  "v_cndmask_b32 v130, 0, v130, s[50:51]\n\t" "v_cndmask_b32 v139, v131, 0, s[50:51]\n\t" "v_cndmask_b32 v131, 0, v131, s[50:51]\n\t" "v_cndmask_b32 v140, v132, 0, s[52:53]\n\t" \
  "v_cndmask_b32 v132, 0, v132, s[52:53]\n\t" "v_cndmask_b32 v141, v133, 0, s[52:53]\n\t" "v_cndmask_b32 v133, 0, v133, s[52:53]\n\t" "v_cndmask_b32 v142, v134, 0, s[52:53]\n\t" \
  "v_cndmask_b32 v134, 0, v134, s[52:53]\n\t" "v_cndmask_b32 v143, v135, 0, s[52:53]\n\t" "v_cndmask_b32 v135, 0, v135, s[52:53]\n\t" "s_cmp_ge_u32 s26, 4\n\t" "s_cbranch_scc1 .Lnw2_%=\n\t" \
  "s_add_u32 m0, s92, 20480\n\t" "s_nop 0\n\t" "global_load_lds_dwordx4 v3, s[12:13]\n\t" "s_add_u32 s12, s12, 81920\n\t" "s_addc_u32 s13, s13, 0\n\t" "s_add_u32 m0, s92, 24576\n\t" "s_nop 0\n\t" \
  "global_load_lds_dwordx4 v4, s[12:13]\n\t" "s_add_u32 s12, s12, 81920\n\t" "s_addc_u32 s13, s13, 0\n\t" "s_add_u32 m0, s92, 28672\n\t" "s_nop 0\n\t" "global_load_lds_dwordx4 v5, s[12:13]\n\t" \
  "s_add_u32 s12, s12, 81920\n\t" "s_addc_u32 s13, s13, 0\n\t" "s_add_u32 m0, s92, 32768\n\t" "s_nop 0\n\t" "global_load_lds_dwordx4 v6, s[12:13]\n\t" "s_add_u32 s12, s12, 81920\n\t" \
  "s_addc_u32 s13, s13, 0\n\t" "s_add_u32 m0, s92, 36864\n\t" "s_nop 0\n\t" "global_load_lds_dwordx4 v7, s[12:13]\n\t" "s_add_u32 s12, s12, 81920\n\t" "s_addc_u32 s13, s13, 0\n\t" \
  "s_add_u32 m0, s92, 61440\n\t" "s_nop 0\n\t" "global_load_lds_dwordx4 v3, s[12:13]\n\t" "s_add_u32 s12, s12, 81920\n\t" "s_addc_u32 s13, s13, 0\n\t" "s_add_u32 m0, s92, 65536\n\t" "s_nop 0\n\t" \
  "global_load_lds_dwordx4 v4, s[12:13]\n\t" "s_add_u32 s12, s12, 81920\n\t" "s_addc_u32 s13, s13, 0\n\t" "s_add_u32 m0, s92, 69632\n\t" "s_nop 0\n\t" "global_load_lds_dwordx4 v5, s[12:13]\n\t" \
  "s_add_u32 s12, s12, 81920\n\t" "s_addc_u32 s13, s13, 0\n\t" "s_add_u32 m0, s92, 73728\n\t" "s_nop 0\n\t" "global_load_lds_dwordx4 v6, s[12:13]\n\t" "s_add_u32 s12, s12, 81920\n\t" \
  "s_addc_u32 s13, s13, 0\n\t" "s_add_u32 m0, s92, 77824\n\t" "s_nop 0\n\t" "global_load_lds_dwordx4 v7, s[12:13]\n\t" ".Lnw2_%=:\n\t" "s_cmp_ge_u32 s26, 4\n\t" "s_waitcnt vmcnt(10)\n\t" \
  "s_cbranch_scc0 .Lnw3_%=\n\t" "s_waitcnt vmcnt(0)\n\t" ".Lnw3_%=:\n\t" "s_setprio 2\n\t" "v_max_u32 v124, v144, v148\n\t" "v_cmp_eq_u32 vcc, -1, v124\n\t" "s_cbranch_vccnz .Lsr_%=\n\t" \
  ".Lrr_%=:\n\t" "v_cndmask_b32 v124, 0, v144, s[48:49]\n\t" "v_cndmask_b32 v125, 0, v145, s[48:49]\n\t" "v_cndmask_b32 v126, v144, 0, s[48:49]\n\t" "v_cndmask_b32 v127, v145, 0, s[48:49]\n\t" \
  "v_mfma_f32_16x16x16_bf16 v[108:111], v[150:151], v[124:125], v[108:111]\n\t" "v_mfma_f32_16x16x16_bf16 v[112:115], v[146:147], v[124:125], v[112:115]\n\t" \
  "v_mfma_f32_16x16x16_bf16 v[116:119], v[150:151], v[126:127], v[116:119]\n\t" "v_mfma_f32_16x16x16_bf16 v[120:123], v[146:147], v[126:127], v[120:123]\n\t" "s_branch .Lk1_%=\n\t" ".Lsv1_%=:\n\t" \
  "ds_read_b32 v39, v37 offset:4\n\t" "s_sub_u32 s39, s39, 1\n\t" "s_cmp_lt_i32 s39, 0\n\t" "s_waitcnt lgkmcnt(0)\n\t" "v_readfirstlane_b32 s38, v39\n\t" "s_cbranch_scc1 .Lrv1_%=\n\t" \
  "s_cmp_ge_u32 s38, 9\n\t" "s_cbranch_scc0 .Lsv1_%=\n\t" "ds_read_b64 v[128:129], v30 offset:24064\n\t" "ds_read_b64 v[130:131], v30 offset:24576\n\t" "ds_read_b64 v[132:133], v30 offset:27136\n\t" \
  "ds_read_b64 v[134:135], v30 offset:27648\n\t" "ds_read_b64 v[136:137], v31 offset:24064\n\t" "ds_read_b64 v[138:139], v31 offset:24576\n\t" "ds_read_b64 v[140:141], v31 offset:27136\n\t" \
  "ds_read_b64 v[142:143], v31 offset:27648\n\t" "s_branch .Lrv1_%=\n\t" ".Lk2_%=:\n\t" "s_waitcnt lgkmcnt(8)\n\t" "v_readfirstlane_b32 s38, v39\n\t" "s_cmp_ge_u32 s38, 9\n\t" \
  "s_cbranch_scc0 .Lsv1_%=\n\t" ".Lrv1_%=:\n\t" "s_waitcnt lgkmcnt(0)\n\t" "v_mfma_f32_16x16x32_bf16 v[152:155], v[128:131], v[112:115], 0\n\t" \
  "v_mfma_f32_16x16x32_bf16 v[164:167], v[140:143], v[120:123], 0\n\t" "v_mfma_f32_16x16x32_bf16 v[156:159], v[132:135], v[112:115], 0\n\t" \
  "v_mfma_f32_16x16x32_bf16 v[160:163], v[136:139], v[120:123], 0\n\t" "v_xor_b32 v0, 32, v35\n\t" "v_xor_b32 v1, 64, v35\n\t" "v_xor_b32 v2, 96, v35\n\t" "s_nop 1\n\t" \
  "v_cvt_pk_bf16_f32 v144, v152, v153\n\t" "v_cvt_pk_bf16_f32 v145, v154, v155\n\t" "v_cvt_pk_bf16_f32 v146, v164, v165\n\t" "v_cvt_pk_bf16_f32 v147, v166, v167\n\t" \
  "global_store_dwordx4 v32, v[144:147], s[60:61] offset:-2048 sc1\n\t" "v_cvt_pk_bf16_f32 v148, v156, v157\n\t" "v_cvt_pk_bf16_f32 v149, v158, v159\n\t" "v_cvt_pk_bf16_f32 v150, v160, v161\n\t" \
  "v_cvt_pk_bf16_f32 v151, v162, v163\n\t" "s_branch .Lk3_%=\n\t" ".Lso0_%=:\n\t" "global_load_dwordx4 v[152:155], v32, s[62:63] offset:-2048 sc1\n\t" "s_sub_u32 s39, s39, 1\n\t" \
  "s_cmp_lt_i32 s39, 0\n\t" "s_waitcnt vmcnt(0)\n\t" "s_cbranch_scc1 .Lro0_%=\n\t" "v_cmp_eq_u32 vcc, -1, v152\n\t" "s_cbranch_vccnz .Lso0_%=\n\t" "s_branch .Lro0_%=\n\t" ".Lso1_%=:\n\t" \
  "global_load_dwordx4 v[156:159], v32, s[62:63] offset:-1024 sc1\n\t" "s_sub_u32 s39, s39, 1\n\t" "s_cmp_lt_i32 s39, 0\n\t" "s_waitcnt vmcnt(0)\n\t" "s_cbranch_scc1 .Lro1_%=\n\t" \
  "v_cmp_eq_u32 vcc, -1, v156\n\t" "s_cbranch_vccnz .Lso1_%=\n\t" "s_branch .Lro1_%=\n\t" ".Lso2_%=:\n\t" "global_load_dwordx4 v[160:163], v32, s[62:63] offset:0 sc1\n\t" "s_sub_u32 s39, s39, 1\n\t" \
  "s_cmp_lt_i32 s39, 0\n\t" "s_waitcnt vmcnt(0)\n\t" "s_cbranch_scc1 .Lro2_%=\n\t" "v_cmp_eq_u32 vcc, -1, v160\n\t" "s_cbranch_vccnz .Lso2_%=\n\t" "s_branch .Lro2_%=\n\t" ".Lso3_%=:\n\t" \
  "global_load_dwordx4 v[164:167], v32, s[62:63] offset:1024 sc1\n\t" "s_sub_u32 s39, s39, 1\n\t" "s_cmp_lt_i32 s39, 0\n\t" "s_waitcnt vmcnt(0)\n\t" "s_cbranch_scc1 .Lro3_%=\n\t" \
  "v_cmp_eq_u32 vcc, -1, v164\n\t" "s_cbranch_vccnz .Lso3_%=\n\t" "s_branch .Lro3_%=\n\t" ".Lso4_%=:\n\t" "global_load_dwordx4 v[152:155], v32, s[62:63] offset:2048 sc1\n\t" \
  "s_sub_u32 s39, s39, 1\n\t" "s_cmp_lt_i32 s39, 0\n\t" "s_waitcnt vmcnt(0)\n\t" "s_cbranch_scc1 .Lro4_%=\n\t" "v_cmp_eq_u32 vcc, -1, v152\n\t" "s_cbranch_vccnz .Lso4_%=\n\t" "s_branch .Lro4_%=\n\t" \
  ".Lso5_%=:\n\t" "global_load_dwordx4 v[156:159], v32, s[64:65] offset:-2048 sc1\n\t" "s_sub_u32 s39, s39, 1\n\t" "s_cmp_lt_i32 s39, 0\n\t" "s_waitcnt vmcnt(0)\n\t" "s_cbranch_scc1 .Lro5_%=\n\t" \
  "v_cmp_eq_u32 vcc, -1, v156\n\t" "s_cbranch_vccnz .Lso5_%=\n\t" "s_branch .Lro5_%=\n\t" ".Lso6_%=:\n\t" "global_load_dwordx4 v[160:163], v32, s[64:65] offset:-1024 sc1\n\t" \
  "s_sub_u32 s39, s39, 1\n\t" "s_cmp_lt_i32 s39, 0\n\t" "s_waitcnt vmcnt(0)\n\t" "s_cbranch_scc1 .Lro6_%=\n\t" "v_cmp_eq_u32 vcc, -1, v160\n\t" "s_cbranch_vccnz .Lso6_%=\n\t" "s_branch .Lro6_%=\n\t" \
  ".Lso7_%=:\n\t" "global_load_dwordx4 v[164:167], v32, s[64:65] offset:0 sc1\n\t" "s_sub_u32 s39, s39, 1\n\t" "s_cmp_lt_i32 s39, 0\n\t" "s_waitcnt vmcnt(0)\n\t" "s_cbranch_scc1 .Lro7_%=\n\t" \
  "v_cmp_eq_u32 vcc, -1, v164\n\t" "s_cbranch_vccnz .Lso7_%=\n\t" "s_branch .Lro7_%=\n\t" ".Lso8_%=:\n\t" "global_load_dwordx4 v[152:155], v32, s[64:65] offset:1024 sc1\n\t" \
  "s_sub_u32 s39, s39, 1\n\t" "s_cmp_lt_i32 s39, 0\n\t" "s_waitcnt vmcnt(0)\n\t" "s_cbranch_scc1 .Lro8_%=\n\t" "v_cmp_eq_u32 vcc, -1, v152\n\t" "s_cbranch_vccnz .Lso8_%=\n\t" "s_branch .Lro8_%=\n\t" \
  ".Lso9_%=:\n\t" "global_load_dwordx4 v[156:159], v32, s[64:65] offset:2048 sc1\n\t" "s_sub_u32 s39, s39, 1\n\t" "s_cmp_lt_i32 s39, 0\n\t" "s_waitcnt vmcnt(0)\n\t" "s_cbranch_scc1 .Lro9_%=\n\t" \
  "v_cmp_eq_u32 vcc, -1, v156\n\t" "s_cbranch_vccnz .Lso9_%=\n\t" "s_branch .Lro9_%=\n\t" ".Lg_%=:\n\t" "v_bfe_u32 v13, s80, v10, 4\n\t" "v_lshlrev_b32 v12, 1, v9\n\t" "s_movk_i32 s76, 24576\n\t" \
  "v_and_b32 v12, 28, v12\n\t" "v_lshl_add_u32 v2, v9, 7, s76\n\t" "v_xor_b32 v12, v12, v10\n\t" "s_mov_b32 s76, 155648\n\t" "v_lshl_add_u32 v2, v12, 2, v2\n\t" "v_lshl_add_u32 v4, v9, 2, s76\n\t" \
  "v_xor_b32 v3, 64, v2\n\t" "v_lshl_add_u32 v5, v10, 2, s76\n\t" "s_mul_i32 s76, s33, 3072\n\t" "v_lshl_add_u32 v6, v8, 3, s76\n\t" "s_sub_u32 s76, 5, s27\n\t" "s_cmp_eq_u32 s27, 0\n\t" \
  "s_cselect_b32 s76, 0, s76\n\t" "s_sub_u32 s77, 15, s27\n\t" "s_cmp_eq_u32 s27, 4\n\t" "s_cselect_b32 s77, 16, s77\n\t" "v_cmp_gt_u32 s[68:69], s76, v13\n\t" "v_cmp_le_u32 vcc, s77, v13\n\t" \
  "v_add3_u32 v13, v13, s27, -1\n\t" "s_or_b64 s[68:69], s[68:69], vcc\n\t" "v_and_b32 v13, 3, v13\n\t" "v_cndmask_b32 v12, 0, 50, vcc\n\t" "v_xor_b32 v13, v13, v12\n\t" "v_lshlrev_b32 v7, 4, v9\n\t" \
  "v_lshl_add_u32 v7, v13, 8, v7\n\t" "v_lshl_add_u32 v12, s33, 8, v5\n\t" "v_lshl_add_u32 v13, s33, 8, v4\n\t" "s_setprio 1\n\t" "s_movk_i32 s38, 9\n\t" "s_mov_b32 s77, 36864\n\t" \
  "v_mov_b64 v[40:41], 0\n\t" "v_mov_b64 v[42:43], 0\n\t" "v_mov_b64 v[44:45], 0\n\t" "v_mov_b64 v[46:47], 0\n\t" "v_mov_b64 v[48:49], 0\n\t" "v_mov_b64 v[50:51], 0\n\t" "v_mov_b64 v[52:53], 0\n\t" \
  "v_mov_b64 v[54:55], 0\n\t" "v_mov_b64 v[56:57], 0\n\t" "v_mov_b64 v[58:59], 0\n\t" "v_mov_b64 v[60:61], 0\n\t" "v_mov_b64 v[62:63], 0\n\t" "v_mov_b64 v[64:65], 0\n\t" "v_mov_b64 v[66:67], 0\n\t" \
  "v_mov_b64 v[68:69], 0\n\t" "v_mov_b64 v[70:71], 0\n\t" "v_mov_b64 v[72:73], 0\n\t" "v_mov_b64 v[74:75], 0\n\t" "v_mov_b64 v[76:77], 0\n\t" "v_mov_b64 v[78:79], 0\n\t" "v_mov_b64 v[80:81], 0\n\t" \
  "v_mov_b64 v[82:83], 0\n\t" "v_mov_b64 v[84:85], 0\n\t" "v_mov_b64 v[86:87], 0\n\t" "v_mov_b64 v[88:89], 0\n\t" "v_mov_b64 v[90:91], 0\n\t" "v_mov_b64 v[92:93], 0\n\t" "v_mov_b64 v[94:95], 0\n\t" \
  "v_mov_b64 v[96:97], 0\n\t" "v_mov_b64 v[98:99], 0\n\t" "v_mov_b64 v[100:101], 0\n\t" "v_mov_b64 v[102:103], 0\n\t" "v_mov_b64 v[104:105], 0\n\t" "v_mov_b64 v[106:107], 0\n\t" \
  "v_mov_b64 v[108:109], 0\n\t" "v_mov_b64 v[110:111], 0\n\t" "s_barrier\n\t" "ds_read_b64_tr_b16 v[136:137], v1 offset:0\n\t" "ds_read_b64_tr_b16 v[138:139], v1 offset:512\n\t" \
  "ds_read_b128 v[112:115], v2 offset:0\n\t" "ds_read_b64_tr_b16 v[140:141], v1 offset:1024\n\t" "ds_read_b64_tr_b16 v[142:143], v1 offset:1536\n\t" "ds_read_b64_tr_b16 v[144:145], v1 offset:2048\n\t" \
  "ds_read_b64_tr_b16 v[146:147], v1 offset:2560\n\t" "ds_read_b128 v[116:119], v2 offset:2048\n\t" "ds_read_b128 v[120:123], v2 offset:4096\n\t" "ds_read_b128 v[124:127], v2 offset:6144\n\t" \
  "ds_read_b128 v[128:131], v2 offset:8192\n\t" "ds_read_b128 v[132:135], v2 offset:10240\n\t" "s_branch .Lb_%=\n\t" ".p2align 6\n\t" ".La_%=:\n\t" "s_waitcnt lgkmcnt(9)\n\t" \
  "v_mfma_f32_16x16x32_bf16 v[40:43], v[148:151], v[112:115], v[40:43]\n\t" "v_xor_b32 v3, 64, v2\n\t" "s_waitcnt lgkmcnt(7)\n\t" \
  "v_mfma_f32_16x16x32_bf16 v[64:67], v[152:155], v[112:115], v[64:67]\n\t" "s_waitcnt lgkmcnt(5)\n\t" "v_mfma_f32_16x16x32_bf16 v[88:91], v[112:115], v[156:159], v[88:91]\n\t" \
  "s_waitcnt lgkmcnt(4)\n\t" "v_mfma_f32_16x16x32_bf16 v[44:47], v[148:151], v[116:119], v[44:47]\n\t" "v_mfma_f32_16x16x32_bf16 v[68:71], v[152:155], v[116:119], v[68:71]\n\t" \
  "v_mfma_f32_16x16x32_bf16 v[92:95], v[116:119], v[156:159], v[92:95]\n\t" "s_waitcnt lgkmcnt(0)\n\t" "s_barrier\n\t" "v_mfma_f32_16x16x32_bf16 v[48:51], v[148:151], v[120:123], v[48:51]\n\t" \
  "ds_read_b64_tr_b16 v[136:137], v1 offset:0\n\t" "ds_read_b64_tr_b16 v[138:139], v1 offset:512\n\t" "v_mfma_f32_16x16x32_bf16 v[72:75], v[152:155], v[120:123], v[72:75]\n\t" \
  "ds_read_b128 v[112:115], v2 offset:0\n\t" "ds_read_b64_tr_b16 v[140:141], v1 offset:1024\n\t" "v_mfma_f32_16x16x32_bf16 v[96:99], v[120:123], v[156:159], v[96:99]\n\t" \
  "ds_read_b64_tr_b16 v[142:143], v1 offset:1536\n\t" "ds_read_b64_tr_b16 v[144:145], v1 offset:2048\n\t" "v_mfma_f32_16x16x32_bf16 v[52:55], v[148:151], v[124:127], v[52:55]\n\t" \
  "ds_read_b64_tr_b16 v[146:147], v1 offset:2560\n\t" "ds_read_b128 v[116:119], v2 offset:2048\n\t" "v_mfma_f32_16x16x32_bf16 v[76:79], v[152:155], v[124:127], v[76:79]\n\t" \
  "ds_read_b128 v[120:123], v2 offset:4096\n\t" "v_mfma_f32_16x16x32_bf16 v[100:103], v[124:127], v[156:159], v[100:103]\n\t" "ds_read_b128 v[124:127], v2 offset:6144\n\t" \
  "v_mfma_f32_16x16x32_bf16 v[56:59], v[148:151], v[128:131], v[56:59]\n\t" "v_mfma_f32_16x16x32_bf16 v[80:83], v[152:155], v[128:131], v[80:83]\n\t" \
  "v_mfma_f32_16x16x32_bf16 v[104:107], v[128:131], v[156:159], v[104:107]\n\t" "ds_read_b128 v[128:131], v2 offset:8192\n\t" "v_mfma_f32_16x16x32_bf16 v[60:63], v[148:151], v[132:135], v[60:63]\n\t" \
  "v_mfma_f32_16x16x32_bf16 v[84:87], v[152:155], v[132:135], v[84:87]\n\t" "v_mfma_f32_16x16x32_bf16 v[108:111], v[132:135], v[156:159], v[108:111]\n\t" "ds_read_b128 v[132:135], v2 offset:10240\n\t" \
  ".Lb_%=:\n\t" "s_waitcnt lgkmcnt(9)\n\t" "v_mfma_f32_16x16x32_bf16 v[40:43], v[136:139], v[112:115], v[40:43]\n\t" "ds_read_b64_tr_b16 v[148:149], v1 offset:12288\n\t" "s_waitcnt lgkmcnt(8)\n\t" \
  "v_mfma_f32_16x16x32_bf16 v[64:67], v[140:143], v[112:115], v[64:67]\n\t" "ds_read_b64_tr_b16 v[150:151], v1 offset:12800\n\t" "s_waitcnt lgkmcnt(7)\n\t" \
  "v_mfma_f32_16x16x32_bf16 v[88:91], v[112:115], v[144:147], v[88:91]\n\t" "ds_read_b128 v[112:115], v3 offset:0\n\t" "s_waitcnt lgkmcnt(7)\n\t" \
  "v_mfma_f32_16x16x32_bf16 v[44:47], v[136:139], v[116:119], v[44:47]\n\t" "ds_read_b64_tr_b16 v[152:153], v1 offset:13312\n\t" \
  "v_mfma_f32_16x16x32_bf16 v[68:71], v[140:143], v[116:119], v[68:71]\n\t" "ds_read_b64_tr_b16 v[154:155], v1 offset:13824\n\t" \
  "v_mfma_f32_16x16x32_bf16 v[92:95], v[116:119], v[144:147], v[92:95]\n\t" "ds_read_b64_tr_b16 v[156:157], v1 offset:14336\n\t" "s_waitcnt lgkmcnt(9)\n\t" \
  "v_mfma_f32_16x16x32_bf16 v[48:51], v[136:139], v[120:123], v[48:51]\n\t" "ds_read_b64_tr_b16 v[158:159], v1 offset:14848\n\t" \
  "v_mfma_f32_16x16x32_bf16 v[72:75], v[140:143], v[120:123], v[72:75]\n\t" "ds_read_b128 v[116:119], v3 offset:2048\n\t" "v_mfma_f32_16x16x32_bf16 v[96:99], v[120:123], v[144:147], v[96:99]\n\t" \
  "ds_read_b128 v[120:123], v3 offset:4096\n\t" "s_waitcnt lgkmcnt(11)\n\t" "v_mfma_f32_16x16x32_bf16 v[52:55], v[136:139], v[124:127], v[52:55]\n\t" "s_bitcmp1_b32 0x44, s38\n\t" \
  "v_mfma_f32_16x16x32_bf16 v[76:79], v[140:143], v[124:127], v[76:79]\n\t" "s_cselect_b32 s76, -110592, s77\n\t" "v_add_u32 v1, s76, v1\n\t" \
  "v_mfma_f32_16x16x32_bf16 v[100:103], v[124:127], v[144:147], v[100:103]\n\t" "ds_read_b128 v[124:127], v3 offset:6144\n\t" "s_waitcnt lgkmcnt(11)\n\t" \
  "v_mfma_f32_16x16x32_bf16 v[56:59], v[136:139], v[128:131], v[56:59]\n\t" "v_add_u32 v2, s76, v2\n\t" "v_mfma_f32_16x16x32_bf16 v[80:83], v[140:143], v[128:131], v[80:83]\n\t" \
  "s_sub_u32 s38, s38, 1\n\t" "v_mfma_f32_16x16x32_bf16 v[104:107], v[128:131], v[144:147], v[104:107]\n\t" "ds_read_b128 v[128:131], v3 offset:8192\n\t" "s_waitcnt lgkmcnt(11)\n\t" \
  "v_mfma_f32_16x16x32_bf16 v[60:63], v[136:139], v[132:135], v[60:63]\n\t" "v_mfma_f32_16x16x32_bf16 v[84:87], v[140:143], v[132:135], v[84:87]\n\t" \
  "v_mfma_f32_16x16x32_bf16 v[108:111], v[132:135], v[144:147], v[108:111]\n\t" "ds_read_b128 v[132:135], v3 offset:10240\n\t" "s_cbranch_scc0 .La_%=\n\t" "s_waitcnt lgkmcnt(9)\n\t" \
  "v_mfma_f32_16x16x32_bf16 v[40:43], v[148:151], v[112:115], v[40:43]\n\t" "s_waitcnt lgkmcnt(7)\n\t" "v_mfma_f32_16x16x32_bf16 v[64:67], v[152:155], v[112:115], v[64:67]\n\t" \
  "s_waitcnt lgkmcnt(4)\n\t" "v_mfma_f32_16x16x32_bf16 v[44:47], v[148:151], v[116:119], v[44:47]\n\t" "v_mfma_f32_16x16x32_bf16 v[68:71], v[152:155], v[116:119], v[68:71]\n\t" \
  "s_waitcnt lgkmcnt(3)\n\t" "v_mfma_f32_16x16x32_bf16 v[48:51], v[148:151], v[120:123], v[48:51]\n\t" "ds_read_b32 v39, v37 offset:12\n\t" "ds_read_b128 v[160:163], v12 offset:1024\n\t" \
  "v_mfma_f32_16x16x32_bf16 v[72:75], v[152:155], v[120:123], v[72:75]\n\t" "ds_read_b128 v[164:167], v12 offset:1088\n\t" "ds_read2_b32 v[136:137], v4 offset0:0 offset1:96\n\t" \
  "s_waitcnt lgkmcnt(6)\n\t" "v_mfma_f32_16x16x32_bf16 v[52:55], v[148:151], v[124:127], v[52:55]\n\t" "ds_read2_b32 v[138:139], v4 offset0:16 offset1:112\n\t" \
  "ds_read2_b32 v[140:141], v4 offset0:32 offset1:128\n\t" "v_mfma_f32_16x16x32_bf16 v[76:79], v[152:155], v[124:127], v[76:79]\n\t" "ds_read2_b32 v[142:143], v4 offset0:48 offset1:144\n\t" \
  "ds_read2_b32 v[144:145], v4 offset0:64 offset1:160\n\t" "s_waitcnt lgkmcnt(9)\n\t" "v_mfma_f32_16x16x32_bf16 v[56:59], v[148:151], v[128:131], v[56:59]\n\t" \
  "ds_read2_b32 v[146:147], v4 offset0:80 offset1:176\n\t" "ds_read_b32 v14, v13 offset:1152\n\t" "v_mfma_f32_16x16x32_bf16 v[80:83], v[152:155], v[128:131], v[80:83]\n\t" "s_waitcnt lgkmcnt(10)\n\t" \
  "v_mfma_f32_16x16x32_bf16 v[60:63], v[148:151], v[132:135], v[60:63]\n\t" "s_waitcnt lgkmcnt(0)\n\t" "v_readfirstlane_b32 s38, v39\n\t" "s_cmp_ge_u32 s38, 4\n\t" "s_cbranch_scc0 .Lts_%=\n\t" \
  ".Ltr_%=:\n\t" "v_fma_f32 v40, -v160, v136, v40\n\t" "v_fma_f32 v41, -v161, v136, v41\n\t" "v_mfma_f32_16x16x32_bf16 v[84:87], v[152:155], v[132:135], v[84:87]\n\t" \
  "v_fma_f32 v42, -v162, v136, v42\n\t" "v_fma_f32 v43, -v163, v136, v43\n\t" "v_mfma_f32_16x16x32_bf16 v[88:91], v[112:115], v[156:159], v[88:91]\n\t" "v_fma_f32 v64, -v164, v136, v64\n\t" \
  "v_fma_f32 v65, -v165, v136, v65\n\t" "v_mfma_f32_16x16x32_bf16 v[92:95], v[116:119], v[156:159], v[92:95]\n\t" "v_fma_f32 v66, -v166, v136, v66\n\t" "v_fma_f32 v67, -v167, v136, v67\n\t" \
  "v_mfma_f32_16x16x32_bf16 v[96:99], v[120:123], v[156:159], v[96:99]\n\t" "v_mul_f32 v40, v40, v137\n\t" "v_mul_f32 v41, v41, v137\n\t" \
  "v_mfma_f32_16x16x32_bf16 v[100:103], v[124:127], v[156:159], v[100:103]\n\t" "v_mul_f32 v42, v42, v137\n\t" "v_mul_f32 v43, v43, v137\n\t" \
  "v_mfma_f32_16x16x32_bf16 v[104:107], v[128:131], v[156:159], v[104:107]\n\t" "v_mul_f32 v64, v64, v137\n\t" "v_mul_f32 v65, v65, v137\n\t" \
  "v_mfma_f32_16x16x32_bf16 v[108:111], v[132:135], v[156:159], v[108:111]\n\t" "v_mul_f32 v66, v66, v137\n\t" "v_mul_f32 v67, v67, v137\n\t" "ds_read_b128 v[112:115], v5 offset:0\n\t" \
  "ds_read_b128 v[116:119], v5 offset:384\n\t" "ds_read_b128 v[120:123], v5 offset:64\n\t" "ds_read_b128 v[124:127], v5 offset:448\n\t" "ds_read_b128 v[128:131], v5 offset:128\n\t" \
  "ds_read_b128 v[132:135], v5 offset:512\n\t" "v_cvt_pk_bf16_f32 v40, v40, v41\n\t" "v_cvt_pk_bf16_f32 v41, v42, v43\n\t" "v_cvt_pk_bf16_f32 v42, v64, v65\n\t" "v_cvt_pk_bf16_f32 v43, v66, v67\n\t" \
  "v_fma_f32 v44, -v160, v138, v44\n\t" "v_fma_f32 v45, -v161, v138, v45\n\t" "v_fma_f32 v46, -v162, v138, v46\n\t" "v_fma_f32 v47, -v163, v138, v47\n\t" "s_mov_b64 exec, s[68:69]\n\t" \
  "global_store_dwordx4 v7, v[40:43], s[66:67] offset:-2048 sc1\n\t" "s_mov_b64 exec, -1\n\t" "ds_write2st64_b64 v6, v[40:41], v[42:43] offset0:0 offset1:24\n\t" "s_mov_b64 exec, 1\n\t" \
  "ds_add_u32 v37, v38 offset:8\n\t" "s_mov_b64 exec, -1\n\t" "v_pk_fma_f32 v[68:69], v[164:165], v[138:139], v[68:69] op_sel_hi:[1,0,1] neg_lo:[1,0,0] neg_hi:[1,0,0]\n\t" \
  "v_pk_fma_f32 v[70:71], v[166:167], v[138:139], v[70:71] op_sel_hi:[1,0,1] neg_lo:[1,0,0] neg_hi:[1,0,0]\n\t" "v_pk_mul_f32 v[44:45], v[44:45], v[138:139] op_sel:[0,1] op_sel_hi:[1,1]\n\t" \
  "v_pk_mul_f32 v[46:47], v[46:47], v[138:139] op_sel:[0,1] op_sel_hi:[1,1]\n\t" "v_pk_mul_f32 v[68:69], v[68:69], v[138:139] op_sel:[0,1] op_sel_hi:[1,1]\n\t" \
  "v_pk_mul_f32 v[70:71], v[70:71], v[138:139] op_sel:[0,1] op_sel_hi:[1,1]\n\t" "v_cvt_pk_bf16_f32 v44, v44, v45\n\t" "v_cvt_pk_bf16_f32 v45, v46, v47\n\t" "v_cvt_pk_bf16_f32 v46, v68, v69\n\t" \
  "v_cvt_pk_bf16_f32 v47, v70, v71\n\t" "s_mov_b64 exec, s[68:69]\n\t" "global_store_dwordx4 v7, v[44:47], s[66:67] offset:-1024 sc1\n\t" "s_mov_b64 exec, -1\n\t" \
  "ds_write2st64_b64 v6, v[44:45], v[46:47] offset0:1 offset1:25\n\t" "ds_read_b128 v[64:67], v5 offset:192\n\t" "ds_read_b128 v[68:71], v5 offset:576\n\t" \
  "v_pk_fma_f32 v[48:49], v[160:161], v[140:141], v[48:49] op_sel_hi:[1,0,1] neg_lo:[1,0,0] neg_hi:[1,0,0]\n\t" \
  "v_pk_fma_f32 v[50:51], v[162:163], v[140:141], v[50:51] op_sel_hi:[1,0,1] neg_lo:[1,0,0] neg_hi:[1,0,0]\n\t" \
  "v_pk_fma_f32 v[72:73], v[164:165], v[140:141], v[72:73] op_sel_hi:[1,0,1] neg_lo:[1,0,0] neg_hi:[1,0,0]\n\t" \
  "v_pk_fma_f32 v[74:75], v[166:167], v[140:141], v[74:75] op_sel_hi:[1,0,1] neg_lo:[1,0,0] neg_hi:[1,0,0]\n\t" "v_pk_mul_f32 v[48:49], v[48:49], v[140:141] op_sel:[0,1] op_sel_hi:[1,1]\n\t" \
  "v_pk_mul_f32 v[50:51], v[50:51], v[140:141] op_sel:[0,1] op_sel_hi:[1,1]\n\t" "v_pk_mul_f32 v[72:73], v[72:73], v[140:141] op_sel:[0,1] op_sel_hi:[1,1]\n\t" \
  "v_pk_mul_f32 v[74:75], v[74:75], v[140:141] op_sel:[0,1] op_sel_hi:[1,1]\n\t" "v_cvt_pk_bf16_f32 v48, v48, v49\n\t" "v_cvt_pk_bf16_f32 v49, v50, v51\n\t" "v_cvt_pk_bf16_f32 v50, v72, v73\n\t" \
  "v_cvt_pk_bf16_f32 v51, v74, v75\n\t" "s_mov_b64 exec, s[68:69]\n\t" "global_store_dwordx4 v7, v[48:51], s[66:67] offset:0 sc1\n\t" "s_mov_b64 exec, -1\n\t" \
  "ds_write2st64_b64 v6, v[48:49], v[50:51] offset0:2 offset1:26\n\t" "v_pk_fma_f32 v[52:53], v[160:161], v[142:143], v[52:53] op_sel_hi:[1,0,1] neg_lo:[1,0,0] neg_hi:[1,0,0]\n\t" \
  "v_pk_fma_f32 v[54:55], v[162:163], v[142:143], v[54:55] op_sel_hi:[1,0,1] neg_lo:[1,0,0] neg_hi:[1,0,0]\n\t" \
  "v_pk_fma_f32 v[76:77], v[164:165], v[142:143], v[76:77] op_sel_hi:[1,0,1] neg_lo:[1,0,0] neg_hi:[1,0,0]\n\t" \
  "v_pk_fma_f32 v[78:79], v[166:167], v[142:143], v[78:79] op_sel_hi:[1,0,1] neg_lo:[1,0,0] neg_hi:[1,0,0]\n\t" "v_pk_mul_f32 v[52:53], v[52:53], v[142:143] op_sel:[0,1] op_sel_hi:[1,1]\n\t" \
  "v_pk_mul_f32 v[54:55], v[54:55], v[142:143] op_sel:[0,1] op_sel_hi:[1,1]\n\t" "v_pk_mul_f32 v[76:77], v[76:77], v[142:143] op_sel:[0,1] op_sel_hi:[1,1]\n\t" \
  "v_pk_mul_f32 v[78:79], v[78:79], v[142:143] op_sel:[0,1] op_sel_hi:[1,1]\n\t" "v_cvt_pk_bf16_f32 v52, v52, v53\n\t" "v_cvt_pk_bf16_f32 v53, v54, v55\n\t" "v_cvt_pk_bf16_f32 v54, v76, v77\n\t" \
  "v_cvt_pk_bf16_f32 v55, v78, v79\n\t" "s_mov_b64 exec, s[68:69]\n\t" "global_store_dwordx4 v7, v[52:55], s[66:67] offset:1024 sc1\n\t" "s_mov_b64 exec, -1\n\t" \
  "ds_write2st64_b64 v6, v[52:53], v[54:55] offset0:3 offset1:27\n\t" "ds_read_b128 v[72:75], v5 offset:256\n\t" "ds_read_b128 v[76:79], v5 offset:640\n\t" \
  "v_pk_fma_f32 v[56:57], v[160:161], v[144:145], v[56:57] op_sel_hi:[1,0,1] neg_lo:[1,0,0] neg_hi:[1,0,0]\n\t" \
  "v_pk_fma_f32 v[58:59], v[162:163], v[144:145], v[58:59] op_sel_hi:[1,0,1] neg_lo:[1,0,0] neg_hi:[1,0,0]\n\t" \
  "v_pk_fma_f32 v[80:81], v[164:165], v[144:145], v[80:81] op_sel_hi:[1,0,1] neg_lo:[1,0,0] neg_hi:[1,0,0]\n\t" \
  "v_pk_fma_f32 v[82:83], v[166:167], v[144:145], v[82:83] op_sel_hi:[1,0,1] neg_lo:[1,0,0] neg_hi:[1,0,0]\n\t" "v_pk_mul_f32 v[56:57], v[56:57], v[144:145] op_sel:[0,1] op_sel_hi:[1,1]\n\t" \
  "v_pk_mul_f32 v[58:59], v[58:59], v[144:145] op_sel:[0,1] op_sel_hi:[1,1]\n\t" "v_pk_mul_f32 v[80:81], v[80:81], v[144:145] op_sel:[0,1] op_sel_hi:[1,1]\n\t" \
  "v_pk_mul_f32 v[82:83], v[82:83], v[144:145] op_sel:[0,1] op_sel_hi:[1,1]\n\t" "v_cvt_pk_bf16_f32 v56, v56, v57\n\t" "v_cvt_pk_bf16_f32 v57, v58, v59\n\t" "v_cvt_pk_bf16_f32 v58, v80, v81\n\t" \
  "v_cvt_pk_bf16_f32 v59, v82, v83\n\t" "s_mov_b64 exec, s[68:69]\n\t" "global_store_dwordx4 v7, v[56:59], s[66:67] offset:2048 sc1\n\t" "s_mov_b64 exec, -1\n\t" \
  "ds_write2st64_b64 v6, v[56:57], v[58:59] offset0:4 offset1:28\n\t" "v_pk_fma_f32 v[60:61], v[160:161], v[146:147], v[60:61] op_sel_hi:[1,0,1] neg_lo:[1,0,0] neg_hi:[1,0,0]\n\t" \
  "v_pk_fma_f32 v[62:63], v[162:163], v[146:147], v[62:63] op_sel_hi:[1,0,1] neg_lo:[1,0,0] neg_hi:[1,0,0]\n\t" \
  "v_pk_fma_f32 v[84:85], v[164:165], v[146:147], v[84:85] op_sel_hi:[1,0,1] neg_lo:[1,0,0] neg_hi:[1,0,0]\n\t" \
  "v_pk_fma_f32 v[86:87], v[166:167], v[146:147], v[86:87] op_sel_hi:[1,0,1] neg_lo:[1,0,0] neg_hi:[1,0,0]\n\t" "v_pk_mul_f32 v[60:61], v[60:61], v[146:147] op_sel:[0,1] op_sel_hi:[1,1]\n\t" \
  "v_pk_mul_f32 v[62:63], v[62:63], v[146:147] op_sel:[0,1] op_sel_hi:[1,1]\n\t" "v_pk_mul_f32 v[84:85], v[84:85], v[146:147] op_sel:[0,1] op_sel_hi:[1,1]\n\t" \
  "v_pk_mul_f32 v[86:87], v[86:87], v[146:147] op_sel:[0,1] op_sel_hi:[1,1]\n\t" "v_cvt_pk_bf16_f32 v60, v60, v61\n\t" "v_cvt_pk_bf16_f32 v61, v62, v63\n\t" "v_cvt_pk_bf16_f32 v62, v84, v85\n\t" \
  "v_cvt_pk_bf16_f32 v63, v86, v87\n\t" "s_mov_b64 exec, s[68:69]\n\t" "global_store_dwordx4 v7, v[60:63], s[66:67] offset:3072 sc1\n\t" "s_mov_b64 exec, -1\n\t" \
  "ds_write2st64_b64 v6, v[60:61], v[62:63] offset0:5 offset1:29\n\t" "ds_read_b128 v[80:83], v5 offset:320\n\t" "ds_read_b128 v[84:87], v5 offset:704\n\t" "s_mov_b64 exec, 1\n\t" \
  "ds_add_u32 v37, v38 offset:0\n\t" "s_mov_b64 exec, -1\n\t" "s_waitcnt lgkmcnt(14)\n\t" "v_pk_fma_f32 v[88:89], v[112:113], v[14:15], v[88:89] op_sel_hi:[1,0,1] neg_lo:[1,0,0] neg_hi:[1,0,0]\n\t" \
  "v_pk_fma_f32 v[90:91], v[114:115], v[14:15], v[90:91] op_sel_hi:[1,0,1] neg_lo:[1,0,0] neg_hi:[1,0,0]\n\t" \
  "v_pk_fma_f32 v[92:93], v[120:121], v[14:15], v[92:93] op_sel_hi:[1,0,1] neg_lo:[1,0,0] neg_hi:[1,0,0]\n\t" \
  "v_pk_fma_f32 v[94:95], v[122:123], v[14:15], v[94:95] op_sel_hi:[1,0,1] neg_lo:[1,0,0] neg_hi:[1,0,0]\n\t" "v_pk_mul_f32 v[88:89], v[88:89], v[116:117]\n\t" \
  "v_pk_mul_f32 v[90:91], v[90:91], v[118:119]\n\t" "v_pk_mul_f32 v[92:93], v[92:93], v[124:125]\n\t" "v_pk_mul_f32 v[94:95], v[94:95], v[126:127]\n\t" "v_cvt_pk_bf16_f32 v88, v88, v89\n\t" \
  "v_cvt_pk_bf16_f32 v89, v90, v91\n\t" "v_cvt_pk_bf16_f32 v92, v92, v93\n\t" "v_cvt_pk_bf16_f32 v93, v94, v95\n\t" "ds_write2st64_b64 v6, v[88:89], v[92:93] offset0:48 offset1:49\n\t" \
  "s_waitcnt lgkmcnt(10)\n\t" "v_pk_fma_f32 v[96:97], v[128:129], v[14:15], v[96:97] op_sel_hi:[1,0,1] neg_lo:[1,0,0] neg_hi:[1,0,0]\n\t" \
  "v_pk_fma_f32 v[98:99], v[130:131], v[14:15], v[98:99] op_sel_hi:[1,0,1] neg_lo:[1,0,0] neg_hi:[1,0,0]\n\t" \
  "v_pk_fma_f32 v[100:101], v[64:65], v[14:15], v[100:101] op_sel_hi:[1,0,1] neg_lo:[1,0,0] neg_hi:[1,0,0]\n\t" \
  "v_pk_fma_f32 v[102:103], v[66:67], v[14:15], v[102:103] op_sel_hi:[1,0,1] neg_lo:[1,0,0] neg_hi:[1,0,0]\n\t" "v_pk_mul_f32 v[96:97], v[96:97], v[132:133]\n\t" \
  "v_pk_mul_f32 v[98:99], v[98:99], v[134:135]\n\t" "v_pk_mul_f32 v[100:101], v[100:101], v[68:69]\n\t" "v_pk_mul_f32 v[102:103], v[102:103], v[70:71]\n\t" "v_cvt_pk_bf16_f32 v96, v96, v97\n\t" \
  "v_cvt_pk_bf16_f32 v97, v98, v99\n\t" "v_cvt_pk_bf16_f32 v100, v100, v101\n\t" "v_cvt_pk_bf16_f32 v101, v102, v103\n\t" "ds_write2st64_b64 v6, v[96:97], v[100:101] offset0:50 offset1:51\n\t" \
  "s_waitcnt lgkmcnt(3)\n\t" "v_pk_fma_f32 v[104:105], v[72:73], v[14:15], v[104:105] op_sel_hi:[1,0,1] neg_lo:[1,0,0] neg_hi:[1,0,0]\n\t" \
  "v_pk_fma_f32 v[106:107], v[74:75], v[14:15], v[106:107] op_sel_hi:[1,0,1] neg_lo:[1,0,0] neg_hi:[1,0,0]\n\t" \
  "v_pk_fma_f32 v[108:109], v[80:81], v[14:15], v[108:109] op_sel_hi:[1,0,1] neg_lo:[1,0,0] neg_hi:[1,0,0]\n\t" \
  "v_pk_fma_f32 v[110:111], v[82:83], v[14:15], v[110:111] op_sel_hi:[1,0,1] neg_lo:[1,0,0] neg_hi:[1,0,0]\n\t" "v_pk_mul_f32 v[104:105], v[104:105], v[76:77]\n\t" \
  "v_pk_mul_f32 v[106:107], v[106:107], v[78:79]\n\t" "v_pk_mul_f32 v[108:109], v[108:109], v[84:85]\n\t" "v_pk_mul_f32 v[110:111], v[110:111], v[86:87]\n\t" "v_cvt_pk_bf16_f32 v104, v104, v105\n\t" \
  "v_cvt_pk_bf16_f32 v105, v106, v107\n\t" "v_cvt_pk_bf16_f32 v108, v108, v109\n\t" "v_cvt_pk_bf16_f32 v109, v110, v111\n\t" "ds_write2st64_b64 v6, v[104:105], v[108:109] offset0:52 offset1:53\n\t" \
  "s_mov_b64 exec, 1\n\t" "ds_add_u32 v37, v38 offset:4\n\t" "s_mov_b64 exec, -1\n\t" "s_setprio 1\n\t" ".Latt_%=:\n\t" "global_load_dwordx4 v[12:15], v33, s[8:9]\n\t" \
  "global_load_dwordx4 v[16:19], v34, s[14:15]\n\t" "s_cmp_eq_u32 s34, 0\n\t" "s_cbranch_scc1 .Lk0_%=\n\t" ".Lq_%=:\n\t" "ds_read_b32 v39, v37\n\t" "ds_read_b64 v[40:41], v30 offset:0\n\t" \
  "ds_read_b64 v[42:43], v30 offset:3072\n\t" "ds_read_b64 v[44:45], v30 offset:11776\n\t" "ds_read_b64 v[46:47], v30 offset:14848\n\t" "ds_read_b64 v[48:49], v30 offset:12288\n\t" \
  "ds_read_b64 v[50:51], v30 offset:15360\n\t" "ds_read_b64 v[52:53], v31 offset:0\n\t" "ds_read_b64 v[54:55], v31 offset:3072\n\t" "ds_read_b64 v[56:57], v31 offset:11776\n\t" \
  "ds_read_b64 v[58:59], v31 offset:14848\n\t" "ds_read_b64 v[60:61], v31 offset:12288\n\t" "ds_read_b64 v[62:63], v31 offset:15360\n\t" "s_waitcnt lgkmcnt(12)\n\t" "v_readfirstlane_b32 s38, v39\n\t" \
  "s_cmp_ge_u32 s38, 4\n\t" "s_cbranch_scc0 .Lsq_%=\n\t" ".Lrq_%=:\n\t" "s_waitcnt lgkmcnt(10)\n\t" "v_cndmask_b32 v42, 0, v42, s[54:55]\n\t" "v_cndmask_b32 v43, 0, v43, s[54:55]\n\t" \
  "s_waitcnt lgkmcnt(8)\n\t" "s_nop 0\n\t" "v_mfma_f32_16x16x32_bf16 v[108:111], v[44:47], v[40:43], v[20:23]\n\t" "s_waitcnt lgkmcnt(6)\n\t" \
  "v_mfma_f32_16x16x32_bf16 v[112:115], v[48:51], v[40:43], v[24:27]\n\t" "s_waitcnt lgkmcnt(4)\n\t" "v_cndmask_b32 v52, 0, v52, s[54:55]\n\t" "v_cndmask_b32 v53, 0, v53, s[54:55]\n\t" \
  "s_waitcnt lgkmcnt(2)\n\t" "s_nop 0\n\t" "v_mfma_f32_16x16x32_bf16 v[116:119], v[56:59], v[52:55], v[20:23]\n\t" "s_waitcnt lgkmcnt(0)\n\t" \
  "v_mfma_f32_16x16x32_bf16 v[120:123], v[60:63], v[52:55], v[24:27]\n\t" "ds_read_b32 v39, v37 offset:4\n\t" "ds_read_b64 v[128:129], v30 offset:24064\n\t" \
  "ds_read_b64 v[130:131], v30 offset:24576\n\t" "ds_read_b64 v[132:133], v30 offset:27136\n\t" "ds_read_b64 v[134:135], v30 offset:27648\n\t" "ds_read_b64 v[136:137], v31 offset:24064\n\t" \
  "ds_read_b64 v[138:139], v31 offset:24576\n\t" "ds_read_b64 v[140:141], v31 offset:27136\n\t" "ds_read_b64 v[142:143], v31 offset:27648\n\t" ".Lk1_%=:\n\t" "s_nop 4\n\t" \
  "v_max_f32 v108, v108, v112\n\t" "v_max_f32 v109, v109, v113\n\t" "v_max_f32 v110, v110, v114\n\t" "v_max_f32 v111, v111, v115\n\t" "v_max_f32 v116, v116, v120\n\t" "v_max_f32 v117, v117, v121\n\t" \
  "v_max_f32 v118, v118, v122\n\t" "v_max_f32 v119, v119, v123\n\t" "v_max3_f32 v124, v108, v109, v110\n\t" "v_max3_f32 v125, v116, v117, v118\n\t" "v_max_f32 v124, v124, v111\n\t" \
  "v_max_f32 v125, v125, v119\n\t" "s_mov_b32 s38, 0x3ea52b5c\n\t" "s_nop 0\n\t" "v_permlane32_swap_b32 v124, v125\n\t" "v_max_f32 v126, v124, v125\n\t" "v_max_f32 v127, v124, v125\n\t" "s_nop 1\n\t" \
  "v_permlane16_swap_b32 v126, v127\n\t" "v_max_f32 v124, v126, v127\n\t" "v_mul_f32 v125, s38, v124\n\t" "v_mul_f32 v124, s38, v124\n\t" "s_nop 1\n\t" "v_permlane32_swap_b32 v124, v125\n\t" \
  "v_fma_f32 v108, v108, s38, -v124\n\t" "v_fma_f32 v116, v116, s38, -v125\n\t" "v_fma_f32 v109, v109, s38, -v124\n\t" "v_fma_f32 v117, v117, s38, -v125\n\t" "v_fma_f32 v110, v110, s38, -v124\n\t" \
  "v_fma_f32 v118, v118, s38, -v125\n\t" "v_fma_f32 v111, v111, s38, -v124\n\t" "v_fma_f32 v119, v119, s38, -v125\n\t" "v_exp_f32 v108, v108\n\t" "v_exp_f32 v116, v116\n\t" "v_exp_f32 v109, v109\n\t" \
  "v_exp_f32 v117, v117\n\t" "v_exp_f32 v110, v110\n\t" "v_exp_f32 v118, v118\n\t" "v_add_f32 v124, v108, v109\n\t" "v_add_f32 v125, v116, v117\n\t" "v_exp_f32 v111, v111\n\t" \
  "v_exp_f32 v119, v119\n\t" "v_add_f32 v124, v124, v110\n\t" "v_add_f32 v125, v125, v118\n\t" "v_add_f32 v124, v124, v111\n\t" "v_add_f32 v125, v125, v119\n\t" "s_cmp_lg_u32 s34, 0\n\t" "s_nop 0\n\t" \
  "v_permlane32_swap_b32 v124, v125\n\t" "v_add_f32 v126, v124, v125\n\t" "v_add_f32 v127, v124, v125\n\t" "s_nop 1\n\t" "v_permlane16_swap_b32 v126, v127\n\t" "v_add_f32 v124, v126, v127\n\t" \
  "v_rcp_f32 v126, v124\n\t" "v_rcp_f32 v124, v124\n\t" "s_nop 1\n\t" "v_permlane32_swap_b32 v124, v126\n\t" "v_pk_mul_f32 v[108:109], v[108:109], v[124:125] op_sel_hi:[1,0]\n\t" \
  "v_pk_mul_f32 v[110:111], v[110:111], v[124:125] op_sel_hi:[1,0]\n\t" "v_pk_mul_f32 v[116:117], v[116:117], v[126:127] op_sel_hi:[1,0]\n\t" \
  "v_pk_mul_f32 v[118:119], v[118:119], v[126:127] op_sel_hi:[1,0]\n\t" "v_cvt_pk_bf16_f32 v112, v108, v109\n\t" "v_cvt_pk_bf16_f32 v113, v110, v111\n\t" "v_cvt_pk_bf16_f32 v120, v116, v117\n\t" \
  "v_cvt_pk_bf16_f32 v121, v118, v119\n\t" "v_and_b32 v114, v112, v28\n\t" "v_and_b32 v115, v113, v29\n\t" "v_and_b32 v122, v120, v28\n\t" "v_and_b32 v123, v121, v29\n\t" \
  "v_xor_b32 v112, v112, v114\n\t" "v_xor_b32 v113, v113, v115\n\t" "v_xor_b32 v120, v120, v122\n\t" "v_xor_b32 v121, v121, v123\n\t" "s_cbranch_scc1 .Lk2_%=\n\t" \
  "v_mfma_f32_16x16x32_bf16 v[152:155], v[128:131], v[112:115], 0\n\t" "v_mfma_f32_16x16x32_bf16 v[156:159], v[132:135], v[112:115], 0\n\t" \
  "v_mfma_f32_16x16x32_bf16 v[152:155], v[136:139], v[120:123], v[152:155]\n\t" "v_mfma_f32_16x16x32_bf16 v[156:159], v[140:143], v[120:123], v[156:159]\n\t" "s_nop 6\n\t" \
  "v_cvt_pk_bf16_f32 v148, v152, v153\n\t" "v_cvt_pk_bf16_f32 v149, v154, v155\n\t" "v_cvt_pk_bf16_f32 v150, v156, v157\n\t" "v_cvt_pk_bf16_f32 v151, v158, v159\n\t" ".Lk3_%=:\n\t" \
  "s_mov_b64 exec, s[54:55]\n\t" "global_store_dwordx4 v36, v[148:151], s[60:61] offset:-2048 sc1\n\t" "s_mov_b64 exec, -1\n\t" "s_cmp_ge_u32 s26, 6\n\t" "s_cbranch_scc1 .Lb3a_%=\n\t" \
  "s_cmp_ge_u32 s26, 4\n\t" "s_cbranch_scc1 .Lv21_%=\n\t" "s_waitcnt vmcnt(1)\n\t" ".Lv21_%=:\n\t" "s_setprio 1\n\t" "ds_read_b64 v[152:153], v37 offset:80\n\t" "s_waitcnt lgkmcnt(0)\n\t" \
  "v_readfirstlane_b32 s80, v152\n\t" "v_readfirstlane_b32 s81, v153\n\t" "s_sub_u32 s81, s81, s80\n\t" "s_sub_u32 s80, s81, 15\n\t" "s_barrier\n\t" "s_cbranch_scc1 .Llx1_%=\n\t" \
  "s_memrealtime s[76:77]\n\t" ".Llw1_%=:\n\t" "s_memrealtime s[78:79]\n\t" "s_waitcnt lgkmcnt(0)\n\t" "s_sub_u32 s78, s78, s76\n\t" "s_cmp_lt_u32 s78, s80\n\t" "s_cbranch_scc1 .Llw1_%=\n\t" \
  ".Llx1_%=:\n\t" "global_load_dwordx4 v[152:155], v32, s[62:63] offset:-2048 sc1\n\t" "global_load_dwordx4 v[156:159], v32, s[62:63] offset:-1024 sc1\n\t" \
  "global_load_dwordx4 v[160:163], v32, s[62:63] offset:0 sc1\n\t" ".Lb3a_%=:\n\t" "ds_read_b64_tr_b16 v[40:41], v35 offset:0\n\t" "ds_read_b64_tr_b16 v[42:43], v35 offset:2048\n\t" \
  "ds_read_b64_tr_b16 v[44:45], v0 offset:0\n\t" "ds_read_b64_tr_b16 v[46:47], v0 offset:2048\n\t" "ds_read_b64_tr_b16 v[48:49], v1 offset:0\n\t" "ds_read_b64_tr_b16 v[50:51], v1 offset:2048\n\t" \
  "ds_read_b64_tr_b16 v[52:53], v2 offset:0\n\t" "ds_read_b64_tr_b16 v[54:55], v2 offset:2048\n\t" "ds_read_b64_tr_b16 v[56:57], v35 offset:4096\n\t" "ds_read_b64_tr_b16 v[58:59], v35 offset:6144\n\t" \
  "ds_read_b64_tr_b16 v[60:61], v0 offset:4096\n\t" "ds_read_b64_tr_b16 v[62:63], v0 offset:6144\n\t" "ds_read_b64_tr_b16 v[64:65], v1 offset:4096\n\t" \
  "ds_read_b64_tr_b16 v[66:67], v1 offset:6144\n\t" "ds_read_b64_tr_b16 v[68:69], v2 offset:4096\n\t" "ds_read_b64_tr_b16 v[70:71], v2 offset:6144\n\t" \
  "ds_read_b64_tr_b16 v[72:73], v35 offset:8192\n\t" "ds_read_b64_tr_b16 v[74:75], v35 offset:10240\n\t" "ds_read_b64_tr_b16 v[76:77], v0 offset:8192\n\t" \
  "ds_read_b64_tr_b16 v[78:79], v0 offset:10240\n\t" "ds_read_b64_tr_b16 v[80:81], v1 offset:8192\n\t" "ds_read_b64_tr_b16 v[82:83], v1 offset:10240\n\t" \
  "ds_read_b64_tr_b16 v[84:85], v2 offset:8192\n\t" "ds_read_b64_tr_b16 v[86:87], v2 offset:10240\n\t" "ds_read_b64_tr_b16 v[88:89], v35 offset:12288\n\t" \
  "ds_read_b64_tr_b16 v[90:91], v35 offset:14336\n\t" "ds_read_b64_tr_b16 v[92:93], v0 offset:12288\n\t" "ds_read_b64_tr_b16 v[94:95], v0 offset:14336\n\t" \
  "ds_read_b64_tr_b16 v[96:97], v1 offset:12288\n\t" "ds_read_b64_tr_b16 v[98:99], v1 offset:14336\n\t" "ds_read_b64_tr_b16 v[100:101], v2 offset:12288\n\t" \
  "ds_read_b64_tr_b16 v[102:103], v2 offset:14336\n\t" "ds_read_b64_tr_b16 v[104:105], v35 offset:16384\n\t" "ds_read_b64_tr_b16 v[106:107], v35 offset:18432\n\t" \
  "ds_read_b64_tr_b16 v[108:109], v0 offset:16384\n\t" "ds_read_b64_tr_b16 v[110:111], v0 offset:18432\n\t" "ds_read_b64_tr_b16 v[112:113], v1 offset:16384\n\t" \
  "ds_read_b64_tr_b16 v[114:115], v1 offset:18432\n\t" "ds_read_b64_tr_b16 v[116:117], v2 offset:16384\n\t" "ds_read_b64_tr_b16 v[118:119], v2 offset:18432\n\t" "s_cmp_lt_u32 s26, 6\n\t" \
  "s_cbranch_scc1 .Lb3b_%=\n\t" "s_setprio 1\n\t" "ds_read_b64 v[152:153], v37 offset:80\n\t" "s_waitcnt lgkmcnt(0)\n\t" "v_readfirstlane_b32 s80, v152\n\t" "v_readfirstlane_b32 s81, v153\n\t" \
  "s_sub_u32 s81, s81, s80\n\t" "s_sub_u32 s80, s81, 15\n\t" "s_barrier\n\t" "s_cbranch_scc1 .Llx2_%=\n\t" "s_memrealtime s[76:77]\n\t" ".Llw2_%=:\n\t" "s_memrealtime s[78:79]\n\t" \
  "s_waitcnt lgkmcnt(0)\n\t" "s_sub_u32 s78, s78, s76\n\t" "s_cmp_lt_u32 s78, s80\n\t" "s_cbranch_scc1 .Llw2_%=\n\t" ".Llx2_%=:\n\t" \
  "global_load_dwordx4 v[152:155], v32, s[62:63] offset:-2048 sc1\n\t" "global_load_dwordx4 v[156:159], v32, s[62:63] offset:-1024 sc1\n\t" \
  "global_load_dwordx4 v[160:163], v32, s[62:63] offset:0 sc1\n\t" ".Lb3b_%=:\n\t" "ds_read_b64_tr_b16 v[120:121], v35 offset:20480\n\t" "ds_read_b64_tr_b16 v[122:123], v35 offset:22528\n\t" \
  "ds_read_b64_tr_b16 v[124:125], v0 offset:20480\n\t" "ds_read_b64_tr_b16 v[126:127], v0 offset:22528\n\t" "ds_read_b64_tr_b16 v[128:129], v1 offset:20480\n\t" \
  "ds_read_b64_tr_b16 v[130:131], v1 offset:22528\n\t" "ds_read_b64_tr_b16 v[132:133], v2 offset:20480\n\t" "ds_read_b64_tr_b16 v[134:135], v2 offset:22528\n\t" "s_waitcnt vmcnt(5)\n\t" \
  "v_permlane16_swap_b32 v12, v14\n\t" "v_permlane16_swap_b32 v13, v15\n\t" "v_mov_b64 v[144:145], 0\n\t" "v_mov_b64 v[146:147], 0\n\t" "v_mov_b64 v[148:149], 0\n\t" "v_mov_b64 v[150:151], 0\n\t" \
  "v_lshlrev_b32 v136, 16, v12\n\t" "v_and_b32 v137, 0xffff0000, v12\n\t" "v_lshlrev_b32 v138, 16, v13\n\t" "v_and_b32 v139, 0xffff0000, v13\n\t" "v_lshlrev_b32 v140, 16, v14\n\t" \
  "v_and_b32 v141, 0xffff0000, v14\n\t" "v_lshlrev_b32 v142, 16, v15\n\t" "v_and_b32 v143, 0xffff0000, v15\n\t" "s_lshl_b32 s38, s26, 11\n\t" "v_and_b32 v28, 0xf0, v32\n\t" \
  "v_add_u32 v24, s38, v32\n\t" "v_permlane16_swap_b32 v16, v18\n\t" "v_permlane16_swap_b32 v17, v19\n\t" "s_lshl_b32 s40, s36, 6\n\t" "s_add_u32 s40, s40, 0\n\t" "v_lshl_add_u32 v27, v10, 1, s40\n\t" \
  "s_mov_b32 s40, 0x3f803f80\n\t" "v_xor_b32 v25, 0x800, v24\n\t" "v_mad_u32_u24 v27, v28, 9, v27\n\t" "s_mul_i32 s38, s37, 2304\n\t" "v_add_u32 v28, v28, v10\n\t" "v_add_u32 v27, s38, v27\n\t" \
  "s_mul_i32 s38, s36, 6\n\t" "s_add_u32 s38, s38, s37\n\t" "s_lshl_b32 s38, s38, 8\n\t" "v_lshl_add_u32 v26, s37, 2, v37\n\t" "v_add3_u32 v28, v28, s38, v37\n\t" "s_mul_i32 s38, s34, 768\n\t" \
  "v_min_u32 v29, 0x2f0, v32\n\t" "v_lshl_add_u32 v30, s34, 2, v37\n\t" "v_add3_u32 v29, v29, s38, v37\n\t" "s_lshl1_add_u32 s41, s29, s34\n\t" "v_min_u32 v20, 47, v8\n\t" \
  "s_mul_i32 s38, s41, 6400\n\t" "s_add_u32 s38, s38, 4829184\n\t" "v_mul_u32_u24 v21, 12, v20\n\t" "s_add_u32 s44, s24, s38\n\t" "s_addc_u32 s45, s25, 0\n\t" "s_add_u32 s42, s44, 2560\n\t" \
  "s_addc_u32 s43, s45, 0\n\t" "s_mul_i32 s38, s28, 640\n\t" "s_add_u32 s44, s44, s38\n\t" "s_addc_u32 s45, s45, 0\n\t" "s_waitcnt vmcnt(2)\n\t" "v_cmp_eq_u32 vcc, -1, v152\n\t" \
  "s_cbranch_vccnz .Lso0_%=\n\t" ".Lro0_%=:\n\t" "s_waitcnt lgkmcnt(14)\n\t" "v_mfma_f32_16x16x32_bf16 v[136:139], v[40:43], v[152:155], v[136:139]\n\t" \
  "global_load_dwordx4 v[164:167], v32, s[62:63] offset:1024 sc1\n\t" "ds_read_b64_tr_b16 v[40:41], v35 offset:24576\n\t" "ds_read_b64_tr_b16 v[42:43], v35 offset:26624\n\t" \
  "v_mfma_f32_16x16x32_bf16 v[140:143], v[44:47], v[152:155], v[140:143]\n\t" "ds_read_b64_tr_b16 v[44:45], v0 offset:24576\n\t" "ds_read_b64_tr_b16 v[46:47], v0 offset:26624\n\t" \
  "v_mfma_f32_16x16x32_bf16 v[144:147], v[48:51], v[152:155], v[144:147]\n\t" "ds_read_b64_tr_b16 v[48:49], v1 offset:24576\n\t" "ds_read_b64_tr_b16 v[50:51], v1 offset:26624\n\t" \
  "v_mfma_f32_16x16x32_bf16 v[148:151], v[52:55], v[152:155], v[148:151]\n\t" "ds_read_b64_tr_b16 v[52:53], v2 offset:24576\n\t" "ds_read_b64_tr_b16 v[54:55], v2 offset:26624\n\t" \
  "s_waitcnt vmcnt(2)\n\t" "v_cmp_eq_u32 vcc, -1, v156\n\t" "s_cbranch_vccnz .Lso1_%=\n\t" ".Lro1_%=:\n\t" "v_mfma_f32_16x16x32_bf16 v[136:139], v[56:59], v[156:159], v[136:139]\n\t" \
  "global_load_dwordx4 v[152:155], v32, s[62:63] offset:2048 sc1\n\t" "ds_read_b64_tr_b16 v[56:57], v35 offset:28672\n\t" "ds_read_b64_tr_b16 v[58:59], v35 offset:30720\n\t" \
  "v_mfma_f32_16x16x32_bf16 v[140:143], v[60:63], v[156:159], v[140:143]\n\t" "ds_read_b64_tr_b16 v[60:61], v0 offset:28672\n\t" "ds_read_b64_tr_b16 v[62:63], v0 offset:30720\n\t" \
  "v_mfma_f32_16x16x32_bf16 v[144:147], v[64:67], v[156:159], v[144:147]\n\t" "ds_read_b64_tr_b16 v[64:65], v1 offset:28672\n\t" "ds_read_b64_tr_b16 v[66:67], v1 offset:30720\n\t" \
  "v_mfma_f32_16x16x32_bf16 v[148:151], v[68:71], v[156:159], v[148:151]\n\t" "ds_read_b64_tr_b16 v[68:69], v2 offset:28672\n\t" "ds_read_b64_tr_b16 v[70:71], v2 offset:30720\n\t" \
  "s_waitcnt vmcnt(2)\n\t" "v_cmp_eq_u32 vcc, -1, v160\n\t" "s_cbranch_vccnz .Lso2_%=\n\t" ".Lro2_%=:\n\t" "v_mfma_f32_16x16x32_bf16 v[136:139], v[72:75], v[160:163], v[136:139]\n\t" \
  "global_load_dwordx4 v[156:159], v32, s[64:65] offset:-2048 sc1\n\t" "ds_read_b64_tr_b16 v[72:73], v35 offset:32768\n\t" "ds_read_b64_tr_b16 v[74:75], v35 offset:34816\n\t" \
  "v_mfma_f32_16x16x32_bf16 v[140:143], v[76:79], v[160:163], v[140:143]\n\t" "ds_read_b64_tr_b16 v[76:77], v0 offset:32768\n\t" "ds_read_b64_tr_b16 v[78:79], v0 offset:34816\n\t" \
  "v_mfma_f32_16x16x32_bf16 v[144:147], v[80:83], v[160:163], v[144:147]\n\t" "ds_read_b64_tr_b16 v[80:81], v1 offset:32768\n\t" "ds_read_b64_tr_b16 v[82:83], v1 offset:34816\n\t" \
  "v_mfma_f32_16x16x32_bf16 v[148:151], v[84:87], v[160:163], v[148:151]\n\t" "ds_read_b64_tr_b16 v[84:85], v2 offset:32768\n\t" "ds_read_b64_tr_b16 v[86:87], v2 offset:34816\n\t" \
  "s_waitcnt vmcnt(2)\n\t" "v_cmp_eq_u32 vcc, -1, v164\n\t" "s_cbranch_vccnz .Lso3_%=\n\t" ".Lro3_%=:\n\t" "v_mfma_f32_16x16x32_bf16 v[136:139], v[88:91], v[164:167], v[136:139]\n\t" \
  "global_load_dwordx4 v[160:163], v32, s[64:65] offset:-1024 sc1\n\t" "ds_read_b64_tr_b16 v[88:89], v35 offset:36864\n\t" "ds_read_b64_tr_b16 v[90:91], v35 offset:38912\n\t" \
  "v_mfma_f32_16x16x32_bf16 v[140:143], v[92:95], v[164:167], v[140:143]\n\t" "ds_read_b64_tr_b16 v[92:93], v0 offset:36864\n\t" "ds_read_b64_tr_b16 v[94:95], v0 offset:38912\n\t" \
  "v_mfma_f32_16x16x32_bf16 v[144:147], v[96:99], v[164:167], v[144:147]\n\t" "ds_read_b64_tr_b16 v[96:97], v1 offset:36864\n\t" "ds_read_b64_tr_b16 v[98:99], v1 offset:38912\n\t" \
  "v_mfma_f32_16x16x32_bf16 v[148:151], v[100:103], v[164:167], v[148:151]\n\t" "ds_read_b64_tr_b16 v[100:101], v2 offset:36864\n\t" "ds_read_b64_tr_b16 v[102:103], v2 offset:38912\n\t" \
  "s_waitcnt vmcnt(2)\n\t" "v_cmp_eq_u32 vcc, -1, v152\n\t" "s_cbranch_vccnz .Lso4_%=\n\t" ".Lro4_%=:\n\t" "v_mfma_f32_16x16x32_bf16 v[136:139], v[104:107], v[152:155], v[136:139]\n\t" \
  "global_load_dwordx4 v[164:167], v32, s[64:65] offset:0 sc1\n\t" "s_waitcnt lgkmcnt(14)\n\t" "v_mfma_f32_16x16x32_bf16 v[140:143], v[108:111], v[152:155], v[140:143]\n\t" \
  "v_mfma_f32_16x16x32_bf16 v[144:147], v[112:115], v[152:155], v[144:147]\n\t" "v_mfma_f32_16x16x32_bf16 v[148:151], v[116:119], v[152:155], v[148:151]\n\t" "s_waitcnt vmcnt(2)\n\t" \
  "v_cmp_eq_u32 vcc, -1, v156\n\t" "s_cbranch_vccnz .Lso5_%=\n\t" ".Lro5_%=:\n\t" "v_mfma_f32_16x16x32_bf16 v[136:139], v[120:123], v[156:159], v[136:139]\n\t" \
  "global_load_dwordx4 v[152:155], v32, s[64:65] offset:1024 sc1\n\t" "v_mfma_f32_16x16x32_bf16 v[140:143], v[124:127], v[156:159], v[140:143]\n\t" \
  "v_mfma_f32_16x16x32_bf16 v[144:147], v[128:131], v[156:159], v[144:147]\n\t" "v_mfma_f32_16x16x32_bf16 v[148:151], v[132:135], v[156:159], v[148:151]\n\t" "s_waitcnt vmcnt(2)\n\t" \
  "v_cmp_eq_u32 vcc, -1, v160\n\t" "s_cbranch_vccnz .Lso6_%=\n\t" ".Lro6_%=:\n\t" "v_mfma_f32_16x16x32_bf16 v[136:139], v[40:43], v[160:163], v[136:139]\n\t" \
  "global_load_dwordx4 v[156:159], v32, s[64:65] offset:2048 sc1\n\t" "v_mfma_f32_16x16x32_bf16 v[140:143], v[44:47], v[160:163], v[140:143]\n\t" \
  "v_mfma_f32_16x16x32_bf16 v[144:147], v[48:51], v[160:163], v[144:147]\n\t" "v_mfma_f32_16x16x32_bf16 v[148:151], v[52:55], v[160:163], v[148:151]\n\t" "s_waitcnt vmcnt(2)\n\t" \
  "v_cmp_eq_u32 vcc, -1, v164\n\t" "s_cbranch_vccnz .Lso7_%=\n\t" ".Lro7_%=:\n\t" "v_mfma_f32_16x16x32_bf16 v[136:139], v[56:59], v[164:167], v[136:139]\n\t" \
  "v_mfma_f32_16x16x32_bf16 v[140:143], v[60:63], v[164:167], v[140:143]\n\t" "v_mfma_f32_16x16x32_bf16 v[144:147], v[64:67], v[164:167], v[144:147]\n\t" \
  "v_mfma_f32_16x16x32_bf16 v[148:151], v[68:71], v[164:167], v[148:151]\n\t" "s_waitcnt vmcnt(1)\n\t" "v_cmp_eq_u32 vcc, -1, v152\n\t" "s_cbranch_vccnz .Lso8_%=\n\t" ".Lro8_%=:\n\t" \
  "v_mfma_f32_16x16x32_bf16 v[136:139], v[72:75], v[152:155], v[136:139]\n\t" "s_waitcnt lgkmcnt(12)\n\t" "v_mfma_f32_16x16x32_bf16 v[140:143], v[76:79], v[152:155], v[140:143]\n\t" \
  "s_waitcnt lgkmcnt(10)\n\t" "v_mfma_f32_16x16x32_bf16 v[144:147], v[80:83], v[152:155], v[144:147]\n\t" "s_waitcnt lgkmcnt(8)\n\t" \
  "v_mfma_f32_16x16x32_bf16 v[148:151], v[84:87], v[152:155], v[148:151]\n\t" "s_waitcnt vmcnt(0)\n\t" "v_cmp_eq_u32 vcc, -1, v156\n\t" "s_cbranch_vccnz .Lso9_%=\n\t" ".Lro9_%=:\n\t" \
  "s_waitcnt lgkmcnt(2)\n\t" "v_mfma_f32_16x16x32_bf16 v[144:147], v[96:99], v[156:159], v[144:147]\n\t" "s_waitcnt lgkmcnt(0)\n\t" \
  "v_mfma_f32_16x16x32_bf16 v[148:151], v[100:103], v[156:159], v[148:151]\n\t" "v_mfma_f32_16x16x32_bf16 v[136:139], v[88:91], v[156:159], v[136:139]\n\t" \
  "v_mfma_f32_16x16x32_bf16 v[140:143], v[92:95], v[156:159], v[140:143]\n\t" "s_nop 3\n\t" "ds_write_b128 v24, v[144:147] offset:36864\n\t" "s_nop 0\n\t" \
  "ds_write_b128 v24, v[148:151] offset:37888\n\t" "s_mov_b64 exec, 1\n\t" "ds_add_u32 v26, v38 offset:48\n\t" "s_mov_b64 exec, -1\n\t" ".Lp_%=:\n\t" "ds_read_b32 v39, v26 offset:48\n\t" \
  "ds_read_b128 v[40:43], v25 offset:36864\n\t" "ds_read_b128 v[44:47], v25 offset:37888\n\t" "s_sub_u32 s39, s39, 1\n\t" "s_cmp_lt_i32 s39, 0\n\t" "s_waitcnt lgkmcnt(2)\n\t" \
  "v_readfirstlane_b32 s38, v39\n\t" "s_cselect_b32 s38, 2, s38\n\t" "s_cmp_ge_u32 s38, 2\n\t" "s_waitcnt lgkmcnt(0)\n\t" "s_cbranch_scc0 .Lp_%=\n\t" \
  "v_pk_add_f32 v[136:137], v[136:137], v[40:41]\n\t" "v_pk_add_f32 v[138:139], v[138:139], v[42:43]\n\t" "v_pk_add_f32 v[140:141], v[140:141], v[44:45]\n\t" \
  "v_pk_add_f32 v[142:143], v[142:143], v[46:47]\n\t" "v_cvt_pk_bf16_f32 v12, v136, v137\n\t" "v_cvt_pk_bf16_f32 v13, v138, v139\n\t" "v_cvt_pk_bf16_f32 v14, v140, v141\n\t" \
  "v_cvt_pk_bf16_f32 v15, v142, v143\n\t" "v_dot2_f32_bf16 v40, v12, s40, 0\n\t" "v_dot2_f32_bf16 v41, v12, v12, 0\n\t" "v_dot2_f32_bf16 v42, v12, v16, 0\n\t" "v_dot2_f32_bf16 v40, v13, s40, v40\n\t" \
  "v_dot2_f32_bf16 v41, v13, v13, v41\n\t" "v_dot2_f32_bf16 v42, v13, v17, v42\n\t" "v_dot2_f32_bf16 v40, v14, s40, v40\n\t" "v_dot2_f32_bf16 v41, v14, v14, v41\n\t" \
  "v_dot2_f32_bf16 v42, v14, v18, v42\n\t" "v_dot2_f32_bf16 v40, v15, s40, v40\n\t" "v_dot2_f32_bf16 v41, v15, v15, v41\n\t" "v_dot2_f32_bf16 v43, v15, v19, v42\n\t" \
  "v_dot2_f32_bf16 v42, v15, v19, v42\n\t" "ds_write2_b64 v27, v[12:13], v[14:15] offset1:4\n\t" "s_nop 0\n\t" "v_permlane32_swap_b32 v40, v41\n\t" "v_permlane32_swap_b32 v42, v43\n\t" \
  "v_add_f32 v40, v40, v41\n\t" "v_add_f32 v42, v42, v43\n\t" "s_nop 1\n\t" "v_permlane16_swap_b32 v40, v42\n\t" "v_add_f32 v40, v40, v42\n\t" "ds_write_b32 v28, v40 offset:256\n\t" \
  "s_mov_b64 exec, 1\n\t" "ds_add_rtn_u32 v39, v30, v38 offset:16\n\t" "s_mov_b64 exec, -1\n\t" "ds_read_b128 v[44:47], v29 offset:256\n\t" "ds_read_b128 v[48:51], v29 offset:1792\n\t" \
  "ds_read_b32 v52, v37 offset:40\n\t" "s_waitcnt lgkmcnt(3)\n\t" "v_readfirstlane_b32 s38, v39\n\t" "s_waitcnt lgkmcnt(0)\n\t" "s_cmp_lg_u32 s38, 5\n\t" "s_setprio 0\n\t" "s_cbranch_scc1 .Lnk_%=\n\t" \
  "s_setprio 3\n\t" "v_pk_add_f32 v[44:45], v[44:45], v[48:49]\n\t" "v_add_f32 v46, v46, v50\n\t" "s_mov_b32 exec_hi, 0xffff\n\t" "global_store_dwordx3 v21, v[44:46], s[44:45] sc1\n\t" \
  "s_mov_b32 exec_hi, -1\n\t" "s_memrealtime s[76:77]\n\t" "s_mul_i32 s50, s34, 48\n\t" "v_lshl_add_u32 v4, s41, 6, v20\n\t" "v_add_u32 v1, s50, v20\n\t" "s_cmp_eq_u32 s29, 0\n\t" \
  "s_cselect_b32 s51, 6, 7\n\t" "v_cmp_le_u32 vcc, s51, v1\n\t" "s_mov_b32 s51, 155648\n\t" "v_lshl_add_u32 v1, v1, 3, s51\n\t" "v_mov_b32 v3, 0xfffff000\n\t" "s_lshl_b32 s51, s41, 7\n\t" \
  "v_cndmask_b32 v3, 0, v3, vcc\n\t" "v_lshl_add_u32 v5, v9, 3, s51\n\t" "v_lshlrev_b32 v7, 2, v8\n\t" "s_mul_i32 s50, s50, 144\n\t" "v_and_b32 v6, 0x7c, v7\n\t" "v_add_u32 v6, s50, v6\n\t" \
  "s_mul_i32 s50, s28, 6144\n\t" "s_add_u32 s50, s50, 5302784\n\t" "s_add_u32 s46, s24, s50\n\t" "s_addc_u32 s47, s25, 0\n\t" "s_mul_i32 s50, s41, 10\n\t" "s_add_u32 s50, s50, s28\n\t" \
  "s_lshl_b32 s50, s50, 8\n\t" "s_add_u32 s50, s50, 5123584\n\t" "s_add_u32 s48, s24, s50\n\t" "s_addc_u32 s49, s25, 0\n\t" "s_mov_b32 s50, 0x3acccccd\n\t" ".Ltk_%=:\n\t" "s_memrealtime s[78:79]\n\t" \
  "s_waitcnt lgkmcnt(0)\n\t" "s_sub_u32 s78, s78, s76\n\t" "s_cmp_lt_u32 s78, 40\n\t" "s_cbranch_scc1 .Ltk_%=\n\t" ".Lkp_%=:\n\t" "global_load_dwordx3 v[60:62], v21, s[42:43] offset:-2560 sc1\n\t" \
  "global_load_dwordx3 v[64:66], v21, s[42:43] offset:-1920 sc1\n\t" "global_load_dwordx3 v[68:70], v21, s[42:43] offset:-1280 sc1\n\t" \
  "global_load_dwordx3 v[72:74], v21, s[42:43] offset:-640 sc1\n\t" "global_load_dwordx3 v[76:78], v21, s[42:43] offset:0 sc1\n\t" "global_load_dwordx3 v[80:82], v21, s[42:43] offset:640 sc1\n\t" \
  "global_load_dwordx3 v[84:86], v21, s[42:43] offset:1280 sc1\n\t" "global_load_dwordx3 v[88:90], v21, s[42:43] offset:1920 sc1\n\t" "global_load_dwordx3 v[92:94], v21, s[42:43] offset:2560 sc1\n\t" \
  "global_load_dwordx3 v[96:98], v21, s[42:43] offset:3200 sc1\n\t" "s_sub_u32 s39, s39, 1\n\t" "s_cmp_lt_i32 s39, 0\n\t" "s_waitcnt vmcnt(0)\n\t" "v_pk_add_f32 v[60:61], v[60:61], v[64:65]\n\t" \
  "v_add_f32 v62, v62, v66\n\t" "v_pk_add_f32 v[68:69], v[68:69], v[72:73]\n\t" "v_add_f32 v70, v70, v74\n\t" "v_pk_add_f32 v[76:77], v[76:77], v[80:81]\n\t" "v_add_f32 v78, v78, v82\n\t" \
  "v_pk_add_f32 v[84:85], v[84:85], v[88:89]\n\t" "v_add_f32 v86, v86, v90\n\t" "v_pk_add_f32 v[92:93], v[92:93], v[96:97]\n\t" "v_add_f32 v94, v94, v98\n\t" \
  "v_pk_add_f32 v[60:61], v[60:61], v[68:69]\n\t" "v_add_f32 v62, v62, v70\n\t" "v_pk_add_f32 v[76:77], v[76:77], v[84:85]\n\t" "v_add_f32 v78, v78, v86\n\t" \
  "v_pk_add_f32 v[60:61], v[60:61], v[92:93]\n\t" "v_add_f32 v62, v62, v94\n\t" "v_pk_add_f32 v[60:61], v[60:61], v[76:77]\n\t" "v_add_f32 v62, v62, v78\n\t" "v_mul_f32 v60, s50, v60\n\t" \
  "v_mul_f32 v63, v60, v60\n\t" "v_fma_f32 v61, -v60, v52, v61\n\t" "v_fma_f32 v62, v62, s50, -v63\n\t" "s_cbranch_scc1 .Lkq_%=\n\t" "v_cmp_u_f32 vcc, v61, v62\n\t" "s_cbranch_vccnz .Lkp_%=\n\t" \
  ".Lkq_%=:\n\t" "v_max_f32 v62, 0, v62\n\t" "v_add_f32 v63, 0x3727c5ac, v62\n\t" "v_rsq_f32 v63, v63\n\t" "v_cvt_pk_bf16_f32 v66, v62, v60\n\t" "v_mul_f32 v61, v61, v63\n\t" \
  "v_ashrrev_i32 v63, 31, v61\n\t" "v_or_b32 v63, 0x80000000, v63\n\t" "v_xor_b32 v61, v61, v63\n\t" "v_and_or_b32 v61, v61, v3, v4\n\t" "v_lshlrev_b32 v67, 16, v66\n\t" \
  "v_and_b32 v66, 0xffff0000, v66\n\t" "v_max_u32_dpp v64, v61, v61 quad_perm:[1,0,3,2] row_mask:0xf bank_mask:0xf\n\t" "v_add_f32 v67, 0x3727c5ac, v67\n\t" "v_cvt_pk_bf16_f32 v67, v67, v67\n\t" \
  "v_max_u32_dpp v64, v64, v64 quad_perm:[2,3,0,1] row_mask:0xf bank_mask:0xf\n\t" "v_lshlrev_b32 v67, 16, v67\n\t" "v_sqrt_f32 v67, v67\n\t" \
  "v_max_u32_dpp v64, v64, v64 row_half_mirror row_mask:0xf bank_mask:0xf\n\t" "s_nop 0\n\t" "v_cvt_pk_bf16_f32 v67, v67, v67\n\t" \
  "v_max_u32_dpp v64, v64, v64 row_mirror row_mask:0xf bank_mask:0xf\n\t" "v_lshlrev_b32 v67, 16, v67\n\t" "v_rcp_f32 v67, v67\n\t" \
  "v_max_u32_dpp v64, v64, v64 row_bcast:15 row_mask:0xa bank_mask:0xf\n\t" "s_nop 1\n\t" "v_max_u32_dpp v64, v64, v64 row_bcast:31 row_mask:0xc bank_mask:0xf\n\t" "v_mov_b32 v65, v64\n\t" \
  "s_mov_b32 exec_lo, 0\n\t" "s_mov_b32 exec_hi, 0xffff0000\n\t" "global_store_dwordx2 v5, v[64:65], s[46:47] sc1\n\t" "s_mov_b64 exec, -1\n\t" "v_readlane_b32 s50, v64, 63\n\t" \
  "s_and_b32 s51, s50, 63\n\t" "s_mul_i32 s52, s51, 144\n\t" "v_add_u32 v6, s52, v6\n\t" "s_or_b32 s50, s50, 0x1000\n\t" "ds_read_b32 v68, v6 offset:0\n\t" "v_mov_b32 v64, s50\n\t" \
  "ds_write_b64 v1, v[66:67]\n\t" "ds_write_b32 v30, v64 offset:24\n\t" "v_readlane_b32 s52, v66, s51\n\t" "v_readlane_b32 s53, v67, s51\n\t" "s_waitcnt lgkmcnt(2)\n\t" \
  "v_lshlrev_b32 v70, 16, v68\n\t" "v_and_b32 v71, 0xffff0000, v68\n\t" "v_subrev_f32 v70, s52, v70\n\t" "v_subrev_f32 v71, s52, v71\n\t" "v_cvt_pk_bf16_f32 v71, v70, v71\n\t" \
  "v_lshlrev_b32 v70, 16, v71\n\t" "v_and_b32 v71, 0xffff0000, v71\n\t" "v_mul_f32 v70, s53, v70\n\t" "v_mul_f32 v71, s53, v71\n\t" "s_mov_b32 exec_hi, 0\n\t" "v_cvt_pk_bf16_f32 v68, v70, v71\n\t" \
  "s_mov_b32 exec_hi, -1\n\t" "global_store_dword v7, v68, s[48:49] sc1\n\t" "s_setprio 0\n\t" ".Lnk_%=:\n\t" "v_lshrrev_b32 v113, 3, v8\n\t" "s_lshl_b32 s40, s26, 3\n\t" "v_add_u32 v2, s40, v113\n\t" \
  "s_mov_b32 s76, 155648\n\t" "v_lshl_add_u32 v1, v2, 3, s76\n\t" "s_cmp_eq_u32 s29, 0\n\t" "s_cselect_b32 s76, 6, 7\n\t" "v_cmp_le_u32 s[72:73], s76, v2\n\t" "v_add_u32 v2, s30, v2\n\t" \
  "v_sub_u32 v2, v2, 6 clamp\n\t" "v_lshrrev_b32 v3, 10, v2\n\t" "v_lshl_or_b32 v2, v2, 1, v3\n\t" "v_and_b32 v2, 0x7ff, v2\n\t" "v_and_b32 v3, 0x70, v32\n\t" "s_lshl_b32 s76, s28, 7\n\t" \
  "v_add_u32 v3, s76, v3\n\t" "s_movk_i32 s76, 1280\n\t" "v_mad_u32_u24 v33, v2, s76, v3\n\t" "s_mul_i32 s76, s26, 1152\n\t" "v_mad_u32_u24 v3, v113, 16, v32\n\t" "v_add_u32 v3, s76, v3\n\t" \
  "s_lshl_b32 s76, s35, 3\n\t" "v_add_u32 v113, s76, v113\n\t" "s_branch .Lw1_%=\n\t" ".Lw_%=:\n\t" "s_sleep 2\n\t" ".Lw1_%=:\n\t" "ds_read_b32 v112, v30 offset:24\n\t" \
  "ds_read_b64 v[110:111], v1\n\t" "ds_read_b128 v[12:15], v3 offset:0\n\t" "s_sub_u32 s39, s39, 1\n\t" "s_cmp_lt_i32 s39, 0\n\t" "s_waitcnt lgkmcnt(0)\n\t" "v_readfirstlane_b32 s66, v112\n\t" \
  "s_cselect_b32 s66, 1, s66\n\t" "s_cmp_eq_u32 s66, 0\n\t" "s_cbranch_scc1 .Lw_%=\n\t" "s_and_b32 s76, s66, 63\n\t" "v_cmp_eq_u32 s[68:69], s76, v113\n\t" "v_lshlrev_b32 v120, 16, v12\n\t" \
  "v_and_b32 v121, 0xffff0000, v12\n\t" "v_lshlrev_b32 v122, 16, v13\n\t" "v_and_b32 v123, 0xffff0000, v13\n\t" "v_lshlrev_b32 v124, 16, v14\n\t" "v_and_b32 v125, 0xffff0000, v14\n\t" \
  "v_lshlrev_b32 v126, 16, v15\n\t" "v_and_b32 v127, 0xffff0000, v15\n\t" "v_pk_add_f32 v[128:129], v[120:121], v[110:111] op_sel_hi:[1,0] neg_lo:[0,1] neg_hi:[0,1]\n\t" \
  "v_pk_add_f32 v[130:131], v[122:123], v[110:111] op_sel_hi:[1,0] neg_lo:[0,1] neg_hi:[0,1]\n\t" "v_pk_add_f32 v[132:133], v[124:125], v[110:111] op_sel_hi:[1,0] neg_lo:[0,1] neg_hi:[0,1]\n\t" \
  "v_pk_add_f32 v[134:135], v[126:127], v[110:111] op_sel_hi:[1,0] neg_lo:[0,1] neg_hi:[0,1]\n\t" "v_cvt_pk_bf16_f32 v129, v128, v129\n\t" "v_cvt_pk_bf16_f32 v131, v130, v131\n\t" \
  "v_cvt_pk_bf16_f32 v133, v132, v133\n\t" "v_cvt_pk_bf16_f32 v135, v134, v135\n\t" "v_lshlrev_b32 v128, 16, v129\n\t" "v_and_b32 v129, 0xffff0000, v129\n\t" "v_lshlrev_b32 v130, 16, v131\n\t" \
  "v_and_b32 v131, 0xffff0000, v131\n\t" "v_lshlrev_b32 v132, 16, v133\n\t" "v_and_b32 v133, 0xffff0000, v133\n\t" "v_lshlrev_b32 v134, 16, v135\n\t" "v_and_b32 v135, 0xffff0000, v135\n\t" \
  "v_pk_mul_f32 v[128:129], v[128:129], v[110:111] op_sel:[0,1] op_sel_hi:[1,1]\n\t" "v_pk_mul_f32 v[130:131], v[130:131], v[110:111] op_sel:[0,1] op_sel_hi:[1,1]\n\t" \
  "v_pk_mul_f32 v[132:133], v[132:133], v[110:111] op_sel:[0,1] op_sel_hi:[1,1]\n\t" "v_pk_mul_f32 v[134:135], v[134:135], v[110:111] op_sel:[0,1] op_sel_hi:[1,1]\n\t" \
  "v_cvt_pk_bf16_f32 v129, v128, v129\n\t" "v_cvt_pk_bf16_f32 v131, v130, v131\n\t" "v_cvt_pk_bf16_f32 v133, v132, v133\n\t" "v_cvt_pk_bf16_f32 v135, v134, v135\n\t" "v_lshlrev_b32 v128, 16, v129\n\t" \
  "v_and_b32 v129, 0xffff0000, v129\n\t" "v_lshlrev_b32 v130, 16, v131\n\t" "v_and_b32 v131, 0xffff0000, v131\n\t" "v_lshlrev_b32 v132, 16, v133\n\t" "v_and_b32 v133, 0xffff0000, v133\n\t" \
  "v_lshlrev_b32 v134, 16, v135\n\t" "v_and_b32 v135, 0xffff0000, v135\n\t" "v_pk_add_f32 v[128:129], v[128:129], v[120:121]\n\t" "v_pk_add_f32 v[130:131], v[130:131], v[122:123]\n\t" \
  "v_pk_add_f32 v[132:133], v[132:133], v[124:125]\n\t" "v_pk_add_f32 v[134:135], v[134:135], v[126:127]\n\t" "v_cvt_pk_bf16_f32 v116, v128, v129\n\t" "v_cvt_pk_bf16_f32 v117, v130, v131\n\t" \
  "v_cvt_pk_bf16_f32 v118, v132, v133\n\t" "v_cvt_pk_bf16_f32 v119, v134, v135\n\t" "s_sleep 16\n\t" "s_andn2_b64 exec, s[72:73], s[68:69]\n\t" "s_cbranch_scc0 .Ln1_%=\n\t" \
  "global_store_dwordx4 v33, v[116:119], s[22:23] sc0 sc1 nt\n\t" ".Ln1_%=:\n\t" "s_mov_b64 exec, -1\n\t" "s_cmp_eq_u32 s26, 11\n\t" "s_cbranch_scc1 .Lrz_%=\n\t" "s_cmp_eq_u64 s[68:69], 0\n\t" \
  "s_cbranch_scc1 .Lend_%=\n\t" ".Lb6_%=:\n\t" "s_waitcnt lgkmcnt(0)\n\t" "s_barrier\n\t" "ds_read_b32 v39, v37 offset:32\n\t" "s_waitcnt lgkmcnt(0)\n\t" "v_readfirstlane_b32 s76, v39\n\t" \
  "s_xor_b32 s76, s76, s66\n\t" "s_and_b32 s76, s76, 4095\n\t" "s_cbranch_scc0 .Lend_%=\n\t" "s_and_b64 exec, s[72:73], s[68:69]\n\t" "s_cbranch_scc0 .Ln2_%=\n\t" \
  "global_store_dwordx4 v33, v[116:119], s[22:23] sc0 sc1 nt\n\t" ".Ln2_%=:\n\t" "s_mov_b64 exec, -1\n\t" "s_branch .Lend_%=\n\t" ".Lrz_%=:\n\t" "s_sleep 35\n\t" ".Lrt_%=:\n\t" \
  "s_mul_i32 s40, s28, 6144\n\t" "s_add_u32 s40, s40, 5302784\n\t" "v_lshlrev_b32 v2, 7, v8\n\t" "s_add_u32 s40, s24, s40\n\t" "s_addc_u32 s41, s25, 0\n\t" "v_mov_b32 v40, -1\n\t" \
  "v_mov_b32 v41, -1\n\t" "v_mov_b32 v42, -1\n\t" "s_mov_b32 exec_hi, 0x3fff\n\t" "global_load_dword v40, v2, s[40:41] sc1\n\t" "s_sleep 2\n\t" "global_load_dword v41, v2, s[40:41] sc1\n\t" \
  "s_sleep 2\n\t" "global_load_dword v42, v2, s[40:41] sc1\n\t" "s_mov_b32 exec_hi, -1\n\t" "v_mov_b32 v43, 0xfc0\n\t" "s_mov_b32 s42, 0\n\t" "s_mov_b32 s43, 0xffff0000\n\t" \
  "v_add_u32 v3, 64, v37\n\t" "v_cndmask_b32 v3, v3, v37, s[42:43]\n\t" "s_mov_b32 s43, 0xffffc000\n\t" ".Lrl_%=:\n\t" "s_waitcnt vmcnt(2)\n\t" "v_cmp_ne_u32 vcc, -1, v40\n\t" \
  "s_or_b64 s[42:43], s[42:43], vcc\n\t" "s_cmp_eq_u64 s[42:43], -1\n\t" "v_cndmask_b32 v43, v43, v40, vcc\n\t" "s_cselect_b32 s44, 1, 0\n\t" "v_mov_b32 v45, s44\n\t" \
  "v_max_u32_dpp v44, v43, v43 quad_perm:[1,0,3,2] row_mask:0xf bank_mask:0xf\n\t" "s_nop 1\n\t" "v_max_u32_dpp v44, v44, v44 quad_perm:[2,3,0,1] row_mask:0xf bank_mask:0xf\n\t" "s_nop 1\n\t" \
  "v_max_u32_dpp v44, v44, v44 row_half_mirror row_mask:0xf bank_mask:0xf\n\t" "s_nop 1\n\t" "v_max_u32_dpp v44, v44, v44 row_mirror row_mask:0xf bank_mask:0xf\n\t" "s_nop 1\n\t" \
  "v_max_u32_dpp v44, v44, v44 row_bcast:15 row_mask:0xa bank_mask:0xf\n\t" "s_nop 1\n\t" "v_max_u32_dpp v44, v44, v44 row_bcast:31 row_mask:0xc bank_mask:0xf\n\t" \
  "ds_write_b64 v3, v[44:45] offset:32\n\t" "s_cbranch_scc1 .Lre_%=\n\t" "s_bcnt1_i32_b64 s45, s[42:43]\n\t" "s_mul_i32 s45, s45, s75\n\t" "s_cmp_ge_u32 s45, 19\n\t" "s_cbranch_scc1 .Lrz0_%=\n\t" \
  "s_sleep 5\n\t" ".Lrz0_%=:\n\t" "s_not_b64 exec, s[42:43]\n\t" "global_load_dword v40, v2, s[40:41] sc1\n\t" "s_mov_b64 exec, -1\n\t" "s_waitcnt vmcnt(2)\n\t" "v_cmp_ne_u32 vcc, -1, v41\n\t" \
  "s_or_b64 s[42:43], s[42:43], vcc\n\t" "s_cmp_eq_u64 s[42:43], -1\n\t" "v_cndmask_b32 v43, v43, v41, vcc\n\t" "s_cselect_b32 s44, 1, 0\n\t" "v_mov_b32 v45, s44\n\t" \
  "v_max_u32_dpp v44, v43, v43 quad_perm:[1,0,3,2] row_mask:0xf bank_mask:0xf\n\t" "s_nop 1\n\t" "v_max_u32_dpp v44, v44, v44 quad_perm:[2,3,0,1] row_mask:0xf bank_mask:0xf\n\t" "s_nop 1\n\t" \
  "v_max_u32_dpp v44, v44, v44 row_half_mirror row_mask:0xf bank_mask:0xf\n\t" "s_nop 1\n\t" "v_max_u32_dpp v44, v44, v44 row_mirror row_mask:0xf bank_mask:0xf\n\t" "s_nop 1\n\t" \
  "v_max_u32_dpp v44, v44, v44 row_bcast:15 row_mask:0xa bank_mask:0xf\n\t" "s_nop 1\n\t" "v_max_u32_dpp v44, v44, v44 row_bcast:31 row_mask:0xc bank_mask:0xf\n\t" \
  "ds_write_b64 v3, v[44:45] offset:32\n\t" "s_cbranch_scc1 .Lre_%=\n\t" "s_bcnt1_i32_b64 s45, s[42:43]\n\t" "s_mul_i32 s45, s45, s75\n\t" "s_cmp_ge_u32 s45, 19\n\t" "s_cbranch_scc1 .Lrz1_%=\n\t" \
  "s_sleep 5\n\t" ".Lrz1_%=:\n\t" "s_not_b64 exec, s[42:43]\n\t" "global_load_dword v41, v2, s[40:41] sc1\n\t" "s_mov_b64 exec, -1\n\t" "s_waitcnt vmcnt(2)\n\t" "v_cmp_ne_u32 vcc, -1, v42\n\t" \
  "s_or_b64 s[42:43], s[42:43], vcc\n\t" "s_cmp_eq_u64 s[42:43], -1\n\t" "v_cndmask_b32 v43, v43, v42, vcc\n\t" "s_cselect_b32 s44, 1, 0\n\t" "v_mov_b32 v45, s44\n\t" \
  "v_max_u32_dpp v44, v43, v43 quad_perm:[1,0,3,2] row_mask:0xf bank_mask:0xf\n\t" "s_nop 1\n\t" "v_max_u32_dpp v44, v44, v44 quad_perm:[2,3,0,1] row_mask:0xf bank_mask:0xf\n\t" "s_nop 1\n\t" \
  "v_max_u32_dpp v44, v44, v44 row_half_mirror row_mask:0xf bank_mask:0xf\n\t" "s_nop 1\n\t" "v_max_u32_dpp v44, v44, v44 row_mirror row_mask:0xf bank_mask:0xf\n\t" "s_nop 1\n\t" \
  "v_max_u32_dpp v44, v44, v44 row_bcast:15 row_mask:0xa bank_mask:0xf\n\t" "s_nop 1\n\t" "v_max_u32_dpp v44, v44, v44 row_bcast:31 row_mask:0xc bank_mask:0xf\n\t" \
  "ds_write_b64 v3, v[44:45] offset:32\n\t" "s_cbranch_scc1 .Lre_%=\n\t" "s_bcnt1_i32_b64 s45, s[42:43]\n\t" "s_mul_i32 s45, s45, s75\n\t" "s_cmp_ge_u32 s45, 19\n\t" "s_cbranch_scc1 .Lrz2_%=\n\t" \
  "s_sleep 5\n\t" ".Lrz2_%=:\n\t" "s_not_b64 exec, s[42:43]\n\t" "global_load_dword v42, v2, s[40:41] sc1\n\t" "s_mov_b64 exec, -1\n\t" "s_sub_u32 s39, s39, 1\n\t" "s_cmp_lt_i32 s39, 0\n\t" \
  "s_cbranch_scc0 .Lrl_%=\n\t" ".Lre_%=:\n\t" "s_cmp_eq_u32 s75, 0\n\t" "s_cbranch_scc1 .Lb6_%=\n\t" "s_waitcnt lgkmcnt(0)\n\t" "s_barrier\n\t" "s_barrier\n\t" "s_cmp_lg_u32 s29, 0\n\t" \
  "s_cbranch_scc1 .Lend_%=\n\t" "v_mov_b32 v1, 0\n\t" "s_mov_b64 exec, 1\n\t" "global_store_dwordx2 v1, v[16:17], s[24:25] sc1\n\t" "s_mov_b64 exec, -1\n\t" "s_branch .Lend_%=\n\t" ".Lsp_%=:\n\t" \
  "v_mov_b32 v1, 0\n\t" "s_add_u32 s40, s28, 16\n\t" "s_xor_b32 s41, %[bid], 2\n\t" "s_sub_u32 s41, s41, 240\n\t" "s_cselect_b32 s29, s40, s41\n\t" "v_lshlrev_b32 v32, 4, v8\n\t" \
  "v_and_b32 v9, 15, v8\n\t" "v_mov_b32 v37, 156416\n\t" "v_mov_b32 v38, 1\n\t" "v_mov_b32 v44, -1\n\t" "v_mov_b32 v45, -1\n\t" "v_mov_b32 v46, -1\n\t" "v_mov_b32 v47, -1\n\t" "s_mov_b32 s75, 1\n\t" \
  "s_mov_b32 s39, 400000\n\t" "s_add_u32 s45, s29, 4\n\t" "s_lshr_b32 s45, s45, 3\n\t" "s_sub_u32 s45, s29, s45\n\t" "s_and_b32 s48, s29, 7\n\t" "s_mul_i32 s46, s45, 232\n\t" "s_movk_i32 s47, 231\n\t" \
  "s_mul_i32 s40, s45, 764\n\t" "s_sub_u32 s40, s40, 11172\n\t" "s_cmp_ge_u32 s29, 24\n\t" "s_cselect_b32 s46, s40, s46\n\t" "s_cselect_b32 s47, 763, s47\n\t" "v_min_u32 v5, s47, v0\n\t" \
  "v_add_u32 v5, s46, v5\n\t" "v_min_u32 v5, 6399, v5\n\t" "v_lshlrev_b32 v5, 7, v5\n\t" "s_lshr_b32 s47, s47, 6\n\t" "s_mul_i32 s40, s45, 12\n\t" "s_add_u32 s40, s40, s26\n\t" \
  "s_lshl_b32 s41, s40, 10\n\t" "s_add_u32 s41, s41, 3416064\n\t" "s_lshl_b32 s44, s45, 7\n\t" "s_add_u32 s44, s44, 4096\n\t" "v_lshl_add_u32 v4, v9, 3, s44\n\t" "s_waitcnt lgkmcnt(0)\n\t" \
  "global_load_dwordx2 v[40:41], v1, s[24:25] sc1\n\t" "s_add_u32 s42, s24, s41\n\t" "s_addc_u32 s43, s25, 0\n\t" "v_lshl_add_u32 v3, v8, 2, v37\n\t" "s_movk_i32 s44, 0xfc0\n\t" "v_mov_b32 v2, 0\n\t" \
  "v_writelane_b32 v2, s44, 8\n\t" "ds_write_b32 v3, v2\n\t" "s_mul_i32 s50, s29, 81920\n\t" "s_mul_i32 s54, s33, 20480\n\t" "s_add_u32 s50, s50, s54\n\t" "s_lshl_b32 s54, s33, 11\n\t" \
  "s_add_u32 s54, s54, 65536\n\t" "s_add_u32 s52, s20, s50\n\t" "s_addc_u32 s53, s21, 0\n\t" "v_lshrrev_b32 v20, 3, v8\n\t" "v_and_b32 v21, 6, v20\n\t" "v_xor_b32 v21, v21, v8\n\t" \
  "v_and_b32 v21, 7, v21\n\t" "v_mul_u32_u24 v20, 1280, v20\n\t" "v_lshl_add_u32 v20, v21, 4, v20\n\t" "v_add_u32 v21, 9216, v20\n\t" "s_memrealtime s[56:57]\n\t" "s_mov_b32 m0, 163072\n\t" \
  "s_cmp_eq_u32 s48, 3\n\t" "s_cbranch_scc1 .Lnf_%=\n\t" ".Lts2_%=:\n\t" "s_sleep 2\n\t" "s_memrealtime s[78:79]\n\t" "s_waitcnt lgkmcnt(0)\n\t" "s_sub_u32 s78, s78, s56\n\t" \
  "s_cmp_lt_u32 s78, 255\n\t" "s_cbranch_scc1 .Lts2_%=\n\t" ".Lpf0_%=:\n\t" "global_store_dwordx4 v32, v[44:47], s[42:43] sc1\n\t" "s_add_u32 s42, s42, 282624\n\t" "s_addc_u32 s43, s43, 0\n\t" \
  "s_add_u32 s40, s40, 276\n\t" "s_cmp_lt_u32 s40, 1380\n\t" "s_cbranch_scc1 .Lpf0_%=\n\t" ".Lpf1_%=:\n\t" "global_store_dwordx4 v32, v[44:47], s[42:43] sc1\n\t" "s_add_u32 s42, s42, 282624\n\t" \
  "s_addc_u32 s43, s43, 0\n\t" "s_add_u32 s40, s40, 276\n\t" "s_cmp_lt_u32 s40, 1903\n\t" "s_cbranch_scc1 .Lpf1_%=\n\t" "s_waitcnt vmcnt(0)\n\t" "v_add_co_u32 v16, vcc, 1, v40\n\t" \
  "s_cmp_lg_u32 s26, 0\n\t" "s_nop 0\n\t" "v_addc_co_u32 v17, vcc, 0, v41, vcc\n\t" "s_barrier\n\t" "s_cbranch_scc1 .Lnf1_%=\n\t" "global_store_dwordx2 v4, v[16:17], s[24:25] sc1\n\t" ".Lnf1_%=:\n\t" \
  "s_cmp_gt_u32 s26, s47\n\t" "s_cbranch_scc1 .Lnt_%=\n\t" "global_load_lds_dword v5, s[12:13]\n\t" ".Lnt_%=:\n\t" ".Lnf_%=:\n\t" "s_cmp_ge_u32 s29, 24\n\t" "s_cbranch_scc1 .Lend_%=\n\t" \
  "s_cmp_ge_u32 s26, 8\n\t" "s_cbranch_scc1 .Lsq_%=x\n\t" "v_lshrrev_b32 v1, 3, v8\n\t" "v_and_b32 v2, 6, v1\n\t" "v_xor_b32 v2, v2, v8\n\t" "v_and_b32 v2, 7, v2\n\t" "v_mul_u32_u24 v1, 3072, v1\n\t" \
  "v_lshl_add_u32 v1, v2, 4, v1\n\t" "v_add_u32 v17, 23552, v1\n\t" "v_add_u32 v18, 47104, v1\n\t" "v_add_u32 v19, 70656, v1\n\t" "s_cmp_ge_u32 s26, 4\n\t" \
  "s_cselect_b64 s[42:43], s[18:19], s[16:17]\n\t" "s_mul_i32 s40, s33, 491520\n\t" "s_lshl_b32 s41, s29, 7\n\t" "s_add_u32 s40, s40, s41\n\t" "s_add_u32 s42, s42, s40\n\t" \
  "s_addc_u32 s43, s43, 0\n\t" "s_lshl_b32 s44, s26, 13\n\t" "s_add_u32 m0, s44, 0\n\t" "s_nop 0\n\t" "global_load_lds_dwordx4 v1, s[42:43] offset:0 nt\n\t" \
  "global_load_lds_dwordx4 v17, s[42:43] offset:1024 nt\n\t" "global_load_lds_dwordx4 v18, s[42:43] offset:2048 nt\n\t" "global_load_lds_dwordx4 v19, s[42:43] offset:3072 nt\n\t" \
  "s_add_u32 s42, s42, 98304\n\t" "s_addc_u32 s43, s43, 0\n\t" "s_add_u32 m0, s44, 4096\n\t" "s_nop 0\n\t" "global_load_lds_dwordx4 v1, s[42:43] offset:0 nt\n\t" \
  "global_load_lds_dwordx4 v17, s[42:43] offset:1024 nt\n\t" "global_load_lds_dwordx4 v18, s[42:43] offset:2048 nt\n\t" "global_load_lds_dwordx4 v19, s[42:43] offset:3072 nt\n\t" \
  "s_add_u32 s42, s42, 98304\n\t" "s_addc_u32 s43, s43, 0\n\t" "v_lshrrev_b32 v10, 2, v8\n\t" "v_lshlrev_b32 v3, 2, v8\n\t" "v_and_b32 v4, 0x60, v3\n\t" "v_and_b32 v3, 12, v3\n\t" \
  "v_lshl_add_u32 v2, v10, 7, v4\n\t" "v_lshl_add_u32 v2, v3, 1, v2\n\t" "s_mul_i32 s40, s26, 5\n\t" "s_add_u32 s40, s40, 0\n\t" "s_and_b32 s41, s40, 3\n\t" "s_lshl_b32 s41, s41, 5\n\t" \
  "s_lshr_b32 s40, s40, 2\n\t" "s_lshl_b32 s40, s40, 13\n\t" "s_add_u32 s40, s40, 65536\n\t" "v_xor_b32 v20, s41, v2\n\t" "v_add_u32 v20, s40, v20\n\t" "s_mul_i32 s40, s26, 5\n\t" \
  "s_add_u32 s40, s40, 1\n\t" "s_and_b32 s41, s40, 3\n\t" "s_lshl_b32 s41, s41, 5\n\t" "s_lshr_b32 s40, s40, 2\n\t" "s_lshl_b32 s40, s40, 13\n\t" "s_add_u32 s40, s40, 65536\n\t" \
  "v_xor_b32 v21, s41, v2\n\t" "v_add_u32 v21, s40, v21\n\t" "s_mul_i32 s40, s26, 5\n\t" "s_add_u32 s40, s40, 2\n\t" "s_and_b32 s41, s40, 3\n\t" "s_lshl_b32 s41, s41, 5\n\t" \
  "s_lshr_b32 s40, s40, 2\n\t" "s_lshl_b32 s40, s40, 13\n\t" "s_add_u32 s40, s40, 65536\n\t" "v_xor_b32 v22, s41, v2\n\t" "v_add_u32 v22, s40, v22\n\t" "s_mul_i32 s40, s26, 5\n\t" \
  "s_add_u32 s40, s40, 3\n\t" "s_and_b32 s41, s40, 3\n\t" "s_lshl_b32 s41, s41, 5\n\t" "s_lshr_b32 s40, s40, 2\n\t" "s_lshl_b32 s40, s40, 13\n\t" "s_add_u32 s40, s40, 65536\n\t" \
  "v_xor_b32 v23, s41, v2\n\t" "v_add_u32 v23, s40, v23\n\t" "s_mul_i32 s40, s26, 5\n\t" "s_add_u32 s40, s40, 4\n\t" "s_and_b32 s41, s40, 3\n\t" "s_lshl_b32 s41, s41, 5\n\t" \
  "s_lshr_b32 s40, s40, 2\n\t" "s_lshl_b32 s40, s40, 13\n\t" "s_add_u32 s40, s40, 65536\n\t" "v_xor_b32 v24, s41, v2\n\t" "v_add_u32 v24, s40, v24\n\t" "v_and_b32 v10, 12, v10\n\t" \
  "v_xor_b32 v12, 0, v2\n\t" "v_add_u32 v12, s44, v12\n\t" "v_xor_b32 v13, 32, v2\n\t" "v_add_u32 v13, s44, v13\n\t" "v_xor_b32 v14, 64, v2\n\t" "v_add_u32 v14, s44, v14\n\t" \
  "v_xor_b32 v15, 96, v2\n\t" "v_add_u32 v15, s44, v15\n\t" "s_waitcnt vmcnt(4)\n\t" "ds_read_b64_tr_b16 v[40:41], v12 offset:0\n\t" "ds_read_b64_tr_b16 v[42:43], v12 offset:2048\n\t" \
  "ds_read_b64_tr_b16 v[44:45], v13 offset:0\n\t" "ds_read_b64_tr_b16 v[46:47], v13 offset:2048\n\t" "ds_read_b64_tr_b16 v[48:49], v14 offset:0\n\t" "ds_read_b64_tr_b16 v[50:51], v14 offset:2048\n\t" \
  "ds_read_b64_tr_b16 v[52:53], v15 offset:0\n\t" "ds_read_b64_tr_b16 v[54:55], v15 offset:2048\n\t" "s_waitcnt lgkmcnt(0)\n\t" "s_add_u32 m0, s44, 0\n\t" "s_nop 0\n\t" \
  "global_load_lds_dwordx4 v1, s[42:43] offset:0 nt\n\t" "global_load_lds_dwordx4 v17, s[42:43] offset:1024 nt\n\t" "global_load_lds_dwordx4 v18, s[42:43] offset:2048 nt\n\t" \
  "global_load_lds_dwordx4 v19, s[42:43] offset:3072 nt\n\t" "s_add_u32 s42, s42, 98304\n\t" "s_addc_u32 s43, s43, 0\n\t" "s_waitcnt vmcnt(4)\n\t" "ds_read_b64_tr_b16 v[56:57], v12 offset:4096\n\t" \
  "ds_read_b64_tr_b16 v[58:59], v12 offset:6144\n\t" "ds_read_b64_tr_b16 v[60:61], v13 offset:4096\n\t" "ds_read_b64_tr_b16 v[62:63], v13 offset:6144\n\t" \
  "ds_read_b64_tr_b16 v[64:65], v14 offset:4096\n\t" "ds_read_b64_tr_b16 v[66:67], v14 offset:6144\n\t" "ds_read_b64_tr_b16 v[68:69], v15 offset:4096\n\t" \
  "ds_read_b64_tr_b16 v[70:71], v15 offset:6144\n\t" "s_waitcnt lgkmcnt(0)\n\t" "s_add_u32 m0, s44, 4096\n\t" "s_nop 0\n\t" "global_load_lds_dwordx4 v1, s[42:43] offset:0 nt\n\t" \
  "global_load_lds_dwordx4 v17, s[42:43] offset:1024 nt\n\t" "global_load_lds_dwordx4 v18, s[42:43] offset:2048 nt\n\t" "global_load_lds_dwordx4 v19, s[42:43] offset:3072 nt\n\t" \
  "s_add_u32 s42, s42, 98304\n\t" "s_addc_u32 s43, s43, 0\n\t" "s_waitcnt vmcnt(4)\n\t" "ds_read_b64_tr_b16 v[72:73], v12 offset:0\n\t" "ds_read_b64_tr_b16 v[74:75], v12 offset:2048\n\t" \
  "ds_read_b64_tr_b16 v[76:77], v13 offset:0\n\t" "ds_read_b64_tr_b16 v[78:79], v13 offset:2048\n\t" "ds_read_b64_tr_b16 v[80:81], v14 offset:0\n\t" "ds_read_b64_tr_b16 v[82:83], v14 offset:2048\n\t" \
  "ds_read_b64_tr_b16 v[84:85], v15 offset:0\n\t" "ds_read_b64_tr_b16 v[86:87], v15 offset:2048\n\t" "s_waitcnt lgkmcnt(0)\n\t" "s_add_u32 m0, s44, 0\n\t" "s_nop 0\n\t" \
  "global_load_lds_dwordx4 v1, s[42:43] offset:0 nt\n\t" "global_load_lds_dwordx4 v17, s[42:43] offset:1024 nt\n\t" "global_load_lds_dwordx4 v18, s[42:43] offset:2048 nt\n\t" \
  "global_load_lds_dwordx4 v19, s[42:43] offset:3072 nt\n\t" "s_barrier\n\t" "ds_read_b64_tr_b16 v[120:121], v20 offset:0\n\t" "ds_read_b64_tr_b16 v[122:123], v20 offset:2048\n\t" \
  "ds_read_b64_tr_b16 v[124:125], v20 offset:4096\n\t" "ds_read_b64_tr_b16 v[126:127], v20 offset:6144\n\t" "ds_read_b64_tr_b16 v[128:129], v21 offset:0\n\t" \
  "ds_read_b64_tr_b16 v[130:131], v21 offset:2048\n\t" "ds_read_b64_tr_b16 v[132:133], v21 offset:4096\n\t" "ds_read_b64_tr_b16 v[134:135], v21 offset:6144\n\t" \
  "ds_read_b64_tr_b16 v[136:137], v22 offset:0\n\t" "ds_read_b64_tr_b16 v[138:139], v22 offset:2048\n\t" "ds_read_b64_tr_b16 v[140:141], v22 offset:4096\n\t" \
  "ds_read_b64_tr_b16 v[142:143], v22 offset:6144\n\t" "ds_read_b64_tr_b16 v[144:145], v23 offset:0\n\t" "ds_read_b64_tr_b16 v[146:147], v23 offset:2048\n\t" \
  "ds_read_b64_tr_b16 v[148:149], v23 offset:4096\n\t" "ds_read_b64_tr_b16 v[150:151], v23 offset:6144\n\t" "ds_read_b64_tr_b16 v[152:153], v24 offset:0\n\t" \
  "ds_read_b64_tr_b16 v[154:155], v24 offset:2048\n\t" "ds_read_b64_tr_b16 v[156:157], v24 offset:4096\n\t" "ds_read_b64_tr_b16 v[158:159], v24 offset:6144\n\t" "s_waitcnt vmcnt(4)\n\t" \
  "ds_read_b64_tr_b16 v[88:89], v12 offset:4096\n\t" "ds_read_b64_tr_b16 v[90:91], v12 offset:6144\n\t" "ds_read_b64_tr_b16 v[92:93], v13 offset:4096\n\t" \
  "ds_read_b64_tr_b16 v[94:95], v13 offset:6144\n\t" "ds_read_b64_tr_b16 v[96:97], v14 offset:4096\n\t" "ds_read_b64_tr_b16 v[98:99], v14 offset:6144\n\t" \
  "ds_read_b64_tr_b16 v[100:101], v15 offset:4096\n\t" "ds_read_b64_tr_b16 v[102:103], v15 offset:6144\n\t" "s_waitcnt vmcnt(0)\n\t" "ds_read_b64_tr_b16 v[104:105], v12 offset:0\n\t" \
  "ds_read_b64_tr_b16 v[106:107], v12 offset:2048\n\t" "ds_read_b64_tr_b16 v[108:109], v13 offset:0\n\t" "ds_read_b64_tr_b16 v[110:111], v13 offset:2048\n\t" \
  "ds_read_b64_tr_b16 v[112:113], v14 offset:0\n\t" "ds_read_b64_tr_b16 v[114:115], v14 offset:2048\n\t" "ds_read_b64_tr_b16 v[116:117], v15 offset:0\n\t" \
  "ds_read_b64_tr_b16 v[118:119], v15 offset:2048\n\t" "s_mul_i32 s40, s33, 320\n\t" "s_add_u32 s40, s40, 159744\n\t" "v_lshl_add_u32 v1, v10, 1, s40\n\t" "s_lshl_b32 s40, s26, 8\n\t" \
  "s_add_u32 s40, s40, 161024\n\t" "v_lshl_add_u32 v2, v10, 2, s40\n\t" "v_and_b32 v4, 12, v8\n\t" "v_lshl_add_u32 v4, v4, 2, v10\n\t" "s_mov_b32 s40, 161024\n\t" "v_lshl_add_u32 v4, v4, 2, s40\n\t" \
  "v_add_u32 v3, v3, v4\n\t" "s_mul_i32 s40, s26, 320\n\t" "v_lshl_add_u32 v4, v9, 6, s40\n\t" "v_lshl_add_u32 v4, v10, 2, v4\n\t" "s_mul_i32 s40, s29, 2560\n\t" "s_add_u32 s40, s40, 5241344\n\t" \
  "s_add_u32 s62, s24, s40\n\t" "s_addc_u32 s63, s25, 0\n\t" "s_mov_b32 s40, 0x10001\n\t" "s_mov_b32 s41, s40\n\t" "s_lshl_b64 s[42:43], s[40:41], 1\n\t" "s_lshl_b64 s[44:45], s[40:41], 2\n\t" \
  "s_lshl_b64 s[46:47], s[40:41], 3\n\t" "s_lshl_b64 s[48:49], s[40:41], 4\n\t" "s_mov_b32 s60, 0x1f001f\n\t" "s_mov_b32 s61, s60\n\t" "s_mov_b32 s76, 0xbfb8aa3b\n\t" "s_setprio 3\n\t" \
  "s_waitcnt lgkmcnt(0)\n\t" "s_barrier\n\t" "ds_read2_b64 v[28:31], v1 offset0:0 offset1:4\n\t" "ds_read2_b64 v[32:35], v1 offset0:8 offset1:12\n\t" \
  "ds_read2_b64 v[36:39], v1 offset0:16 offset1:20\n\t" "ds_read2_b64 v[160:163], v1 offset0:24 offset1:28\n\t" "ds_read2_b64 v[164:167], v1 offset0:32 offset1:36\n\t" "s_waitcnt lgkmcnt(4)\n\t" \
  "v_mfma_f32_16x16x32_bf16 v[12:15], v[40:43], v[28:31], 0\n\t" "v_mfma_f32_16x16x32_bf16 v[16:19], v[44:47], v[28:31], 0\n\t" "v_mfma_f32_16x16x32_bf16 v[20:23], v[48:51], v[28:31], 0\n\t" \
  "v_mfma_f32_16x16x32_bf16 v[24:27], v[52:55], v[28:31], 0\n\t" "s_waitcnt lgkmcnt(3)\n\t" "v_mfma_f32_16x16x32_bf16 v[12:15], v[56:59], v[32:35], v[12:15]\n\t" \
  "v_mfma_f32_16x16x32_bf16 v[16:19], v[60:63], v[32:35], v[16:19]\n\t" "v_mfma_f32_16x16x32_bf16 v[20:23], v[64:67], v[32:35], v[20:23]\n\t" \
  "v_mfma_f32_16x16x32_bf16 v[24:27], v[68:71], v[32:35], v[24:27]\n\t" "s_waitcnt lgkmcnt(2)\n\t" "v_mfma_f32_16x16x32_bf16 v[12:15], v[72:75], v[36:39], v[12:15]\n\t" \
  "v_mfma_f32_16x16x32_bf16 v[16:19], v[76:79], v[36:39], v[16:19]\n\t" "v_mfma_f32_16x16x32_bf16 v[20:23], v[80:83], v[36:39], v[20:23]\n\t" \
  "v_mfma_f32_16x16x32_bf16 v[24:27], v[84:87], v[36:39], v[24:27]\n\t" "s_waitcnt lgkmcnt(1)\n\t" "v_mfma_f32_16x16x32_bf16 v[12:15], v[88:91], v[160:163], v[12:15]\n\t" \
  "v_mfma_f32_16x16x32_bf16 v[16:19], v[92:95], v[160:163], v[16:19]\n\t" "v_mfma_f32_16x16x32_bf16 v[20:23], v[96:99], v[160:163], v[20:23]\n\t" \
  "v_mfma_f32_16x16x32_bf16 v[24:27], v[100:103], v[160:163], v[24:27]\n\t" "s_waitcnt lgkmcnt(0)\n\t" "v_mfma_f32_16x16x32_bf16 v[12:15], v[104:107], v[164:167], v[12:15]\n\t" \
  "v_mfma_f32_16x16x32_bf16 v[16:19], v[108:111], v[164:167], v[16:19]\n\t" "v_mfma_f32_16x16x32_bf16 v[20:23], v[112:115], v[164:167], v[20:23]\n\t" \
  "v_mfma_f32_16x16x32_bf16 v[24:27], v[116:119], v[164:167], v[24:27]\n\t" "s_mov_b64 exec, s[40:41]\n\t" "s_nop 3\n\t" "ds_write_b128 v2, v[12:15] offset:0\n\t" \
  "ds_write_b128 v2, v[16:19] offset:64\n\t" "ds_write_b128 v2, v[20:23] offset:128\n\t" "ds_write_b128 v2, v[24:27] offset:192\n\t" "s_mov_b64 exec, -1\n\t" "s_waitcnt lgkmcnt(0)\n\t" "s_barrier\n\t" \
  "ds_read2st64_b32 v[40:41], v3 offset0:0 offset1:4\n\t" "ds_read2st64_b32 v[42:43], v3 offset0:1 offset1:5\n\t" "ds_read2st64_b32 v[44:45], v3 offset0:2 offset1:6\n\t" \
  "ds_read2st64_b32 v[46:47], v3 offset0:3 offset1:7\n\t" "s_waitcnt lgkmcnt(0)\n\t" "v_pk_add_f32 v[40:41], v[40:41], v[42:43]\n\t" "v_pk_add_f32 v[44:45], v[44:45], v[46:47]\n\t" \
  "v_pk_add_f32 v[40:41], v[40:41], v[44:45]\n\t" "v_cvt_pk_bf16_f32 v41, v40, v41\n\t" "v_lshlrev_b32 v40, 16, v41\n\t" "v_mul_f32 v42, s76, v40\n\t" "v_exp_f32 v42, v42\n\t" \
  "v_and_b32 v41, 0xffff0000, v41\n\t" "v_add_f32 v42, 1.0, v42\n\t" "v_rcp_f32 v42, v42\n\t" "s_nop 0\n\t" "v_mul_f32 v40, v40, v42\n\t" "v_cvt_pk_bf16_f32 v40, v40, v40\n\t" \
  "v_lshlrev_b32 v40, 16, v40\n\t" "v_mul_f32 v40, v40, v41\n\t" "s_nop 1\n\t" "v_mov_b32_dpp v41, v40 quad_perm:[1,1,3,3] row_mask:0xf bank_mask:0xf\n\t" "v_cvt_pk_bf16_f32 v40, v40, v41\n\t" \
  "s_nop 1\n\t" "v_mov_b32_dpp v44, v40 row_newbcast:0 row_mask:0xf bank_mask:0xf\n\t" "v_mov_b32_dpp v45, v40 row_newbcast:2 row_mask:0xf bank_mask:0xf\n\t" \
  "v_mov_b32_dpp v46, v40 row_newbcast:4 row_mask:0xf bank_mask:0xf\n\t" "v_mov_b32_dpp v47, v40 row_newbcast:6 row_mask:0xf bank_mask:0xf\n\t" \
  "v_mov_b32_dpp v48, v40 row_newbcast:8 row_mask:0xf bank_mask:0xf\n\t" "v_mov_b32_dpp v49, v40 row_newbcast:10 row_mask:0xf bank_mask:0xf\n\t" \
  "v_mov_b32_dpp v50, v40 row_newbcast:12 row_mask:0xf bank_mask:0xf\n\t" "v_mov_b32_dpp v51, v40 row_newbcast:14 row_mask:0xf bank_mask:0xf\n\t" \
  "v_mfma_f32_16x16x32_bf16 v[56:59], v[120:123], v[44:47], 0\n\t" "s_nop 0\n\t" "v_mfma_f32_16x16x32_bf16 v[56:59], v[124:127], v[48:51], v[56:59]\n\t" \
  "v_mfma_f32_16x16x32_bf16 v[60:63], v[128:131], v[44:47], 0\n\t" "v_mfma_f32_16x16x32_bf16 v[60:63], v[132:135], v[48:51], v[60:63]\n\t" \
  "v_mfma_f32_16x16x32_bf16 v[64:67], v[136:139], v[44:47], 0\n\t" "v_mfma_f32_16x16x32_bf16 v[64:67], v[140:143], v[48:51], v[64:67]\n\t" \
  "v_mfma_f32_16x16x32_bf16 v[68:71], v[144:147], v[44:47], 0\n\t" "v_mfma_f32_16x16x32_bf16 v[68:71], v[148:151], v[48:51], v[68:71]\n\t" \
  "v_mfma_f32_16x16x32_bf16 v[72:75], v[152:155], v[44:47], 0\n\t" "v_mfma_f32_16x16x32_bf16 v[72:75], v[156:159], v[48:51], v[72:75]\n\t" "s_nop 1\n\t" "v_cndmask_b32 v56, v56, v60, s[42:43]\n\t" \
  "v_cndmask_b32 v57, v57, v61, s[42:43]\n\t" "v_cndmask_b32 v58, v58, v62, s[42:43]\n\t" "v_cndmask_b32 v59, v59, v63, s[42:43]\n\t" "v_cndmask_b32 v56, v56, v64, s[44:45]\n\t" \
  "v_cndmask_b32 v57, v57, v65, s[44:45]\n\t" "v_cndmask_b32 v58, v58, v66, s[44:45]\n\t" "v_cndmask_b32 v59, v59, v67, s[44:45]\n\t" "v_cndmask_b32 v56, v56, v68, s[46:47]\n\t" \
  "v_cndmask_b32 v57, v57, v69, s[46:47]\n\t" "v_cndmask_b32 v58, v58, v70, s[46:47]\n\t" "v_cndmask_b32 v59, v59, v71, s[46:47]\n\t" "v_cndmask_b32 v56, v56, v72, s[48:49]\n\t" \
  "v_cndmask_b32 v57, v57, v73, s[48:49]\n\t" "v_cndmask_b32 v58, v58, v74, s[48:49]\n\t" "v_cndmask_b32 v59, v59, v75, s[48:49]\n\t" "s_mov_b64 exec, s[60:61]\n\t" \
  "global_store_dwordx4 v4, v[56:59], s[62:63] sc1\n\t" "s_mov_b64 exec, -1\n\t" "s_branch .Lend_%=\n\t" ".Lsq_%=x:\n\t" "s_setprio 1\n\t" "s_movk_i32 s50, 10\n\t" ".Ld3_%=:\n\t" \
  "s_mov_b32 m0, s54\n\t" "s_add_u32 s54, s54, 8192\n\t" "global_load_lds_dwordx4 v20, s[52:53] nt\n\t" "global_load_lds_dwordx4 v21, s[52:53] offset:1024 nt\n\t" "s_add_u32 s52, s52, 128\n\t" \
  "s_addc_u32 s53, s53, 0\n\t" "s_sub_u32 s50, s50, 1\n\t" "s_cmp_lg_u32 s50, 0\n\t" "s_cbranch_scc1 .Ld3_%=\n\t" "s_movk_i32 s40, 4096\n\t" "v_min_u32 v3, 22, v8\n\t" \
  "v_lshl_add_u32 v3, v3, 7, s40\n\t" "s_cmp_lg_u32 s26, 11\n\t" "s_cbranch_scc1 .Lf11_%=\n\t" "global_load_dwordx2 v[42:43], v3, s[24:25] sc1\n\t" ".Lf11_%=:\n\t" "s_waitcnt vmcnt(0) lgkmcnt(0)\n\t" \
  "s_barrier\n\t" "v_add_co_u32 v16, vcc, 1, v40\n\t" "s_nop 1\n\t" "v_addc_co_u32 v17, vcc, 0, v41, vcc\n\t" "s_mul_i32 s40, s29, 205\n\t" "s_lshr_b32 s40, s40, 11\n\t" "s_mul_i32 s40, s40, 10\n\t" \
  "s_sub_u32 s28, s29, s40\n\t" "s_cmp_lt_u32 s26, 10\n\t" "s_cbranch_scc1 .Lcb_%=\n\t" "s_setprio 3\n\t" "s_cmp_eq_u32 s26, 10\n\t" "s_cbranch_scc1 .Lfe_%=\n\t" "s_branch .Lsf1_%=\n\t" ".Lsf_%=:\n\t" \
  "global_load_dwordx2 v[42:43], v3, s[24:25] sc1\n\t" "s_sub_u32 s39, s39, 1\n\t" "s_cmp_lt_i32 s39, 0\n\t" "s_waitcnt vmcnt(0)\n\t" "s_cbranch_scc1 .Lrt_%=\n\t" ".Lsf1_%=:\n\t" \
  "v_cmp_ne_u64 vcc, v[42:43], v[16:17]\n\t" "s_cbranch_vccnz .Lsf_%=\n\t" "s_branch .Lrt_%=\n\t" "s_xor_b32 s40, s28, 1\n\t" "s_mul_i32 s40, s40, 6144\n\t" "s_add_u32 s40, s40, 5302784\n\t" \
  "v_and_b32 v3, 7, v8\n\t" "v_mul_u32_u24 v3, 640, v3\n\t" "v_add_u32 v3, s40, v3\n\t" "s_branch .Lsk1_%=\n\t" ".Lsk_%=:\n\t" "s_sleep 6\n\t" ".Lsk1_%=:\n\t" \
  "global_load_dword v40, v3, s[24:25] sc1\n\t" "s_sub_u32 s39, s39, 1\n\t" "s_cmp_lt_i32 s39, 0\n\t" "s_waitcnt vmcnt(0)\n\t" "s_cbranch_scc1 .Lrt_%=\n\t" "v_cmp_ne_u32 vcc, -1, v40\n\t" \
  "s_cbranch_vccz .Lsk_%=\n\t" "s_branch .Lrt_%=\n\t" ".Lfe_%=:\n\t" "v_lshrrev_b32 v2, 3, v8\n\t" "v_lshl_add_u32 v2, v2, 7, v32\n\t" "v_lshlrev_b32 v4, 2, v8\n\t" "v_lshrrev_b32 v3, 5, v8\n\t" \
  "v_lshl_add_u32 v3, v3, 7, v4\n\t" "v_add_u32 v4, 160768, v4\n\t" "v_add_u32 v5, 159744, v32\n\t" "s_movk_i32 s44, 63\n\t" ".Lf_%=:\n\t" "ds_read_b64 v[40:41], v37 offset:32\n\t" \
  "s_sub_u32 s39, s39, 1\n\t" "s_cmp_lt_i32 s39, 0\n\t" "s_cselect_b32 s46, 1, 0\n\t" "s_waitcnt lgkmcnt(0)\n\t" "v_readfirstlane_b32 s45, v40\n\t" "v_readfirstlane_b32 s47, v41\n\t" \
  "s_bfe_u32 s45, s45, 0x60006\n\t" "s_or_b32 s46, s46, s47\n\t" "s_xor_b32 s47, s45, s44\n\t" "s_or_b32 s48, s47, s46\n\t" "s_cbranch_scc0 .Lf_%=\n\t" "s_cmp_eq_u32 s47, 0\n\t" \
  "s_cbranch_scc1 .Lfd_%=\n\t" "s_mul_i32 s40, s45, 2560\n\t" "s_add_u32 s40, s40, 5123584\n\t" "s_add_u32 s40, s24, s40\n\t" "s_addc_u32 s41, s25, 0\n\t" \
  "global_load_dwordx4 v[44:47], v2, s[40:41] sc1\n\t" "global_load_dword v48, v3, s[40:41] offset:2048 sc1\n\t" "s_add_u32 s49, s45, 1\n\t" "v_mov_b32 v50, s49\n\t" "v_mov_b32 v51, s46\n\t" \
  "s_cmp_lt_i32 s39, 0\n\t" "s_waitcnt vmcnt(0)\n\t" "ds_write_b128 v5, v[44:47]\n\t" "ds_write_b32 v4, v48\n\t" "s_cbranch_scc1 .Lfd_%=\n\t" "v_max3_u32 v49, v44, v45, v46\n\t" \
  "v_max3_u32 v49, v49, v47, v48\n\t" "s_movk_i32 s44, 99\n\t" "v_cmp_eq_u32 vcc, -1, v49\n\t" "s_cbranch_vccnz .Lf_%=\n\t" "s_mov_b32 s44, s45\n\t" "s_cmp_eq_u32 s46, 0\n\t" \
  "s_cbranch_scc1 .Lf_%=\n\t" ".Lfd_%=:\n\t" "s_waitcnt lgkmcnt(0)\n\t" "s_barrier\n\t" "s_barrier\n\t" "s_branch .Lend_%=\n\t" ".Lcb_%=:\n\t" "s_cmp_ge_u32 s29, 20\n\t" "s_cbranch_scc1 .Lend_%=\n\t" \
  "global_load_dwordx4 v[20:23], v32, s[14:15]\n\t" "v_lshlrev_b32 v6, 2, v8\n\t" "global_load_dword v24, v6, s[14:15] offset:1024\n\t" "v_add_u32 v5, 159744, v32\n\t" "v_add_u32 v4, 160768, v6\n\t" \
  "s_lshl1_add_u32 s40, s29, s33\n\t" "v_and_b32 v1, 3, v8\n\t" "v_lshrrev_b32 v2, 2, v8\n\t" "v_and_b32 v3, 4, v2\n\t" "v_lshrrev_b32 v7, 4, v8\n\t" "v_and_b32 v7, 2, v7\n\t" \
  "v_lshl_add_u32 v3, v1, 3, v3\n\t" "v_add_u32 v3, v3, v7\n\t" "v_mul_u32_u24 v2, 2560, v2\n\t" "v_lshl_add_u32 v2, v1, 4, v2\n\t" "s_lshl_b32 s41, s40, 5\n\t" "s_add_u32 s42, s41, 159744\n\t" \
  "v_add_u32 v1, s42, v3\n\t" "s_lshl_b32 s42, s40, 6\n\t" "s_add_u32 s42, s42, 5241344\n\t" "s_add_u32 s44, s24, s42\n\t" "s_addc_u32 s45, s25, 0\n\t" "s_add_u32 s46, s44, 40960\n\t" \
  "s_addc_u32 s47, s45, 0\n\t" "v_mov_b64 v[64:65], 0\n\t" "v_mov_b64 v[66:67], 0\n\t" "s_waitcnt vmcnt(0)\n\t" "s_barrier\n\t" "s_barrier\n\t" "s_memrealtime s[64:65]\n\t" \
  "ds_read_b32 v39, v37 offset:32\n\t" "ds_read_b128 v[44:47], v5\n\t" "ds_read_b32 v48, v4\n\t" "ds_read_u16_d16_hi v49, v1\n\t" "s_waitcnt lgkmcnt(0)\n\t" "v_readfirstlane_b32 s42, v39\n\t" \
  "v_dot2_f32_bf16 v50, v48, v24, 0\n\t" "v_dot2_f32_bf16 v50, v44, v20, v50\n\t" "v_dot2_f32_bf16 v50, v45, v21, v50\n\t" "v_dot2_f32_bf16 v50, v46, v22, v50\n\t" \
  "v_dot2_f32_bf16 v50, v47, v23, v50\n\t" "s_bfe_u32 s43, s42, 0x60006\n\t" "s_lshr_b32 s56, s43, 1\n\t" "s_mul_i32 s56, s56, 89\n\t" "s_and_b32 s57, s43, 1\n\t" "s_mul_i32 s57, s57, 48\n\t" \
  "s_add_u32 s56, s56, s57\n\t" "s_and_b32 s42, s42, 63\n\t" "s_add_u32 s56, s56, s42\n\t" "s_sub_u32 s56, s56, 6\n\t" "v_add_f32_dpp v50, v50, v50 quad_perm:[1,0,3,2] row_mask:0xf bank_mask:0xf\n\t" \
  "s_nop 1\n\t" "v_add_f32_dpp v50, v50, v50 quad_perm:[2,3,0,1] row_mask:0xf bank_mask:0xf\n\t" "s_nop 1\n\t" "v_add_f32_dpp v50, v50, v50 row_half_mirror row_mask:0xf bank_mask:0xf\n\t" \
  "s_nop 1\n\t" "v_add_f32_dpp v50, v50, v50 row_mirror row_mask:0xf bank_mask:0xf\n\t" "s_bfe_u32 s57, s56, 0x1000a\n\t" "s_lshl1_add_u32 s56, s56, s57\n\t" "s_and_b32 s56, s56, 2047\n\t" \
  "v_mov_b32 v51, v50\n\t" "s_mul_i32 s56, s56, 1280\n\t" "s_add_u32 s56, s56, s41\n\t" "v_permlane32_swap_b32 v50, v51\n\t" "v_add_f32 v50, v50, v51\n\t" "v_mov_b32 v51, v50\n\t" \
  "s_add_u32 s56, s22, s56\n\t" "s_addc_u32 s57, s23, 0\n\t" "v_permlane16_swap_b32 v50, v51\n\t" "v_add_f32 v50, v50, v51\n\t" "v_cvt_pk_bf16_f32 v50, v50, v50\n\t" "v_lshlrev_b32 v50, 16, v50\n\t" \
  "v_mul_f32 v50, 0xbfb8aa3b, v50\n\t" "v_exp_f32 v50, v50\n\t" "s_mul_i32 s43, s43, 2560\n\t" "s_lshr_b32 s42, s40, 2\n\t" "s_lshl_b32 s42, s42, 7\n\t" "s_add_u32 s43, s43, s42\n\t" \
  "s_add_u32 s43, s43, s41\n\t" "v_add_f32 v50, 1.0, v50\n\t" "v_rcp_f32 v50, v50\n\t" "s_add_u32 s43, s43, 5123712\n\t" "s_add_u32 s58, s24, s43\n\t" "s_addc_u32 s59, s25, 0\n\t" \
  "v_cvt_pk_bf16_f32 v50, v50, v50\n\t" "v_lshlrev_b32 v50, 16, v50\n\t" "v_and_b32 v49, 0xffff0000, v49\n\t" ".Ltc_%=:\n\t" "s_memrealtime s[78:79]\n\t" "s_waitcnt lgkmcnt(0)\n\t" \
  "s_sub_u32 s78, s78, s64\n\t" "s_cmp_lt_u32 s78, 60\n\t" "s_cbranch_scc1 .Ltc_%=\n\t" ".Lcp_%=:\n\t" "global_load_short_d16_hi v51, v3, s[58:59] sc1\n\t" \
  "global_load_dwordx4 v[60:63], v2, s[44:45] sc1\n\t" "s_mov_b32 exec_hi, 0\n\t" "global_load_dwordx4 v[64:67], v2, s[46:47] sc1\n\t" "s_mov_b32 exec_hi, -1\n\t" "s_sub_u32 s39, s39, 1\n\t" \
  "s_cmp_lt_i32 s39, 0\n\t" "s_waitcnt vmcnt(0)\n\t" "v_pk_add_f32 v[60:61], v[60:61], v[64:65]\n\t" "v_pk_add_f32 v[62:63], v[62:63], v[66:67]\n\t" "v_and_b32 v51, 0xffff0000, v51\n\t" \
  "v_add_f32_dpp v60, v60, v60 row_ror:4 row_mask:0xf bank_mask:0xf\n\t" "v_add_f32_dpp v61, v61, v61 row_ror:4 row_mask:0xf bank_mask:0xf\n\t" \
  "v_add_f32_dpp v62, v62, v62 row_ror:4 row_mask:0xf bank_mask:0xf\n\t" "v_add_f32_dpp v63, v63, v63 row_ror:4 row_mask:0xf bank_mask:0xf\n\t" \
  "v_add_f32_dpp v60, v60, v60 row_ror:8 row_mask:0xf bank_mask:0xf\n\t" "v_add_f32_dpp v61, v61, v61 row_ror:8 row_mask:0xf bank_mask:0xf\n\t" \
  "v_add_f32_dpp v62, v62, v62 row_ror:8 row_mask:0xf bank_mask:0xf\n\t" "v_add_f32_dpp v63, v63, v63 row_ror:8 row_mask:0xf bank_mask:0xf\n\t" "v_permlane32_swap_b32 v60, v61\n\t" "s_nop 0\n\t" \
  "v_permlane32_swap_b32 v62, v63\n\t" "v_add_f32 v60, v60, v61\n\t" "v_add_f32 v62, v62, v63\n\t" "s_nop 1\n\t" "v_permlane16_swap_b32 v60, v62\n\t" "v_add_f32 v60, v60, v62\n\t" \
  "v_cvt_pk_bf16_f32 v60, v60, v60\n\t" "v_lshlrev_b32 v60, 16, v60\n\t" "v_mul_f32 v60, v60, v50\n\t" "v_cvt_pk_bf16_f32 v60, v60, v60\n\t" "v_lshlrev_b32 v60, 16, v60\n\t" \
  "v_add_f32 v60, v60, v49\n\t" "v_cvt_pk_bf16_f32 v60, v60, v60\n\t" "v_lshlrev_b32 v60, 16, v60\n\t" "v_add_f32 v60, v60, v51\n\t" "s_cbranch_scc1 .Lcq_%=\n\t" "v_cmp_u_f32 vcc, v60, v60\n\t" \
  "s_cbranch_vccnz .Lcp_%=\n\t" ".Lcq_%=:\n\t" "v_cvt_pk_bf16_f32 v60, v60, v60\n\t" "s_mov_b32 exec_lo, 0xf000f\n\t" "s_mov_b32 exec_hi, 0xf000f\n\t" "global_store_short v3, v60, s[56:57]\n\t" \
  "s_mov_b64 exec, -1\n\t" ".Lend_%=:\n\t"

extern "C" __global__ __launch_bounds__(768, 3) void fused_block(const u16* x, const u16* wqkv, const u16* wo, const u16* w_r,
                                                                 const u16* w_gate, const u16* w_up, const u16* w_down, u16* out,
                                                                 char* ws) {
  u32 tid = threadIdx.x;
  asm volatile(BODY
    : "+{v0}"(tid)
    : [ka] "s"(__builtin_amdgcn_kernarg_segment_ptr()), [bid] "s"(blockIdx.x)
    : "memory", "scc", "vcc", "v1", "v2", "v3", "v4", "v5", "v6", "v7", "v8", "v9", CL10(1), CL10(2), CL10(3), CL10(4), CL10(5), CL10(6),
      CL10(7), CL10(8), CL10(9), CL10(10), CL10(11), CL10(12), CL10(13), CL10(14), CL10(15), CL8(16), "s8", "s9", SL10(1), SL10(2), "s30",
      "s31", "s33", "s34", "s35", "s36", "s37", "s38", "s39", SL10(4), SL10(5), SL10(6), SL10(7), SL10(8), SL10(9));
}

extern "C" void kernel_launch(void* const* d_in, const int* in_sizes, int n_in,
                              void* d_out, int out_size, void* d_ws, size_t ws_size,
                              hipStream_t stream) {
  const u16* x = (const u16*)d_in[0];
  const u16* wqkv = (const u16*)d_in[1];
  const u16* wo = (const u16*)d_in[2];
  const u16* w_r = (const u16*)d_in[4];
  const u16* w_gate = (const u16*)d_in[5];
  const u16* w_up = (const u16*)d_in[6];
  const u16* w_down = (const u16*)d_in[7];
  (void)hipFuncSetAttribute((const void*)fused_block, hipFuncAttributeMaxDynamicSharedMemorySize, LDS_TOTAL);
  fused_block<<<256, 768, LDS_TOTAL, stream>>>(x, wqkv, wo, w_r, w_gate, w_up, w_down, (u16*)d_out, (char*)d_ws);
}
```
